```python
import torch

def _softmax(x, axis=-1):
    return torch.softmax(x, dim=axis)
def _gelu(x):
    return torch.nn.functional.gelu(x, approximate="tanh")
def _silu(x):
    return torch.nn.functional.silu(x)
def _sigmoid(x):
    return torch.sigmoid(x)
def _layernorm(x, axis=-1, eps=1e-5):
    m = x.mean(dim=axis, keepdim=True)
    v = x.var(dim=axis, keepdim=True, unbiased=False)
    return (x - m) / torch.sqrt(v + eps)
def _scatter_add(tgt, idx, src, axis=0):
    return tgt.clone().index_add_(axis, idx, src)
def _segment_sum(src, seg, n):
    out = torch.zeros((n,) + src.shape[1:], dtype=src.dtype)
    return out.index_add_(0, seg, src)
def _pad(x, pw):
    flat = tuple(v for pair in reversed(pw) for v in pair)
    return torch.nn.functional.pad(x, flat)

kernel_name = 'standalone__detection_seg_fpn_lateral__vision'

def setup_inputs():

    _w = (lambda a: (lambda t: t.to(torch.bfloat16) if t.is_floating_point() else t)(torch.as_tensor(a)))
    H = W = (384 // 2) * 2
    C = 384
    k = 1
    return {
        "c_fine": _w((lambda *s: torch.randn(*s, dtype=torch.bfloat16) * 0.05)(1, H, W, C)),
        "p_coarse": _w((lambda *s: torch.randn(*s, dtype=torch.bfloat16) * 0.05)(1, H // 2, W // 2, C)),
        "w_lat": _w((lambda *s: torch.randn(*s, dtype=torch.bfloat16) * 0.05)(C, C)),
        "w_out": _w((lambda *s: torch.randn(*s, dtype=torch.bfloat16) * 0.05)(k, k, C, C)),
    }

def reference(c_fine, p_coarse, w_lat, w_out):
    B, H, W, C = c_fine.shape
    k = 1
    lat = torch.einsum("bhwc,cd->bhwd", c_fine, w_lat)
    up = (lambda x, n, axis: torch.repeat_interleave(x, n, dim=axis))((lambda x, n, axis: torch.repeat_interleave(x, n, dim=axis))(p_coarse, 2, axis=1), 2, axis=2)
    m = lat + up
    pad = k // 2
    mp = _pad(m, ((0, 0), (pad, pad), (pad, pad), (0, 0)))
    o = None
    for di in range(k):
        for dj in range(k):
            t = torch.einsum("bhwc,cd->bhwd", mp[:, di:di + H, dj:dj + W, :], w_out[di, dj])
            o = t if o is None else o + t
    return o
```

```cpp
#include <hip/hip_runtime.h>
#include <hip/hip_bf16.h>

__global__ __launch_bounds__(256, 1) __attribute__((aligned(4096))) void fpn_kernel(const char* cf, const char* pc, const char* w1, char* out, char* ws, const char* w2) {
  const void* ka = (const void*)__builtin_amdgcn_kernarg_segment_ptr();
  unsigned b = blockIdx.x, t = threadIdx.x;
  asm volatile(
"s_load_dwordx8 s[20:27], s[0:1], 0x0\n"
"s_load_dwordx2 s[28:29], s[0:1], 0x20\n"
"s_load_dwordx2 s[66:67], s[0:1], 0x28\n"
"s_memrealtime s[84:85]\n"
"v_readfirstlane_b32 s76, v0\n"
"v_and_b32 v1, 63, v0\n"
"s_mov_b32 s51, s2\n"
"v_lshlrev_b32 v239, 4, v1\n"
"s_lshr_b32 s76, s76, 6\n"
"v_lshrrev_b32 v2, 4, v1\n"
"s_movk_i32 s4, 0x300\n"
"v_and_b32 v3, 15, v1\n"
"s_lshl_b32 s3, s76, 2\n"
"s_cmpk_ge_u32 s51, 144\n"
"s_cbranch_scc1 LU1\n"
"s_mul_i32 s9, s51, 0xab\n"
"v_lshrrev_b32 v110, 2, v1\n"
"s_lshr_b32 s9, s9, 11\n"
"v_and_b32 v111, 48, v239\n"
"s_mul_i32 s10, s9, 12\n"
"v_mad_u32_u24 v100, v110, s4, v111\n"
"s_sub_u32 s10, s51, s10\n"
"s_mul_i32 s11, s76, 0x12000\n"
"s_lshl_b32 s12, s9, 6\n"
"s_add_u32 s12, s12, s11\n"
"s_mul_i32 s13, s10, 0x6000\n"
"s_mul_i32 s14, s76, 0xc0\n"
"s_mul_i32 s75, s76, 0x1800\n"
"s_add_u32 s13, s13, s14\n"
"s_add_u32 s75, s75, 0x1b000\n"
"s_lshr_b32 s86, s51, 3\n"
"s_mul_i32 s87, s86, 0x56\n"
"s_lshr_b32 s87, s87, 8\n"
"s_mul_i32 s87, s87, 3\n"
"s_sub_u32 s86, s86, s87\n"
"s_lshl_b32 s86, s86, 11\n"
"s_add_u32 s87, s86, 0x800\n"
"s_cmp_eq_u32 s87, 0x1800\n"
"s_cselect_b32 s87, 0, s87\n"
"s_add_u32 s90, s87, 0x800\n"
"s_cmp_eq_u32 s90, 0x1800\n"
"s_cselect_b32 s90, 0, s90\n"
"v_bitop3_b32 v110, s3, v1, v2 bitop3:0x96\n"
"s_mul_i32 s5, s51, 0x3000\n"
"v_lshlrev_b32 v110, 4, v110\n"
"s_mul_i32 s6, s76, 0xc00\n"
"v_and_b32 v110, 0xf0, v110\n"
"s_lshl_b32 s31, s76, 10\n"
"v_mad_u32_u24 v236, v2, s4, v110\n"
"s_add_u32 s5, s5, s6\n"
"s_add_u32 s30, s31, 0x12000\n"
"s_movk_i32 s7, 0x240\n"
"v_mul_u32_u24 v110, 0xab, v239\n"
"v_add_u32 v111, 0x400, v239\n"
"v_add_u32 v112, 0x800, v239\n"
"v_lshrrev_b32 v110, 15, v110\n"
"v_mul_u32_u24 v113, 0xab, v111\n"
"v_mul_u32_u24 v114, 0xab, v112\n"
"v_mad_u32_u24 v240, v110, s7, v239\n"
"v_lshrrev_b32 v113, 15, v113\n"
"v_lshrrev_b32 v114, 15, v114\n"
"s_mul_i32 s74, s76, 0x1800\n"
"v_mad_u32_u24 v241, v113, s7, v239\n"
"v_mad_u32_u24 v242, v114, s7, v239\n"
"s_mul_i32 s8, s76, 0xc0\n"
"v_mov_b32 v0, 0\n"
"s_waitcnt lgkmcnt(0)\n"
"s_mov_b32 s83, s84\n"
"global_load_dwordx2 v[252:253], v0, s[28:29] sc1\n"
"s_add_u32 s68, s66, s12\n"
"s_addc_u32 s69, s67, 0\n"
"s_add_u32 m0, s75, s86\n"
"s_mul_i32 s18, s86, 12\n"
"s_add_u32 s56, s68, s18\n"
"s_addc_u32 s57, s69, 0\n"
"global_load_lds_dwordx4 v100, s[56:57]\n"
"s_add_u32 m0, m0, 0x400\n"
"s_add_u32 s56, s56, 0x3000\n"
"s_addc_u32 s57, s57, 0\n"
"global_load_lds_dwordx4 v100, s[56:57]\n"
"s_add_u32 m0, s74, 0xc000\n"
"s_add_u32 s70, s24, s13\n"
"s_addc_u32 s71, s25, 0\n"
"global_load_lds_dwordx4 v240, s[70:71] offset:0\n"
"global_load_lds_dwordx4 v241, s[70:71] offset:1024\n"
"global_load_lds_dwordx4 v242, s[70:71] offset:2048\n"
"s_add_u32 m0, s74, 0xcc00\n"
"s_add_u32 s70, s70, 0x3000\n"
"s_addc_u32 s71, s71, 0\n"
"global_load_lds_dwordx4 v240, s[70:71] offset:0\n"
"global_load_lds_dwordx4 v241, s[70:71] offset:1024\n"
"global_load_lds_dwordx4 v242, s[70:71] offset:2048\n"
"v_lshlrev_b32 v110, 1, v1\n"
"v_and_b32 v111, 3, v1\n"
"s_movk_i32 s14, 0xc0\n"
"v_and_or_b32 v110, v110, 24, v111\n"
"v_and_b32 v111, 48, v1\n"
"v_mad_u32_u24 v110, v110, s14, v111\n"
"v_add_u32 v101, s74, v110\n"
"v_bfe_u32 v110, v1, 2, 2\n"
"s_mul_i32 s14, s9, 0x56\n"
"v_lshl_or_b32 v110, v2, 3, v110\n"
"s_lshr_b32 s14, s14, 8\n"
"v_and_b32 v111, 48, v239\n"
"s_mul_i32 s15, s14, 3\n"
"v_lshl_add_u32 v110, v110, 6, v111\n"
"s_sub_u32 s15, s9, s15\n"
"v_add_u32 v102, s75, v110\n"
"s_lshl_b32 s15, s15, 1\n"
"s_and_b32 s16, s76, 1\n"
"v_add_u32 v110, 0x21000, v239\n"
"s_add_u32 s15, s15, s16\n"
"v_lshl_add_u32 v103, s76, 12, v110\n"
"s_mul_i32 s15, s15, 12\n"
"v_lshl_add_u32 v104, s16, 11, v110\n"
"s_mul_i32 s14, s14, 72\n"
"s_add_u32 s15, s15, s10\n"
"s_add_u32 s15, s15, s14\n"
"s_lshl_b32 s15, s15, 10\n"
"s_add_u32 s15, s15, 0x10000\n"
"s_add_u32 s72, s28, s15\n"
"s_addc_u32 s73, s29, 0\n"
"v_bfe_u32 v110, v1, 2, 2\n"
"s_movk_i32 s14, 0xc0\n"
"v_lshl_or_b32 v110, v2, 3, v110\n"
"v_and_b32 v111, 48, v239\n"
"v_lshlrev_b32 v112, 8, v3\n"
"v_mad_u32_u24 v110, v110, s14, v111\n"
"v_xor_b32 v113, v2, v3\n"
"v_add_u32 v243, s74, v110\n"
"v_add_u32 v249, 0xfffee000, v112\n"
"v_add_u32 v112, 0x12000, v112\n"
"v_lshl_add_u32 v232, v113, 4, v112\n"
"v_xor_b32 v233, 64, v232\n"
"v_xor_b32 v234, 0x80, v232\n"
"v_xor_b32 v235, 0xc0, v232\n"
"s_lshl_b32 s77, s51, 4\n"
"s_add_u32 s77, s77, 0x1000\n"
"s_lshl_b32 s14, s76, 1\n"
"v_lshrrev_b32 v110, 5, v1\n"
"v_add_u32 v111, s3, v2\n"
"v_bitop3_b32 v110, s14, v1, v110 bitop3:0x96\n"
"s_mul_i32 s15, s76, 0x240\n"
"v_lshlrev_b32 v110, 4, v110\n"
"v_and_b32 v112, 0x70, v239\n"
"v_and_b32 v110, 0xf0, v110\n"
"v_add_u32 v113, s15, v239\n"
"v_mad_u32_u24 v237, v111, s4, v110\n"
"v_cmp_gt_u32 vcc, 36, v1\n"
"v_xor_b32 v238, 0x80, v237\n"
"s_mul_i32 s14, s76, 12\n"
"v_cndmask_b32 v112, v112, v113, vcc\n"
"v_add_u32 v238, 0x3000, v238\n"
"v_add_u32 v248, 0x1000, v112\n"
"v_add_u32 v110, s14, v2\n"
"v_mul_u32_u24 v112, 0x300, v3\n"
"v_add_u32 v111, 4, v110\n"
"v_add_u32 v112, 0x18000, v112\n"
"v_bitop3_b32 v110, v110, v1, 7 bitop3:0x78\n"
"v_bitop3_b32 v111, v111, v1, 7 bitop3:0x78\n"
"s_add_u32 s14, s6, 0x18000\n"
"v_lshl_add_u32 v116, v110, 4, v112\n"
"v_lshl_add_u32 v117, v111, 4, v112\n"
"v_add_u32 v118, s14, v239\n"
"s_movk_i32 s14, 0x600\n"
"v_add_u32 v110, 0, v1\n"
"v_mul_u32_u24 v111, 0xab, v110\n"
"v_lshrrev_b32 v111, 13, v111\n"
"v_mul_u32_u24 v112, 48, v111\n"
"v_add_u32 v111, s3, v111\n"
"v_sub_u32 v110, v110, v112\n"
"v_bitop3_b32 v110, v110, v111, 7 bitop3:0x78\n"
"v_lshlrev_b32 v110, 4, v110\n"
"v_mad_u32_u24 v119, v111, s14, v110\n"
"v_add_u32 v110, 64, v1\n"
"v_mul_u32_u24 v111, 0xab, v110\n"
"v_lshrrev_b32 v111, 13, v111\n"
"v_mul_u32_u24 v112, 48, v111\n"
"v_add_u32 v111, s3, v111\n"
"v_sub_u32 v110, v110, v112\n"
"v_bitop3_b32 v110, v110, v111, 7 bitop3:0x78\n"
"v_lshlrev_b32 v110, 4, v110\n"
"v_mad_u32_u24 v120, v111, s14, v110\n"
"v_add_u32 v110, 128, v1\n"
"v_mul_u32_u24 v111, 0xab, v110\n"
"v_lshrrev_b32 v111, 13, v111\n"
"v_mul_u32_u24 v112, 48, v111\n"
"v_add_u32 v111, s3, v111\n"
"v_sub_u32 v110, v110, v112\n"
"v_bitop3_b32 v110, v110, v111, 7 bitop3:0x78\n"
"v_lshlrev_b32 v110, 4, v110\n"
"v_mad_u32_u24 v121, v111, s14, v110\n"
"s_add_u32 s15, s3, 0x27000\n"
"v_lshlrev_b32 v110, 2, v1\n"
"s_lshl_b32 s14, s76, 11\n"
"v_and_b32 v110, 12, v110\n"
"s_add_u32 s14, s14, 0x25000\n"
"v_mov_b32 v122, s15\n"
"v_add_u32 v123, 0x27000, v110\n"
"v_lshl_add_u32 v250, v1, 5, s14\n"
"v_mov_b32 v110, 0\n"
"ds_write_b128 v250, v[116:119]\n"
"ds_write_b128 v250, v[120:123] offset:16\n"
"ds_write_b32 v122, v110\n"
"s_add_u32 m0, s75, s87\n"
"s_mul_i32 s18, s87, 12\n"
"s_add_u32 s56, s68, s18\n"
"s_addc_u32 s57, s69, 0\n"
"global_load_lds_dwordx4 v100, s[56:57]\n"
"s_add_u32 m0, m0, 0x400\n"
"s_add_u32 s56, s56, 0x3000\n"
"s_addc_u32 s57, s57, 0\n"
"global_load_lds_dwordx4 v100, s[56:57]\n"
"s_add_u32 m0, s75, s90\n"
"s_mul_i32 s18, s90, 12\n"
"s_add_u32 s56, s68, s18\n"
"s_addc_u32 s57, s69, 0\n"
"global_load_lds_dwordx4 v100, s[56:57]\n"
"s_add_u32 m0, m0, 0x400\n"
"s_add_u32 s56, s56, 0x3000\n"
"s_addc_u32 s57, s57, 0\n"
"global_load_lds_dwordx4 v100, s[56:57]\n"
"s_add_u32 s22, s22, s5\n"
"s_addc_u32 s23, s23, 0\n"
"s_add_u32 m0, s30, 0x0\n"
"s_add_u32 s32, s22, 0x0\n"
"s_addc_u32 s33, s23, 0\n"
"global_load_lds_dwordx4 v236, s[32:33] offset:0 nt\n"
"s_add_u32 m0, s30, 0x1000\n"
"s_add_u32 s34, s22, 0x300000\n"
"s_addc_u32 s35, s23, 0\n"
"global_load_lds_dwordx4 v236, s[34:35] offset:0 nt\n"
"s_add_u32 m0, s30, 0x2000\n"
"s_add_u32 s36, s22, 0x600000\n"
"s_addc_u32 s37, s23, 0\n"
"global_load_lds_dwordx4 v236, s[36:37] offset:0 nt\n"
"s_add_u32 m0, s30, 0x3000\n"
"s_add_u32 s38, s22, 0x900000\n"
"s_addc_u32 s39, s23, 0\n"
"global_load_lds_dwordx4 v236, s[38:39] offset:0 nt\n"
"s_add_u32 m0, s30, 0x4000\n"
"s_add_u32 s40, s22, 0xc00000\n"
"s_addc_u32 s41, s23, 0\n"
"global_load_lds_dwordx4 v236, s[40:41] offset:0 nt\n"
"s_add_u32 m0, s30, 0x5000\n"
"s_add_u32 s42, s22, 0xf00000\n"
"s_addc_u32 s43, s23, 0\n"
"global_load_lds_dwordx4 v236, s[42:43] offset:0 nt\n"
"s_add_u32 m0, s30, 0x6000\n"
"s_add_u32 s44, s22, 0x1200000\n"
"s_addc_u32 s45, s23, 0\n"
"global_load_lds_dwordx4 v236, s[44:45] offset:0 nt\n"
"s_add_u32 m0, s30, 0x7000\n"
"s_add_u32 s46, s22, 0x1500000\n"
"s_addc_u32 s47, s23, 0\n"
"global_load_lds_dwordx4 v236, s[46:47] offset:0 nt\n"
"s_add_u32 m0, s30, 0x8000\n"
"s_add_u32 s48, s22, 0x1800000\n"
"s_addc_u32 s49, s23, 0\n"
"global_load_lds_dwordx4 v236, s[48:49] offset:0 nt\n"
"s_branch LV0\n"
"LU1:\n"
"s_lshr_b32 s9, s51, 3\n"
"s_lshl_b32 s9, s9, 2\n"
"s_add_u32 s9, s9, s76\n"
"s_sub_u32 s9, s9, 72\n"
"s_mul_i32 s10, s9, 12\n"
"v_min_u32 v106, 11, v1\n"
"s_mul_i32 s9, s9, 42\n"
"v_add_lshl_u32 v106, s10, v106, 7\n"
"v_min_u32 v105, 41, v1\n"
"v_add_u32 v105, s9, v105\n"
"v_min_u32 v105, 0x8ff, v105\n"
"v_lshlrev_b32 v105, 7, v105\n"
"s_getpc_b64 s[56:57]\n"
"LC0_1:\n"
"v_min_u32 v106, LCEND-LC0_1-4, v106\n"
"global_load_dword v106, v106, s[56:57]\n"
"v_bitop3_b32 v110, s3, v1, v2 bitop3:0x96\n"
"s_mul_i32 s5, s51, 0x3000\n"
"v_lshlrev_b32 v110, 4, v110\n"
"s_mul_i32 s6, s76, 0xc00\n"
"v_and_b32 v110, 0xf0, v110\n"
"s_lshl_b32 s31, s76, 10\n"
"v_mad_u32_u24 v236, v2, s4, v110\n"
"s_add_u32 s5, s5, s6\n"
"s_add_u32 s30, s31, 0x12000\n"
"s_movk_i32 s7, 0x240\n"
"v_mul_u32_u24 v110, 0xab, v239\n"
"v_add_u32 v111, 0x400, v239\n"
"v_add_u32 v112, 0x800, v239\n"
"v_lshrrev_b32 v110, 15, v110\n"
"v_mul_u32_u24 v113, 0xab, v111\n"
"v_mul_u32_u24 v114, 0xab, v112\n"
"v_mad_u32_u24 v240, v110, s7, v239\n"
"v_lshrrev_b32 v113, 15, v113\n"
"v_lshrrev_b32 v114, 15, v114\n"
"s_mul_i32 s74, s76, 0x1800\n"
"v_mad_u32_u24 v241, v113, s7, v239\n"
"v_mad_u32_u24 v242, v114, s7, v239\n"
"s_mul_i32 s8, s76, 0xc0\n"
"v_mov_b32 v0, 0\n"
"s_waitcnt lgkmcnt(0)\n"
"s_mov_b32 s83, s84\n"
"global_load_dwordx2 v[252:253], v0, s[28:29] sc1\n"
"v_bfe_u32 v110, v1, 2, 2\n"
"s_movk_i32 s14, 0xc0\n"
"v_lshl_or_b32 v110, v2, 3, v110\n"
"v_and_b32 v111, 48, v239\n"
"v_lshlrev_b32 v112, 8, v3\n"
"v_mad_u32_u24 v110, v110, s14, v111\n"
"v_xor_b32 v113, v2, v3\n"
"v_add_u32 v243, s74, v110\n"
"v_add_u32 v249, 0xfffee000, v112\n"
"v_add_u32 v112, 0x12000, v112\n"
"v_lshl_add_u32 v232, v113, 4, v112\n"
"v_xor_b32 v233, 64, v232\n"
"v_xor_b32 v234, 0x80, v232\n"
"v_xor_b32 v235, 0xc0, v232\n"
"s_lshl_b32 s77, s51, 3\n"
"s_add_u32 s77, s77, 0x2000\n"
"s_lshl_b32 s14, s76, 1\n"
"v_lshrrev_b32 v110, 5, v1\n"
"v_add_u32 v111, s3, v2\n"
"v_bitop3_b32 v110, s14, v1, v110 bitop3:0x96\n"
"s_mul_i32 s15, s76, 0x240\n"
"v_lshlrev_b32 v110, 4, v110\n"
"v_and_b32 v112, 0x70, v239\n"
"v_and_b32 v110, 0xf0, v110\n"
"v_add_u32 v113, s15, v239\n"
"v_mad_u32_u24 v237, v111, s4, v110\n"
"v_cmp_gt_u32 vcc, 36, v1\n"
"v_xor_b32 v238, 0x80, v237\n"
"s_mul_i32 s14, s76, 12\n"
"v_cndmask_b32 v112, v112, v113, vcc\n"
"v_add_u32 v238, 0x3000, v238\n"
"v_add_u32 v248, 0x1000, v112\n"
"v_add_u32 v110, s14, v2\n"
"v_mul_u32_u24 v112, 0x300, v3\n"
"v_add_u32 v111, 4, v110\n"
"v_add_u32 v112, 0x18000, v112\n"
"v_bitop3_b32 v110, v110, v1, 7 bitop3:0x78\n"
"v_bitop3_b32 v111, v111, v1, 7 bitop3:0x78\n"
"s_add_u32 s14, s6, 0x18000\n"
"v_lshl_add_u32 v116, v110, 4, v112\n"
"v_lshl_add_u32 v117, v111, 4, v112\n"
"v_add_u32 v118, s14, v239\n"
"s_movk_i32 s14, 0x600\n"
"v_add_u32 v110, 0, v1\n"
"v_mul_u32_u24 v111, 0xab, v110\n"
"v_lshrrev_b32 v111, 13, v111\n"
"v_mul_u32_u24 v112, 48, v111\n"
"v_add_u32 v111, s3, v111\n"
"v_sub_u32 v110, v110, v112\n"
"v_bitop3_b32 v110, v110, v111, 7 bitop3:0x78\n"
"v_lshlrev_b32 v110, 4, v110\n"
"v_mad_u32_u24 v119, v111, s14, v110\n"
"v_add_u32 v110, 64, v1\n"
"v_mul_u32_u24 v111, 0xab, v110\n"
"v_lshrrev_b32 v111, 13, v111\n"
"v_mul_u32_u24 v112, 48, v111\n"
"v_add_u32 v111, s3, v111\n"
"v_sub_u32 v110, v110, v112\n"
"v_bitop3_b32 v110, v110, v111, 7 bitop3:0x78\n"
"v_lshlrev_b32 v110, 4, v110\n"
"v_mad_u32_u24 v120, v111, s14, v110\n"
"v_add_u32 v110, 128, v1\n"
"v_mul_u32_u24 v111, 0xab, v110\n"
"v_lshrrev_b32 v111, 13, v111\n"
"v_mul_u32_u24 v112, 48, v111\n"
"v_add_u32 v111, s3, v111\n"
"v_sub_u32 v110, v110, v112\n"
"v_bitop3_b32 v110, v110, v111, 7 bitop3:0x78\n"
"v_lshlrev_b32 v110, 4, v110\n"
"v_mad_u32_u24 v121, v111, s14, v110\n"
"s_add_u32 s15, s3, 0x27000\n"
"v_lshlrev_b32 v110, 2, v1\n"
"s_lshl_b32 s14, s76, 11\n"
"v_and_b32 v110, 12, v110\n"
"s_add_u32 s14, s14, 0x25000\n"
"v_mov_b32 v122, s15\n"
"v_add_u32 v123, 0x27000, v110\n"
"v_lshl_add_u32 v250, v1, 5, s14\n"
"v_mov_b32 v110, 0\n"
"ds_write_b128 v250, v[116:119]\n"
"ds_write_b128 v250, v[120:123] offset:16\n"
"ds_write_b32 v122, v110\n"
"s_add_u32 s22, s22, s5\n"
"s_addc_u32 s23, s23, 0\n"
"s_add_u32 m0, s30, 0x0\n"
"s_add_u32 s32, s22, 0x0\n"
"s_addc_u32 s33, s23, 0\n"
"global_load_lds_dwordx4 v236, s[32:33] offset:0 nt\n"
"s_add_u32 m0, s30, 0x1000\n"
"s_add_u32 s34, s22, 0x300000\n"
"s_addc_u32 s35, s23, 0\n"
"global_load_lds_dwordx4 v236, s[34:35] offset:0 nt\n"
"s_add_u32 m0, s30, 0x2000\n"
"s_add_u32 s36, s22, 0x600000\n"
"s_addc_u32 s37, s23, 0\n"
"global_load_lds_dwordx4 v236, s[36:37] offset:0 nt\n"
"s_add_u32 m0, s30, 0x3000\n"
"s_add_u32 s38, s22, 0x900000\n"
"s_addc_u32 s39, s23, 0\n"
"global_load_lds_dwordx4 v236, s[38:39] offset:0 nt\n"
"s_add_u32 m0, s30, 0x4000\n"
"s_add_u32 s40, s22, 0xc00000\n"
"s_addc_u32 s41, s23, 0\n"
"global_load_lds_dwordx4 v236, s[40:41] offset:0 nt\n"
"s_add_u32 m0, s30, 0x5000\n"
"s_add_u32 s42, s22, 0xf00000\n"
"s_addc_u32 s43, s23, 0\n"
"global_load_lds_dwordx4 v236, s[42:43] offset:0 nt\n"
"s_add_u32 m0, s30, 0x6000\n"
"s_add_u32 s44, s22, 0x1200000\n"
"s_addc_u32 s45, s23, 0\n"
"global_load_lds_dwordx4 v236, s[44:45] offset:0 nt\n"
"s_add_u32 m0, s30, 0x7000\n"
"s_add_u32 s46, s22, 0x1500000\n"
"s_addc_u32 s47, s23, 0\n"
"global_load_lds_dwordx4 v236, s[46:47] offset:0 nt\n"
"s_add_u32 m0, s30, 0x8000\n"
"s_add_u32 s48, s22, 0x1800000\n"
"s_addc_u32 s49, s23, 0\n"
"global_load_lds_dwordx4 v236, s[48:49] offset:0 nt\n"
"s_branch LV1\n"
"LV0:\n"
"s_waitcnt vmcnt(21)\n"
"v_readfirstlane_b32 s80, v252\n"
"v_readfirstlane_b32 s81, v253\n"
"s_nop 3\n"
"s_add_u32 s80, s80, 0x7f4a7c15\n"
"s_addc_u32 s81, s81, 0x9e3779b9\n"
"v_mov_b32 v252, s80\n"
"v_mov_b32 v253, s81\n"
"s_waitcnt vmcnt(9)\n"
"ds_read_b128 v[4:7], v101 offset:49152\n"
"ds_read_b128 v[8:11], v101 offset:49920\n"
"ds_read_b64_tr_b16 v[28:29], v102 offset:0\n"
"ds_read_b64_tr_b16 v[30:31], v102 offset:256\n"
"ds_read_b64_tr_b16 v[32:33], v102 offset:8\n"
"ds_read_b64_tr_b16 v[34:35], v102 offset:264\n"
"ds_read_b128 v[12:15], v101 offset:49216\n"
"ds_read_b128 v[16:19], v101 offset:49984\n"
"ds_read_b64_tr_b16 v[36:37], v102 offset:2048\n"
"ds_read_b64_tr_b16 v[38:39], v102 offset:2304\n"
"ds_read_b64_tr_b16 v[40:41], v102 offset:2056\n"
"ds_read_b64_tr_b16 v[42:43], v102 offset:2312\n"
"ds_read_b128 v[20:23], v101 offset:49280\n"
"ds_read_b128 v[24:27], v101 offset:50048\n"
"ds_read_b64_tr_b16 v[44:45], v102 offset:4096\n"
"ds_read_b64_tr_b16 v[46:47], v102 offset:4352\n"
"ds_read_b64_tr_b16 v[48:49], v102 offset:4104\n"
"ds_read_b64_tr_b16 v[50:51], v102 offset:4360\n"
"s_waitcnt lgkmcnt(0)\n"
"v_mfma_f32_16x16x32_bf16 v[52:55], v[4:7], v[28:31], 0\n"
"v_mfma_f32_16x16x32_bf16 v[56:59], v[8:11], v[28:31], 0\n"
"v_mfma_f32_16x16x32_bf16 v[60:63], v[4:7], v[32:35], 0\n"
"v_mfma_f32_16x16x32_bf16 v[64:67], v[8:11], v[32:35], 0\n"
"v_mfma_f32_16x16x32_bf16 v[52:55], v[12:15], v[36:39], v[52:55]\n"
"v_mfma_f32_16x16x32_bf16 v[56:59], v[16:19], v[36:39], v[56:59]\n"
"v_mfma_f32_16x16x32_bf16 v[60:63], v[12:15], v[40:43], v[60:63]\n"
"v_mfma_f32_16x16x32_bf16 v[64:67], v[16:19], v[40:43], v[64:67]\n"
"v_mfma_f32_16x16x32_bf16 v[52:55], v[20:23], v[44:47], v[52:55]\n"
"v_mfma_f32_16x16x32_bf16 v[56:59], v[24:27], v[44:47], v[56:59]\n"
"v_mfma_f32_16x16x32_bf16 v[60:63], v[20:23], v[48:51], v[60:63]\n"
"v_mfma_f32_16x16x32_bf16 v[64:67], v[24:27], v[48:51], v[64:67]\n"
"s_nop 7\n"
"ds_write_b128 v103, v[52:55] offset:0\n"
"ds_write_b128 v103, v[56:59] offset:1024\n"
"ds_write_b128 v103, v[60:63] offset:2048\n"
"ds_write_b128 v103, v[64:67] offset:3072\n"
"s_add_u32 s64, s66, s8\n"
"s_addc_u32 s65, s67, 0\n"
"s_add_u32 m0, s74, 0x0\n"
"s_add_u32 s78, s64, 0x0\n"
"s_addc_u32 s79, s65, 0\n"
"global_load_lds_dwordx4 v240, s[78:79] offset:0\n"
"global_load_lds_dwordx4 v241, s[78:79] offset:1024\n"
"global_load_lds_dwordx4 v242, s[78:79] offset:2048\n"
"s_add_u32 m0, s74, 0xc00\n"
"s_add_u32 s78, s64, 0x3000\n"
"s_addc_u32 s79, s65, 0\n"
"global_load_lds_dwordx4 v240, s[78:79] offset:0\n"
"global_load_lds_dwordx4 v241, s[78:79] offset:1024\n"
"global_load_lds_dwordx4 v242, s[78:79] offset:2048\n"
"s_waitcnt lgkmcnt(0)\n"
"s_barrier\n"
"s_cmp_ge_u32 s76, 2\n"
"s_cbranch_scc1 LJ1_0\n"
"ds_read_b128 v[4:7], v104 offset:0\n"
"ds_read_b128 v[8:11], v104 offset:1024\n"
"ds_read_b128 v[12:15], v104 offset:4096\n"
"ds_read_b128 v[16:19], v104 offset:5120\n"
"ds_read_b128 v[20:23], v104 offset:8192\n"
"ds_read_b128 v[24:27], v104 offset:9216\n"
"ds_read_b128 v[28:31], v104 offset:12288\n"
"ds_read_b128 v[32:35], v104 offset:13312\n"
"s_waitcnt lgkmcnt(0)\n"
"v_add_f32 v4, v4, v12\n"
"v_add_f32 v5, v5, v13\n"
"v_add_f32 v6, v6, v14\n"
"v_add_f32 v7, v7, v15\n"
"v_add_f32 v8, v8, v16\n"
"v_add_f32 v9, v9, v17\n"
"v_add_f32 v10, v10, v18\n"
"v_add_f32 v11, v11, v19\n"
"v_add_f32 v4, v4, v20\n"
"v_add_f32 v5, v5, v21\n"
"v_add_f32 v6, v6, v22\n"
"v_add_f32 v7, v7, v23\n"
"v_add_f32 v8, v8, v24\n"
"v_add_f32 v9, v9, v25\n"
"v_add_f32 v10, v10, v26\n"
"v_add_f32 v11, v11, v27\n"
"v_add_f32 v4, v4, v28\n"
"v_add_f32 v5, v5, v29\n"
"v_add_f32 v6, v6, v30\n"
"v_add_f32 v7, v7, v31\n"
"v_add_f32 v8, v8, v32\n"
"v_add_f32 v9, v9, v33\n"
"v_add_f32 v10, v10, v34\n"
"v_add_f32 v11, v11, v35\n"
"v_cvt_pk_bf16_f32 v40, v4, v5\n"
"v_cvt_pk_bf16_f32 v41, v6, v7\n"
"v_cvt_pk_bf16_f32 v42, v8, v9\n"
"v_cvt_pk_bf16_f32 v43, v10, v11\n"
"LJ1_0:\n"
"s_cselect_b64 exec, 0, -1\n"
"global_store_dwordx4 v239, v[40:43], s[72:73] sc1\n"
"s_mov_b64 exec, -1\n"
"s_add_u32 m0, s74, 0x6000\n"
"s_add_u32 s78, s64, 0x6000\n"
"s_addc_u32 s79, s65, 0\n"
"global_load_lds_dwordx4 v240, s[78:79] offset:0\n"
"global_load_lds_dwordx4 v241, s[78:79] offset:1024\n"
"global_load_lds_dwordx4 v242, s[78:79] offset:2048\n"
"s_add_u32 m0, s74, 0x6c00\n"
"s_add_u32 s78, s64, 0x9000\n"
"s_addc_u32 s79, s65, 0\n"
"global_load_lds_dwordx4 v240, s[78:79] offset:0\n"
"global_load_lds_dwordx4 v241, s[78:79] offset:1024\n"
"global_load_lds_dwordx4 v242, s[78:79] offset:2048\n"
"s_add_u32 m0, s74, 0xc000\n"
"s_add_u32 s78, s64, 0xc000\n"
"s_addc_u32 s79, s65, 0\n"
"global_load_lds_dwordx4 v240, s[78:79] offset:0\n"
"global_load_lds_dwordx4 v241, s[78:79] offset:1024\n"
"global_load_lds_dwordx4 v242, s[78:79] offset:2048\n"
"s_add_u32 m0, s74, 0xcc00\n"
"s_add_u32 s78, s64, 0xf000\n"
"s_addc_u32 s79, s65, 0\n"
"global_load_lds_dwordx4 v240, s[78:79] offset:0\n"
"global_load_lds_dwordx4 v241, s[78:79] offset:1024\n"
"global_load_lds_dwordx4 v242, s[78:79] offset:2048\n"
"s_memrealtime s[84:85]\n"
"s_waitcnt vmcnt(13)\n"
"s_waitcnt lgkmcnt(0)\n"
"ds_read_b64_tr_b16 a[0:1], v243 offset:0\n"
"ds_read_b64_tr_b16 a[2:3], v243 offset:768\n"
"ds_read_b64_tr_b16 a[4:5], v243 offset:8\n"
"ds_read_b64_tr_b16 a[6:7], v243 offset:776\n"
"ds_read_b64_tr_b16 a[8:9], v243 offset:64\n"
"ds_read_b64_tr_b16 a[10:11], v243 offset:832\n"
"ds_read_b64_tr_b16 a[12:13], v243 offset:72\n"
"ds_read_b64_tr_b16 a[14:15], v243 offset:840\n"
"ds_read_b64_tr_b16 a[16:17], v243 offset:128\n"
"ds_read_b64_tr_b16 a[18:19], v243 offset:896\n"
"ds_read_b64_tr_b16 a[20:21], v243 offset:136\n"
"ds_read_b64_tr_b16 a[22:23], v243 offset:904\n"
"s_barrier\n"
"s_cmp_eq_u32 s76, 0\n"
"s_cselect_b32 s12, 1, 0\n"
"s_mov_b32 s13, 0\n"
"s_cmp_lg_u32 s76, 0\n"
"s_cselect_b64 s[4:5], -1, 0\n"
"s_cmp_lg_u32 s76, 1\n"
"s_cselect_b64 s[6:7], -1, 0\n"
"s_cmp_lg_u32 s76, 2\n"
"s_cselect_b64 s[8:9], -1, 0\n"
"s_cmp_lg_u32 s76, 3\n"
"s_cselect_b64 s[10:11], -1, 0\n"
"v_mov_b32 v192, s77\n"
"v_mov_b32 v196, s80\n"
"v_mov_b32 v197, s81\n"
"v_mov_b32 v198, s84\n"
"v_mov_b32 v199, 0\n"
"ds_read_b128 v[216:219], v232 offset:0\n"
"ds_read_b128 v[220:223], v232 offset:4096\n"
"ds_read_b128 v[224:227], v232 offset:8192\n"
"s_waitcnt lgkmcnt(2)\n"
"v_mfma_f32_16x16x32_bf16 v[0:3], a[0:3], v[216:219], 0\n"
"s_add_u32 s78, s64, 0x12000\n"
"s_addc_u32 s79, s65, 0\n"
"s_add_u32 m0, s74, 0x0\n"
"v_mfma_f32_16x16x32_bf16 v[4:7], a[4:7], v[216:219], 0\n"
"global_load_lds_dwordx4 v240, s[78:79] offset:0\n"
"v_mfma_f32_16x16x32_bf16 v[8:11], a[8:11], v[216:219], 0\n"
"ds_read_b128 v[228:231], v232 offset:12288\n"
"v_mfma_f32_16x16x32_bf16 v[12:15], a[12:15], v[216:219], 0\n"
"v_mfma_f32_16x16x32_bf16 v[16:19], a[16:19], v[216:219], 0\n"
"v_mfma_f32_16x16x32_bf16 v[20:23], a[20:23], v[216:219], 0\n"
"global_load_lds_dwordx4 v241, s[78:79] offset:1024\n"
"s_waitcnt lgkmcnt(2)\n"
"v_mfma_f32_16x16x32_bf16 v[24:27], a[0:3], v[220:223], 0\n"
"s_waitcnt vmcnt(8)\n"
"ds_read_b64_tr_b16 a[24:25], v243 offset:24576\n"
"v_mfma_f32_16x16x32_bf16 v[28:31], a[4:7], v[220:223], 0\n"
"ds_read_b64_tr_b16 a[26:27], v243 offset:25344\n"
"v_mfma_f32_16x16x32_bf16 v[32:35], a[8:11], v[220:223], 0\n"
"s_barrier\n"
"ds_read_b128 v[216:219], v232 offset:16384\n"
"v_mfma_f32_16x16x32_bf16 v[36:39], a[12:15], v[220:223], 0\n"
"s_mov_b64 exec, s[12:13]\n"
"global_store_dwordx4 v192, v[196:199], s[28:29] sc1\n"
"s_mov_b64 exec, -1\n"
"global_load_lds_dwordx4 v242, s[78:79] offset:2048\n"
"v_mfma_f32_16x16x32_bf16 v[40:43], a[16:19], v[220:223], 0\n"
"ds_read_b64_tr_b16 a[28:29], v243 offset:24584\n"
"v_mfma_f32_16x16x32_bf16 v[44:47], a[20:23], v[220:223], 0\n"
"s_add_u32 s78, s64, 0x15000\n"
"s_addc_u32 s79, s65, 0\n"
"s_waitcnt lgkmcnt(5)\n"
"v_mfma_f32_16x16x32_bf16 v[48:51], a[0:3], v[224:227], 0\n"
"s_add_u32 m0, s74, 0xc00\n"
"ds_read_b64_tr_b16 a[30:31], v243 offset:25352\n"
"v_mfma_f32_16x16x32_bf16 v[52:55], a[4:7], v[224:227], 0\n"
"global_load_lds_dwordx4 v240, s[78:79] offset:0\n"
"ds_read_b64_tr_b16 a[32:33], v243 offset:24640\n"
"v_mfma_f32_16x16x32_bf16 v[56:59], a[8:11], v[224:227], 0\n"
"ds_read_b128 v[220:223], v232 offset:20480\n"
"ds_read_b64_tr_b16 a[34:35], v243 offset:25408\n"
"v_mfma_f32_16x16x32_bf16 v[60:63], a[12:15], v[224:227], 0\n"
"ds_read_b64_tr_b16 a[36:37], v243 offset:24648\n"
"v_mfma_f32_16x16x32_bf16 v[64:67], a[16:19], v[224:227], 0\n"
"ds_read_b64_tr_b16 a[38:39], v243 offset:25416\n"
"v_mfma_f32_16x16x32_bf16 v[68:71], a[20:23], v[224:227], 0\n"
"global_load_lds_dwordx4 v241, s[78:79] offset:1024\n"
"s_waitcnt lgkmcnt(10)\n"
"v_mfma_f32_16x16x32_bf16 v[72:75], a[0:3], v[228:231], 0\n"
"ds_read_b64_tr_b16 a[40:41], v243 offset:24704\n"
"v_mfma_f32_16x16x32_bf16 v[76:79], a[4:7], v[228:231], 0\n"
"ds_read_b64_tr_b16 a[42:43], v243 offset:25472\n"
"v_mfma_f32_16x16x32_bf16 v[80:83], a[8:11], v[228:231], 0\n"
"ds_read_b128 v[224:227], v232 offset:24576\n"
"ds_read_b64_tr_b16 a[44:45], v243 offset:24712\n"
"v_mfma_f32_16x16x32_bf16 v[84:87], a[12:15], v[228:231], 0\n"
"global_load_lds_dwordx4 v242, s[78:79] offset:2048\n"
"ds_read_b64_tr_b16 a[46:47], v243 offset:25480\n"
"v_mfma_f32_16x16x32_bf16 v[88:91], a[16:19], v[228:231], 0\n"
"s_waitcnt vmcnt(7)\n"
"ds_read_b64_tr_b16 a[48:49], v243 offset:49152\n"
"v_mfma_f32_16x16x32_bf16 v[92:95], a[20:23], v[228:231], 0\n"
"ds_read_b64_tr_b16 a[50:51], v243 offset:49920\n"
"s_waitcnt lgkmcnt(14)\n"
"v_mfma_f32_16x16x32_bf16 v[96:99], a[0:3], v[216:219], 0\n"
"ds_read_b64_tr_b16 a[52:53], v243 offset:49160\n"
"v_mfma_f32_16x16x32_bf16 v[100:103], a[4:7], v[216:219], 0\n"
"ds_read_b64_tr_b16 a[54:55], v243 offset:49928\n"
"v_mfma_f32_16x16x32_bf16 v[104:107], a[8:11], v[216:219], 0\n"
"ds_read_b128 v[228:231], v232 offset:28672\n"
"ds_read_b64_tr_b16 a[56:57], v243 offset:49216\n"
"v_mfma_f32_16x16x32_bf16 v[108:111], a[12:15], v[216:219], 0\n"
"ds_read_b64_tr_b16 a[58:59], v243 offset:49984\n"
"v_mfma_f32_16x16x32_bf16 v[112:115], a[16:19], v[216:219], 0\n"
"s_add_u32 s78, s64, 0x18000\n"
"s_addc_u32 s79, s65, 0\n"
"v_mfma_f32_16x16x32_bf16 v[116:119], a[20:23], v[216:219], 0\n"
"s_add_u32 m0, s74, 0x6000\n"
"s_waitcnt lgkmcnt(15)\n"
"v_mfma_f32_16x16x32_bf16 v[120:123], a[0:3], v[220:223], 0\n"
"s_waitcnt lgkmcnt(7)\n"
"global_load_lds_dwordx4 v240, s[78:79] offset:0\n"
"v_mfma_f32_16x16x32_bf16 v[124:127], a[4:7], v[220:223], 0\n"
"ds_read_b64_tr_b16 a[60:61], v243 offset:49224\n"
"v_mfma_f32_16x16x32_bf16 v[128:131], a[8:11], v[220:223], 0\n"
"ds_read_b128 v[216:219], v232 offset:32768\n"
"v_add_u32 v232, 0x9000, v232\n"
"v_mfma_f32_16x16x32_bf16 v[132:135], a[12:15], v[220:223], 0\n"
"ds_read_b64_tr_b16 a[62:63], v243 offset:49992\n"
"v_mfma_f32_16x16x32_bf16 v[136:139], a[16:19], v[220:223], 0\n"
"global_load_lds_dwordx4 v241, s[78:79] offset:1024\n"
"ds_read_b64_tr_b16 a[64:65], v243 offset:49280\n"
"v_mfma_f32_16x16x32_bf16 v[140:143], a[20:23], v[220:223], 0\n"
"ds_read_b64_tr_b16 a[66:67], v243 offset:50048\n"
"v_mfma_f32_16x16x32_bf16 v[144:147], a[0:3], v[224:227], 0\n"
"ds_read_b64_tr_b16 a[68:69], v243 offset:49288\n"
"v_mfma_f32_16x16x32_bf16 v[148:151], a[4:7], v[224:227], 0\n"
"ds_read_b64_tr_b16 a[70:71], v243 offset:50056\n"
"v_mfma_f32_16x16x32_bf16 v[152:155], a[8:11], v[224:227], 0\n"
"ds_read_b128 v[220:223], v233 offset:0\n"
"global_load_lds_dwordx4 v242, s[78:79] offset:2048\n"
"v_mfma_f32_16x16x32_bf16 v[156:159], a[12:15], v[224:227], 0\n"
"s_mul_i32 s14, s76, 0x12000\n"
"v_mfma_f32_16x16x32_bf16 v[160:163], a[16:19], v[224:227], 0\n"
"s_add_u32 s78, s64, 0x1b000\n"
"s_addc_u32 s79, s65, 0\n"
"v_mfma_f32_16x16x32_bf16 v[164:167], a[20:23], v[224:227], 0\n"
"s_add_u32 m0, s74, 0x6c00\n"
"s_waitcnt lgkmcnt(10)\n"
"v_mfma_f32_16x16x32_bf16 v[168:171], a[0:3], v[228:231], 0\n"
"global_load_lds_dwordx4 v240, s[78:79] offset:0\n"
"v_mfma_f32_16x16x32_bf16 v[172:175], a[4:7], v[228:231], 0\n"
"s_add_u32 s14, s14, 0x10000\n"
"v_mfma_f32_16x16x32_bf16 v[176:179], a[8:11], v[228:231], 0\n"
"ds_read_b128 v[224:227], v233 offset:4096\n"
"v_mfma_f32_16x16x32_bf16 v[180:183], a[12:15], v[228:231], 0\n"
"s_add_u32 s24, s28, s14\n"
"s_addc_u32 s25, s29, 0\n"
"v_mfma_f32_16x16x32_bf16 v[184:187], a[16:19], v[228:231], 0\n"
"global_load_lds_dwordx4 v241, s[78:79] offset:1024\n"
"v_mfma_f32_16x16x32_bf16 v[188:191], a[20:23], v[228:231], 0\n"
"s_waitcnt lgkmcnt(7)\n"
"v_mfma_f32_16x16x32_bf16 v[192:195], a[0:3], v[216:219], 0\n"
"s_mov_b32 s88, 0\n"
"v_mfma_f32_16x16x32_bf16 v[196:199], a[4:7], v[216:219], 0\n"
"s_mov_b32 s19, 30\n"
"v_mfma_f32_16x16x32_bf16 v[200:203], a[8:11], v[216:219], 0\n"
"ds_read_b128 v[228:231], v233 offset:8192\n"
"global_load_lds_dwordx4 v242, s[78:79] offset:2048\n"
"v_mfma_f32_16x16x32_bf16 v[204:207], a[12:15], v[216:219], 0\n"
"v_mfma_f32_16x16x32_bf16 v[208:211], a[16:19], v[216:219], 0\n"
"v_mfma_f32_16x16x32_bf16 v[212:215], a[20:23], v[216:219], 0\n"
"s_add_u32 m0, s30, 0x8f00\n"
"s_waitcnt lgkmcnt(2)\n"
"v_mfma_f32_16x16x32_bf16 v[0:3], a[24:27], v[220:223], v[0:3]\n"
"global_load_lds_dwordx4 v236, s[32:33] offset:256 nt\n"
"v_mfma_f32_16x16x32_bf16 v[4:7], a[28:31], v[220:223], v[4:7]\n"
"v_mfma_f32_16x16x32_bf16 v[8:11], a[32:35], v[220:223], v[8:11]\n"
"ds_read_b128 v[216:219], v233 offset:12288\n"
"s_add_u32 m0, s30, 0x9f00\n"
"s_waitcnt vmcnt(7)\n"
"ds_read_b64_tr_b16 a[72:73], v243 offset:0\n"
"v_mfma_f32_16x16x32_bf16 v[12:15], a[36:39], v[220:223], v[12:15]\n"
"global_load_lds_dwordx4 v236, s[34:35] offset:256 nt\n"
"ds_read_b64_tr_b16 a[74:75], v243 offset:768\n"
"v_mfma_f32_16x16x32_bf16 v[16:19], a[40:43], v[220:223], v[16:19]\n"
"ds_read_b64_tr_b16 a[76:77], v243 offset:8\n"
"v_mfma_f32_16x16x32_bf16 v[20:23], a[44:47], v[220:223], v[20:23]\n"
"s_add_u32 m0, s30, 0xaf00\n"
"ds_read_b64_tr_b16 a[78:79], v243 offset:776\n"
"s_waitcnt lgkmcnt(6)\n"
"v_mfma_f32_16x16x32_bf16 v[24:27], a[24:27], v[224:227], v[24:27]\n"
"global_load_lds_dwordx4 v236, s[36:37] offset:256 nt\n"
"ds_read_b64_tr_b16 a[80:81], v243 offset:64\n"
"v_mfma_f32_16x16x32_bf16 v[28:31], a[28:31], v[224:227], v[28:31]\n"
"ds_read_b64_tr_b16 a[82:83], v243 offset:832\n"
"v_mfma_f32_16x16x32_bf16 v[32:35], a[32:35], v[224:227], v[32:35]\n"
"ds_read_b128 v[220:223], v233 offset:16384\n"
"s_add_u32 m0, s30, 0xbf00\n"
"v_mfma_f32_16x16x32_bf16 v[36:39], a[36:39], v[224:227], v[36:39]\n"
"global_load_lds_dwordx4 v236, s[38:39] offset:256 nt\n"
"ds_read_b64_tr_b16 a[84:85], v243 offset:72\n"
"v_mfma_f32_16x16x32_bf16 v[40:43], a[40:43], v[224:227], v[40:43]\n"
"ds_read_b64_tr_b16 a[86:87], v243 offset:840\n"
"v_mfma_f32_16x16x32_bf16 v[44:47], a[44:47], v[224:227], v[44:47]\n"
"s_add_u32 m0, s30, 0xcf00\n"
"s_waitcnt lgkmcnt(10)\n"
"v_mfma_f32_16x16x32_bf16 v[48:51], a[24:27], v[228:231], v[48:51]\n"
"global_load_lds_dwordx4 v236, s[40:41] offset:256 nt\n"
"ds_read_b64_tr_b16 a[88:89], v243 offset:128\n"
"v_mfma_f32_16x16x32_bf16 v[52:55], a[28:31], v[228:231], v[52:55]\n"
"ds_read_b64_tr_b16 a[90:91], v243 offset:896\n"
"v_mfma_f32_16x16x32_bf16 v[56:59], a[32:35], v[228:231], v[56:59]\n"
"ds_read_b128 v[224:227], v233 offset:20480\n"
"s_add_u32 m0, s30, 0xdf00\n"
"v_mfma_f32_16x16x32_bf16 v[60:63], a[36:39], v[228:231], v[60:63]\n"
"global_load_lds_dwordx4 v236, s[42:43] offset:256 nt\n"
"ds_read_b64_tr_b16 a[92:93], v243 offset:136\n"
"v_mfma_f32_16x16x32_bf16 v[64:67], a[40:43], v[228:231], v[64:67]\n"
"ds_read_b64_tr_b16 a[94:95], v243 offset:904\n"
"v_mfma_f32_16x16x32_bf16 v[68:71], a[44:47], v[228:231], v[68:71]\n"
"s_add_u32 m0, s30, 0xef00\n"
"s_waitcnt lgkmcnt(14)\n"
"v_mfma_f32_16x16x32_bf16 v[72:75], a[24:27], v[216:219], v[72:75]\n"
"global_load_lds_dwordx4 v236, s[44:45] offset:256 nt\n"
"v_mfma_f32_16x16x32_bf16 v[76:79], a[28:31], v[216:219], v[76:79]\n"
"v_mfma_f32_16x16x32_bf16 v[80:83], a[32:35], v[216:219], v[80:83]\n"
"ds_read_b128 v[228:231], v233 offset:24576\n"
"s_add_u32 m0, s30, 0xff00\n"
"v_mfma_f32_16x16x32_bf16 v[84:87], a[36:39], v[216:219], v[84:87]\n"
"global_load_lds_dwordx4 v236, s[46:47] offset:256 nt\n"
"v_mfma_f32_16x16x32_bf16 v[88:91], a[40:43], v[216:219], v[88:91]\n"
"v_mfma_f32_16x16x32_bf16 v[92:95], a[44:47], v[216:219], v[92:95]\n"
"s_add_u32 m0, s30, 0x10f00\n"
"s_waitcnt lgkmcnt(8)\n"
"v_mfma_f32_16x16x32_bf16 v[96:99], a[24:27], v[220:223], v[96:99]\n"
"global_load_lds_dwordx4 v236, s[48:49] offset:256 nt\n"
"v_mfma_f32_16x16x32_bf16 v[100:103], a[28:31], v[220:223], v[100:103]\n"
"v_mfma_f32_16x16x32_bf16 v[104:107], a[32:35], v[220:223], v[104:107]\n"
"ds_read_b128 v[216:219], v233 offset:28672\n"
"s_add_u32 s78, s64, 0x1e000\n"
"s_addc_u32 s79, s65, 0\n"
"v_mfma_f32_16x16x32_bf16 v[108:111], a[36:39], v[220:223], v[108:111]\n"
"s_add_u32 m0, s74, 0xc000\n"
"v_mfma_f32_16x16x32_bf16 v[112:115], a[40:43], v[220:223], v[112:115]\n"
"global_load_lds_dwordx4 v240, s[78:79] offset:0\n"
"v_mfma_f32_16x16x32_bf16 v[116:119], a[44:47], v[220:223], v[116:119]\n"
"s_waitcnt lgkmcnt(4)\n"
"v_mfma_f32_16x16x32_bf16 v[120:123], a[24:27], v[224:227], v[120:123]\n"
"v_mfma_f32_16x16x32_bf16 v[124:127], a[28:31], v[224:227], v[124:127]\n"
"s_waitcnt vmcnt(10)\n"
"ds_read_b64_tr_b16 a[96:97], v243 offset:24576\n"
"v_mfma_f32_16x16x32_bf16 v[128:131], a[32:35], v[224:227], v[128:131]\n"
"ds_read_b128 v[220:223], v233 offset:32768\n"
"v_add_u32 v233, 0x9000, v233\n"
"global_load_lds_dwordx4 v241, s[78:79] offset:1024\n"
"v_mfma_f32_16x16x32_bf16 v[132:135], a[36:39], v[224:227], v[132:135]\n"
"ds_read_b64_tr_b16 a[98:99], v243 offset:25344\n"
"v_mfma_f32_16x16x32_bf16 v[136:139], a[40:43], v[224:227], v[136:139]\n"
"ds_read_b64_tr_b16 a[100:101], v243 offset:24584\n"
"v_mfma_f32_16x16x32_bf16 v[140:143], a[44:47], v[224:227], v[140:143]\n"
"ds_read_b64_tr_b16 a[102:103], v243 offset:25352\n"
"s_waitcnt lgkmcnt(6)\n"
"v_mfma_f32_16x16x32_bf16 v[144:147], a[24:27], v[228:231], v[144:147]\n"
"global_load_lds_dwordx4 v242, s[78:79] offset:2048\n"
"ds_read_b64_tr_b16 a[104:105], v243 offset:24640\n"
"v_mfma_f32_16x16x32_bf16 v[148:151], a[28:31], v[228:231], v[148:151]\n"
"ds_read_b64_tr_b16 a[106:107], v243 offset:25408\n"
"v_mfma_f32_16x16x32_bf16 v[152:155], a[32:35], v[228:231], v[152:155]\n"
"ds_read_b128 v[224:227], v234 offset:0\n"
"s_add_u32 s78, s64, 0x21000\n"
"s_addc_u32 s79, s65, 0\n"
"v_mfma_f32_16x16x32_bf16 v[156:159], a[36:39], v[228:231], v[156:159]\n"
"s_add_u32 m0, s74, 0xcc00\n"
"ds_read_b64_tr_b16 a[108:109], v243 offset:24648\n"
"v_mfma_f32_16x16x32_bf16 v[160:163], a[40:43], v[228:231], v[160:163]\n"
"global_load_lds_dwordx4 v240, s[78:79] offset:0\n"
"ds_read_b64_tr_b16 a[110:111], v243 offset:25416\n"
"v_mfma_f32_16x16x32_bf16 v[164:167], a[44:47], v[228:231], v[164:167]\n"
"s_waitcnt lgkmcnt(10)\n"
"v_mfma_f32_16x16x32_bf16 v[168:171], a[24:27], v[216:219], v[168:171]\n"
"ds_read_b64_tr_b16 a[112:113], v243 offset:24704\n"
"v_mfma_f32_16x16x32_bf16 v[172:175], a[28:31], v[216:219], v[172:175]\n"
"ds_read_b64_tr_b16 a[114:115], v243 offset:25472\n"
"v_mfma_f32_16x16x32_bf16 v[176:179], a[32:35], v[216:219], v[176:179]\n"
"ds_read_b128 v[228:231], v234 offset:4096\n"
"global_load_lds_dwordx4 v241, s[78:79] offset:1024\n"
"v_mfma_f32_16x16x32_bf16 v[180:183], a[36:39], v[216:219], v[180:183]\n"
"ds_read_b64_tr_b16 a[116:117], v243 offset:24712\n"
"v_mfma_f32_16x16x32_bf16 v[184:187], a[40:43], v[216:219], v[184:187]\n"
"ds_read_b64_tr_b16 a[118:119], v243 offset:25480\n"
"v_mfma_f32_16x16x32_bf16 v[188:191], a[44:47], v[216:219], v[188:191]\n"
"s_waitcnt lgkmcnt(13)\n"
"v_mfma_f32_16x16x32_bf16 v[192:195], a[24:27], v[220:223], v[192:195]\n"
"global_load_lds_dwordx4 v242, s[78:79] offset:2048\n"
"v_mfma_f32_16x16x32_bf16 v[196:199], a[28:31], v[220:223], v[196:199]\n"
"v_mfma_f32_16x16x32_bf16 v[200:203], a[32:35], v[220:223], v[200:203]\n"
"ds_read_b128 v[216:219], v234 offset:8192\n"
"s_add_u32 s78, s64, 0x24000\n"
"s_addc_u32 s79, s65, 0\n"
"v_mfma_f32_16x16x32_bf16 v[204:207], a[36:39], v[220:223], v[204:207]\n"
"s_add_u32 m0, s74, 0x0\n"
"v_mfma_f32_16x16x32_bf16 v[208:211], a[40:43], v[220:223], v[208:211]\n"
"global_load_lds_dwordx4 v240, s[78:79] offset:0\n"
"v_mfma_f32_16x16x32_bf16 v[212:215], a[44:47], v[220:223], v[212:215]\n"
"s_waitcnt lgkmcnt(8)\n"
"v_mfma_f32_16x16x32_bf16 v[0:3], a[48:51], v[224:227], v[0:3]\n"
"v_mfma_f32_16x16x32_bf16 v[4:7], a[52:55], v[224:227], v[4:7]\n"
"v_mfma_f32_16x16x32_bf16 v[8:11], a[56:59], v[224:227], v[8:11]\n"
"ds_read_b128 v[220:223], v234 offset:12288\n"
"global_load_lds_dwordx4 v241, s[78:79] offset:1024\n"
"v_mfma_f32_16x16x32_bf16 v[12:15], a[60:63], v[224:227], v[12:15]\n"
"v_mfma_f32_16x16x32_bf16 v[16:19], a[64:67], v[224:227], v[16:19]\n"
"v_mfma_f32_16x16x32_bf16 v[20:23], a[68:71], v[224:227], v[20:23]\n"
"s_waitcnt lgkmcnt(4)\n"
"v_mfma_f32_16x16x32_bf16 v[24:27], a[48:51], v[228:231], v[24:27]\n"
"global_load_lds_dwordx4 v242, s[78:79] offset:2048\n"
"v_mfma_f32_16x16x32_bf16 v[28:31], a[52:55], v[228:231], v[28:31]\n"
"v_mfma_f32_16x16x32_bf16 v[32:35], a[56:59], v[228:231], v[32:35]\n"
"ds_read_b128 v[224:227], v234 offset:16384\n"
"s_add_u32 s78, s64, 0x27000\n"
"s_addc_u32 s79, s65, 0\n"
"v_mfma_f32_16x16x32_bf16 v[36:39], a[60:63], v[228:231], v[36:39]\n"
"s_add_u32 m0, s74, 0xc00\n"
"v_mfma_f32_16x16x32_bf16 v[40:43], a[64:67], v[228:231], v[40:43]\n"
"global_load_lds_dwordx4 v240, s[78:79] offset:0\n"
"v_mfma_f32_16x16x32_bf16 v[44:47], a[68:71], v[228:231], v[44:47]\n"
"s_waitcnt lgkmcnt(2)\n"
"v_mfma_f32_16x16x32_bf16 v[48:51], a[48:51], v[216:219], v[48:51]\n"
"v_mfma_f32_16x16x32_bf16 v[52:55], a[52:55], v[216:219], v[52:55]\n"
"v_mfma_f32_16x16x32_bf16 v[56:59], a[56:59], v[216:219], v[56:59]\n"
"ds_read_b128 v[228:231], v234 offset:20480\n"
"global_load_lds_dwordx4 v241, s[78:79] offset:1024\n"
"v_mfma_f32_16x16x32_bf16 v[60:63], a[60:63], v[216:219], v[60:63]\n"
"v_mfma_f32_16x16x32_bf16 v[64:67], a[64:67], v[216:219], v[64:67]\n"
"v_mfma_f32_16x16x32_bf16 v[68:71], a[68:71], v[216:219], v[68:71]\n"
"s_waitcnt lgkmcnt(2)\n"
"v_mfma_f32_16x16x32_bf16 v[72:75], a[48:51], v[220:223], v[72:75]\n"
"global_load_lds_dwordx4 v242, s[78:79] offset:2048\n"
"v_mfma_f32_16x16x32_bf16 v[76:79], a[52:55], v[220:223], v[76:79]\n"
"v_mfma_f32_16x16x32_bf16 v[80:83], a[56:59], v[220:223], v[80:83]\n"
"ds_read_b128 v[216:219], v234 offset:24576\n"
"s_add_u32 s78, s64, 0x2a000\n"
"s_addc_u32 s79, s65, 0\n"
"v_mfma_f32_16x16x32_bf16 v[84:87], a[60:63], v[220:223], v[84:87]\n"
"s_add_u32 m0, s74, 0x6000\n"
"v_mfma_f32_16x16x32_bf16 v[88:91], a[64:67], v[220:223], v[88:91]\n"
"global_load_lds_dwordx4 v240, s[78:79] offset:0\n"
"v_mfma_f32_16x16x32_bf16 v[92:95], a[68:71], v[220:223], v[92:95]\n"
"s_waitcnt lgkmcnt(2)\n"
"v_mfma_f32_16x16x32_bf16 v[96:99], a[48:51], v[224:227], v[96:99]\n"
"v_mfma_f32_16x16x32_bf16 v[100:103], a[52:55], v[224:227], v[100:103]\n"
"v_mfma_f32_16x16x32_bf16 v[104:107], a[56:59], v[224:227], v[104:107]\n"
"ds_read_b128 v[220:223], v234 offset:28672\n"
"global_load_lds_dwordx4 v241, s[78:79] offset:1024\n"
"v_mfma_f32_16x16x32_bf16 v[108:111], a[60:63], v[224:227], v[108:111]\n"
"v_mfma_f32_16x16x32_bf16 v[112:115], a[64:67], v[224:227], v[112:115]\n"
"v_mfma_f32_16x16x32_bf16 v[116:119], a[68:71], v[224:227], v[116:119]\n"
"s_waitcnt vmcnt(8)\n"
"ds_read_b64_tr_b16 a[120:121], v243 offset:49152\n"
"s_waitcnt lgkmcnt(3)\n"
"v_mfma_f32_16x16x32_bf16 v[120:123], a[48:51], v[228:231], v[120:123]\n"
"global_load_lds_dwordx4 v242, s[78:79] offset:2048\n"
"ds_read_b64_tr_b16 a[122:123], v243 offset:49920\n"
"v_mfma_f32_16x16x32_bf16 v[124:127], a[52:55], v[228:231], v[124:127]\n"
"ds_read_b64_tr_b16 a[124:125], v243 offset:49160\n"
"v_mfma_f32_16x16x32_bf16 v[128:131], a[56:59], v[228:231], v[128:131]\n"
"ds_read_b128 v[224:227], v234 offset:32768\n"
"v_add_u32 v234, 0x9000, v234\n"
"s_add_u32 s78, s64, 0x2d000\n"
"s_addc_u32 s79, s65, 0\n"
"v_mfma_f32_16x16x32_bf16 v[132:135], a[60:63], v[228:231], v[132:135]\n"
"s_add_u32 m0, s74, 0x6c00\n"
"ds_read_b64_tr_b16 a[126:127], v243 offset:49928\n"
"v_mfma_f32_16x16x32_bf16 v[136:139], a[64:67], v[228:231], v[136:139]\n"
"global_load_lds_dwordx4 v240, s[78:79] offset:0\n"
"ds_read_b64_tr_b16 a[128:129], v243 offset:49216\n"
"v_mfma_f32_16x16x32_bf16 v[140:143], a[68:71], v[228:231], v[140:143]\n"
"ds_read_b64_tr_b16 a[130:131], v243 offset:49984\n"
"s_waitcnt lgkmcnt(8)\n"
"v_mfma_f32_16x16x32_bf16 v[144:147], a[48:51], v[216:219], v[144:147]\n"
"ds_read_b64_tr_b16 a[132:133], v243 offset:49224\n"
"v_mfma_f32_16x16x32_bf16 v[148:151], a[52:55], v[216:219], v[148:151]\n"
"ds_read_b64_tr_b16 a[134:135], v243 offset:49992\n"
"v_mfma_f32_16x16x32_bf16 v[152:155], a[56:59], v[216:219], v[152:155]\n"
"ds_read_b128 v[228:231], v235 offset:0\n"
"global_load_lds_dwordx4 v241, s[78:79] offset:1024\n"
"v_mfma_f32_16x16x32_bf16 v[156:159], a[60:63], v[216:219], v[156:159]\n"
"ds_read_b64_tr_b16 a[136:137], v243 offset:49280\n"
"v_mfma_f32_16x16x32_bf16 v[160:163], a[64:67], v[216:219], v[160:163]\n"
"ds_read_b64_tr_b16 a[138:139], v243 offset:50048\n"
"v_mfma_f32_16x16x32_bf16 v[164:167], a[68:71], v[216:219], v[164:167]\n"
"ds_read_b64_tr_b16 a[140:141], v243 offset:49288\n"
"s_waitcnt lgkmcnt(13)\n"
"v_mfma_f32_16x16x32_bf16 v[168:171], a[48:51], v[220:223], v[168:171]\n"
"global_load_lds_dwordx4 v242, s[78:79] offset:2048\n"
"v_mfma_f32_16x16x32_bf16 v[172:175], a[52:55], v[220:223], v[172:175]\n"
"ds_read_b64_tr_b16 a[142:143], v243 offset:50056\n"
"v_mfma_f32_16x16x32_bf16 v[176:179], a[56:59], v[220:223], v[176:179]\n"
"ds_read_b128 v[216:219], v235 offset:4096\n"
"v_mfma_f32_16x16x32_bf16 v[180:183], a[60:63], v[220:223], v[180:183]\n"
"global_load_dwordx4 v[244:247], v248, s[28:29] sc1\n"
"v_mfma_f32_16x16x32_bf16 v[184:187], a[64:67], v[220:223], v[184:187]\n"
"v_mfma_f32_16x16x32_bf16 v[188:191], a[68:71], v[220:223], v[188:191]\n"
"s_waitcnt lgkmcnt(11)\n"
"v_mfma_f32_16x16x32_bf16 v[192:195], a[48:51], v[224:227], v[192:195]\n"
"v_mfma_f32_16x16x32_bf16 v[196:199], a[52:55], v[224:227], v[196:199]\n"
"v_mfma_f32_16x16x32_bf16 v[200:203], a[56:59], v[224:227], v[200:203]\n"
"ds_read_b128 v[220:223], v235 offset:8192\n"
"v_mfma_f32_16x16x32_bf16 v[204:207], a[60:63], v[224:227], v[204:207]\n"
"s_add_u32 s78, s64, 0x30000\n"
"s_addc_u32 s79, s65, 0\n"
"v_mfma_f32_16x16x32_bf16 v[208:211], a[64:67], v[224:227], v[208:211]\n"
"s_add_u32 m0, s74, 0xc000\n"
"v_mfma_f32_16x16x32_bf16 v[212:215], a[68:71], v[224:227], v[212:215]\n"
"s_waitcnt lgkmcnt(2)\n"
"global_load_lds_dwordx4 v240, s[78:79] offset:0\n"
"v_mfma_f32_16x16x32_bf16 v[0:3], a[72:75], v[228:231], v[0:3]\n"
"s_waitcnt vmcnt(8)\n"
"ds_read_b64_tr_b16 a[144:145], v243 offset:0\n"
"v_mfma_f32_16x16x32_bf16 v[4:7], a[76:79], v[228:231], v[4:7]\n"
"ds_read_b64_tr_b16 a[146:147], v243 offset:768\n"
"v_mfma_f32_16x16x32_bf16 v[8:11], a[80:83], v[228:231], v[8:11]\n"
"ds_read_b128 v[224:227], v235 offset:12288\n"
"ds_read_b64_tr_b16 a[148:149], v243 offset:8\n"
"v_mfma_f32_16x16x32_bf16 v[12:15], a[84:87], v[228:231], v[12:15]\n"
"global_load_lds_dwordx4 v241, s[78:79] offset:1024\n"
"ds_read_b64_tr_b16 a[150:151], v243 offset:776\n"
"v_mfma_f32_16x16x32_bf16 v[16:19], a[88:91], v[228:231], v[16:19]\n"
"ds_read_b64_tr_b16 a[152:153], v243 offset:64\n"
"v_mfma_f32_16x16x32_bf16 v[20:23], a[92:95], v[228:231], v[20:23]\n"
"ds_read_b64_tr_b16 a[154:155], v243 offset:832\n"
"s_waitcnt lgkmcnt(8)\n"
"v_mfma_f32_16x16x32_bf16 v[24:27], a[72:75], v[216:219], v[24:27]\n"
"ds_read_b64_tr_b16 a[156:157], v243 offset:72\n"
"v_mfma_f32_16x16x32_bf16 v[28:31], a[76:79], v[216:219], v[28:31]\n"
"global_load_lds_dwordx4 v242, s[78:79] offset:2048\n"
"ds_read_b64_tr_b16 a[158:159], v243 offset:840\n"
"v_mfma_f32_16x16x32_bf16 v[32:35], a[80:83], v[216:219], v[32:35]\n"
"ds_read_b128 v[228:231], v235 offset:16384\n"
"ds_read_b64_tr_b16 a[160:161], v243 offset:128\n"
"v_mfma_f32_16x16x32_bf16 v[36:39], a[84:87], v[216:219], v[36:39]\n"
"s_add_u32 s78, s64, 0x33000\n"
"s_addc_u32 s79, s65, 0\n"
"v_mfma_f32_16x16x32_bf16 v[40:43], a[88:91], v[216:219], v[40:43]\n"
"s_add_u32 m0, s74, 0xcc00\n"
"ds_read_b64_tr_b16 a[162:163], v243 offset:896\n"
"v_mfma_f32_16x16x32_bf16 v[44:47], a[92:95], v[216:219], v[44:47]\n"
"global_load_lds_dwordx4 v240, s[78:79] offset:0\n"
"s_waitcnt lgkmcnt(12)\n"
"v_mfma_f32_16x16x32_bf16 v[48:51], a[72:75], v[220:223], v[48:51]\n"
"ds_read_b64_tr_b16 a[164:165], v243 offset:136\n"
"v_mfma_f32_16x16x32_bf16 v[52:55], a[76:79], v[220:223], v[52:55]\n"
"ds_read_b64_tr_b16 a[166:167], v243 offset:904\n"
"v_mfma_f32_16x16x32_bf16 v[56:59], a[80:83], v[220:223], v[56:59]\n"
"ds_read_b128 v[216:219], v235 offset:20480\n"
"v_mfma_f32_16x16x32_bf16 v[60:63], a[84:87], v[220:223], v[60:63]\n"
"global_load_lds_dwordx4 v241, s[78:79] offset:1024\n"
"v_mfma_f32_16x16x32_bf16 v[64:67], a[88:91], v[220:223], v[64:67]\n"
"v_mfma_f32_16x16x32_bf16 v[68:71], a[92:95], v[220:223], v[68:71]\n"
"s_waitcnt lgkmcnt(12)\n"
"v_mfma_f32_16x16x32_bf16 v[72:75], a[72:75], v[224:227], v[72:75]\n"
"v_mfma_f32_16x16x32_bf16 v[76:79], a[76:79], v[224:227], v[76:79]\n"
"global_load_lds_dwordx4 v242, s[78:79] offset:2048\n"
"v_mfma_f32_16x16x32_bf16 v[80:83], a[80:83], v[224:227], v[80:83]\n"
"ds_read_b128 v[220:223], v235 offset:24576\n"
"v_mfma_f32_16x16x32_bf16 v[84:87], a[84:87], v[224:227], v[84:87]\n"
"s_add_u32 s78, s64, 0x36000\n"
"s_addc_u32 s79, s65, 0\n"
"v_mfma_f32_16x16x32_bf16 v[88:91], a[88:91], v[224:227], v[88:91]\n"
"s_add_u32 m0, s74, 0x0\n"
"v_mfma_f32_16x16x32_bf16 v[92:95], a[92:95], v[224:227], v[92:95]\n"
"s_waitcnt lgkmcnt(2)\n"
"global_load_lds_dwordx4 v240, s[78:79] offset:0\n"
"v_mfma_f32_16x16x32_bf16 v[96:99], a[72:75], v[228:231], v[96:99]\n"
"s_waitcnt vmcnt(8)\n"
"ds_read_b64_tr_b16 a[168:169], v243 offset:24576\n"
"v_mfma_f32_16x16x32_bf16 v[100:103], a[76:79], v[228:231], v[100:103]\n"
"ds_read_b64_tr_b16 a[170:171], v243 offset:25344\n"
"v_mfma_f32_16x16x32_bf16 v[104:107], a[80:83], v[228:231], v[104:107]\n"
"ds_read_b128 v[224:227], v235 offset:28672\n"
"ds_read_b64_tr_b16 a[172:173], v243 offset:24584\n"
"v_mfma_f32_16x16x32_bf16 v[108:111], a[84:87], v[228:231], v[108:111]\n"
"global_load_lds_dwordx4 v241, s[78:79] offset:1024\n"
"ds_read_b64_tr_b16 a[174:175], v243 offset:25352\n"
"v_mfma_f32_16x16x32_bf16 v[112:115], a[88:91], v[228:231], v[112:115]\n"
"ds_read_b64_tr_b16 a[176:177], v243 offset:24640\n"
"v_mfma_f32_16x16x32_bf16 v[116:119], a[92:95], v[228:231], v[116:119]\n"
"ds_read_b64_tr_b16 a[178:179], v243 offset:25408\n"
"s_waitcnt lgkmcnt(8)\n"
"v_mfma_f32_16x16x32_bf16 v[120:123], a[72:75], v[216:219], v[120:123]\n"
"ds_read_b64_tr_b16 a[180:181], v243 offset:24648\n"
"v_mfma_f32_16x16x32_bf16 v[124:127], a[76:79], v[216:219], v[124:127]\n"
"global_load_lds_dwordx4 v242, s[78:79] offset:2048\n"
"ds_read_b64_tr_b16 a[182:183], v243 offset:25416\n"
"v_mfma_f32_16x16x32_bf16 v[128:131], a[80:83], v[216:219], v[128:131]\n"
"ds_read_b128 v[228:231], v235 offset:32768\n"
"v_add_u32 v235, 0x9000, v235\n"
"ds_read_b64_tr_b16 a[184:185], v243 offset:24704\n"
"v_mfma_f32_16x16x32_bf16 v[132:135], a[84:87], v[216:219], v[132:135]\n"
"s_add_u32 s78, s64, 0x39000\n"
"s_addc_u32 s79, s65, 0\n"
"v_mfma_f32_16x16x32_bf16 v[136:139], a[88:91], v[216:219], v[136:139]\n"
"s_add_u32 m0, s74, 0xc00\n"
"ds_read_b64_tr_b16 a[186:187], v243 offset:25472\n"
"v_mfma_f32_16x16x32_bf16 v[140:143], a[92:95], v[216:219], v[140:143]\n"
"global_load_lds_dwordx4 v240, s[78:79] offset:0\n"
"s_waitcnt lgkmcnt(12)\n"
"v_mfma_f32_16x16x32_bf16 v[144:147], a[72:75], v[220:223], v[144:147]\n"
"ds_read_b64_tr_b16 a[188:189], v243 offset:24712\n"
"v_mfma_f32_16x16x32_bf16 v[148:151], a[76:79], v[220:223], v[148:151]\n"
"s_barrier\n"
"ds_read_b64_tr_b16 a[190:191], v243 offset:25480\n"
"v_mfma_f32_16x16x32_bf16 v[152:155], a[80:83], v[220:223], v[152:155]\n"
"ds_read_b128 v[216:219], v232 offset:0\n"
"s_waitcnt vmcnt(10)\n"
"v_cmp_ne_u64 vcc, s[80:81], v[244:245]\n"
"v_cmp_ne_u64 s[56:57], s[80:81], v[244:245]\n"
"s_nop 3\n"
"s_or_b64 vcc, vcc, s[56:57]\n"
"s_cbranch_scc0 LP1_0\n"
"s_mov_b32 s82, 0x40000\n"
"LP0_0:\n"
"s_sleep 2\n"
"global_load_dwordx4 v[244:247], v248, s[28:29] sc1\n"
"s_sub_u32 s82, s82, 1\n"
"s_cbranch_scc1 LP2_0\n"
"s_waitcnt vmcnt(0)\n"
"v_cmp_ne_u64 vcc, s[80:81], v[244:245]\n"
"v_cmp_ne_u64 s[56:57], s[80:81], v[244:245]\n"
"s_nop 3\n"
"s_or_b64 vcc, vcc, s[56:57]\n"
"s_cbranch_scc1 LP0_0\n"
"LP2_0:\n"
"s_waitcnt vmcnt(0)\n"
"LP1_0:\n"
"s_nop 0\n"
"v_mfma_f32_16x16x32_bf16 v[156:159], a[84:87], v[220:223], v[156:159]\n"
"s_mov_b64 exec, s[4:5]\n"
"s_cbranch_execz LHT0\n"
"LHR0:\n"
"s_mov_b64 exec, -1\n"
"v_mfma_f32_16x16x32_bf16 v[160:163], a[88:91], v[220:223], v[160:163]\n"
"v_subrev_u32 v246, s83, v246\n"
"global_load_lds_dwordx4 v241, s[78:79] offset:1024\n"
"v_mfma_f32_16x16x32_bf16 v[164:167], a[92:95], v[220:223], v[164:167]\n"
"s_waitcnt lgkmcnt(12)\n"
"v_mfma_f32_16x16x32_bf16 v[168:171], a[72:75], v[224:227], v[168:171]\n"
"v_add_u32_dpp v246, v246, v246 row_shr:1 row_mask:0xf bank_mask:0xf\n"
"v_mfma_f32_16x16x32_bf16 v[172:175], a[76:79], v[224:227], v[172:175]\n"
"v_mfma_f32_16x16x32_bf16 v[176:179], a[80:83], v[224:227], v[176:179]\n"
"ds_read_b128 v[220:223], v232 offset:4096\n"
"v_add_u32_dpp v246, v246, v246 row_shr:2 row_mask:0xf bank_mask:0xf\n"
"global_load_lds_dwordx4 v242, s[78:79] offset:2048\n"
"v_mfma_f32_16x16x32_bf16 v[180:183], a[84:87], v[224:227], v[180:183]\n"
"v_mfma_f32_16x16x32_bf16 v[184:187], a[88:91], v[224:227], v[184:187]\n"
"v_add_u32_dpp v246, v246, v246 row_shr:4 row_mask:0xf bank_mask:0xf\n"
"s_add_u32 s78, s64, 0x3c000\n"
"s_addc_u32 s79, s65, 0\n"
"v_mfma_f32_16x16x32_bf16 v[188:191], a[92:95], v[224:227], v[188:191]\n"
"s_add_u32 m0, s74, 0x6000\n"
"s_waitcnt lgkmcnt(6)\n"
"v_mfma_f32_16x16x32_bf16 v[192:195], a[72:75], v[228:231], v[192:195]\n"
"v_readlane_b32 s86, v246, 63\n"
"s_waitcnt lgkmcnt(2)\n"
"global_load_lds_dwordx4 v240, s[78:79] offset:0\n"
"v_mfma_f32_16x16x32_bf16 v[196:199], a[76:79], v[228:231], v[196:199]\n"
"v_mfma_f32_16x16x32_bf16 v[200:203], a[80:83], v[228:231], v[200:203]\n"
"ds_read_b128 v[224:227], v232 offset:8192\n"
"s_ashr_i32 s86, s86, 3\n"
"v_mfma_f32_16x16x32_bf16 v[204:207], a[84:87], v[228:231], v[204:207]\n"
"v_mfma_f32_16x16x32_bf16 v[208:211], a[88:91], v[228:231], v[208:211]\n"
"s_max_i32 s86, s86, 100\n"
"global_load_lds_dwordx4 v241, s[78:79] offset:1024\n"
"v_mfma_f32_16x16x32_bf16 v[212:215], a[92:95], v[228:231], v[212:215]\n"
"s_waitcnt lgkmcnt(2)\n"
"v_mfma_f32_16x16x32_bf16 v[0:3], a[96:99], v[216:219], v[0:3]\n"
"s_min_i32 s86, s86, 600\n"
"s_waitcnt vmcnt(8)\n"
"ds_read_b64_tr_b16 a[192:193], v243 offset:49152\n"
"v_mfma_f32_16x16x32_bf16 v[4:7], a[100:103], v[216:219], v[4:7]\n"
"s_barrier\n"
"ds_read_b64_tr_b16 a[194:195], v243 offset:49920\n"
"v_mfma_f32_16x16x32_bf16 v[8:11], a[104:107], v[216:219], v[8:11]\n"
"ds_read_b128 v[228:231], v232 offset:12288\n"
"s_add_u32 s83, s83, s86\n"
"global_load_lds_dwordx4 v242, s[78:79] offset:2048\n"
"v_mfma_f32_16x16x32_bf16 v[12:15], a[108:111], v[216:219], v[12:15]\n"
"ds_read_b64_tr_b16 a[196:197], v243 offset:49160\n"
"v_mfma_f32_16x16x32_bf16 v[16:19], a[112:115], v[216:219], v[16:19]\n"
"s_add_u32 s83, s83, 3318\n"
"s_add_u32 s78, s64, 0x3f000\n"
"s_addc_u32 s79, s65, 0\n"
"v_mfma_f32_16x16x32_bf16 v[20:23], a[116:119], v[216:219], v[20:23]\n"
"s_add_u32 m0, s74, 0x6c00\n"
"s_waitcnt lgkmcnt(5)\n"
"v_mfma_f32_16x16x32_bf16 v[24:27], a[96:99], v[220:223], v[24:27]\n"
"global_load_lds_dwordx4 v240, s[78:79] offset:0\n"
"ds_read_b64_tr_b16 a[198:199], v243 offset:49928\n"
"v_mfma_f32_16x16x32_bf16 v[28:31], a[100:103], v[220:223], v[28:31]\n"
"ds_read_b64_tr_b16 a[200:201], v243 offset:49216\n"
"v_mfma_f32_16x16x32_bf16 v[32:35], a[104:107], v[220:223], v[32:35]\n"
"ds_read_b128 v[216:219], v232 offset:16384\n"
"ds_read_b64_tr_b16 a[202:203], v243 offset:49984\n"
"v_mfma_f32_16x16x32_bf16 v[36:39], a[108:111], v[220:223], v[36:39]\n"
"ds_read_b64_tr_b16 a[204:205], v243 offset:49224\n"
"v_mfma_f32_16x16x32_bf16 v[40:43], a[112:115], v[220:223], v[40:43]\n"
"global_load_lds_dwordx4 v241, s[78:79] offset:1024\n"
"ds_read_b64_tr_b16 a[206:207], v243 offset:49992\n"
"v_mfma_f32_16x16x32_bf16 v[44:47], a[116:119], v[220:223], v[44:47]\n"
"ds_read_b64_tr_b16 a[208:209], v243 offset:49280\n"
"s_waitcnt lgkmcnt(11)\n"
"v_mfma_f32_16x16x32_bf16 v[48:51], a[96:99], v[224:227], v[48:51]\n"
"ds_read_b64_tr_b16 a[210:211], v243 offset:50048\n"
"v_mfma_f32_16x16x32_bf16 v[52:55], a[100:103], v[224:227], v[52:55]\n"
"ds_read_b64_tr_b16 a[212:213], v243 offset:49288\n"
"v_mfma_f32_16x16x32_bf16 v[56:59], a[104:107], v[224:227], v[56:59]\n"
"ds_read_b128 v[220:223], v232 offset:20480\n"
"global_load_lds_dwordx4 v242, s[78:79] offset:2048\n"
"v_mfma_f32_16x16x32_bf16 v[60:63], a[108:111], v[224:227], v[60:63]\n"
"ds_read_b64_tr_b16 a[214:215], v243 offset:50056\n"
"v_mfma_f32_16x16x32_bf16 v[64:67], a[112:115], v[224:227], v[64:67]\n"
"s_add_u32 s58, s24, 0x1000\n"
"s_addc_u32 s59, s25, 0\n"
"v_mfma_f32_16x16x32_bf16 v[68:71], a[116:119], v[224:227], v[68:71]\n"
"global_load_dwordx4 a[240:243], v239, s[58:59] offset:-4096 sc1\n"
"s_waitcnt lgkmcnt(12)\n"
"v_mfma_f32_16x16x32_bf16 v[72:75], a[96:99], v[228:231], v[72:75]\n"
"v_mfma_f32_16x16x32_bf16 v[76:79], a[100:103], v[228:231], v[76:79]\n"
"v_mfma_f32_16x16x32_bf16 v[80:83], a[104:107], v[228:231], v[80:83]\n"
"ds_read_b128 v[224:227], v232 offset:24576\n"
"v_mfma_f32_16x16x32_bf16 v[84:87], a[108:111], v[228:231], v[84:87]\n"
"global_load_dwordx4 a[244:247], v239, s[58:59] offset:-3072 sc1\n"
"v_mfma_f32_16x16x32_bf16 v[88:91], a[112:115], v[228:231], v[88:91]\n"
"v_mfma_f32_16x16x32_bf16 v[92:95], a[116:119], v[228:231], v[92:95]\n"
"s_waitcnt lgkmcnt(9)\n"
"v_mfma_f32_16x16x32_bf16 v[96:99], a[96:99], v[216:219], v[96:99]\n"
"s_add_u32 s78, s64, 0x42000\n"
"s_addc_u32 s79, s65, 0\n"
"v_mfma_f32_16x16x32_bf16 v[100:103], a[100:103], v[216:219], v[100:103]\n"
"s_add_u32 m0, s74, 0xc000\n"
"s_waitcnt vmcnt(8)\n"
"ds_read_b64_tr_b16 a[216:217], v243 offset:0\n"
"v_mfma_f32_16x16x32_bf16 v[104:107], a[104:107], v[216:219], v[104:107]\n"
"ds_read_b128 v[228:231], v232 offset:28672\n"
"s_waitcnt lgkmcnt(3)\n"
"global_load_lds_dwordx4 v240, s[78:79] offset:0\n"
"v_mfma_f32_16x16x32_bf16 v[108:111], a[108:111], v[216:219], v[108:111]\n"
"ds_read_b64_tr_b16 a[218:219], v243 offset:768\n"
"v_mfma_f32_16x16x32_bf16 v[112:115], a[112:115], v[216:219], v[112:115]\n"
"ds_read_b64_tr_b16 a[220:221], v243 offset:8\n"
"v_mfma_f32_16x16x32_bf16 v[116:119], a[116:119], v[216:219], v[116:119]\n"
"ds_read_b64_tr_b16 a[222:223], v243 offset:776\n"
"v_mfma_f32_16x16x32_bf16 v[120:123], a[96:99], v[220:223], v[120:123]\n"
"global_load_lds_dwordx4 v241, s[78:79] offset:1024\n"
"ds_read_b64_tr_b16 a[224:225], v243 offset:64\n"
"v_mfma_f32_16x16x32_bf16 v[124:127], a[100:103], v[220:223], v[124:127]\n"
"ds_read_b64_tr_b16 a[226:227], v243 offset:832\n"
"v_mfma_f32_16x16x32_bf16 v[128:131], a[104:107], v[220:223], v[128:131]\n"
"ds_read_b128 v[216:219], v232 offset:32768\n"
"v_add_u32 v232, 0xffff7000, v232\n"
"v_mfma_f32_16x16x32_bf16 v[132:135], a[108:111], v[220:223], v[132:135]\n"
"ds_read_b64_tr_b16 a[228:229], v243 offset:72\n"
"v_mfma_f32_16x16x32_bf16 v[136:139], a[112:115], v[220:223], v[136:139]\n"
"global_load_lds_dwordx4 v242, s[78:79] offset:2048\n"
"ds_read_b64_tr_b16 a[230:231], v243 offset:840\n"
"v_mfma_f32_16x16x32_bf16 v[140:143], a[116:119], v[220:223], v[140:143]\n"
"ds_read_b64_tr_b16 a[232:233], v243 offset:128\n"
"s_waitcnt lgkmcnt(11)\n"
"v_mfma_f32_16x16x32_bf16 v[144:147], a[96:99], v[224:227], v[144:147]\n"
"s_add_u32 s78, s64, 0x45000\n"
"s_addc_u32 s79, s65, 0\n"
"LHT0:\n"
"s_cbranch_execz LHR0\n"
"v_mfma_f32_16x16x32_bf16 v[148:151], a[100:103], v[224:227], v[148:151]\n"
"s_add_u32 m0, s74, 0xcc00\n"
"ds_read_b64_tr_b16 a[234:235], v243 offset:896\n"
"v_mfma_f32_16x16x32_bf16 v[152:155], a[104:107], v[224:227], v[152:155]\n"
"ds_read_b128 v[220:223], v233 offset:0\n"
"global_load_lds_dwordx4 v240, s[78:79] offset:0\n"
"v_mfma_f32_16x16x32_bf16 v[156:159], a[108:111], v[224:227], v[156:159]\n"
"ds_read_b64_tr_b16 a[236:237], v243 offset:136\n"
"v_mfma_f32_16x16x32_bf16 v[160:163], a[112:115], v[224:227], v[160:163]\n"
"ds_read_b64_tr_b16 a[238:239], v243 offset:904\n"
"v_mfma_f32_16x16x32_bf16 v[164:167], a[116:119], v[224:227], v[164:167]\n"
"s_waitcnt lgkmcnt(13)\n"
"v_mfma_f32_16x16x32_bf16 v[168:171], a[96:99], v[228:231], v[168:171]\n"
"global_load_lds_dwordx4 v241, s[78:79] offset:1024\n"
"v_mfma_f32_16x16x32_bf16 v[172:175], a[100:103], v[228:231], v[172:175]\n"
"v_mfma_f32_16x16x32_bf16 v[176:179], a[104:107], v[228:231], v[176:179]\n"
"ds_read_b128 v[224:227], v233 offset:4096\n"
"v_mfma_f32_16x16x32_bf16 v[180:183], a[108:111], v[228:231], v[180:183]\n"
"v_mfma_f32_16x16x32_bf16 v[184:187], a[112:115], v[228:231], v[184:187]\n"
"global_load_lds_dwordx4 v242, s[78:79] offset:2048\n"
"v_mfma_f32_16x16x32_bf16 v[188:191], a[116:119], v[228:231], v[188:191]\n"
"s_waitcnt lgkmcnt(8)\n"
"v_mfma_f32_16x16x32_bf16 v[192:195], a[96:99], v[216:219], v[192:195]\n"
"v_mfma_f32_16x16x32_bf16 v[196:199], a[100:103], v[216:219], v[196:199]\n"
"global_load_dwordx4 a[248:251], v239, s[58:59] offset:-2048 sc1\n"
"s_waitcnt vmcnt(9)\n"
"ds_read_b64_tr_b16 a[0:1], v243 offset:24576\n"
"v_mfma_f32_16x16x32_bf16 v[200:203], a[104:107], v[216:219], v[200:203]\n"
"ds_read_b128 v[228:231], v233 offset:8192\n"
"ds_read_b64_tr_b16 a[2:3], v243 offset:25344\n"
"v_mfma_f32_16x16x32_bf16 v[204:207], a[108:111], v[216:219], v[204:207]\n"
"ds_read_b64_tr_b16 a[4:5], v243 offset:24584\n"
"v_mfma_f32_16x16x32_bf16 v[208:211], a[112:115], v[216:219], v[208:211]\n"
"ds_read_b64_tr_b16 a[6:7], v243 offset:25352\n"
"v_mfma_f32_16x16x32_bf16 v[212:215], a[116:119], v[216:219], v[212:215]\n"
"global_load_dwordx4 a[252:255], v239, s[58:59] offset:-1024 sc1\n"
"s_waitcnt lgkmcnt(8)\n"
"v_mfma_f32_16x16x32_bf16 v[0:3], a[120:123], v[220:223], v[0:3]\n"
"ds_read_b64_tr_b16 a[8:9], v243 offset:24640\n"
"v_mfma_f32_16x16x32_bf16 v[4:7], a[124:127], v[220:223], v[4:7]\n"
"ds_read_b64_tr_b16 a[10:11], v243 offset:25408\n"
"v_mfma_f32_16x16x32_bf16 v[8:11], a[128:131], v[220:223], v[8:11]\n"
"ds_read_b128 v[216:219], v233 offset:12288\n"
"ds_read_b64_tr_b16 a[12:13], v243 offset:24648\n"
"v_mfma_f32_16x16x32_bf16 v[12:15], a[132:135], v[220:223], v[12:15]\n"
"global_load_dwordx4 a[48:51], v239, s[58:59] offset:0 sc1\n"
"ds_read_b64_tr_b16 a[14:15], v243 offset:25416\n"
"v_mfma_f32_16x16x32_bf16 v[16:19], a[136:139], v[220:223], v[16:19]\n"
"ds_read_b64_tr_b16 a[16:17], v243 offset:24704\n"
"v_mfma_f32_16x16x32_bf16 v[20:23], a[140:143], v[220:223], v[20:23]\n"
"s_waitcnt lgkmcnt(11)\n"
"v_mfma_f32_16x16x32_bf16 v[24:27], a[120:123], v[224:227], v[24:27]\n"
"ds_read_b64_tr_b16 a[18:19], v243 offset:25472\n"
"v_mfma_f32_16x16x32_bf16 v[28:31], a[124:127], v[224:227], v[28:31]\n"
"global_load_dwordx4 a[52:55], v239, s[58:59] offset:1024 sc1\n"
"v_mfma_f32_16x16x32_bf16 v[32:35], a[128:131], v[224:227], v[32:35]\n"
"ds_read_b128 v[220:223], v233 offset:16384\n"
"v_mfma_f32_16x16x32_bf16 v[36:39], a[132:135], v[224:227], v[36:39]\n"
"ds_read_b64_tr_b16 a[20:21], v243 offset:24712\n"
"v_mfma_f32_16x16x32_bf16 v[40:43], a[136:139], v[224:227], v[40:43]\n"
"ds_read_b64_tr_b16 a[22:23], v243 offset:25480\n"
"v_mfma_f32_16x16x32_bf16 v[44:47], a[140:143], v[224:227], v[44:47]\n"
"s_add_u32 m0, s30, 0xfffffe00\n"
"s_waitcnt lgkmcnt(13)\n"
"v_mfma_f32_16x16x32_bf16 v[48:51], a[120:123], v[228:231], v[48:51]\n"
"global_load_lds_dwordx4 v236, s[32:33] offset:512 nt\n"
"v_mfma_f32_16x16x32_bf16 v[52:55], a[124:127], v[228:231], v[52:55]\n"
"v_mfma_f32_16x16x32_bf16 v[56:59], a[128:131], v[228:231], v[56:59]\n"
"ds_read_b128 v[224:227], v233 offset:20480\n"
"s_add_u32 m0, s30, 0xe00\n"
"v_mfma_f32_16x16x32_bf16 v[60:63], a[132:135], v[228:231], v[60:63]\n"
"global_load_lds_dwordx4 v236, s[34:35] offset:512 nt\n"
"v_mfma_f32_16x16x32_bf16 v[64:67], a[136:139], v[228:231], v[64:67]\n"
"v_mfma_f32_16x16x32_bf16 v[68:71], a[140:143], v[228:231], v[68:71]\n"
"s_add_u32 m0, s30, 0x1e00\n"
"s_waitcnt lgkmcnt(8)\n"
"v_mfma_f32_16x16x32_bf16 v[72:75], a[120:123], v[216:219], v[72:75]\n"
"global_load_lds_dwordx4 v236, s[36:37] offset:512 nt\n"
"v_mfma_f32_16x16x32_bf16 v[76:79], a[124:127], v[216:219], v[76:79]\n"
"v_mfma_f32_16x16x32_bf16 v[80:83], a[128:131], v[216:219], v[80:83]\n"
"ds_read_b128 v[228:231], v233 offset:24576\n"
"s_add_u32 m0, s30, 0x2e00\n"
"v_mfma_f32_16x16x32_bf16 v[84:87], a[132:135], v[216:219], v[84:87]\n"
"global_load_lds_dwordx4 v236, s[38:39] offset:512 nt\n"
"v_mfma_f32_16x16x32_bf16 v[88:91], a[136:139], v[216:219], v[88:91]\n"
"v_mfma_f32_16x16x32_bf16 v[92:95], a[140:143], v[216:219], v[92:95]\n"
"s_add_u32 m0, s30, 0x3e00\n"
"s_waitcnt lgkmcnt(4)\n"
"v_mfma_f32_16x16x32_bf16 v[96:99], a[120:123], v[220:223], v[96:99]\n"
"global_load_lds_dwordx4 v236, s[40:41] offset:512 nt\n"
"v_mfma_f32_16x16x32_bf16 v[100:103], a[124:127], v[220:223], v[100:103]\n"
"v_mfma_f32_16x16x32_bf16 v[104:107], a[128:131], v[220:223], v[104:107]\n"
"ds_read_b128 v[216:219], v233 offset:28672\n"
"s_add_u32 m0, s30, 0x4e00\n"
"v_mfma_f32_16x16x32_bf16 v[108:111], a[132:135], v[220:223], v[108:111]\n"
"global_load_lds_dwordx4 v236, s[42:43] offset:512 nt\n"
"s_waitcnt vmcnt(10)\n"
"ds_read_b64_tr_b16 a[24:25], v243 offset:49152\n"
"v_mfma_f32_16x16x32_bf16 v[112:115], a[136:139], v[220:223], v[112:115]\n"
"ds_read_b64_tr_b16 a[26:27], v243 offset:49920\n"
"v_mfma_f32_16x16x32_bf16 v[116:119], a[140:143], v[220:223], v[116:119]\n"
"s_add_u32 m0, s30, 0x5e00\n"
"s_waitcnt lgkmcnt(4)\n"
"v_mfma_f32_16x16x32_bf16 v[120:123], a[120:123], v[224:227], v[120:123]\n"
"global_load_lds_dwordx4 v236, s[44:45] offset:512 nt\n"
"ds_read_b64_tr_b16 a[28:29], v243 offset:49160\n"
"v_mfma_f32_16x16x32_bf16 v[124:127], a[124:127], v[224:227], v[124:127]\n"
"ds_read_b64_tr_b16 a[30:31], v243 offset:49928\n"
"v_mfma_f32_16x16x32_bf16 v[128:131], a[128:131], v[224:227], v[128:131]\n"
"ds_read_b128 v[220:223], v233 offset:32768\n"
"v_add_u32 v233, 0xffff7000, v233\n"
"s_add_u32 m0, s30, 0x6e00\n"
"v_mfma_f32_16x16x32_bf16 v[132:135], a[132:135], v[224:227], v[132:135]\n"
"global_load_lds_dwordx4 v236, s[46:47] offset:512 nt\n"
"ds_read_b64_tr_b16 a[32:33], v243 offset:49216\n"
"v_mfma_f32_16x16x32_bf16 v[136:139], a[136:139], v[224:227], v[136:139]\n"
"ds_read_b64_tr_b16 a[34:35], v243 offset:49984\n"
"v_mfma_f32_16x16x32_bf16 v[140:143], a[140:143], v[224:227], v[140:143]\n"
"s_add_u32 m0, s30, 0x7e00\n"
"s_waitcnt lgkmcnt(8)\n"
"v_mfma_f32_16x16x32_bf16 v[144:147], a[120:123], v[228:231], v[144:147]\n"
"global_load_lds_dwordx4 v236, s[48:49] offset:512 nt\n"
"ds_read_b64_tr_b16 a[36:37], v243 offset:49224\n"
"v_mfma_f32_16x16x32_bf16 v[148:151], a[124:127], v[228:231], v[148:151]\n"
"ds_read_b64_tr_b16 a[38:39], v243 offset:49992\n"
"v_mfma_f32_16x16x32_bf16 v[152:155], a[128:131], v[228:231], v[152:155]\n"
"ds_read_b128 v[224:227], v234 offset:0\n"
"ds_read_b64_tr_b16 a[40:41], v243 offset:49280\n"
"v_mfma_f32_16x16x32_bf16 v[156:159], a[132:135], v[228:231], v[156:159]\n"
"global_load_dwordx4 a[56:59], v239, s[58:59] offset:2048 sc1\n"
"ds_read_b64_tr_b16 a[42:43], v243 offset:50048\n"
"v_mfma_f32_16x16x32_bf16 v[160:163], a[136:139], v[228:231], v[160:163]\n"
"ds_read_b64_tr_b16 a[44:45], v243 offset:49288\n"
"v_mfma_f32_16x16x32_bf16 v[164:167], a[140:143], v[228:231], v[164:167]\n"
"s_waitcnt lgkmcnt(13)\n"
"v_mfma_f32_16x16x32_bf16 v[168:171], a[120:123], v[216:219], v[168:171]\n"
"ds_read_b64_tr_b16 a[46:47], v243 offset:50056\n"
"v_mfma_f32_16x16x32_bf16 v[172:175], a[124:127], v[216:219], v[172:175]\n"
"global_load_dwordx4 a[60:63], v239, s[58:59] offset:3072 sc1\n"
"v_mfma_f32_16x16x32_bf16 v[176:179], a[128:131], v[216:219], v[176:179]\n"
"ds_read_b128 v[228:231], v234 offset:4096\n"
"v_mfma_f32_16x16x32_bf16 v[180:183], a[132:135], v[216:219], v[180:183]\n"
"ds_read_b64 v[240:241], v250 offset:24\n"
"v_mfma_f32_16x16x32_bf16 v[184:187], a[136:139], v[216:219], v[184:187]\n"
"s_add_u32 s58, s24, 0x3000\n"
"s_addc_u32 s59, s25, 0\n"
"v_mfma_f32_16x16x32_bf16 v[188:191], a[140:143], v[216:219], v[188:191]\n"
"global_load_dwordx4 a[64:67], v239, s[58:59] offset:-4096 sc1\n"
"s_waitcnt lgkmcnt(11)\n"
"v_mfma_f32_16x16x32_bf16 v[192:195], a[120:123], v[220:223], v[192:195]\n"
"s_add_u32 s14, s51, 0\n"
"v_mfma_f32_16x16x32_bf16 v[196:199], a[124:127], v[220:223], v[196:199]\n"
"s_mul_i32 s15, s14, 0xaaab\n"
"v_mfma_f32_16x16x32_bf16 v[200:203], a[128:131], v[220:223], v[200:203]\n"
"ds_read_b128 v[216:219], v234 offset:8192\n"
"v_mfma_f32_16x16x32_bf16 v[204:207], a[132:135], v[220:223], v[204:207]\n"
"global_load_dwordx4 a[68:71], v239, s[58:59] offset:-3072 sc1\n"
"v_mfma_f32_16x16x32_bf16 v[208:211], a[136:139], v[220:223], v[208:211]\n"
"s_lshr_b32 s15, s15, 19\n"
"v_mfma_f32_16x16x32_bf16 v[212:215], a[140:143], v[220:223], v[212:215]\n"
"s_waitcnt lgkmcnt(7)\n"
"v_mfma_f32_16x16x32_bf16 v[0:3], a[144:147], v[224:227], v[0:3]\n"
"s_mul_i32 s15, s15, 0x48000\n"
"v_mfma_f32_16x16x32_bf16 v[4:7], a[148:151], v[224:227], v[4:7]\n"
"global_load_dwordx4 a[72:75], v239, s[58:59] offset:-2048 sc1\n"
"v_mfma_f32_16x16x32_bf16 v[8:11], a[152:155], v[224:227], v[8:11]\n"
"ds_read_b128 v[220:223], v234 offset:12288\n"
"v_mfma_f32_16x16x32_bf16 v[12:15], a[156:159], v[224:227], v[12:15]\n"
"s_mul_i32 s14, s14, 0x6000\n"
"v_mfma_f32_16x16x32_bf16 v[16:19], a[160:163], v[224:227], v[16:19]\n"
"s_add_u32 s32, s14, s15\n"
"v_mfma_f32_16x16x32_bf16 v[20:23], a[164:167], v[224:227], v[20:23]\n"
"global_load_dwordx4 a[76:79], v239, s[58:59] offset:-1024 sc1\n"
"s_waitcnt lgkmcnt(3)\n"
"v_mfma_f32_16x16x32_bf16 v[24:27], a[144:147], v[228:231], v[24:27]\n"
"s_add_u32 s33, s32, 0x48000\n"
"v_mfma_f32_16x16x32_bf16 v[28:31], a[148:151], v[228:231], v[28:31]\n"
"s_add_u32 s14, s51, 256\n"
"v_mfma_f32_16x16x32_bf16 v[32:35], a[152:155], v[228:231], v[32:35]\n"
"ds_read_b128 v[224:227], v234 offset:16384\n"
"v_mfma_f32_16x16x32_bf16 v[36:39], a[156:159], v[228:231], v[36:39]\n"
"global_load_dwordx4 a[80:83], v239, s[58:59] offset:0 sc1\n"
"v_mfma_f32_16x16x32_bf16 v[40:43], a[160:163], v[228:231], v[40:43]\n"
"s_mul_i32 s15, s14, 0xaaab\n"
"v_mfma_f32_16x16x32_bf16 v[44:47], a[164:167], v[228:231], v[44:47]\n"
"s_waitcnt lgkmcnt(2)\n"
"v_mfma_f32_16x16x32_bf16 v[48:51], a[144:147], v[216:219], v[48:51]\n"
"s_lshr_b32 s15, s15, 19\n"
"v_mfma_f32_16x16x32_bf16 v[52:55], a[148:151], v[216:219], v[52:55]\n"
"global_load_dwordx4 a[84:87], v239, s[58:59] offset:1024 sc1\n"
"v_mfma_f32_16x16x32_bf16 v[56:59], a[152:155], v[216:219], v[56:59]\n"
"ds_read_b128 v[228:231], v234 offset:20480\n"
"v_mfma_f32_16x16x32_bf16 v[60:63], a[156:159], v[216:219], v[60:63]\n"
"s_mul_i32 s15, s15, 0x48000\n"
"v_mfma_f32_16x16x32_bf16 v[64:67], a[160:163], v[216:219], v[64:67]\n"
"s_mul_i32 s14, s14, 0x6000\n"
"v_mfma_f32_16x16x32_bf16 v[68:71], a[164:167], v[216:219], v[68:71]\n"
"global_load_dwordx4 a[88:91], v239, s[58:59] offset:2048 sc1\n"
"s_waitcnt lgkmcnt(2)\n"
"v_mfma_f32_16x16x32_bf16 v[72:75], a[144:147], v[220:223], v[72:75]\n"
"s_add_u32 s34, s14, s15\n"
"v_mfma_f32_16x16x32_bf16 v[76:79], a[148:151], v[220:223], v[76:79]\n"
"s_add_u32 s35, s34, 0x48000\n"
"v_mfma_f32_16x16x32_bf16 v[80:83], a[152:155], v[220:223], v[80:83]\n"
"ds_read_b128 v[216:219], v234 offset:24576\n"
"v_mfma_f32_16x16x32_bf16 v[84:87], a[156:159], v[220:223], v[84:87]\n"
"global_load_dwordx4 a[92:95], v239, s[58:59] offset:3072 sc1\n"
"v_mfma_f32_16x16x32_bf16 v[88:91], a[160:163], v[220:223], v[88:91]\n"
"s_add_u32 s14, s51, 512\n"
"v_mfma_f32_16x16x32_bf16 v[92:95], a[164:167], v[220:223], v[92:95]\n"
"s_waitcnt lgkmcnt(2)\n"
"v_mfma_f32_16x16x32_bf16 v[96:99], a[144:147], v[224:227], v[96:99]\n"
"s_add_u32 s58, s24, 0x5000\n"
"s_addc_u32 s59, s25, 0\n"
"v_mfma_f32_16x16x32_bf16 v[100:103], a[148:151], v[224:227], v[100:103]\n"
"global_load_dwordx4 a[96:99], v239, s[58:59] offset:-4096 sc1\n"
"v_mfma_f32_16x16x32_bf16 v[104:107], a[152:155], v[224:227], v[104:107]\n"
"ds_read_b128 v[220:223], v234 offset:28672\n"
"v_mfma_f32_16x16x32_bf16 v[108:111], a[156:159], v[224:227], v[108:111]\n"
"s_mul_i32 s15, s14, 0xaaab\n"
"v_mfma_f32_16x16x32_bf16 v[112:115], a[160:163], v[224:227], v[112:115]\n"
"s_lshr_b32 s15, s15, 19\n"
"v_mfma_f32_16x16x32_bf16 v[116:119], a[164:167], v[224:227], v[116:119]\n"
"global_load_dwordx4 a[100:103], v239, s[58:59] offset:-3072 sc1\n"
"s_waitcnt lgkmcnt(2)\n"
"v_mfma_f32_16x16x32_bf16 v[120:123], a[144:147], v[228:231], v[120:123]\n"
"s_mul_i32 s15, s15, 0x48000\n"
"v_mfma_f32_16x16x32_bf16 v[124:127], a[148:151], v[228:231], v[124:127]\n"
"s_mul_i32 s14, s14, 0x6000\n"
"v_mfma_f32_16x16x32_bf16 v[128:131], a[152:155], v[228:231], v[128:131]\n"
"ds_read_b128 v[224:227], v234 offset:32768\n"
"v_add_u32 v234, 0xffff7000, v234\n"
"v_mfma_f32_16x16x32_bf16 v[132:135], a[156:159], v[228:231], v[132:135]\n"
"global_load_dwordx4 a[104:107], v239, s[58:59] offset:-2048 sc1\n"
"v_mfma_f32_16x16x32_bf16 v[136:139], a[160:163], v[228:231], v[136:139]\n"
"s_add_u32 s36, s14, s15\n"
"v_mfma_f32_16x16x32_bf16 v[140:143], a[164:167], v[228:231], v[140:143]\n"
"s_waitcnt lgkmcnt(2)\n"
"v_mfma_f32_16x16x32_bf16 v[144:147], a[144:147], v[216:219], v[144:147]\n"
"s_add_u32 s37, s36, 0x48000\n"
"v_mfma_f32_16x16x32_bf16 v[148:151], a[148:151], v[216:219], v[148:151]\n"
"global_load_dwordx4 a[108:111], v239, s[58:59] offset:-1024 sc1\n"
"v_mfma_f32_16x16x32_bf16 v[152:155], a[152:155], v[216:219], v[152:155]\n"
"ds_read_b128 v[228:231], v235 offset:0\n"
"v_mfma_f32_16x16x32_bf16 v[156:159], a[156:159], v[216:219], v[156:159]\n"
"s_add_u32 s38, s32, 0x2400000\n"
"v_mfma_f32_16x16x32_bf16 v[160:163], a[160:163], v[216:219], v[160:163]\n"
"s_add_u32 s39, s33, 0x2400000\n"
"v_mfma_f32_16x16x32_bf16 v[164:167], a[164:167], v[216:219], v[164:167]\n"
"global_load_dwordx4 a[112:115], v239, s[58:59] offset:0 sc1\n"
"s_waitcnt lgkmcnt(2)\n"
"v_mfma_f32_16x16x32_bf16 v[168:171], a[144:147], v[220:223], v[168:171]\n"
"s_add_u32 s40, s34, 0x2400000\n"
"v_mfma_f32_16x16x32_bf16 v[172:175], a[148:151], v[220:223], v[172:175]\n"
"s_add_u32 s41, s35, 0x2400000\n"
"v_mfma_f32_16x16x32_bf16 v[176:179], a[152:155], v[220:223], v[176:179]\n"
"ds_read_b128 v[216:219], v235 offset:4096\n"
"v_mfma_f32_16x16x32_bf16 v[180:183], a[156:159], v[220:223], v[180:183]\n"
"global_load_dwordx4 a[116:119], v239, s[58:59] offset:1024 sc1\n"
"v_mfma_f32_16x16x32_bf16 v[184:187], a[160:163], v[220:223], v[184:187]\n"
"s_add_u32 s42, s36, 0x2400000\n"
"v_mfma_f32_16x16x32_bf16 v[188:191], a[164:167], v[220:223], v[188:191]\n"
"s_waitcnt lgkmcnt(2)\n"
"v_mfma_f32_16x16x32_bf16 v[192:195], a[144:147], v[224:227], v[192:195]\n"
"s_add_u32 s43, s37, 0x2400000\n"
"v_mfma_f32_16x16x32_bf16 v[196:199], a[148:151], v[224:227], v[196:199]\n"
"global_load_dwordx4 a[120:123], v239, s[58:59] offset:2048 sc1\n"
"v_mfma_f32_16x16x32_bf16 v[200:203], a[152:155], v[224:227], v[200:203]\n"
"ds_read_b128 v[220:223], v235 offset:8192\n"
"v_mfma_f32_16x16x32_bf16 v[204:207], a[156:159], v[224:227], v[204:207]\n"
"s_add_u32 s44, s38, 0x2400000\n"
"v_mfma_f32_16x16x32_bf16 v[208:211], a[160:163], v[224:227], v[208:211]\n"
"s_add_u32 s45, s39, 0x2400000\n"
"v_mfma_f32_16x16x32_bf16 v[212:215], a[164:167], v[224:227], v[212:215]\n"
"global_load_dwordx4 a[124:127], v239, s[58:59] offset:3072 sc1\n"
"s_waitcnt lgkmcnt(2)\n"
"v_mfma_f32_16x16x32_bf16 v[0:3], a[168:171], v[228:231], v[0:3]\n"
"s_add_u32 s46, s40, 0x2400000\n"
"v_mfma_f32_16x16x32_bf16 v[4:7], a[172:175], v[228:231], v[4:7]\n"
"s_add_u32 s47, s41, 0x2400000\n"
"v_mfma_f32_16x16x32_bf16 v[8:11], a[176:179], v[228:231], v[8:11]\n"
"ds_read_b128 v[224:227], v235 offset:12288\n"
"s_add_u32 s58, s24, 0x7000\n"
"s_addc_u32 s59, s25, 0\n"
"v_mfma_f32_16x16x32_bf16 v[12:15], a[180:183], v[228:231], v[12:15]\n"
"global_load_dwordx4 a[128:131], v239, s[58:59] offset:-4096 sc1\n"
"v_mfma_f32_16x16x32_bf16 v[16:19], a[184:187], v[228:231], v[16:19]\n"
"s_add_u32 s48, s42, 0x2400000\n"
"v_mfma_f32_16x16x32_bf16 v[20:23], a[188:191], v[228:231], v[20:23]\n"
"s_waitcnt lgkmcnt(2)\n"
"v_mfma_f32_16x16x32_bf16 v[24:27], a[168:171], v[216:219], v[24:27]\n"
"s_add_u32 s49, s43, 0x2400000\n"
"v_mfma_f32_16x16x32_bf16 v[28:31], a[172:175], v[216:219], v[28:31]\n"
"global_load_dwordx4 a[132:135], v239, s[58:59] offset:-3072 sc1\n"
"v_mfma_f32_16x16x32_bf16 v[32:35], a[176:179], v[216:219], v[32:35]\n"
"ds_read_b128 v[228:231], v235 offset:16384\n"
"v_mfma_f32_16x16x32_bf16 v[36:39], a[180:183], v[216:219], v[36:39]\n"
"v_mfma_f32_16x16x32_bf16 v[40:43], a[184:187], v[216:219], v[40:43]\n"
"v_mfma_f32_16x16x32_bf16 v[44:47], a[188:191], v[216:219], v[44:47]\n"
"global_load_dwordx4 a[136:139], v239, s[58:59] offset:-2048 sc1\n"
"s_waitcnt lgkmcnt(2)\n"
"v_mfma_f32_16x16x32_bf16 v[48:51], a[168:171], v[220:223], v[48:51]\n"
"v_mfma_f32_16x16x32_bf16 v[52:55], a[172:175], v[220:223], v[52:55]\n"
"v_mfma_f32_16x16x32_bf16 v[56:59], a[176:179], v[220:223], v[56:59]\n"
"ds_read_b128 v[216:219], v235 offset:20480\n"
"v_mfma_f32_16x16x32_bf16 v[60:63], a[180:183], v[220:223], v[60:63]\n"
"global_load_dwordx4 a[140:143], v239, s[58:59] offset:-1024 sc1\n"
"v_mfma_f32_16x16x32_bf16 v[64:67], a[184:187], v[220:223], v[64:67]\n"
"v_mfma_f32_16x16x32_bf16 v[68:71], a[188:191], v[220:223], v[68:71]\n"
"s_waitcnt lgkmcnt(2)\n"
"v_mfma_f32_16x16x32_bf16 v[72:75], a[168:171], v[224:227], v[72:75]\n"
"v_mfma_f32_16x16x32_bf16 v[76:79], a[172:175], v[224:227], v[76:79]\n"
"global_load_dwordx4 a[144:147], v239, s[58:59] offset:0 sc1\n"
"v_mfma_f32_16x16x32_bf16 v[80:83], a[176:179], v[224:227], v[80:83]\n"
"ds_read_b128 v[220:223], v235 offset:24576\n"
"v_mfma_f32_16x16x32_bf16 v[84:87], a[180:183], v[224:227], v[84:87]\n"
"v_mfma_f32_16x16x32_bf16 v[88:91], a[184:187], v[224:227], v[88:91]\n"
"v_mfma_f32_16x16x32_bf16 v[92:95], a[188:191], v[224:227], v[92:95]\n"
"global_load_dwordx4 a[148:151], v239, s[58:59] offset:1024 sc1\n"
"s_waitcnt lgkmcnt(2)\n"
"v_mfma_f32_16x16x32_bf16 v[96:99], a[168:171], v[228:231], v[96:99]\n"
"v_mfma_f32_16x16x32_bf16 v[100:103], a[172:175], v[228:231], v[100:103]\n"
"v_mfma_f32_16x16x32_bf16 v[104:107], a[176:179], v[228:231], v[104:107]\n"
"ds_read_b128 v[224:227], v235 offset:28672\n"
"v_mfma_f32_16x16x32_bf16 v[108:111], a[180:183], v[228:231], v[108:111]\n"
"global_load_dwordx4 a[152:155], v239, s[58:59] offset:2048 sc1\n"
"v_mfma_f32_16x16x32_bf16 v[112:115], a[184:187], v[228:231], v[112:115]\n"
"v_mfma_f32_16x16x32_bf16 v[116:119], a[188:191], v[228:231], v[116:119]\n"
"s_waitcnt lgkmcnt(2)\n"
"v_mfma_f32_16x16x32_bf16 v[120:123], a[168:171], v[216:219], v[120:123]\n"
"v_mfma_f32_16x16x32_bf16 v[124:127], a[172:175], v[216:219], v[124:127]\n"
"global_load_dwordx4 a[156:159], v239, s[58:59] offset:3072 sc1\n"
"v_mfma_f32_16x16x32_bf16 v[128:131], a[176:179], v[216:219], v[128:131]\n"
"ds_read_b128 v[228:231], v235 offset:32768\n"
"v_add_u32 v235, 0xffff7000, v235\n"
"v_mfma_f32_16x16x32_bf16 v[132:135], a[180:183], v[216:219], v[132:135]\n"
"v_mfma_f32_16x16x32_bf16 v[136:139], a[184:187], v[216:219], v[136:139]\n"
"s_add_u32 s58, s24, 0x9000\n"
"s_addc_u32 s59, s25, 0\n"
"v_mfma_f32_16x16x32_bf16 v[140:143], a[188:191], v[216:219], v[140:143]\n"
"global_load_dwordx4 a[160:163], v239, s[58:59] offset:-4096 sc1\n"
"s_waitcnt lgkmcnt(2)\n"
"v_mfma_f32_16x16x32_bf16 v[144:147], a[168:171], v[220:223], v[144:147]\n"
"v_mfma_f32_16x16x32_bf16 v[148:151], a[172:175], v[220:223], v[148:151]\n"
"s_waitcnt vmcnt(27)\n"
"s_barrier\n"
"v_mfma_f32_16x16x32_bf16 v[152:155], a[176:179], v[220:223], v[152:155]\n"
"ds_read_b128 v[216:219], v232 offset:0\n"
"v_mfma_f32_16x16x32_bf16 v[156:159], a[180:183], v[220:223], v[156:159]\n"
"global_load_dwordx4 a[164:167], v239, s[58:59] offset:-3072 sc1\n"
"v_mfma_f32_16x16x32_bf16 v[160:163], a[184:187], v[220:223], v[160:163]\n"
"v_mfma_f32_16x16x32_bf16 v[164:167], a[188:191], v[220:223], v[164:167]\n"
"s_waitcnt lgkmcnt(2)\n"
"v_mfma_f32_16x16x32_bf16 v[168:171], a[168:171], v[224:227], v[168:171]\n"
"v_mfma_f32_16x16x32_bf16 v[172:175], a[172:175], v[224:227], v[172:175]\n"
"v_mfma_f32_16x16x32_bf16 v[176:179], a[176:179], v[224:227], v[176:179]\n"
"ds_read_b128 v[220:223], v232 offset:4096\n"
"v_mfma_f32_16x16x32_bf16 v[180:183], a[180:183], v[224:227], v[180:183]\n"
"v_mfma_f32_16x16x32_bf16 v[184:187], a[184:187], v[224:227], v[184:187]\n"
"v_mfma_f32_16x16x32_bf16 v[188:191], a[188:191], v[224:227], v[188:191]\n"
"s_waitcnt lgkmcnt(2)\n"
"v_mfma_f32_16x16x32_bf16 v[192:195], a[168:171], v[228:231], v[192:195]\n"
"v_mfma_f32_16x16x32_bf16 v[196:199], a[172:175], v[228:231], v[196:199]\n"
"v_mfma_f32_16x16x32_bf16 v[200:203], a[176:179], v[228:231], v[200:203]\n"
"ds_read_b128 v[224:227], v232 offset:8192\n"
"v_mfma_f32_16x16x32_bf16 v[204:207], a[180:183], v[228:231], v[204:207]\n"
"v_mfma_f32_16x16x32_bf16 v[208:211], a[184:187], v[228:231], v[208:211]\n"
"v_mfma_f32_16x16x32_bf16 v[212:215], a[188:191], v[228:231], v[212:215]\n"
"s_waitcnt lgkmcnt(2)\n"
"v_mfma_f32_16x16x32_bf16 v[0:3], a[192:195], v[216:219], v[0:3]\n"
"global_load_dwordx4 a[168:171], v239, s[58:59] offset:-2048 sc1\n"
"v_mfma_f32_16x16x32_bf16 v[4:7], a[196:199], v[216:219], v[4:7]\n"
"v_mfma_f32_16x16x32_bf16 v[8:11], a[200:203], v[216:219], v[8:11]\n"
"ds_read_b128 v[228:231], v232 offset:12288\n"
"v_mfma_f32_16x16x32_bf16 v[12:15], a[204:207], v[216:219], v[12:15]\n"
"v_mfma_f32_16x16x32_bf16 v[16:19], a[208:211], v[216:219], v[16:19]\n"
"global_load_dwordx4 a[172:175], v239, s[58:59] offset:-1024 sc1\n"
"v_mfma_f32_16x16x32_bf16 v[20:23], a[212:215], v[216:219], v[20:23]\n"
"s_waitcnt lgkmcnt(2)\n"
"v_mfma_f32_16x16x32_bf16 v[24:27], a[192:195], v[220:223], v[24:27]\n"
"v_mfma_f32_16x16x32_bf16 v[28:31], a[196:199], v[220:223], v[28:31]\n"
"v_mfma_f32_16x16x32_bf16 v[32:35], a[200:203], v[220:223], v[32:35]\n"
"ds_read_b128 v[216:219], v232 offset:16384\n"
"global_load_dwordx4 a[176:179], v239, s[58:59] offset:0 sc1\n"
"v_mfma_f32_16x16x32_bf16 v[36:39], a[204:207], v[220:223], v[36:39]\n"
"v_mfma_f32_16x16x32_bf16 v[40:43], a[208:211], v[220:223], v[40:43]\n"
"v_mfma_f32_16x16x32_bf16 v[44:47], a[212:215], v[220:223], v[44:47]\n"
"s_waitcnt lgkmcnt(2)\n"
"v_mfma_f32_16x16x32_bf16 v[48:51], a[192:195], v[224:227], v[48:51]\n"
"global_load_dwordx4 a[180:183], v239, s[58:59] offset:1024 sc1\n"
"v_mfma_f32_16x16x32_bf16 v[52:55], a[196:199], v[224:227], v[52:55]\n"
"v_mfma_f32_16x16x32_bf16 v[56:59], a[200:203], v[224:227], v[56:59]\n"
"ds_read_b128 v[220:223], v232 offset:20480\n"
"v_mfma_f32_16x16x32_bf16 v[60:63], a[204:207], v[224:227], v[60:63]\n"
"v_mfma_f32_16x16x32_bf16 v[64:67], a[208:211], v[224:227], v[64:67]\n"
"global_load_dwordx4 a[184:187], v239, s[58:59] offset:2048 sc1\n"
"v_mfma_f32_16x16x32_bf16 v[68:71], a[212:215], v[224:227], v[68:71]\n"
"s_waitcnt lgkmcnt(2)\n"
"v_mfma_f32_16x16x32_bf16 v[72:75], a[192:195], v[228:231], v[72:75]\n"
"v_mfma_f32_16x16x32_bf16 v[76:79], a[196:199], v[228:231], v[76:79]\n"
"v_mfma_f32_16x16x32_bf16 v[80:83], a[200:203], v[228:231], v[80:83]\n"
"ds_read_b128 v[224:227], v232 offset:24576\n"
"global_load_dwordx4 a[188:191], v239, s[58:59] offset:3072 sc1\n"
"v_mfma_f32_16x16x32_bf16 v[84:87], a[204:207], v[228:231], v[84:87]\n"
"v_mfma_f32_16x16x32_bf16 v[88:91], a[208:211], v[228:231], v[88:91]\n"
"v_mfma_f32_16x16x32_bf16 v[92:95], a[212:215], v[228:231], v[92:95]\n"
"s_waitcnt lgkmcnt(2)\n"
"v_mfma_f32_16x16x32_bf16 v[96:99], a[192:195], v[216:219], v[96:99]\n"
"s_add_u32 s52, s20, s32\n"
"s_addc_u32 s53, s21, 0\n"
"s_add_u32 m0, s31, 0x0\n"
"v_mfma_f32_16x16x32_bf16 v[100:103], a[196:199], v[216:219], v[100:103]\n"
"global_load_lds_dwordx4 v237, s[52:53] offset:0\n"
"v_mfma_f32_16x16x32_bf16 v[104:107], a[200:203], v[216:219], v[104:107]\n"
"ds_read_b128 v[228:231], v232 offset:28672\n"
"s_add_u32 m0, s31, 0x1f00\n"
"v_mfma_f32_16x16x32_bf16 v[108:111], a[204:207], v[216:219], v[108:111]\n"
"global_load_lds_dwordx4 v237, s[52:53] offset:256\n"
"v_mfma_f32_16x16x32_bf16 v[112:115], a[208:211], v[216:219], v[112:115]\n"
"s_add_u32 m0, s31, 0x3e00\n"
"v_mfma_f32_16x16x32_bf16 v[116:119], a[212:215], v[216:219], v[116:119]\n"
"global_load_lds_dwordx4 v237, s[52:53] offset:512\n"
"s_waitcnt lgkmcnt(2)\n"
"v_mfma_f32_16x16x32_bf16 v[120:123], a[192:195], v[220:223], v[120:123]\n"
"s_add_u32 m0, s31, 0x1000\n"
"v_mfma_f32_16x16x32_bf16 v[124:127], a[196:199], v[220:223], v[124:127]\n"
"global_load_lds_dwordx4 v238, s[52:53] offset:0\n"
"v_mfma_f32_16x16x32_bf16 v[128:131], a[200:203], v[220:223], v[128:131]\n"
"ds_read_b128 v[216:219], v232 offset:32768\n"
"v_add_u32 v232, v249, v232\n"
"s_add_u32 m0, s31, 0x2f00\n"
"v_mfma_f32_16x16x32_bf16 v[132:135], a[204:207], v[220:223], v[132:135]\n"
"global_load_lds_dwordx4 v238, s[52:53] offset:256\n"
"v_mfma_f32_16x16x32_bf16 v[136:139], a[208:211], v[220:223], v[136:139]\n"
"s_add_u32 m0, s31, 0x4e00\n"
"v_mfma_f32_16x16x32_bf16 v[140:143], a[212:215], v[220:223], v[140:143]\n"
"global_load_lds_dwordx4 v238, s[52:53] offset:512\n"
"s_waitcnt lgkmcnt(2)\n"
"v_mfma_f32_16x16x32_bf16 v[144:147], a[192:195], v[224:227], v[144:147]\n"
"v_mfma_f32_16x16x32_bf16 v[148:151], a[196:199], v[224:227], v[148:151]\n"
"v_mfma_f32_16x16x32_bf16 v[152:155], a[200:203], v[224:227], v[152:155]\n"
"ds_read_b128 v[220:223], v233 offset:0\n"
"v_mfma_f32_16x16x32_bf16 v[156:159], a[204:207], v[224:227], v[156:159]\n"
"v_mfma_f32_16x16x32_bf16 v[160:163], a[208:211], v[224:227], v[160:163]\n"
"v_mfma_f32_16x16x32_bf16 v[164:167], a[212:215], v[224:227], v[164:167]\n"
"s_waitcnt lgkmcnt(2)\n"
"v_mfma_f32_16x16x32_bf16 v[168:171], a[192:195], v[228:231], v[168:171]\n"
"v_mfma_f32_16x16x32_bf16 v[172:175], a[196:199], v[228:231], v[172:175]\n"
"v_mfma_f32_16x16x32_bf16 v[176:179], a[200:203], v[228:231], v[176:179]\n"
"ds_read_b128 v[224:227], v233 offset:4096\n"
"v_mfma_f32_16x16x32_bf16 v[180:183], a[204:207], v[228:231], v[180:183]\n"
"v_mfma_f32_16x16x32_bf16 v[184:187], a[208:211], v[228:231], v[184:187]\n"
"s_memrealtime s[84:85]\n"
"v_mfma_f32_16x16x32_bf16 v[188:191], a[212:215], v[228:231], v[188:191]\n"
"v_mov_b32 v244, s80\n"
"s_waitcnt lgkmcnt(2)\n"
"v_mfma_f32_16x16x32_bf16 v[192:195], a[192:195], v[216:219], v[192:195]\n"
"v_mfma_f32_16x16x32_bf16 v[196:199], a[196:199], v[216:219], v[196:199]\n"
"v_mov_b32 v245, s81\n"
"v_mfma_f32_16x16x32_bf16 v[200:203], a[200:203], v[216:219], v[200:203]\n"
"ds_read_b128 v[228:231], v233 offset:8192\n"
"v_mfma_f32_16x16x32_bf16 v[204:207], a[204:207], v[216:219], v[204:207]\n"
"v_mov_b32 v251, s77\n"
"v_mfma_f32_16x16x32_bf16 v[208:211], a[208:211], v[216:219], v[208:211]\n"
"s_mov_b64 exec, s[6:7]\n"
"s_cbranch_execz LHT1\n"
"LHR1:\n"
"s_mov_b64 exec, -1\n"
"v_mfma_f32_16x16x32_bf16 v[212:215], a[212:215], v[216:219], v[212:215]\n"
"s_waitcnt lgkmcnt(0)\n"
"v_mov_b32 v246, s84\n"
"s_add_u32 s58, s24, 0xb000\n"
"s_addc_u32 s59, s25, 0\n"
"v_mfma_f32_16x16x32_bf16 v[0:3], a[216:219], v[220:223], v[0:3]\n"
"global_load_dwordx4 a[192:195], v239, s[58:59] offset:-4096 sc1\n"
"v_mfma_f32_16x16x32_bf16 v[4:7], a[220:223], v[220:223], v[4:7]\n"
"s_lshl_b32 s90, s76, 7\n"
"s_add_u32 s90, s90, s77\n"
"s_cmp_ge_u32 s90, 4224\n"
"s_cbranch_scc1 LQ_0\n"
"s_mov_b64 exec, 1\n"
"global_store_dwordx4 v251, v[244:247], s[28:29] offset:3072 sc1\n"
"s_mov_b64 exec, -1\n"
"LQ_0:\n"
"s_nop 0\n"
"v_mfma_f32_16x16x32_bf16 v[8:11], a[224:227], v[220:223], v[8:11]\n"
"ds_read_b128 v[216:219], v233 offset:12288\n"
"v_mfma_f32_16x16x32_bf16 v[12:15], a[228:231], v[220:223], v[12:15]\n"
"v_mfma_f32_16x16x32_bf16 v[16:19], a[232:235], v[220:223], v[16:19]\n"
"global_load_dwordx4 a[196:199], v239, s[58:59] offset:-3072 sc1\n"
"v_mfma_f32_16x16x32_bf16 v[20:23], a[236:239], v[220:223], v[20:23]\n"
"v_mfma_f32_16x16x32_bf16 v[24:27], a[216:219], v[224:227], v[24:27]\n"
"v_mfma_f32_16x16x32_bf16 v[28:31], a[220:223], v[224:227], v[28:31]\n"
"v_mfma_f32_16x16x32_bf16 v[32:35], a[224:227], v[224:227], v[32:35]\n"
"ds_read_b128 v[220:223], v233 offset:16384\n"
"global_load_dwordx4 a[200:203], v239, s[58:59] offset:-2048 sc1\n"
"v_mfma_f32_16x16x32_bf16 v[36:39], a[228:231], v[224:227], v[36:39]\n"
"v_mfma_f32_16x16x32_bf16 v[40:43], a[232:235], v[224:227], v[40:43]\n"
"v_mfma_f32_16x16x32_bf16 v[44:47], a[236:239], v[224:227], v[44:47]\n"
"v_mfma_f32_16x16x32_bf16 v[48:51], a[216:219], v[228:231], v[48:51]\n"
"global_load_dwordx4 a[204:207], v239, s[58:59] offset:-1024 sc1\n"
"v_mfma_f32_16x16x32_bf16 v[52:55], a[220:223], v[228:231], v[52:55]\n"
"v_mfma_f32_16x16x32_bf16 v[56:59], a[224:227], v[228:231], v[56:59]\n"
"ds_read_b128 v[224:227], v233 offset:20480\n"
"v_mfma_f32_16x16x32_bf16 v[60:63], a[228:231], v[228:231], v[60:63]\n"
"v_mfma_f32_16x16x32_bf16 v[64:67], a[232:235], v[228:231], v[64:67]\n"
"global_load_dwordx4 a[208:211], v239, s[58:59] offset:0 sc1\n"
"v_mfma_f32_16x16x32_bf16 v[68:71], a[236:239], v[228:231], v[68:71]\n"
"s_waitcnt lgkmcnt(2)\n"
"v_mfma_f32_16x16x32_bf16 v[72:75], a[216:219], v[216:219], v[72:75]\n"
"v_mfma_f32_16x16x32_bf16 v[76:79], a[220:223], v[216:219], v[76:79]\n"
"v_mfma_f32_16x16x32_bf16 v[80:83], a[224:227], v[216:219], v[80:83]\n"
"ds_read_b128 v[228:231], v233 offset:24576\n"
"global_load_dwordx4 a[212:215], v239, s[58:59] offset:1024 sc1\n"
"v_mfma_f32_16x16x32_bf16 v[84:87], a[228:231], v[216:219], v[84:87]\n"
"v_mfma_f32_16x16x32_bf16 v[88:91], a[232:235], v[216:219], v[88:91]\n"
"v_mfma_f32_16x16x32_bf16 v[92:95], a[236:239], v[216:219], v[92:95]\n"
"s_waitcnt lgkmcnt(2)\n"
"v_mfma_f32_16x16x32_bf16 v[96:99], a[216:219], v[220:223], v[96:99]\n"
"v_mfma_f32_16x16x32_bf16 v[100:103], a[220:223], v[220:223], v[100:103]\n"
"v_mfma_f32_16x16x32_bf16 v[104:107], a[224:227], v[220:223], v[104:107]\n"
"ds_read_b128 v[216:219], v233 offset:28672\n"
"v_mfma_f32_16x16x32_bf16 v[108:111], a[228:231], v[220:223], v[108:111]\n"
"v_mfma_f32_16x16x32_bf16 v[112:115], a[232:235], v[220:223], v[112:115]\n"
"v_mfma_f32_16x16x32_bf16 v[116:119], a[236:239], v[220:223], v[116:119]\n"
"s_waitcnt lgkmcnt(2)\n"
"v_mfma_f32_16x16x32_bf16 v[120:123], a[216:219], v[224:227], v[120:123]\n"
"v_mfma_f32_16x16x32_bf16 v[124:127], a[220:223], v[224:227], v[124:127]\n"
"v_mfma_f32_16x16x32_bf16 v[128:131], a[224:227], v[224:227], v[128:131]\n"
"ds_read_b128 v[220:223], v233 offset:32768\n"
"v_add_u32 v233, v249, v233\n"
"v_mfma_f32_16x16x32_bf16 v[132:135], a[228:231], v[224:227], v[132:135]\n"
"v_mfma_f32_16x16x32_bf16 v[136:139], a[232:235], v[224:227], v[136:139]\n"
"v_mfma_f32_16x16x32_bf16 v[140:143], a[236:239], v[224:227], v[140:143]\n"
"s_waitcnt lgkmcnt(2)\n"
"v_mfma_f32_16x16x32_bf16 v[144:147], a[216:219], v[228:231], v[144:147]\n"
"v_mfma_f32_16x16x32_bf16 v[148:151], a[220:223], v[228:231], v[148:151]\n"
"v_mfma_f32_16x16x32_bf16 v[152:155], a[224:227], v[228:231], v[152:155]\n"
"ds_read_b128 v[224:227], v234 offset:0\n"
"v_mfma_f32_16x16x32_bf16 v[156:159], a[228:231], v[228:231], v[156:159]\n"
"v_mfma_f32_16x16x32_bf16 v[160:163], a[232:235], v[228:231], v[160:163]\n"
"v_mfma_f32_16x16x32_bf16 v[164:167], a[236:239], v[228:231], v[164:167]\n"
"s_waitcnt lgkmcnt(2)\n"
"v_mfma_f32_16x16x32_bf16 v[168:171], a[216:219], v[216:219], v[168:171]\n"
"v_mfma_f32_16x16x32_bf16 v[172:175], a[220:223], v[216:219], v[172:175]\n"
"v_mfma_f32_16x16x32_bf16 v[176:179], a[224:227], v[216:219], v[176:179]\n"
"ds_read_b128 v[228:231], v234 offset:4096\n"
"v_mfma_f32_16x16x32_bf16 v[180:183], a[228:231], v[216:219], v[180:183]\n"
"v_mfma_f32_16x16x32_bf16 v[184:187], a[232:235], v[216:219], v[184:187]\n"
"s_add_u32 s52, s20, s33\n"
"s_addc_u32 s53, s21, 0\n"
"s_add_u32 m0, s31, 0x6000\n"
"LHT1:\n"
"s_cbranch_execz LHR1\n"
"v_mfma_f32_16x16x32_bf16 v[188:191], a[236:239], v[216:219], v[188:191]\n"
"global_load_lds_dwordx4 v237, s[52:53] offset:0\n"
"s_waitcnt lgkmcnt(2)\n"
"v_mfma_f32_16x16x32_bf16 v[192:195], a[216:219], v[220:223], v[192:195]\n"
"s_add_u32 m0, s31, 0x7f00\n"
"v_mfma_f32_16x16x32_bf16 v[196:199], a[220:223], v[220:223], v[196:199]\n"
"global_load_lds_dwordx4 v237, s[52:53] offset:256\n"
"v_mfma_f32_16x16x32_bf16 v[200:203], a[224:227], v[220:223], v[200:203]\n"
"ds_read_b128 v[216:219], v234 offset:8192\n"
"s_add_u32 m0, s31, 0x9e00\n"
"v_mfma_f32_16x16x32_bf16 v[204:207], a[228:231], v[220:223], v[204:207]\n"
"global_load_lds_dwordx4 v237, s[52:53] offset:512\n"
"v_mfma_f32_16x16x32_bf16 v[208:211], a[232:235], v[220:223], v[208:211]\n"
"s_add_u32 m0, s31, 0x7000\n"
"v_mfma_f32_16x16x32_bf16 v[212:215], a[236:239], v[220:223], v[212:215]\n"
"global_load_lds_dwordx4 v238, s[52:53] offset:0\n"
"s_waitcnt lgkmcnt(2)\n"
"v_mfma_f32_16x16x32_bf16 v[0:3], a[0:3], v[224:227], v[0:3]\n"
"s_add_u32 m0, s31, 0x8f00\n"
"ds_read_b128 v[244:247], v250\n"
"v_mfma_f32_16x16x32_bf16 v[4:7], a[4:7], v[224:227], v[4:7]\n"
"global_load_lds_dwordx4 v238, s[52:53] offset:256\n"
"ds_read_b64 v[252:253], v250 offset:16\n"
"v_mfma_f32_16x16x32_bf16 v[8:11], a[8:11], v[224:227], v[8:11]\n"
"ds_read_b128 v[220:223], v234 offset:12288\n"
"s_add_u32 m0, s31, 0xae00\n"
"v_mfma_f32_16x16x32_bf16 v[12:15], a[12:15], v[224:227], v[12:15]\n"
"global_load_lds_dwordx4 v238, s[52:53] offset:512\n"
"v_mfma_f32_16x16x32_bf16 v[16:19], a[16:19], v[224:227], v[16:19]\n"
"v_mfma_f32_16x16x32_bf16 v[20:23], a[20:23], v[224:227], v[20:23]\n"
"global_load_dwordx4 a[216:219], v239, s[58:59] offset:2048 sc1\n"
"s_waitcnt lgkmcnt(4)\n"
"v_mfma_f32_16x16x32_bf16 v[24:27], a[0:3], v[228:231], v[24:27]\n"
"v_mfma_f32_16x16x32_bf16 v[28:31], a[4:7], v[228:231], v[28:31]\n"
"v_mfma_f32_16x16x32_bf16 v[32:35], a[8:11], v[228:231], v[32:35]\n"
"ds_read_b128 v[224:227], v234 offset:16384\n"
"v_mfma_f32_16x16x32_bf16 v[36:39], a[12:15], v[228:231], v[36:39]\n"
"global_load_dwordx4 a[220:223], v239, s[58:59] offset:3072 sc1\n"
"v_mfma_f32_16x16x32_bf16 v[40:43], a[16:19], v[228:231], v[40:43]\n"
"v_mfma_f32_16x16x32_bf16 v[44:47], a[20:23], v[228:231], v[44:47]\n"
"s_waitcnt lgkmcnt(4)\n"
"v_mfma_f32_16x16x32_bf16 v[48:51], a[0:3], v[216:219], v[48:51]\n"
"s_add_u32 s58, s24, 0xd000\n"
"s_addc_u32 s59, s25, 0\n"
"v_mfma_f32_16x16x32_bf16 v[52:55], a[4:7], v[216:219], v[52:55]\n"
"global_load_dwordx4 a[224:227], v239, s[58:59] offset:-4096 sc1\n"
"v_mfma_f32_16x16x32_bf16 v[56:59], a[8:11], v[216:219], v[56:59]\n"
"ds_read_b128 v[228:231], v234 offset:20480\n"
"v_mfma_f32_16x16x32_bf16 v[60:63], a[12:15], v[216:219], v[60:63]\n"
"v_mfma_f32_16x16x32_bf16 v[64:67], a[16:19], v[216:219], v[64:67]\n"
"v_mfma_f32_16x16x32_bf16 v[68:71], a[20:23], v[216:219], v[68:71]\n"
"global_load_dwordx4 a[228:231], v239, s[58:59] offset:-3072 sc1\n"
"s_waitcnt lgkmcnt(2)\n"
"v_mfma_f32_16x16x32_bf16 v[72:75], a[0:3], v[220:223], v[72:75]\n"
"v_mfma_f32_16x16x32_bf16 v[76:79], a[4:7], v[220:223], v[76:79]\n"
"v_mfma_f32_16x16x32_bf16 v[80:83], a[8:11], v[220:223], v[80:83]\n"
"ds_read_b128 v[216:219], v234 offset:24576\n"
"v_mfma_f32_16x16x32_bf16 v[84:87], a[12:15], v[220:223], v[84:87]\n"
"global_load_dwordx4 a[232:235], v239, s[58:59] offset:-2048 sc1\n"
"v_mfma_f32_16x16x32_bf16 v[88:91], a[16:19], v[220:223], v[88:91]\n"
"v_mfma_f32_16x16x32_bf16 v[92:95], a[20:23], v[220:223], v[92:95]\n"
"s_waitcnt lgkmcnt(2)\n"
"v_mfma_f32_16x16x32_bf16 v[96:99], a[0:3], v[224:227], v[96:99]\n"
"v_mfma_f32_16x16x32_bf16 v[100:103], a[4:7], v[224:227], v[100:103]\n"
"global_load_dwordx4 a[236:239], v239, s[58:59] offset:-1024 sc1\n"
"v_mfma_f32_16x16x32_bf16 v[104:107], a[8:11], v[224:227], v[104:107]\n"
"ds_read_b128 v[220:223], v234 offset:28672\n"
"v_mfma_f32_16x16x32_bf16 v[108:111], a[12:15], v[224:227], v[108:111]\n"
"v_mfma_f32_16x16x32_bf16 v[112:115], a[16:19], v[224:227], v[112:115]\n"
"v_mfma_f32_16x16x32_bf16 v[116:119], a[20:23], v[224:227], v[116:119]\n"
"s_waitcnt lgkmcnt(2)\n"
"v_mfma_f32_16x16x32_bf16 v[120:123], a[0:3], v[228:231], v[120:123]\n"
"v_mfma_f32_16x16x32_bf16 v[124:127], a[4:7], v[228:231], v[124:127]\n"
"v_mfma_f32_16x16x32_bf16 v[128:131], a[8:11], v[228:231], v[128:131]\n"
"ds_read_b128 v[224:227], v234 offset:32768\n"
"v_add_u32 v234, v249, v234\n"
"v_mfma_f32_16x16x32_bf16 v[132:135], a[12:15], v[228:231], v[132:135]\n"
"v_mfma_f32_16x16x32_bf16 v[136:139], a[16:19], v[228:231], v[136:139]\n"
"v_mfma_f32_16x16x32_bf16 v[140:143], a[20:23], v[228:231], v[140:143]\n"
"s_waitcnt lgkmcnt(2)\n"
"v_mfma_f32_16x16x32_bf16 v[144:147], a[0:3], v[216:219], v[144:147]\n"
"v_mfma_f32_16x16x32_bf16 v[148:151], a[4:7], v[216:219], v[148:151]\n"
"v_mfma_f32_16x16x32_bf16 v[152:155], a[8:11], v[216:219], v[152:155]\n"
"ds_read_b128 v[228:231], v235 offset:4096\n"
"v_mfma_f32_16x16x32_bf16 v[156:159], a[12:15], v[216:219], v[156:159]\n"
"v_mfma_f32_16x16x32_bf16 v[160:163], a[16:19], v[216:219], v[160:163]\n"
"v_mfma_f32_16x16x32_bf16 v[164:167], a[20:23], v[216:219], v[164:167]\n"
"s_waitcnt lgkmcnt(2)\n"
"v_mfma_f32_16x16x32_bf16 v[168:171], a[0:3], v[220:223], v[168:171]\n"
"v_mfma_f32_16x16x32_bf16 v[172:175], a[4:7], v[220:223], v[172:175]\n"
"v_mfma_f32_16x16x32_bf16 v[176:179], a[8:11], v[220:223], v[176:179]\n"
"ds_read_b128 v[216:219], v235 offset:8192\n"
"v_mfma_f32_16x16x32_bf16 v[180:183], a[12:15], v[220:223], v[180:183]\n"
"v_mfma_f32_16x16x32_bf16 v[184:187], a[16:19], v[220:223], v[184:187]\n"
"v_mfma_f32_16x16x32_bf16 v[188:191], a[20:23], v[220:223], v[188:191]\n"
"s_waitcnt lgkmcnt(2)\n"
"v_mfma_f32_16x16x32_bf16 v[192:195], a[0:3], v[224:227], v[192:195]\n"
"v_mfma_f32_16x16x32_bf16 v[196:199], a[4:7], v[224:227], v[196:199]\n"
"v_mfma_f32_16x16x32_bf16 v[200:203], a[8:11], v[224:227], v[200:203]\n"
"ds_read_b128 v[220:223], v235 offset:12288\n"
"v_mfma_f32_16x16x32_bf16 v[204:207], a[12:15], v[224:227], v[204:207]\n"
"v_mfma_f32_16x16x32_bf16 v[208:211], a[16:19], v[224:227], v[208:211]\n"
"v_mfma_f32_16x16x32_bf16 v[212:215], a[20:23], v[224:227], v[212:215]\n"
"s_waitcnt lgkmcnt(2)\n"
"v_mfma_f32_16x16x32_bf16 v[24:27], a[24:27], v[228:231], v[24:27]\n"
"global_load_dwordx4 a[0:3], v239, s[58:59] offset:0 sc1\n"
"v_mfma_f32_16x16x32_bf16 v[28:31], a[28:31], v[228:231], v[28:31]\n"
"v_mfma_f32_16x16x32_bf16 v[32:35], a[32:35], v[228:231], v[32:35]\n"
"ds_read_b128 v[224:227], v235 offset:16384\n"
"v_mfma_f32_16x16x32_bf16 v[36:39], a[36:39], v[228:231], v[36:39]\n"
"v_mfma_f32_16x16x32_bf16 v[40:43], a[40:43], v[228:231], v[40:43]\n"
"global_load_dwordx4 a[4:7], v239, s[58:59] offset:1024 sc1\n"
"v_mfma_f32_16x16x32_bf16 v[44:47], a[44:47], v[228:231], v[44:47]\n"
"s_waitcnt lgkmcnt(2)\n"
"v_mfma_f32_16x16x32_bf16 v[48:51], a[24:27], v[216:219], v[48:51]\n"
"v_cvt_pk_bf16_f32 v24, v24, v25\n"
"v_cvt_pk_bf16_f32 v25, v26, v27\n"
"v_mfma_f32_16x16x32_bf16 v[52:55], a[28:31], v[216:219], v[52:55]\n"
"v_cvt_pk_bf16_f32 v26, v28, v29\n"
"v_cvt_pk_bf16_f32 v27, v30, v31\n"
"v_mfma_f32_16x16x32_bf16 v[56:59], a[32:35], v[216:219], v[56:59]\n"
"ds_read_b128 v[228:231], v235 offset:20480\n"
"v_cvt_pk_bf16_f32 v28, v32, v33\n"
"v_cvt_pk_bf16_f32 v29, v34, v35\n"
"global_load_dwordx4 a[8:11], v239, s[58:59] offset:2048 sc1\n"
"v_mfma_f32_16x16x32_bf16 v[60:63], a[36:39], v[216:219], v[60:63]\n"
"v_cvt_pk_bf16_f32 v30, v36, v37\n"
"v_cvt_pk_bf16_f32 v31, v38, v39\n"
"v_mfma_f32_16x16x32_bf16 v[64:67], a[40:43], v[216:219], v[64:67]\n"
"v_cvt_pk_bf16_f32 v32, v40, v41\n"
"v_cvt_pk_bf16_f32 v33, v42, v43\n"
"v_mfma_f32_16x16x32_bf16 v[68:71], a[44:47], v[216:219], v[68:71]\n"
"v_cvt_pk_bf16_f32 v34, v44, v45\n"
"v_cvt_pk_bf16_f32 v35, v46, v47\n"
"s_waitcnt lgkmcnt(2)\n"
"v_mfma_f32_16x16x32_bf16 v[72:75], a[24:27], v[220:223], v[72:75]\n"
"v_cvt_pk_bf16_f32 v48, v48, v49\n"
"v_cvt_pk_bf16_f32 v49, v50, v51\n"
"global_load_dwordx4 a[12:15], v239, s[58:59] offset:3072 sc1\n"
"v_mfma_f32_16x16x32_bf16 v[76:79], a[28:31], v[220:223], v[76:79]\n"
"v_cvt_pk_bf16_f32 v50, v52, v53\n"
"v_cvt_pk_bf16_f32 v51, v54, v55\n"
"v_mfma_f32_16x16x32_bf16 v[80:83], a[32:35], v[220:223], v[80:83]\n"
"ds_read_b128 v[216:219], v235 offset:24576\n"
"v_cvt_pk_bf16_f32 v52, v56, v57\n"
"v_cvt_pk_bf16_f32 v53, v58, v59\n"
"v_mfma_f32_16x16x32_bf16 v[84:87], a[36:39], v[220:223], v[84:87]\n"
"v_cvt_pk_bf16_f32 v54, v60, v61\n"
"v_cvt_pk_bf16_f32 v55, v62, v63\n"
"s_add_u32 s58, s24, 0xf000\n"
"s_addc_u32 s59, s25, 0\n"
"v_mfma_f32_16x16x32_bf16 v[88:91], a[40:43], v[220:223], v[88:91]\n"
"v_cvt_pk_bf16_f32 v56, v64, v65\n"
"v_cvt_pk_bf16_f32 v57, v66, v67\n"
"global_load_dwordx4 a[16:19], v239, s[58:59] offset:-4096 sc1\n"
"v_mfma_f32_16x16x32_bf16 v[92:95], a[44:47], v[220:223], v[92:95]\n"
"v_cvt_pk_bf16_f32 v58, v68, v69\n"
"v_cvt_pk_bf16_f32 v59, v70, v71\n"
"s_waitcnt lgkmcnt(2)\n"
"v_mfma_f32_16x16x32_bf16 v[96:99], a[24:27], v[224:227], v[96:99]\n"
"v_cvt_pk_bf16_f32 v72, v72, v73\n"
"v_cvt_pk_bf16_f32 v73, v74, v75\n"
"v_mfma_f32_16x16x32_bf16 v[100:103], a[28:31], v[224:227], v[100:103]\n"
"v_cvt_pk_bf16_f32 v74, v76, v77\n"
"v_cvt_pk_bf16_f32 v75, v78, v79\n"
"v_mfma_f32_16x16x32_bf16 v[104:107], a[32:35], v[224:227], v[104:107]\n"
"ds_read_b128 v[220:223], v235 offset:28672\n"
"v_cvt_pk_bf16_f32 v76, v80, v81\n"
"v_cvt_pk_bf16_f32 v77, v82, v83\n"
"global_load_dwordx4 a[20:23], v239, s[58:59] offset:-3072 sc1\n"
"v_mfma_f32_16x16x32_bf16 v[108:111], a[36:39], v[224:227], v[108:111]\n"
"v_cvt_pk_bf16_f32 v78, v84, v85\n"
"v_cvt_pk_bf16_f32 v79, v86, v87\n"
"v_mfma_f32_16x16x32_bf16 v[112:115], a[40:43], v[224:227], v[112:115]\n"
"v_cvt_pk_bf16_f32 v80, v88, v89\n"
"v_cvt_pk_bf16_f32 v81, v90, v91\n"
"v_mfma_f32_16x16x32_bf16 v[116:119], a[44:47], v[224:227], v[116:119]\n"
"v_cvt_pk_bf16_f32 v82, v92, v93\n"
"v_cvt_pk_bf16_f32 v83, v94, v95\n"
"s_waitcnt lgkmcnt(2)\n"
"v_mfma_f32_16x16x32_bf16 v[120:123], a[24:27], v[228:231], v[120:123]\n"
"s_memrealtime s[84:85]\n"
"v_cvt_pk_bf16_f32 v96, v96, v97\n"
"v_cvt_pk_bf16_f32 v97, v98, v99\n"
"v_mfma_f32_16x16x32_bf16 v[124:127], a[28:31], v[228:231], v[124:127]\n"
"v_cvt_pk_bf16_f32 v98, v100, v101\n"
"v_cvt_pk_bf16_f32 v99, v102, v103\n"
"v_mfma_f32_16x16x32_bf16 v[128:131], a[32:35], v[228:231], v[128:131]\n"
"ds_read_b128 v[224:227], v235 offset:32768\n"
"v_cvt_pk_bf16_f32 v100, v104, v105\n"
"v_cvt_pk_bf16_f32 v101, v106, v107\n"
"v_mfma_f32_16x16x32_bf16 v[132:135], a[36:39], v[228:231], v[132:135]\n"
"v_cvt_pk_bf16_f32 v102, v108, v109\n"
"v_cvt_pk_bf16_f32 v103, v110, v111\n"
"v_mfma_f32_16x16x32_bf16 v[136:139], a[40:43], v[228:231], v[136:139]\n"
"v_cvt_pk_bf16_f32 v104, v112, v113\n"
"v_cvt_pk_bf16_f32 v105, v114, v115\n"
"v_mfma_f32_16x16x32_bf16 v[140:143], a[44:47], v[228:231], v[140:143]\n"
"v_cvt_pk_bf16_f32 v106, v116, v117\n"
"v_cvt_pk_bf16_f32 v107, v118, v119\n"
"s_waitcnt lgkmcnt(2)\n"
"v_mfma_f32_16x16x32_bf16 v[144:147], a[24:27], v[216:219], v[144:147]\n"
"v_cvt_pk_bf16_f32 v120, v120, v121\n"
"v_cvt_pk_bf16_f32 v121, v122, v123\n"
"v_mfma_f32_16x16x32_bf16 v[148:151], a[28:31], v[216:219], v[148:151]\n"
"s_waitcnt lgkmcnt(0)\n"
"s_sub_u32 s86, s83, s84\n"
"v_cvt_pk_bf16_f32 v122, v124, v125\n"
"v_cvt_pk_bf16_f32 v123, v126, v127\n"
"v_mfma_f32_16x16x32_bf16 v[152:155], a[32:35], v[216:219], v[152:155]\n"
"ds_read_b128 v[228:231], v235 offset:0\n"
"v_add_u32 v235, v249, v235\n"
"s_waitcnt vmcnt(24)\n"
"s_barrier\n"
"v_cvt_pk_bf16_f32 v124, v128, v129\n"
"v_cvt_pk_bf16_f32 v125, v130, v131\n"
"v_mfma_f32_16x16x32_bf16 v[156:159], a[36:39], v[216:219], v[156:159]\n"
"ds_read_b128 v[36:39], v232 offset:0\n"
"v_cvt_pk_bf16_f32 v126, v132, v133\n"
"v_cvt_pk_bf16_f32 v127, v134, v135\n"
"v_mfma_f32_16x16x32_bf16 v[160:163], a[40:43], v[216:219], v[160:163]\n"
"v_cvt_pk_bf16_f32 v128, v136, v137\n"
"v_cvt_pk_bf16_f32 v129, v138, v139\n"
"v_mfma_f32_16x16x32_bf16 v[164:167], a[44:47], v[216:219], v[164:167]\n"
"ds_read_b128 v[40:43], v233 offset:0\n"
"v_cvt_pk_bf16_f32 v130, v140, v141\n"
"v_cvt_pk_bf16_f32 v131, v142, v143\n"
"v_mfma_f32_16x16x32_bf16 v[168:171], a[24:27], v[220:223], v[168:171]\n"
"v_cvt_pk_bf16_f32 v144, v144, v145\n"
"v_cvt_pk_bf16_f32 v145, v146, v147\n"
"global_load_dwordx4 v[140:143], v239, s[58:59] offset:-2048 sc1\n"
"v_mfma_f32_16x16x32_bf16 v[172:175], a[28:31], v[220:223], v[172:175]\n"
"ds_read_b128 v[44:47], v234 offset:0\n"
"v_cvt_pk_bf16_f32 v146, v148, v149\n"
"v_cvt_pk_bf16_f32 v147, v150, v151\n"
"v_mfma_f32_16x16x32_bf16 v[176:179], a[32:35], v[220:223], v[176:179]\n"
"v_cvt_pk_bf16_f32 v148, v152, v153\n"
"v_cvt_pk_bf16_f32 v149, v154, v155\n"
"v_mfma_f32_16x16x32_bf16 v[180:183], a[36:39], v[220:223], v[180:183]\n"
"ds_read_b128 v[60:63], v235 offset:0\n"
"v_cvt_pk_bf16_f32 v150, v156, v157\n"
"v_cvt_pk_bf16_f32 v151, v158, v159\n"
"v_mfma_f32_16x16x32_bf16 v[184:187], a[40:43], v[220:223], v[184:187]\n"
"v_cvt_pk_bf16_f32 v152, v160, v161\n"
"v_cvt_pk_bf16_f32 v153, v162, v163\n"
"v_mfma_f32_16x16x32_bf16 v[188:191], a[44:47], v[220:223], v[188:191]\n"
"ds_read_b128 v[64:67], v232 offset:8192\n"
"v_cvt_pk_bf16_f32 v154, v164, v165\n"
"v_cvt_pk_bf16_f32 v155, v166, v167\n"
"v_mfma_f32_16x16x32_bf16 v[192:195], a[24:27], v[224:227], v[192:195]\n"
"v_cvt_pk_bf16_f32 v168, v168, v169\n"
"v_cvt_pk_bf16_f32 v169, v170, v171\n"
"global_load_dwordx4 v[156:159], v239, s[58:59] offset:-1024 sc1\n"
"v_mfma_f32_16x16x32_bf16 v[196:199], a[28:31], v[224:227], v[196:199]\n"
"ds_read_b128 v[68:71], v233 offset:8192\n"
"v_cvt_pk_bf16_f32 v170, v172, v173\n"
"v_cvt_pk_bf16_f32 v171, v174, v175\n"
"v_mfma_f32_16x16x32_bf16 v[200:203], a[32:35], v[224:227], v[200:203]\n"
"v_cvt_pk_bf16_f32 v172, v176, v177\n"
"v_cvt_pk_bf16_f32 v173, v178, v179\n"
"v_mfma_f32_16x16x32_bf16 v[204:207], a[36:39], v[224:227], v[204:207]\n"
"ds_read_b128 v[84:87], v234 offset:8192\n"
"v_cvt_pk_bf16_f32 v174, v180, v181\n"
"v_cvt_pk_bf16_f32 v175, v182, v183\n"
"v_mfma_f32_16x16x32_bf16 v[208:211], a[40:43], v[224:227], v[208:211]\n"
"v_cvt_pk_bf16_f32 v176, v184, v185\n"
"v_cvt_pk_bf16_f32 v177, v186, v187\n"
"global_load_dwordx4 v[160:163], v239, s[58:59] offset:0 sc1\n"
"v_mfma_f32_16x16x32_bf16 v[212:215], a[44:47], v[224:227], v[212:215]\n"
"ds_read_b128 v[88:91], v235 offset:8192\n"
"v_cvt_pk_bf16_f32 v178, v188, v189\n"
"v_cvt_pk_bf16_f32 v179, v190, v191\n"
"s_waitcnt lgkmcnt(8)\n"
"v_mfma_f32_16x16x32_bf16 v[0:3], a[24:27], v[228:231], v[0:3]\n"
"v_cvt_pk_bf16_f32 v192, v192, v193\n"
"v_cvt_pk_bf16_f32 v193, v194, v195\n"
"v_mfma_f32_16x16x32_bf16 v[4:7], a[28:31], v[228:231], v[4:7]\n"
"ds_read_b128 v[92:95], v232 offset:16384\n"
"v_cvt_pk_bf16_f32 v194, v196, v197\n"
"v_cvt_pk_bf16_f32 v195, v198, v199\n"
"v_mfma_f32_16x16x32_bf16 v[8:11], a[32:35], v[228:231], v[8:11]\n"
"v_cvt_pk_bf16_f32 v196, v200, v201\n"
"v_cvt_pk_bf16_f32 v197, v202, v203\n"
"global_load_dwordx4 v[164:167], v239, s[58:59] offset:1024 sc1\n"
"v_mfma_f32_16x16x32_bf16 v[12:15], a[36:39], v[228:231], v[12:15]\n"
"ds_read_b128 v[108:111], v233 offset:16384\n"
"v_cvt_pk_bf16_f32 v198, v204, v205\n"
"v_cvt_pk_bf16_f32 v199, v206, v207\n"
"v_mfma_f32_16x16x32_bf16 v[16:19], a[40:43], v[228:231], v[16:19]\n"
"ds_read_b128 v[112:115], v234 offset:16384\n"
"v_cvt_pk_bf16_f32 v200, v208, v209\n"
"v_cvt_pk_bf16_f32 v201, v210, v211\n"
"v_mfma_f32_16x16x32_bf16 v[20:23], a[44:47], v[228:231], v[20:23]\n"
"ds_read_b128 v[116:119], v235 offset:16384\n"
"v_cvt_pk_bf16_f32 v202, v212, v213\n"
"v_cvt_pk_bf16_f32 v203, v214, v215\n"
"s_waitcnt lgkmcnt(8)\n"
"v_mfma_f32_16x16x32_bf16 v[132:135], a[240:243], v[36:39], v[0:3]\n"
"global_load_dwordx4 v[180:183], v239, s[58:59] offset:2048 sc1\n"
"v_mfma_f32_16x16x32_bf16 v[132:135], a[244:247], v[40:43], v[132:135]\n"
"v_mfma_f32_16x16x32_bf16 v[132:135], a[248:251], v[44:47], v[132:135]\n"
"v_mfma_f32_16x16x32_bf16 v[132:135], a[252:255], v[60:63], v[132:135]\n"
"global_load_dwordx4 v[184:187], v239, s[58:59] offset:3072 sc1\n"
"s_waitcnt lgkmcnt(4)\n"
"v_mfma_f32_16x16x32_bf16 v[132:135], a[48:51], v[64:67], v[132:135]\n"
"v_mfma_f32_16x16x32_bf16 v[132:135], a[52:55], v[68:71], v[132:135]\n"
"v_mfma_f32_16x16x32_bf16 v[132:135], a[56:59], v[84:87], v[132:135]\n"
"s_add_u32 s58, s24, 0x11000\n"
"s_addc_u32 s59, s25, 0\n"
"global_load_dwordx4 v[188:191], v239, s[58:59] offset:-4096 sc1\n"
"v_mfma_f32_16x16x32_bf16 v[132:135], a[60:63], v[88:91], v[132:135]\n"
"s_waitcnt lgkmcnt(0)\n"
"v_mfma_f32_16x16x32_bf16 v[132:135], a[64:67], v[92:95], v[132:135]\n"
"v_mfma_f32_16x16x32_bf16 v[132:135], a[68:71], v[108:111], v[132:135]\n"
"global_load_dwordx4 v[204:207], v239, s[58:59] offset:-3072 sc1\n"
"v_mfma_f32_16x16x32_bf16 v[132:135], a[72:75], v[112:115], v[132:135]\n"
"v_mfma_f32_16x16x32_bf16 v[132:135], a[76:79], v[116:119], v[132:135]\n"
"v_mfma_f32_16x16x32_bf16 v[136:139], a[80:83], v[36:39], v[4:7]\n"
"global_load_dwordx4 a[24:27], v239, s[58:59] offset:-2048 sc1\n"
"v_mfma_f32_16x16x32_bf16 v[136:139], a[84:87], v[40:43], v[136:139]\n"
"v_mfma_f32_16x16x32_bf16 v[136:139], a[88:91], v[44:47], v[136:139]\n"
"v_mfma_f32_16x16x32_bf16 v[136:139], a[92:95], v[60:63], v[136:139]\n"
"global_load_dwordx4 a[28:31], v239, s[58:59] offset:-1024 sc1\n"
"v_mfma_f32_16x16x32_bf16 v[136:139], a[96:99], v[64:67], v[136:139]\n"
"v_cvt_pk_bf16_f32 v208, v132, v133\n"
"v_mfma_f32_16x16x32_bf16 v[136:139], a[100:103], v[68:71], v[136:139]\n"
"v_cvt_pk_bf16_f32 v209, v134, v135\n"
"v_mfma_f32_16x16x32_bf16 v[136:139], a[104:107], v[84:87], v[136:139]\n"
"global_load_dwordx4 a[32:35], v239, s[58:59] offset:0 sc1\n"
"v_mfma_f32_16x16x32_bf16 v[136:139], a[108:111], v[88:91], v[136:139]\n"
"v_mfma_f32_16x16x32_bf16 v[136:139], a[112:115], v[92:95], v[136:139]\n"
"v_mfma_f32_16x16x32_bf16 v[136:139], a[116:119], v[108:111], v[136:139]\n"
"global_load_dwordx4 a[36:39], v239, s[58:59] offset:1024 sc1\n"
"v_mfma_f32_16x16x32_bf16 v[136:139], a[120:123], v[112:115], v[136:139]\n"
"ds_write_b64 v244, v[208:209] offset:12288\n"
"v_mfma_f32_16x16x32_bf16 v[136:139], a[124:127], v[116:119], v[136:139]\n"
"v_mfma_f32_16x16x32_bf16 v[132:135], a[128:131], v[36:39], v[8:11]\n"
"global_load_dwordx4 a[40:43], v239, s[58:59] offset:2048 sc1\n"
"v_mfma_f32_16x16x32_bf16 v[132:135], a[132:135], v[40:43], v[132:135]\n"
"v_mfma_f32_16x16x32_bf16 v[132:135], a[136:139], v[44:47], v[132:135]\n"
"v_mfma_f32_16x16x32_bf16 v[132:135], a[140:143], v[60:63], v[132:135]\n"
"global_load_dwordx4 a[44:47], v239, s[58:59] offset:3072 sc1\n"
"v_mfma_f32_16x16x32_bf16 v[132:135], a[144:147], v[64:67], v[132:135]\n"
"v_cvt_pk_bf16_f32 v210, v136, v137\n"
"v_mfma_f32_16x16x32_bf16 v[132:135], a[148:151], v[68:71], v[132:135]\n"
"v_cvt_pk_bf16_f32 v211, v138, v139\n"
"v_mfma_f32_16x16x32_bf16 v[132:135], a[152:155], v[84:87], v[132:135]\n"
"global_load_dwordx4 v[228:231], v248, s[28:29] offset:3072 sc1\n"
"v_mfma_f32_16x16x32_bf16 v[132:135], a[156:159], v[88:91], v[132:135]\n"
"v_mfma_f32_16x16x32_bf16 v[132:135], a[160:163], v[92:95], v[132:135]\n"
"v_mfma_f32_16x16x32_bf16 v[132:135], a[164:167], v[108:111], v[132:135]\n"
"s_add_u32 s52, s20, s34\n"
"s_addc_u32 s53, s21, 0\n"
"s_add_u32 m0, s31, 0xc000\n"
"v_mfma_f32_16x16x32_bf16 v[132:135], a[168:171], v[112:115], v[132:135]\n"
"global_load_lds_dwordx4 v237, s[52:53] offset:0\n"
"v_mfma_f32_16x16x32_bf16 v[132:135], a[172:175], v[116:119], v[132:135]\n"
"ds_write_b64 v244, v[210:211] offset:12296\n"
"v_mfma_f32_16x16x32_bf16 v[136:139], a[176:179], v[36:39], v[12:15]\n"
"v_mfma_f32_16x16x32_bf16 v[136:139], a[180:183], v[40:43], v[136:139]\n"
"v_mfma_f32_16x16x32_bf16 v[136:139], a[184:187], v[44:47], v[136:139]\n"
"v_mfma_f32_16x16x32_bf16 v[136:139], a[188:191], v[60:63], v[136:139]\n"
"s_add_u32 m0, s31, 0xdf00\n"
"s_waitcnt vmcnt(36)\n"
"v_mfma_f32_16x16x32_bf16 v[136:139], a[192:195], v[64:67], v[136:139]\n"
"global_load_lds_dwordx4 v237, s[52:53] offset:256\n"
"v_mfma_f32_16x16x32_bf16 v[136:139], a[196:199], v[68:71], v[136:139]\n"
"v_cvt_pk_bf16_f32 v212, v132, v133\n"
"v_mfma_f32_16x16x32_bf16 v[136:139], a[200:203], v[84:87], v[136:139]\n"
"v_cvt_pk_bf16_f32 v213, v134, v135\n"
"v_mfma_f32_16x16x32_bf16 v[136:139], a[204:207], v[88:91], v[136:139]\n"
"s_waitcnt vmcnt(27)\n"
"v_mfma_f32_16x16x32_bf16 v[136:139], a[208:211], v[92:95], v[136:139]\n"
"v_mfma_f32_16x16x32_bf16 v[136:139], a[212:215], v[108:111], v[136:139]\n"
"s_add_u32 m0, s31, 0xfe00\n"
"v_mfma_f32_16x16x32_bf16 v[136:139], a[216:219], v[112:115], v[136:139]\n"
"global_load_lds_dwordx4 v237, s[52:53] offset:512\n"
"v_mfma_f32_16x16x32_bf16 v[136:139], a[220:223], v[116:119], v[136:139]\n"
"ds_write_b64 v245, v[212:213] offset:12288\n"
"s_waitcnt vmcnt(24)\n"
"v_mfma_f32_16x16x32_bf16 v[132:135], a[224:227], v[36:39], v[16:19]\n"
"v_mfma_f32_16x16x32_bf16 v[132:135], a[228:231], v[40:43], v[132:135]\n"
"v_mfma_f32_16x16x32_bf16 v[132:135], a[232:235], v[44:47], v[132:135]\n"
"v_mfma_f32_16x16x32_bf16 v[132:135], a[236:239], v[60:63], v[132:135]\n"
"s_add_u32 m0, s31, 0xd000\n"
"s_waitcnt vmcnt(20)\n"
"v_mfma_f32_16x16x32_bf16 v[132:135], a[0:3], v[64:67], v[132:135]\n"
"global_load_lds_dwordx4 v238, s[52:53] offset:0\n"
"v_mfma_f32_16x16x32_bf16 v[132:135], a[4:7], v[68:71], v[132:135]\n"
"v_cvt_pk_bf16_f32 v214, v136, v137\n"
"v_mfma_f32_16x16x32_bf16 v[132:135], a[8:11], v[84:87], v[132:135]\n"
"v_cvt_pk_bf16_f32 v215, v138, v139\n"
"v_mfma_f32_16x16x32_bf16 v[132:135], a[12:15], v[88:91], v[132:135]\n"
"s_waitcnt vmcnt(17)\n"
"v_mfma_f32_16x16x32_bf16 v[132:135], a[16:19], v[92:95], v[132:135]\n"
"v_mfma_f32_16x16x32_bf16 v[132:135], a[20:23], v[108:111], v[132:135]\n"
"s_add_u32 m0, s31, 0xef00\n"
"v_mfma_f32_16x16x32_bf16 v[132:135], v[140:143], v[112:115], v[132:135]\n"
"global_load_lds_dwordx4 v238, s[52:53] offset:256\n"
"v_mfma_f32_16x16x32_bf16 v[132:135], v[156:159], v[116:119], v[132:135]\n"
"s_barrier\n"
"ds_write_b64 v245, v[214:215] offset:12296\n"
"s_waitcnt vmcnt(14)\n"
"v_mfma_f32_16x16x32_bf16 v[136:139], v[160:163], v[36:39], v[20:23]\n"
"ds_read_b128 v[36:39], v232 offset:256\n"
"v_mfma_f32_16x16x32_bf16 v[136:139], v[164:167], v[40:43], v[136:139]\n"
"ds_read_b128 v[40:43], v233 offset:256\n"
"v_mfma_f32_16x16x32_bf16 v[136:139], v[180:183], v[44:47], v[136:139]\n"
"ds_read_b128 v[44:47], v234 offset:256\n"
"v_mfma_f32_16x16x32_bf16 v[136:139], v[184:187], v[60:63], v[136:139]\n"
"ds_read_b128 v[60:63], v235 offset:256\n"
"s_add_u32 m0, s31, 0x10e00\n"
"s_waitcnt vmcnt(10)\n"
"v_mfma_f32_16x16x32_bf16 v[136:139], v[188:191], v[64:67], v[136:139]\n"
"ds_read_b128 v[64:67], v232 offset:8448\n"
"global_load_lds_dwordx4 v238, s[52:53] offset:512\n"
"v_mfma_f32_16x16x32_bf16 v[136:139], v[204:207], v[68:71], v[136:139]\n"
"ds_read_b128 v[68:71], v233 offset:8448\n"
"v_cvt_pk_bf16_f32 v208, v132, v133\n"
"v_mfma_f32_16x16x32_bf16 v[136:139], a[24:27], v[84:87], v[136:139]\n"
"ds_read_b128 v[84:87], v234 offset:8448\n"
"v_cvt_pk_bf16_f32 v209, v134, v135\n"
"v_mfma_f32_16x16x32_bf16 v[136:139], a[28:31], v[88:91], v[136:139]\n"
"ds_read_b128 v[88:91], v235 offset:8448\n"
"s_waitcnt vmcnt(7)\n"
"v_mfma_f32_16x16x32_bf16 v[136:139], a[32:35], v[92:95], v[136:139]\n"
"ds_read_b128 v[92:95], v232 offset:16640\n"
"v_mfma_f32_16x16x32_bf16 v[136:139], a[36:39], v[108:111], v[136:139]\n"
"ds_read_b128 v[108:111], v233 offset:16640\n"
"v_mfma_f32_16x16x32_bf16 v[136:139], a[40:43], v[112:115], v[136:139]\n"
"ds_read_b128 v[112:115], v234 offset:16640\n"
"v_mfma_f32_16x16x32_bf16 v[136:139], a[44:47], v[116:119], v[136:139]\n"
"ds_read_b128 v[116:119], v235 offset:16640\n"
"s_waitcnt lgkmcnt(8)\n"
"v_mfma_f32_16x16x32_bf16 v[132:135], a[240:243], v[36:39], v[0:3]\n"
"s_add_u32 s52, s20, s35\n"
"s_addc_u32 s53, s21, 0\n"
"s_add_u32 m0, s31, 0x12000\n"
"v_mfma_f32_16x16x32_bf16 v[132:135], a[244:247], v[40:43], v[132:135]\n"
"global_load_lds_dwordx4 v237, s[52:53] offset:0\n"
"v_mfma_f32_16x16x32_bf16 v[132:135], a[248:251], v[44:47], v[132:135]\n"
"ds_write_b64 v244, v[208:209] offset:12416\n"
"v_mfma_f32_16x16x32_bf16 v[132:135], a[252:255], v[60:63], v[132:135]\n"
"v_cvt_pk_bf16_f32 v210, v136, v137\n"
"s_waitcnt lgkmcnt(5)\n"
"v_mfma_f32_16x16x32_bf16 v[132:135], a[48:51], v[64:67], v[132:135]\n"
"v_cvt_pk_bf16_f32 v211, v138, v139\n"
"v_mfma_f32_16x16x32_bf16 v[132:135], a[52:55], v[68:71], v[132:135]\n"
"s_mov_b64 exec, s[8:9]\n"
"s_cbranch_execz LHT2\n"
"LHR2:\n"
"s_mov_b64 exec, -1\n"
"v_mfma_f32_16x16x32_bf16 v[132:135], a[56:59], v[84:87], v[132:135]\n"
"s_add_u32 m0, s31, 0x13f00\n"
"v_mfma_f32_16x16x32_bf16 v[132:135], a[60:63], v[88:91], v[132:135]\n"
"global_load_lds_dwordx4 v237, s[52:53] offset:256\n"
"s_waitcnt lgkmcnt(1)\n"
"v_mfma_f32_16x16x32_bf16 v[132:135], a[64:67], v[92:95], v[132:135]\n"
"v_mfma_f32_16x16x32_bf16 v[132:135], a[68:71], v[108:111], v[132:135]\n"
"ds_write_b64 v244, v[210:211] offset:12424\n"
"v_mfma_f32_16x16x32_bf16 v[132:135], a[72:75], v[112:115], v[132:135]\n"
"v_mfma_f32_16x16x32_bf16 v[132:135], a[76:79], v[116:119], v[132:135]\n"
"v_mfma_f32_16x16x32_bf16 v[136:139], a[124:127], v[116:119], v[4:7]\n"
"s_add_u32 m0, s31, 0x15e00\n"
"v_mfma_f32_16x16x32_bf16 v[136:139], a[120:123], v[112:115], v[136:139]\n"
"global_load_lds_dwordx4 v237, s[52:53] offset:512\n"
"v_mfma_f32_16x16x32_bf16 v[136:139], a[116:119], v[108:111], v[136:139]\n"
"v_mfma_f32_16x16x32_bf16 v[136:139], a[112:115], v[92:95], v[136:139]\n"
"v_cvt_pk_bf16_f32 v208, v132, v133\n"
"v_mfma_f32_16x16x32_bf16 v[136:139], a[108:111], v[88:91], v[136:139]\n"
"v_cvt_pk_bf16_f32 v209, v134, v135\n"
"v_mfma_f32_16x16x32_bf16 v[136:139], a[104:107], v[84:87], v[136:139]\n"
"v_mfma_f32_16x16x32_bf16 v[136:139], a[100:103], v[68:71], v[136:139]\n"
"s_add_u32 m0, s31, 0x13000\n"
"v_mfma_f32_16x16x32_bf16 v[136:139], a[96:99], v[64:67], v[136:139]\n"
"global_load_lds_dwordx4 v238, s[52:53] offset:0\n"
"v_mfma_f32_16x16x32_bf16 v[136:139], a[92:95], v[60:63], v[136:139]\n"
"s_waitcnt vmcnt(10)\n"
"v_mfma_f32_16x16x32_bf16 v[136:139], a[88:91], v[44:47], v[136:139]\n"
"v_cmp_ne_u64 s[58:59], s[80:81], v[228:229]\n"
"v_mfma_f32_16x16x32_bf16 v[136:139], a[84:87], v[40:43], v[136:139]\n"
"v_subrev_u32 v230, s83, v230\n"
"v_mfma_f32_16x16x32_bf16 v[136:139], a[80:83], v[36:39], v[136:139]\n"
"ds_write_b64 v244, v[208:209] offset:24576\n"
"v_mfma_f32_16x16x32_bf16 v[132:135], a[128:131], v[36:39], v[8:11]\n"
"v_add_u32_dpp v230, v230, v230 row_shr:1 row_mask:0xf bank_mask:0xf\n"
"s_add_u32 m0, s31, 0x14f00\n"
"v_mfma_f32_16x16x32_bf16 v[132:135], a[132:135], v[40:43], v[132:135]\n"
"global_load_lds_dwordx4 v238, s[52:53] offset:256\n"
"v_mfma_f32_16x16x32_bf16 v[132:135], a[136:139], v[44:47], v[132:135]\n"
"v_add_u32_dpp v230, v230, v230 row_shr:2 row_mask:0xf bank_mask:0xf\n"
"v_mfma_f32_16x16x32_bf16 v[132:135], a[140:143], v[60:63], v[132:135]\n"
"v_cvt_pk_bf16_f32 v210, v136, v137\n"
"v_mfma_f32_16x16x32_bf16 v[132:135], a[144:147], v[64:67], v[132:135]\n"
"v_add_u32_dpp v230, v230, v230 row_shr:4 row_mask:0xf bank_mask:0xf\n"
"v_mfma_f32_16x16x32_bf16 v[132:135], a[148:151], v[68:71], v[132:135]\n"
"v_cvt_pk_bf16_f32 v211, v138, v139\n"
"v_mfma_f32_16x16x32_bf16 v[132:135], a[152:155], v[84:87], v[132:135]\n"
"v_readlane_b32 s90, v230, 63\n"
"s_add_u32 m0, s31, 0x16e00\n"
"v_mfma_f32_16x16x32_bf16 v[132:135], a[156:159], v[88:91], v[132:135]\n"
"global_load_lds_dwordx4 v238, s[52:53] offset:512\n"
"v_mfma_f32_16x16x32_bf16 v[132:135], a[160:163], v[92:95], v[132:135]\n"
"s_ashr_i32 s90, s90, 3\n"
"v_mfma_f32_16x16x32_bf16 v[132:135], a[164:167], v[108:111], v[132:135]\n"
"s_add_i32 s90, s90, 2678\n"
"v_mfma_f32_16x16x32_bf16 v[132:135], a[168:171], v[112:115], v[132:135]\n"
"s_max_i32 s90, s90, 0\n"
"ds_write_b64 v244, v[210:211] offset:24584\n"
"v_mfma_f32_16x16x32_bf16 v[132:135], a[172:175], v[116:119], v[132:135]\n"
"s_min_i32 s90, s90, 120\n"
"v_mfma_f32_16x16x32_bf16 v[136:139], a[220:223], v[116:119], v[12:15]\n"
"s_mul_i32 s90, s90, 12\n"
"v_mfma_f32_16x16x32_bf16 v[136:139], a[216:219], v[112:115], v[136:139]\n"
"s_ashr_i32 s90, s90, 3\n"
"v_mfma_f32_16x16x32_bf16 v[136:139], a[212:215], v[108:111], v[136:139]\n"
"s_lshr_b32 s59, s59, 24\n"
"s_cselect_b32 s90, 0, s90\n"
"v_mfma_f32_16x16x32_bf16 v[136:139], a[208:211], v[92:95], v[136:139]\n"
"s_add_i32 s86, s86, s90\n"
"v_mfma_f32_16x16x32_bf16 v[136:139], a[204:207], v[88:91], v[136:139]\n"
"s_ashr_i32 s86, s86, 4\n"
"v_mov_b32 v242, 2\n"
"v_mfma_f32_16x16x32_bf16 v[136:139], a[200:203], v[84:87], v[136:139]\n"
"s_max_i32 s86, s86, 144\n"
"v_mfma_f32_16x16x32_bf16 v[136:139], a[196:199], v[68:71], v[136:139]\n"
"s_min_i32 s86, s86, 185\n"
"s_waitcnt lgkmcnt(2)\n"
"ds_write_b32 v240, v242\n"
"v_mfma_f32_16x16x32_bf16 v[136:139], a[192:195], v[64:67], v[136:139]\n"
"s_mov_b32 s83, s84\n"
"v_mfma_f32_16x16x32_bf16 v[136:139], a[188:191], v[60:63], v[136:139]\n"
"v_cvt_pk_bf16_f32 v212, v132, v133\n"
"v_mfma_f32_16x16x32_bf16 v[136:139], a[184:187], v[44:47], v[136:139]\n"
"v_cvt_pk_bf16_f32 v213, v134, v135\n"
"v_mfma_f32_16x16x32_bf16 v[136:139], a[180:183], v[40:43], v[136:139]\n"
"v_mfma_f32_16x16x32_bf16 v[136:139], a[176:179], v[36:39], v[136:139]\n"
"v_mfma_f32_16x16x32_bf16 v[132:135], a[224:227], v[36:39], v[16:19]\n"
"v_mfma_f32_16x16x32_bf16 v[132:135], a[228:231], v[40:43], v[132:135]\n"
"v_mfma_f32_16x16x32_bf16 v[132:135], a[232:235], v[44:47], v[132:135]\n"
"ds_read_b32 v243, v241\n"
"s_add_u32 s83, s83, s86\n"
"v_mfma_f32_16x16x32_bf16 v[132:135], a[236:239], v[60:63], v[132:135]\n"
"s_memrealtime s[84:85]\n"
"v_cvt_pk_bf16_f32 v215, v138, v139\n"
"LHT2:\n"
"s_cbranch_execz LHR2\n"
"v_mfma_f32_16x16x32_bf16 v[132:135], a[0:3], v[64:67], v[132:135]\n"
"ds_write_b64 v245, v[212:213] offset:24576\n"
"s_sub_i32 s88, s88, 6\n"
"v_mfma_f32_16x16x32_bf16 v[132:135], a[4:7], v[68:71], v[132:135]\n"
"v_cvt_pk_bf16_f32 v214, v136, v137\n"
"s_max_i32 s88, s88, 0\n"
"v_mfma_f32_16x16x32_bf16 v[132:135], a[8:11], v[84:87], v[132:135]\n"
"s_waitcnt lgkmcnt(0)\n"
"v_cmp_gt_u32 s[56:57], 2, v243\n"
"v_mfma_f32_16x16x32_bf16 v[132:135], a[12:15], v[88:91], v[132:135]\n"
"s_sub_u32 s84, s84, s83\n"
"v_mfma_f32_16x16x32_bf16 v[132:135], a[16:19], v[92:95], v[132:135]\n"
"s_cmp_lg_u64 s[56:57], 0\n"
"s_cbranch_scc1 LB2_0\n"
"LB2_0R:\n"
"v_mfma_f32_16x16x32_bf16 v[132:135], a[20:23], v[108:111], v[132:135]\n"
"s_cmp_lt_i32 s84, s88\n"
"s_cselect_b64 vcc, -1, 0\n"
"v_mfma_f32_16x16x32_bf16 v[132:135], v[140:143], v[112:115], v[132:135]\n"
"ds_write_b64 v245, v[214:215] offset:24584\n"
"v_mfma_f32_16x16x32_bf16 v[132:135], v[156:159], v[116:119], v[132:135]\n"
"v_mfma_f32_16x16x32_bf16 v[136:139], a[44:47], v[116:119], v[20:23]\n"
"ds_read_b128 v[116:119], v235 offset:40960\n"
"v_mfma_f32_16x16x32_bf16 v[136:139], a[40:43], v[112:115], v[136:139]\n"
"ds_read_b128 v[112:115], v234 offset:40960\n"
"v_mfma_f32_16x16x32_bf16 v[136:139], a[36:39], v[108:111], v[136:139]\n"
"ds_read_b128 v[108:111], v233 offset:40960\n"
"v_mfma_f32_16x16x32_bf16 v[136:139], a[32:35], v[92:95], v[136:139]\n"
"ds_read_b128 v[92:95], v232 offset:40960\n"
"v_cvt_pk_bf16_f32 v208, v132, v133\n"
"v_mfma_f32_16x16x32_bf16 v[136:139], a[28:31], v[88:91], v[136:139]\n"
"ds_read_b128 v[88:91], v235 offset:32768\n"
"v_cvt_pk_bf16_f32 v209, v134, v135\n"
"v_mfma_f32_16x16x32_bf16 v[136:139], a[24:27], v[84:87], v[136:139]\n"
"ds_read_b128 v[84:87], v234 offset:32768\n"
"v_mfma_f32_16x16x32_bf16 v[136:139], v[204:207], v[68:71], v[136:139]\n"
"ds_read_b128 v[68:71], v233 offset:32768\n"
"v_mfma_f32_16x16x32_bf16 v[136:139], v[188:191], v[64:67], v[136:139]\n"
"ds_read_b128 v[64:67], v232 offset:32768\n"
"v_mfma_f32_16x16x32_bf16 v[136:139], v[184:187], v[60:63], v[136:139]\n"
"ds_read_b128 v[60:63], v235 offset:24576\n"
"v_mfma_f32_16x16x32_bf16 v[136:139], v[180:183], v[44:47], v[136:139]\n"
"ds_read_b128 v[44:47], v234 offset:24576\n"
"v_mfma_f32_16x16x32_bf16 v[136:139], v[164:167], v[40:43], v[136:139]\n"
"ds_read_b128 v[40:43], v233 offset:24576\n"
"v_mfma_f32_16x16x32_bf16 v[136:139], v[160:163], v[36:39], v[136:139]\n"
"ds_read_b128 v[36:39], v232 offset:24576\n"
"s_branch LJOIN\n"
"LV1:\n"
"s_add_u32 s64, s66, s8\n"
"s_addc_u32 s65, s67, 0\n"
"s_add_u32 m0, s74, 0x0\n"
"s_add_u32 s78, s64, 0x0\n"
"s_addc_u32 s79, s65, 0\n"
"global_load_lds_dwordx4 v240, s[78:79] offset:0\n"
"global_load_lds_dwordx4 v241, s[78:79] offset:1024\n"
"global_load_lds_dwordx4 v242, s[78:79] offset:2048\n"
"s_add_u32 m0, s74, 0xc00\n"
"s_add_u32 s78, s64, 0x3000\n"
"s_addc_u32 s79, s65, 0\n"
"global_load_lds_dwordx4 v240, s[78:79] offset:0\n"
"global_load_lds_dwordx4 v241, s[78:79] offset:1024\n"
"global_load_lds_dwordx4 v242, s[78:79] offset:2048\n"
"s_add_u32 m0, s74, 0x6000\n"
"s_add_u32 s78, s64, 0x6000\n"
"s_addc_u32 s79, s65, 0\n"
"global_load_lds_dwordx4 v240, s[78:79] offset:0\n"
"global_load_lds_dwordx4 v241, s[78:79] offset:1024\n"
"global_load_lds_dwordx4 v242, s[78:79] offset:2048\n"
"s_add_u32 m0, s74, 0x6c00\n"
"s_add_u32 s78, s64, 0x9000\n"
"s_addc_u32 s79, s65, 0\n"
"global_load_lds_dwordx4 v240, s[78:79] offset:0\n"
"global_load_lds_dwordx4 v241, s[78:79] offset:1024\n"
"global_load_lds_dwordx4 v242, s[78:79] offset:2048\n"
"s_add_u32 m0, s74, 0xc000\n"
"s_add_u32 s78, s64, 0xc000\n"
"s_addc_u32 s79, s65, 0\n"
"global_load_lds_dwordx4 v240, s[78:79] offset:0\n"
"global_load_lds_dwordx4 v241, s[78:79] offset:1024\n"
"global_load_lds_dwordx4 v242, s[78:79] offset:2048\n"
"s_add_u32 m0, s74, 0xcc00\n"
"s_add_u32 s78, s64, 0xf000\n"
"s_addc_u32 s79, s65, 0\n"
"global_load_lds_dwordx4 v240, s[78:79] offset:0\n"
"global_load_lds_dwordx4 v241, s[78:79] offset:1024\n"
"global_load_lds_dwordx4 v242, s[78:79] offset:2048\n"
"s_waitcnt vmcnt(27)\n"
"v_readfirstlane_b32 s80, v252\n"
"v_readfirstlane_b32 s81, v253\n"
"s_nop 3\n"
"s_add_u32 s80, s80, 0x7f4a7c15\n"
"s_addc_u32 s81, s81, 0x9e3779b9\n"
"v_mov_b32 v252, s80\n"
"v_mov_b32 v253, s81\n"
"s_waitcnt vmcnt(12)\n"
"ds_read_b64_tr_b16 a[0:1], v243 offset:0\n"
"ds_read_b64_tr_b16 a[2:3], v243 offset:768\n"
"ds_read_b64_tr_b16 a[4:5], v243 offset:8\n"
"ds_read_b64_tr_b16 a[6:7], v243 offset:776\n"
"ds_read_b64_tr_b16 a[8:9], v243 offset:64\n"
"ds_read_b64_tr_b16 a[10:11], v243 offset:832\n"
"ds_read_b64_tr_b16 a[12:13], v243 offset:72\n"
"ds_read_b64_tr_b16 a[14:15], v243 offset:840\n"
"ds_read_b64_tr_b16 a[16:17], v243 offset:128\n"
"ds_read_b64_tr_b16 a[18:19], v243 offset:896\n"
"ds_read_b64_tr_b16 a[20:21], v243 offset:136\n"
"ds_read_b64_tr_b16 a[22:23], v243 offset:904\n"
"s_waitcnt lgkmcnt(12)\n"
"s_barrier\n"
"s_cmp_eq_u32 s76, 0\n"
"s_cselect_b32 s12, 1, 0\n"
"s_mov_b32 s13, 0\n"
"s_cmp_lg_u32 s76, 0\n"
"s_cselect_b64 s[4:5], -1, 0\n"
"s_cmp_lg_u32 s76, 1\n"
"s_cselect_b64 s[6:7], -1, 0\n"
"s_cmp_lg_u32 s76, 2\n"
"s_cselect_b64 s[8:9], -1, 0\n"
"s_cmp_lg_u32 s76, 3\n"
"s_cselect_b64 s[10:11], -1, 0\n"
"v_mov_b32 v251, s77\n"
"ds_read_b128 v[216:219], v232 offset:0\n"
"ds_read_b128 v[220:223], v232 offset:4096\n"
"ds_read_b128 v[224:227], v232 offset:8192\n"
"s_waitcnt lgkmcnt(2)\n"
"v_mfma_f32_16x16x32_bf16 v[0:3], a[0:3], v[216:219], 0\n"
"s_add_u32 s78, s64, 0x12000\n"
"s_addc_u32 s79, s65, 0\n"
"s_add_u32 m0, s74, 0x0\n"
"v_mfma_f32_16x16x32_bf16 v[4:7], a[4:7], v[216:219], 0\n"
"global_load_lds_dwordx4 v240, s[78:79] offset:0\n"
"v_mfma_f32_16x16x32_bf16 v[8:11], a[8:11], v[216:219], 0\n"
"ds_read_b128 v[228:231], v232 offset:12288\n"
"v_mfma_f32_16x16x32_bf16 v[12:15], a[12:15], v[216:219], 0\n"
"v_mfma_f32_16x16x32_bf16 v[16:19], a[16:19], v[216:219], 0\n"
"v_mfma_f32_16x16x32_bf16 v[20:23], a[20:23], v[216:219], 0\n"
"global_load_lds_dwordx4 v241, s[78:79] offset:1024\n"
"s_waitcnt lgkmcnt(2)\n"
"v_mfma_f32_16x16x32_bf16 v[24:27], a[0:3], v[220:223], 0\n"
"s_waitcnt vmcnt(8)\n"
"ds_read_b64_tr_b16 a[24:25], v243 offset:24576\n"
"v_mfma_f32_16x16x32_bf16 v[28:31], a[4:7], v[220:223], 0\n"
"ds_read_b64_tr_b16 a[26:27], v243 offset:25344\n"
"v_mfma_f32_16x16x32_bf16 v[32:35], a[8:11], v[220:223], 0\n"
"ds_read_b128 v[216:219], v232 offset:16384\n"
"ds_read_b64_tr_b16 a[28:29], v243 offset:24584\n"
"v_mfma_f32_16x16x32_bf16 v[36:39], a[12:15], v[220:223], 0\n"
"global_load_lds_dwordx4 v242, s[78:79] offset:2048\n"
"ds_read_b64_tr_b16 a[30:31], v243 offset:25352\n"
"v_mfma_f32_16x16x32_bf16 v[40:43], a[16:19], v[220:223], 0\n"
"ds_read_b64_tr_b16 a[32:33], v243 offset:24640\n"
"v_mfma_f32_16x16x32_bf16 v[44:47], a[20:23], v[220:223], 0\n"
"s_add_u32 s78, s64, 0x15000\n"
"s_addc_u32 s79, s65, 0\n"
"s_waitcnt lgkmcnt(7)\n"
"v_mfma_f32_16x16x32_bf16 v[48:51], a[0:3], v[224:227], 0\n"
"s_add_u32 m0, s74, 0xc00\n"
"ds_read_b64_tr_b16 a[34:35], v243 offset:25408\n"
"v_mfma_f32_16x16x32_bf16 v[52:55], a[4:7], v[224:227], 0\n"
"global_load_lds_dwordx4 v240, s[78:79] offset:0\n"
"ds_read_b64_tr_b16 a[36:37], v243 offset:24648\n"
"v_mfma_f32_16x16x32_bf16 v[56:59], a[8:11], v[224:227], 0\n"
"ds_read_b128 v[220:223], v232 offset:20480\n"
"ds_read_b64_tr_b16 a[38:39], v243 offset:25416\n"
"v_mfma_f32_16x16x32_bf16 v[60:63], a[12:15], v[224:227], 0\n"
"ds_read_b64_tr_b16 a[40:41], v243 offset:24704\n"
"v_mfma_f32_16x16x32_bf16 v[64:67], a[16:19], v[224:227], 0\n"
"ds_read_b64_tr_b16 a[42:43], v243 offset:25472\n"
"v_mfma_f32_16x16x32_bf16 v[68:71], a[20:23], v[224:227], 0\n"
"global_load_lds_dwordx4 v241, s[78:79] offset:1024\n"
"s_waitcnt lgkmcnt(12)\n"
"v_mfma_f32_16x16x32_bf16 v[72:75], a[0:3], v[228:231], 0\n"
"ds_read_b64_tr_b16 a[44:45], v243 offset:24712\n"
"v_mfma_f32_16x16x32_bf16 v[76:79], a[4:7], v[228:231], 0\n"
"ds_read_b64_tr_b16 a[46:47], v243 offset:25480\n"
"v_mfma_f32_16x16x32_bf16 v[80:83], a[8:11], v[228:231], 0\n"
"ds_read_b128 v[224:227], v232 offset:24576\n"
"v_mfma_f32_16x16x32_bf16 v[84:87], a[12:15], v[228:231], 0\n"
"global_load_lds_dwordx4 v242, s[78:79] offset:2048\n"
"v_mfma_f32_16x16x32_bf16 v[88:91], a[16:19], v[228:231], 0\n"
"s_waitcnt vmcnt(6)\n"
"ds_read_b64_tr_b16 a[48:49], v243 offset:49152\n"
"v_mfma_f32_16x16x32_bf16 v[92:95], a[20:23], v[228:231], 0\n"
"ds_read_b64_tr_b16 a[50:51], v243 offset:49920\n"
"s_waitcnt lgkmcnt(14)\n"
"v_mfma_f32_16x16x32_bf16 v[96:99], a[0:3], v[216:219], 0\n"
"ds_read_b64_tr_b16 a[52:53], v243 offset:49160\n"
"v_mfma_f32_16x16x32_bf16 v[100:103], a[4:7], v[216:219], 0\n"
"ds_read_b64_tr_b16 a[54:55], v243 offset:49928\n"
"v_mfma_f32_16x16x32_bf16 v[104:107], a[8:11], v[216:219], 0\n"
"ds_read_b128 v[228:231], v232 offset:28672\n"
"ds_read_b64_tr_b16 a[56:57], v243 offset:49216\n"
"v_mfma_f32_16x16x32_bf16 v[108:111], a[12:15], v[216:219], 0\n"
"ds_read_b64_tr_b16 a[58:59], v243 offset:49984\n"
"v_mfma_f32_16x16x32_bf16 v[112:115], a[16:19], v[216:219], 0\n"
"s_add_u32 s78, s64, 0x18000\n"
"s_addc_u32 s79, s65, 0\n"
"v_mfma_f32_16x16x32_bf16 v[116:119], a[20:23], v[216:219], 0\n"
"s_add_u32 m0, s74, 0x6000\n"
"s_waitcnt lgkmcnt(13)\n"
"v_mfma_f32_16x16x32_bf16 v[120:123], a[0:3], v[220:223], 0\n"
"s_waitcnt lgkmcnt(8)\n"
"global_load_lds_dwordx4 v240, s[78:79] offset:0\n"
"v_mfma_f32_16x16x32_bf16 v[124:127], a[4:7], v[220:223], 0\n"
"ds_read_b64_tr_b16 a[60:61], v243 offset:49224\n"
"v_mfma_f32_16x16x32_bf16 v[128:131], a[8:11], v[220:223], 0\n"
"ds_read_b128 v[216:219], v232 offset:32768\n"
"v_add_u32 v232, 0x9000, v232\n"
"v_mfma_f32_16x16x32_bf16 v[132:135], a[12:15], v[220:223], 0\n"
"ds_read_b64_tr_b16 a[62:63], v243 offset:49992\n"
"v_mfma_f32_16x16x32_bf16 v[136:139], a[16:19], v[220:223], 0\n"
"global_load_lds_dwordx4 v241, s[78:79] offset:1024\n"
"ds_read_b64_tr_b16 a[64:65], v243 offset:49280\n"
"v_mfma_f32_16x16x32_bf16 v[140:143], a[20:23], v[220:223], 0\n"
"ds_read_b64_tr_b16 a[66:67], v243 offset:50048\n"
"s_waitcnt lgkmcnt(12)\n"
"v_mfma_f32_16x16x32_bf16 v[144:147], a[0:3], v[224:227], 0\n"
"ds_read_b64_tr_b16 a[68:69], v243 offset:49288\n"
"v_mfma_f32_16x16x32_bf16 v[148:151], a[4:7], v[224:227], 0\n"
"ds_read_b64_tr_b16 a[70:71], v243 offset:50056\n"
"v_mfma_f32_16x16x32_bf16 v[152:155], a[8:11], v[224:227], 0\n"
"ds_read_b128 v[220:223], v233 offset:0\n"
"global_load_lds_dwordx4 v242, s[78:79] offset:2048\n"
"v_mfma_f32_16x16x32_bf16 v[156:159], a[12:15], v[224:227], 0\n"
"s_mul_i32 s14, s76, 0x12000\n"
"v_mfma_f32_16x16x32_bf16 v[160:163], a[16:19], v[224:227], 0\n"
"s_add_u32 s78, s64, 0x1b000\n"
"s_addc_u32 s79, s65, 0\n"
"v_mfma_f32_16x16x32_bf16 v[164:167], a[20:23], v[224:227], 0\n"
"s_add_u32 m0, s74, 0x6c00\n"
"s_waitcnt lgkmcnt(10)\n"
"v_mfma_f32_16x16x32_bf16 v[168:171], a[0:3], v[228:231], 0\n"
"global_load_lds_dwordx4 v240, s[78:79] offset:0\n"
"v_mfma_f32_16x16x32_bf16 v[172:175], a[4:7], v[228:231], 0\n"
"s_add_u32 s14, s14, 0x10000\n"
"v_mfma_f32_16x16x32_bf16 v[176:179], a[8:11], v[228:231], 0\n"
"ds_read_b128 v[224:227], v233 offset:4096\n"
"v_mfma_f32_16x16x32_bf16 v[180:183], a[12:15], v[228:231], 0\n"
"s_add_u32 s24, s28, s14\n"
"s_addc_u32 s25, s29, 0\n"
"v_mfma_f32_16x16x32_bf16 v[184:187], a[16:19], v[228:231], 0\n"
"global_load_lds_dwordx4 v241, s[78:79] offset:1024\n"
"v_mfma_f32_16x16x32_bf16 v[188:191], a[20:23], v[228:231], 0\n"
"s_waitcnt lgkmcnt(7)\n"
"v_mfma_f32_16x16x32_bf16 v[192:195], a[0:3], v[216:219], 0\n"
"s_mov_b32 s88, 0\n"
"v_mfma_f32_16x16x32_bf16 v[196:199], a[4:7], v[216:219], 0\n"
"s_mov_b32 s19, 30\n"
"v_mfma_f32_16x16x32_bf16 v[200:203], a[8:11], v[216:219], 0\n"
"ds_read_b128 v[228:231], v233 offset:8192\n"
"global_load_lds_dwordx4 v242, s[78:79] offset:2048\n"
"v_mfma_f32_16x16x32_bf16 v[204:207], a[12:15], v[216:219], 0\n"
"v_mfma_f32_16x16x32_bf16 v[208:211], a[16:19], v[216:219], 0\n"
"v_mfma_f32_16x16x32_bf16 v[212:215], a[20:23], v[216:219], 0\n"
"s_add_u32 m0, s30, 0x8f00\n"
"s_waitcnt lgkmcnt(2)\n"
"v_mfma_f32_16x16x32_bf16 v[0:3], a[24:27], v[220:223], v[0:3]\n"
"global_load_lds_dwordx4 v236, s[32:33] offset:256 nt\n"
"v_mfma_f32_16x16x32_bf16 v[4:7], a[28:31], v[220:223], v[4:7]\n"
"v_mfma_f32_16x16x32_bf16 v[8:11], a[32:35], v[220:223], v[8:11]\n"
"ds_read_b128 v[216:219], v233 offset:12288\n"
"s_add_u32 m0, s30, 0x9f00\n"
"s_waitcnt vmcnt(7)\n"
"ds_read_b64_tr_b16 a[72:73], v243 offset:0\n"
"v_mfma_f32_16x16x32_bf16 v[12:15], a[36:39], v[220:223], v[12:15]\n"
"global_load_lds_dwordx4 v236, s[34:35] offset:256 nt\n"
"ds_read_b64_tr_b16 a[74:75], v243 offset:768\n"
"v_mfma_f32_16x16x32_bf16 v[16:19], a[40:43], v[220:223], v[16:19]\n"
"ds_read_b64_tr_b16 a[76:77], v243 offset:8\n"
"v_mfma_f32_16x16x32_bf16 v[20:23], a[44:47], v[220:223], v[20:23]\n"
"s_add_u32 m0, s30, 0xaf00\n"
"ds_read_b64_tr_b16 a[78:79], v243 offset:776\n"
"s_waitcnt lgkmcnt(6)\n"
"v_mfma_f32_16x16x32_bf16 v[24:27], a[24:27], v[224:227], v[24:27]\n"
"global_load_lds_dwordx4 v236, s[36:37] offset:256 nt\n"
"ds_read_b64_tr_b16 a[80:81], v243 offset:64\n"
"v_mfma_f32_16x16x32_bf16 v[28:31], a[28:31], v[224:227], v[28:31]\n"
"ds_read_b64_tr_b16 a[82:83], v243 offset:832\n"
"v_mfma_f32_16x16x32_bf16 v[32:35], a[32:35], v[224:227], v[32:35]\n"
"ds_read_b128 v[220:223], v233 offset:16384\n"
"s_add_u32 m0, s30, 0xbf00\n"
"v_mfma_f32_16x16x32_bf16 v[36:39], a[36:39], v[224:227], v[36:39]\n"
"global_load_lds_dwordx4 v236, s[38:39] offset:256 nt\n"
"ds_read_b64_tr_b16 a[84:85], v243 offset:72\n"
"v_mfma_f32_16x16x32_bf16 v[40:43], a[40:43], v[224:227], v[40:43]\n"
"ds_read_b64_tr_b16 a[86:87], v243 offset:840\n"
"v_mfma_f32_16x16x32_bf16 v[44:47], a[44:47], v[224:227], v[44:47]\n"
"s_add_u32 m0, s30, 0xcf00\n"
"s_waitcnt lgkmcnt(10)\n"
"v_mfma_f32_16x16x32_bf16 v[48:51], a[24:27], v[228:231], v[48:51]\n"
"global_load_lds_dwordx4 v236, s[40:41] offset:256 nt\n"
"ds_read_b64_tr_b16 a[88:89], v243 offset:128\n"
"v_mfma_f32_16x16x32_bf16 v[52:55], a[28:31], v[228:231], v[52:55]\n"
"ds_read_b64_tr_b16 a[90:91], v243 offset:896\n"
"v_mfma_f32_16x16x32_bf16 v[56:59], a[32:35], v[228:231], v[56:59]\n"
"ds_read_b128 v[224:227], v233 offset:20480\n"
"s_add_u32 m0, s30, 0xdf00\n"
"v_mfma_f32_16x16x32_bf16 v[60:63], a[36:39], v[228:231], v[60:63]\n"
"global_load_lds_dwordx4 v236, s[42:43] offset:256 nt\n"
"ds_read_b64_tr_b16 a[92:93], v243 offset:136\n"
"v_mfma_f32_16x16x32_bf16 v[64:67], a[40:43], v[228:231], v[64:67]\n"
"ds_read_b64_tr_b16 a[94:95], v243 offset:904\n"
"v_mfma_f32_16x16x32_bf16 v[68:71], a[44:47], v[228:231], v[68:71]\n"
"s_add_u32 m0, s30, 0xef00\n"
"s_waitcnt lgkmcnt(14)\n"
"v_mfma_f32_16x16x32_bf16 v[72:75], a[24:27], v[216:219], v[72:75]\n"
"global_load_lds_dwordx4 v236, s[44:45] offset:256 nt\n"
"v_mfma_f32_16x16x32_bf16 v[76:79], a[28:31], v[216:219], v[76:79]\n"
"v_mfma_f32_16x16x32_bf16 v[80:83], a[32:35], v[216:219], v[80:83]\n"
"ds_read_b128 v[228:231], v233 offset:24576\n"
"s_add_u32 m0, s30, 0xff00\n"
"v_mfma_f32_16x16x32_bf16 v[84:87], a[36:39], v[216:219], v[84:87]\n"
"global_load_lds_dwordx4 v236, s[46:47] offset:256 nt\n"
"v_mfma_f32_16x16x32_bf16 v[88:91], a[40:43], v[216:219], v[88:91]\n"
"v_mfma_f32_16x16x32_bf16 v[92:95], a[44:47], v[216:219], v[92:95]\n"
"s_add_u32 m0, s30, 0x10f00\n"
"s_waitcnt lgkmcnt(8)\n"
"v_mfma_f32_16x16x32_bf16 v[96:99], a[24:27], v[220:223], v[96:99]\n"
"global_load_lds_dwordx4 v236, s[48:49] offset:256 nt\n"
"v_mfma_f32_16x16x32_bf16 v[100:103], a[28:31], v[220:223], v[100:103]\n"
"v_mfma_f32_16x16x32_bf16 v[104:107], a[32:35], v[220:223], v[104:107]\n"
"ds_read_b128 v[216:219], v233 offset:28672\n"
"s_add_u32 s78, s64, 0x1e000\n"
"s_addc_u32 s79, s65, 0\n"
"v_mfma_f32_16x16x32_bf16 v[108:111], a[36:39], v[220:223], v[108:111]\n"
"s_mov_b64 exec, s[12:13]\n"
"global_store_dwordx2 v251, v[252:253], s[28:29] sc1\n"
"s_mov_b64 exec, -1\n"
"s_add_u32 m0, s74, 0xc000\n"
"v_mfma_f32_16x16x32_bf16 v[112:115], a[40:43], v[220:223], v[112:115]\n"
"global_load_lds_dwordx4 v240, s[78:79] offset:0\n"
"v_mfma_f32_16x16x32_bf16 v[116:119], a[44:47], v[220:223], v[116:119]\n"
"s_waitcnt lgkmcnt(4)\n"
"v_mfma_f32_16x16x32_bf16 v[120:123], a[24:27], v[224:227], v[120:123]\n"
"v_mfma_f32_16x16x32_bf16 v[124:127], a[28:31], v[224:227], v[124:127]\n"
"s_waitcnt vmcnt(11)\n"
"ds_read_b64_tr_b16 a[96:97], v243 offset:24576\n"
"v_mfma_f32_16x16x32_bf16 v[128:131], a[32:35], v[224:227], v[128:131]\n"
"ds_read_b128 v[220:223], v233 offset:32768\n"
"v_add_u32 v233, 0x9000, v233\n"
"global_load_lds_dwordx4 v241, s[78:79] offset:1024\n"
"v_mfma_f32_16x16x32_bf16 v[132:135], a[36:39], v[224:227], v[132:135]\n"
"ds_read_b64_tr_b16 a[98:99], v243 offset:25344\n"
"v_mfma_f32_16x16x32_bf16 v[136:139], a[40:43], v[224:227], v[136:139]\n"
"ds_read_b64_tr_b16 a[100:101], v243 offset:24584\n"
"v_mfma_f32_16x16x32_bf16 v[140:143], a[44:47], v[224:227], v[140:143]\n"
"ds_read_b64_tr_b16 a[102:103], v243 offset:25352\n"
"s_waitcnt lgkmcnt(6)\n"
"v_mfma_f32_16x16x32_bf16 v[144:147], a[24:27], v[228:231], v[144:147]\n"
"global_load_lds_dwordx4 v242, s[78:79] offset:2048\n"
"ds_read_b64_tr_b16 a[104:105], v243 offset:24640\n"
"v_mfma_f32_16x16x32_bf16 v[148:151], a[28:31], v[228:231], v[148:151]\n"
"ds_read_b64_tr_b16 a[106:107], v243 offset:25408\n"
"v_mfma_f32_16x16x32_bf16 v[152:155], a[32:35], v[228:231], v[152:155]\n"
"ds_read_b128 v[224:227], v234 offset:0\n"
"s_add_u32 s78, s64, 0x21000\n"
"s_addc_u32 s79, s65, 0\n"
"v_mfma_f32_16x16x32_bf16 v[156:159], a[36:39], v[228:231], v[156:159]\n"
"s_add_u32 m0, s74, 0xcc00\n"
"ds_read_b64_tr_b16 a[108:109], v243 offset:24648\n"
"v_mfma_f32_16x16x32_bf16 v[160:163], a[40:43], v[228:231], v[160:163]\n"
"global_load_lds_dwordx4 v240, s[78:79] offset:0\n"
"ds_read_b64_tr_b16 a[110:111], v243 offset:25416\n"
"v_mfma_f32_16x16x32_bf16 v[164:167], a[44:47], v[228:231], v[164:167]\n"
"s_waitcnt lgkmcnt(10)\n"
"v_mfma_f32_16x16x32_bf16 v[168:171], a[24:27], v[216:219], v[168:171]\n"
"ds_read_b64_tr_b16 a[112:113], v243 offset:24704\n"
"v_mfma_f32_16x16x32_bf16 v[172:175], a[28:31], v[216:219], v[172:175]\n"
"ds_read_b64_tr_b16 a[114:115], v243 offset:25472\n"
"v_mfma_f32_16x16x32_bf16 v[176:179], a[32:35], v[216:219], v[176:179]\n"
"ds_read_b128 v[228:231], v234 offset:4096\n"
"global_load_lds_dwordx4 v241, s[78:79] offset:1024\n"
"v_mfma_f32_16x16x32_bf16 v[180:183], a[36:39], v[216:219], v[180:183]\n"
"ds_read_b64_tr_b16 a[116:117], v243 offset:24712\n"
"v_mfma_f32_16x16x32_bf16 v[184:187], a[40:43], v[216:219], v[184:187]\n"
"ds_read_b64_tr_b16 a[118:119], v243 offset:25480\n"
"v_mfma_f32_16x16x32_bf16 v[188:191], a[44:47], v[216:219], v[188:191]\n"
"s_waitcnt lgkmcnt(13)\n"
"v_mfma_f32_16x16x32_bf16 v[192:195], a[24:27], v[220:223], v[192:195]\n"
"global_load_lds_dwordx4 v242, s[78:79] offset:2048\n"
"v_mfma_f32_16x16x32_bf16 v[196:199], a[28:31], v[220:223], v[196:199]\n"
"v_mfma_f32_16x16x32_bf16 v[200:203], a[32:35], v[220:223], v[200:203]\n"
"ds_read_b128 v[216:219], v234 offset:8192\n"
"s_add_u32 s78, s64, 0x24000\n"
"s_addc_u32 s79, s65, 0\n"
"v_mfma_f32_16x16x32_bf16 v[204:207], a[36:39], v[220:223], v[204:207]\n"
"s_add_u32 m0, s74, 0x0\n"
"v_mfma_f32_16x16x32_bf16 v[208:211], a[40:43], v[220:223], v[208:211]\n"
"global_load_lds_dwordx4 v240, s[78:79] offset:0\n"
"v_mfma_f32_16x16x32_bf16 v[212:215], a[44:47], v[220:223], v[212:215]\n"
"s_waitcnt lgkmcnt(8)\n"
"v_mfma_f32_16x16x32_bf16 v[0:3], a[48:51], v[224:227], v[0:3]\n"
"v_mfma_f32_16x16x32_bf16 v[4:7], a[52:55], v[224:227], v[4:7]\n"
"v_mfma_f32_16x16x32_bf16 v[8:11], a[56:59], v[224:227], v[8:11]\n"
"ds_read_b128 v[220:223], v234 offset:12288\n"
"global_load_lds_dwordx4 v241, s[78:79] offset:1024\n"
"v_mfma_f32_16x16x32_bf16 v[12:15], a[60:63], v[224:227], v[12:15]\n"
"v_mfma_f32_16x16x32_bf16 v[16:19], a[64:67], v[224:227], v[16:19]\n"
"v_mfma_f32_16x16x32_bf16 v[20:23], a[68:71], v[224:227], v[20:23]\n"
"s_waitcnt lgkmcnt(4)\n"
"v_mfma_f32_16x16x32_bf16 v[24:27], a[48:51], v[228:231], v[24:27]\n"
"global_load_lds_dwordx4 v242, s[78:79] offset:2048\n"
"v_mfma_f32_16x16x32_bf16 v[28:31], a[52:55], v[228:231], v[28:31]\n"
"v_mfma_f32_16x16x32_bf16 v[32:35], a[56:59], v[228:231], v[32:35]\n"
"ds_read_b128 v[224:227], v234 offset:16384\n"
"s_add_u32 s78, s64, 0x27000\n"
"s_addc_u32 s79, s65, 0\n"
"v_mfma_f32_16x16x32_bf16 v[36:39], a[60:63], v[228:231], v[36:39]\n"
"s_add_u32 m0, s74, 0xc00\n"
"v_mfma_f32_16x16x32_bf16 v[40:43], a[64:67], v[228:231], v[40:43]\n"
"global_load_lds_dwordx4 v240, s[78:79] offset:0\n"
"v_mfma_f32_16x16x32_bf16 v[44:47], a[68:71], v[228:231], v[44:47]\n"
"s_waitcnt lgkmcnt(2)\n"
"v_mfma_f32_16x16x32_bf16 v[48:51], a[48:51], v[216:219], v[48:51]\n"
"v_mfma_f32_16x16x32_bf16 v[52:55], a[52:55], v[216:219], v[52:55]\n"
"v_mfma_f32_16x16x32_bf16 v[56:59], a[56:59], v[216:219], v[56:59]\n"
"ds_read_b128 v[228:231], v234 offset:20480\n"
"global_load_lds_dwordx4 v241, s[78:79] offset:1024\n"
"v_mfma_f32_16x16x32_bf16 v[60:63], a[60:63], v[216:219], v[60:63]\n"
"v_mfma_f32_16x16x32_bf16 v[64:67], a[64:67], v[216:219], v[64:67]\n"
"v_mfma_f32_16x16x32_bf16 v[68:71], a[68:71], v[216:219], v[68:71]\n"
"s_waitcnt lgkmcnt(2)\n"
"v_mfma_f32_16x16x32_bf16 v[72:75], a[48:51], v[220:223], v[72:75]\n"
"global_load_lds_dwordx4 v242, s[78:79] offset:2048\n"
"v_mfma_f32_16x16x32_bf16 v[76:79], a[52:55], v[220:223], v[76:79]\n"
"v_mfma_f32_16x16x32_bf16 v[80:83], a[56:59], v[220:223], v[80:83]\n"
"ds_read_b128 v[216:219], v234 offset:24576\n"
"s_add_u32 s78, s64, 0x2a000\n"
"s_addc_u32 s79, s65, 0\n"
"v_mfma_f32_16x16x32_bf16 v[84:87], a[60:63], v[220:223], v[84:87]\n"
"s_add_u32 m0, s74, 0x6000\n"
"v_mfma_f32_16x16x32_bf16 v[88:91], a[64:67], v[220:223], v[88:91]\n"
"global_load_lds_dwordx4 v240, s[78:79] offset:0\n"
"v_mfma_f32_16x16x32_bf16 v[92:95], a[68:71], v[220:223], v[92:95]\n"
"s_waitcnt lgkmcnt(2)\n"
"v_mfma_f32_16x16x32_bf16 v[96:99], a[48:51], v[224:227], v[96:99]\n"
"v_mfma_f32_16x16x32_bf16 v[100:103], a[52:55], v[224:227], v[100:103]\n"
"v_mfma_f32_16x16x32_bf16 v[104:107], a[56:59], v[224:227], v[104:107]\n"
"ds_read_b128 v[220:223], v234 offset:28672\n"
"global_load_lds_dwordx4 v241, s[78:79] offset:1024\n"
"v_mfma_f32_16x16x32_bf16 v[108:111], a[60:63], v[224:227], v[108:111]\n"
"v_mfma_f32_16x16x32_bf16 v[112:115], a[64:67], v[224:227], v[112:115]\n"
"s_mov_b64 exec, s[10:11]\n"
"s_cbranch_execz LHT3\n"
"LHR3:\n"
"s_mov_b64 exec, -1\n"
"v_mfma_f32_16x16x32_bf16 v[116:119], a[68:71], v[224:227], v[116:119]\n"
"s_waitcnt vmcnt(8)\n"
"ds_read_b64_tr_b16 a[120:121], v243 offset:49152\n"
"s_waitcnt lgkmcnt(3)\n"
"v_mfma_f32_16x16x32_bf16 v[120:123], a[48:51], v[228:231], v[120:123]\n"
"global_load_lds_dwordx4 v242, s[78:79] offset:2048\n"
"ds_read_b64_tr_b16 a[122:123], v243 offset:49920\n"
"v_mfma_f32_16x16x32_bf16 v[124:127], a[52:55], v[228:231], v[124:127]\n"
"ds_read_b64_tr_b16 a[124:125], v243 offset:49160\n"
"v_mfma_f32_16x16x32_bf16 v[128:131], a[56:59], v[228:231], v[128:131]\n"
"ds_read_b128 v[224:227], v234 offset:32768\n"
"v_add_u32 v234, 0x9000, v234\n"
"s_add_u32 s78, s64, 0x2d000\n"
"s_addc_u32 s79, s65, 0\n"
"v_mfma_f32_16x16x32_bf16 v[132:135], a[60:63], v[228:231], v[132:135]\n"
"s_add_u32 m0, s74, 0x6c00\n"
"ds_read_b64_tr_b16 a[126:127], v243 offset:49928\n"
"v_mfma_f32_16x16x32_bf16 v[136:139], a[64:67], v[228:231], v[136:139]\n"
"global_load_lds_dwordx4 v240, s[78:79] offset:0\n"
"ds_read_b64_tr_b16 a[128:129], v243 offset:49216\n"
"v_mfma_f32_16x16x32_bf16 v[140:143], a[68:71], v[228:231], v[140:143]\n"
"ds_read_b64_tr_b16 a[130:131], v243 offset:49984\n"
"s_waitcnt lgkmcnt(8)\n"
"v_mfma_f32_16x16x32_bf16 v[144:147], a[48:51], v[216:219], v[144:147]\n"
"ds_read_b64_tr_b16 a[132:133], v243 offset:49224\n"
"v_mfma_f32_16x16x32_bf16 v[148:151], a[52:55], v[216:219], v[148:151]\n"
"ds_read_b64_tr_b16 a[134:135], v243 offset:49992\n"
"v_mfma_f32_16x16x32_bf16 v[152:155], a[56:59], v[216:219], v[152:155]\n"
"ds_read_b128 v[228:231], v235 offset:0\n"
"global_load_lds_dwordx4 v241, s[78:79] offset:1024\n"
"v_mfma_f32_16x16x32_bf16 v[156:159], a[60:63], v[216:219], v[156:159]\n"
"ds_read_b64_tr_b16 a[136:137], v243 offset:49280\n"
"v_mfma_f32_16x16x32_bf16 v[160:163], a[64:67], v[216:219], v[160:163]\n"
"ds_read_b64_tr_b16 a[138:139], v243 offset:50048\n"
"v_mfma_f32_16x16x32_bf16 v[164:167], a[68:71], v[216:219], v[164:167]\n"
"ds_read_b64_tr_b16 a[140:141], v243 offset:49288\n"
"s_waitcnt lgkmcnt(13)\n"
"v_mfma_f32_16x16x32_bf16 v[168:171], a[48:51], v[220:223], v[168:171]\n"
"global_load_lds_dwordx4 v242, s[78:79] offset:2048\n"
"v_mfma_f32_16x16x32_bf16 v[172:175], a[52:55], v[220:223], v[172:175]\n"
"ds_read_b64_tr_b16 a[142:143], v243 offset:50056\n"
"v_mfma_f32_16x16x32_bf16 v[176:179], a[56:59], v[220:223], v[176:179]\n"
"ds_read_b128 v[216:219], v235 offset:4096\n"
"v_mfma_f32_16x16x32_bf16 v[180:183], a[60:63], v[220:223], v[180:183]\n"
"global_load_dwordx4 v[244:247], v248, s[28:29] sc1\n"
"v_mfma_f32_16x16x32_bf16 v[184:187], a[64:67], v[220:223], v[184:187]\n"
"v_mfma_f32_16x16x32_bf16 v[188:191], a[68:71], v[220:223], v[188:191]\n"
"s_waitcnt lgkmcnt(11)\n"
"v_mfma_f32_16x16x32_bf16 v[192:195], a[48:51], v[224:227], v[192:195]\n"
"v_mfma_f32_16x16x32_bf16 v[196:199], a[52:55], v[224:227], v[196:199]\n"
"v_mfma_f32_16x16x32_bf16 v[200:203], a[56:59], v[224:227], v[200:203]\n"
"ds_read_b128 v[220:223], v235 offset:8192\n"
"v_mfma_f32_16x16x32_bf16 v[204:207], a[60:63], v[224:227], v[204:207]\n"
"s_add_u32 s78, s64, 0x30000\n"
"s_addc_u32 s79, s65, 0\n"
"v_mfma_f32_16x16x32_bf16 v[208:211], a[64:67], v[224:227], v[208:211]\n"
"s_add_u32 m0, s74, 0xc000\n"
"v_mfma_f32_16x16x32_bf16 v[212:215], a[68:71], v[224:227], v[212:215]\n"
"s_waitcnt lgkmcnt(2)\n"
"global_load_lds_dwordx4 v240, s[78:79] offset:0\n"
"v_mfma_f32_16x16x32_bf16 v[0:3], a[72:75], v[228:231], v[0:3]\n"
"s_waitcnt vmcnt(8)\n"
"ds_read_b64_tr_b16 a[144:145], v243 offset:0\n"
"v_mfma_f32_16x16x32_bf16 v[4:7], a[76:79], v[228:231], v[4:7]\n"
"ds_read_b64_tr_b16 a[146:147], v243 offset:768\n"
"v_mfma_f32_16x16x32_bf16 v[8:11], a[80:83], v[228:231], v[8:11]\n"
"ds_read_b128 v[224:227], v235 offset:12288\n"
"ds_read_b64_tr_b16 a[148:149], v243 offset:8\n"
"v_mfma_f32_16x16x32_bf16 v[12:15], a[84:87], v[228:231], v[12:15]\n"
"global_load_lds_dwordx4 v241, s[78:79] offset:1024\n"
"ds_read_b64_tr_b16 a[150:151], v243 offset:776\n"
"v_mfma_f32_16x16x32_bf16 v[16:19], a[88:91], v[228:231], v[16:19]\n"
"ds_read_b64_tr_b16 a[152:153], v243 offset:64\n"
"v_mfma_f32_16x16x32_bf16 v[20:23], a[92:95], v[228:231], v[20:23]\n"
"ds_read_b64_tr_b16 a[154:155], v243 offset:832\n"
"s_waitcnt lgkmcnt(8)\n"
"v_mfma_f32_16x16x32_bf16 v[24:27], a[72:75], v[216:219], v[24:27]\n"
"ds_read_b64_tr_b16 a[156:157], v243 offset:72\n"
"v_mfma_f32_16x16x32_bf16 v[28:31], a[76:79], v[216:219], v[28:31]\n"
"global_load_lds_dwordx4 v242, s[78:79] offset:2048\n"
"ds_read_b64_tr_b16 a[158:159], v243 offset:840\n"
"v_mfma_f32_16x16x32_bf16 v[32:35], a[80:83], v[216:219], v[32:35]\n"
"ds_read_b128 v[228:231], v235 offset:16384\n"
"ds_read_b64_tr_b16 a[160:161], v243 offset:128\n"
"v_mfma_f32_16x16x32_bf16 v[36:39], a[84:87], v[216:219], v[36:39]\n"
"s_add_u32 s78, s64, 0x33000\n"
"s_addc_u32 s79, s65, 0\n"
"v_mfma_f32_16x16x32_bf16 v[40:43], a[88:91], v[216:219], v[40:43]\n"
"s_add_u32 m0, s74, 0xcc00\n"
"ds_read_b64_tr_b16 a[162:163], v243 offset:896\n"
"v_mfma_f32_16x16x32_bf16 v[44:47], a[92:95], v[216:219], v[44:47]\n"
"global_load_lds_dwordx4 v240, s[78:79] offset:0\n"
"s_waitcnt lgkmcnt(12)\n"
"v_mfma_f32_16x16x32_bf16 v[48:51], a[72:75], v[220:223], v[48:51]\n"
"ds_read_b64_tr_b16 a[164:165], v243 offset:136\n"
"v_mfma_f32_16x16x32_bf16 v[52:55], a[76:79], v[220:223], v[52:55]\n"
"ds_read_b64_tr_b16 a[166:167], v243 offset:904\n"
"v_mfma_f32_16x16x32_bf16 v[56:59], a[80:83], v[220:223], v[56:59]\n"
"ds_read_b128 v[216:219], v235 offset:20480\n"
"v_mfma_f32_16x16x32_bf16 v[60:63], a[84:87], v[220:223], v[60:63]\n"
"global_load_lds_dwordx4 v241, s[78:79] offset:1024\n"
"v_mfma_f32_16x16x32_bf16 v[64:67], a[88:91], v[220:223], v[64:67]\n"
"v_mfma_f32_16x16x32_bf16 v[68:71], a[92:95], v[220:223], v[68:71]\n"
"s_waitcnt lgkmcnt(12)\n"
"v_mfma_f32_16x16x32_bf16 v[72:75], a[72:75], v[224:227], v[72:75]\n"
"v_mfma_f32_16x16x32_bf16 v[76:79], a[76:79], v[224:227], v[76:79]\n"
"global_load_lds_dwordx4 v242, s[78:79] offset:2048\n"
"v_mfma_f32_16x16x32_bf16 v[80:83], a[80:83], v[224:227], v[80:83]\n"
"ds_read_b128 v[220:223], v235 offset:24576\n"
"v_mfma_f32_16x16x32_bf16 v[84:87], a[84:87], v[224:227], v[84:87]\n"
"s_add_u32 s78, s64, 0x36000\n"
"s_addc_u32 s79, s65, 0\n"
"v_mfma_f32_16x16x32_bf16 v[88:91], a[88:91], v[224:227], v[88:91]\n"
"s_add_u32 m0, s74, 0x0\n"
"v_mfma_f32_16x16x32_bf16 v[92:95], a[92:95], v[224:227], v[92:95]\n"
"s_waitcnt lgkmcnt(2)\n"
"global_load_lds_dwordx4 v240, s[78:79] offset:0\n"
"v_mfma_f32_16x16x32_bf16 v[96:99], a[72:75], v[228:231], v[96:99]\n"
"s_waitcnt vmcnt(8)\n"
"ds_read_b64_tr_b16 a[168:169], v243 offset:24576\n"
"LHT3:\n"
"s_cbranch_execz LHR3\n"
"v_mfma_f32_16x16x32_bf16 v[100:103], a[76:79], v[228:231], v[100:103]\n"
"ds_read_b64_tr_b16 a[170:171], v243 offset:25344\n"
"v_mfma_f32_16x16x32_bf16 v[104:107], a[80:83], v[228:231], v[104:107]\n"
"ds_read_b128 v[224:227], v235 offset:28672\n"
"ds_read_b64_tr_b16 a[172:173], v243 offset:24584\n"
"v_mfma_f32_16x16x32_bf16 v[108:111], a[84:87], v[228:231], v[108:111]\n"
"global_load_lds_dwordx4 v241, s[78:79] offset:1024\n"
"ds_read_b64_tr_b16 a[174:175], v243 offset:25352\n"
"v_mfma_f32_16x16x32_bf16 v[112:115], a[88:91], v[228:231], v[112:115]\n"
"ds_read_b64_tr_b16 a[176:177], v243 offset:24640\n"
"v_mfma_f32_16x16x32_bf16 v[116:119], a[92:95], v[228:231], v[116:119]\n"
"ds_read_b64_tr_b16 a[178:179], v243 offset:25408\n"
"s_waitcnt lgkmcnt(8)\n"
"v_mfma_f32_16x16x32_bf16 v[120:123], a[72:75], v[216:219], v[120:123]\n"
"ds_read_b64_tr_b16 a[180:181], v243 offset:24648\n"
"v_mfma_f32_16x16x32_bf16 v[124:127], a[76:79], v[216:219], v[124:127]\n"
"global_load_lds_dwordx4 v242, s[78:79] offset:2048\n"
"ds_read_b64_tr_b16 a[182:183], v243 offset:25416\n"
"v_mfma_f32_16x16x32_bf16 v[128:131], a[80:83], v[216:219], v[128:131]\n"
"ds_read_b128 v[228:231], v235 offset:32768\n"
"v_add_u32 v235, 0x9000, v235\n"
"ds_read_b64_tr_b16 a[184:185], v243 offset:24704\n"
"v_mfma_f32_16x16x32_bf16 v[132:135], a[84:87], v[216:219], v[132:135]\n"
"s_add_u32 s78, s64, 0x39000\n"
"s_addc_u32 s79, s65, 0\n"
"v_mfma_f32_16x16x32_bf16 v[136:139], a[88:91], v[216:219], v[136:139]\n"
"s_add_u32 m0, s74, 0xc00\n"
"ds_read_b64_tr_b16 a[186:187], v243 offset:25472\n"
"v_mfma_f32_16x16x32_bf16 v[140:143], a[92:95], v[216:219], v[140:143]\n"
"global_load_lds_dwordx4 v240, s[78:79] offset:0\n"
"s_waitcnt lgkmcnt(12)\n"
"v_mfma_f32_16x16x32_bf16 v[144:147], a[72:75], v[220:223], v[144:147]\n"
"ds_read_b64_tr_b16 a[188:189], v243 offset:24712\n"
"v_mfma_f32_16x16x32_bf16 v[148:151], a[76:79], v[220:223], v[148:151]\n"
"s_barrier\n"
"ds_read_b64_tr_b16 a[190:191], v243 offset:25480\n"
"v_mfma_f32_16x16x32_bf16 v[152:155], a[80:83], v[220:223], v[152:155]\n"
"ds_read_b128 v[216:219], v232 offset:0\n"
"s_waitcnt vmcnt(10)\n"
"v_cmp_ne_u64 vcc, s[80:81], v[244:245]\n"
"v_cmp_ne_u64 s[56:57], s[80:81], v[244:245]\n"
"s_nop 3\n"
"s_or_b64 vcc, vcc, s[56:57]\n"
"s_cbranch_scc0 LP1_1\n"
"s_mov_b32 s82, 0x40000\n"
"LP0_1:\n"
"s_sleep 2\n"
"global_load_dwordx4 v[244:247], v248, s[28:29] sc1\n"
"s_sub_u32 s82, s82, 1\n"
"s_cbranch_scc1 LP2_1\n"
"s_waitcnt vmcnt(0)\n"
"v_cmp_ne_u64 vcc, s[80:81], v[244:245]\n"
"v_cmp_ne_u64 s[56:57], s[80:81], v[244:245]\n"
"s_nop 3\n"
"s_or_b64 vcc, vcc, s[56:57]\n"
"s_cbranch_scc1 LP0_1\n"
"LP2_1:\n"
"s_waitcnt vmcnt(0)\n"
"LP1_1:\n"
"s_nop 0\n"
"v_mfma_f32_16x16x32_bf16 v[156:159], a[84:87], v[220:223], v[156:159]\n"
"v_mfma_f32_16x16x32_bf16 v[160:163], a[88:91], v[220:223], v[160:163]\n"
"v_subrev_u32 v246, s83, v246\n"
"global_load_lds_dwordx4 v241, s[78:79] offset:1024\n"
"v_mfma_f32_16x16x32_bf16 v[164:167], a[92:95], v[220:223], v[164:167]\n"
"s_waitcnt lgkmcnt(12)\n"
"v_mfma_f32_16x16x32_bf16 v[168:171], a[72:75], v[224:227], v[168:171]\n"
"v_add_u32_dpp v246, v246, v246 row_shr:1 row_mask:0xf bank_mask:0xf\n"
"v_mfma_f32_16x16x32_bf16 v[172:175], a[76:79], v[224:227], v[172:175]\n"
"v_mfma_f32_16x16x32_bf16 v[176:179], a[80:83], v[224:227], v[176:179]\n"
"ds_read_b128 v[220:223], v232 offset:4096\n"
"v_add_u32_dpp v246, v246, v246 row_shr:2 row_mask:0xf bank_mask:0xf\n"
"global_load_lds_dwordx4 v242, s[78:79] offset:2048\n"
"v_mfma_f32_16x16x32_bf16 v[180:183], a[84:87], v[224:227], v[180:183]\n"
"v_mfma_f32_16x16x32_bf16 v[184:187], a[88:91], v[224:227], v[184:187]\n"
"v_add_u32_dpp v246, v246, v246 row_shr:4 row_mask:0xf bank_mask:0xf\n"
"s_add_u32 s78, s64, 0x3c000\n"
"s_addc_u32 s79, s65, 0\n"
"v_mfma_f32_16x16x32_bf16 v[188:191], a[92:95], v[224:227], v[188:191]\n"
"s_add_u32 m0, s74, 0x6000\n"
"s_waitcnt lgkmcnt(6)\n"
"v_mfma_f32_16x16x32_bf16 v[192:195], a[72:75], v[228:231], v[192:195]\n"
"v_readlane_b32 s86, v246, 63\n"
"s_waitcnt lgkmcnt(2)\n"
"global_load_lds_dwordx4 v240, s[78:79] offset:0\n"
"v_mfma_f32_16x16x32_bf16 v[196:199], a[76:79], v[228:231], v[196:199]\n"
"v_mfma_f32_16x16x32_bf16 v[200:203], a[80:83], v[228:231], v[200:203]\n"
"ds_read_b128 v[224:227], v232 offset:8192\n"
"s_ashr_i32 s86, s86, 3\n"
"v_mfma_f32_16x16x32_bf16 v[204:207], a[84:87], v[228:231], v[204:207]\n"
"v_mfma_f32_16x16x32_bf16 v[208:211], a[88:91], v[228:231], v[208:211]\n"
"s_max_i32 s86, s86, 100\n"
"global_load_lds_dwordx4 v241, s[78:79] offset:1024\n"
"v_mfma_f32_16x16x32_bf16 v[212:215], a[92:95], v[228:231], v[212:215]\n"
"s_waitcnt lgkmcnt(2)\n"
"v_mfma_f32_16x16x32_bf16 v[0:3], a[96:99], v[216:219], v[0:3]\n"
"s_min_i32 s86, s86, 600\n"
"s_waitcnt vmcnt(8)\n"
"ds_read_b64_tr_b16 a[192:193], v243 offset:49152\n"
"v_mfma_f32_16x16x32_bf16 v[4:7], a[100:103], v[216:219], v[4:7]\n"
"s_barrier\n"
"ds_read_b64_tr_b16 a[194:195], v243 offset:49920\n"
"v_mfma_f32_16x16x32_bf16 v[8:11], a[104:107], v[216:219], v[8:11]\n"
"ds_read_b128 v[228:231], v232 offset:12288\n"
"s_add_u32 s83, s83, s86\n"
"global_load_lds_dwordx4 v242, s[78:79] offset:2048\n"
"v_mfma_f32_16x16x32_bf16 v[12:15], a[108:111], v[216:219], v[12:15]\n"
"ds_read_b64_tr_b16 a[196:197], v243 offset:49160\n"
"v_mfma_f32_16x16x32_bf16 v[16:19], a[112:115], v[216:219], v[16:19]\n"
"s_add_u32 s83, s83, 3318\n"
"s_add_u32 s78, s64, 0x3f000\n"
"s_addc_u32 s79, s65, 0\n"
"v_mfma_f32_16x16x32_bf16 v[20:23], a[116:119], v[216:219], v[20:23]\n"
"s_add_u32 m0, s74, 0x6c00\n"
"s_waitcnt lgkmcnt(5)\n"
"v_mfma_f32_16x16x32_bf16 v[24:27], a[96:99], v[220:223], v[24:27]\n"
"global_load_lds_dwordx4 v240, s[78:79] offset:0\n"
"ds_read_b64_tr_b16 a[198:199], v243 offset:49928\n"
"v_mfma_f32_16x16x32_bf16 v[28:31], a[100:103], v[220:223], v[28:31]\n"
"ds_read_b64_tr_b16 a[200:201], v243 offset:49216\n"
"v_mfma_f32_16x16x32_bf16 v[32:35], a[104:107], v[220:223], v[32:35]\n"
"ds_read_b128 v[216:219], v232 offset:16384\n"
"ds_read_b64_tr_b16 a[202:203], v243 offset:49984\n"
"v_mfma_f32_16x16x32_bf16 v[36:39], a[108:111], v[220:223], v[36:39]\n"
"ds_read_b64_tr_b16 a[204:205], v243 offset:49224\n"
"v_mfma_f32_16x16x32_bf16 v[40:43], a[112:115], v[220:223], v[40:43]\n"
"global_load_lds_dwordx4 v241, s[78:79] offset:1024\n"
"ds_read_b64_tr_b16 a[206:207], v243 offset:49992\n"
"v_mfma_f32_16x16x32_bf16 v[44:47], a[116:119], v[220:223], v[44:47]\n"
"ds_read_b64_tr_b16 a[208:209], v243 offset:49280\n"
"s_waitcnt lgkmcnt(11)\n"
"v_mfma_f32_16x16x32_bf16 v[48:51], a[96:99], v[224:227], v[48:51]\n"
"ds_read_b64_tr_b16 a[210:211], v243 offset:50048\n"
"v_mfma_f32_16x16x32_bf16 v[52:55], a[100:103], v[224:227], v[52:55]\n"
"ds_read_b64_tr_b16 a[212:213], v243 offset:49288\n"
"v_mfma_f32_16x16x32_bf16 v[56:59], a[104:107], v[224:227], v[56:59]\n"
"ds_read_b128 v[220:223], v232 offset:20480\n"
"global_load_lds_dwordx4 v242, s[78:79] offset:2048\n"
"v_mfma_f32_16x16x32_bf16 v[60:63], a[108:111], v[224:227], v[60:63]\n"
"ds_read_b64_tr_b16 a[214:215], v243 offset:50056\n"
"v_mfma_f32_16x16x32_bf16 v[64:67], a[112:115], v[224:227], v[64:67]\n"
"s_add_u32 s58, s24, 0x1000\n"
"s_addc_u32 s59, s25, 0\n"
"v_mfma_f32_16x16x32_bf16 v[68:71], a[116:119], v[224:227], v[68:71]\n"
"global_load_dwordx4 a[240:243], v239, s[58:59] offset:-4096 sc1\n"
"s_waitcnt lgkmcnt(12)\n"
"v_mfma_f32_16x16x32_bf16 v[72:75], a[96:99], v[228:231], v[72:75]\n"
"v_mfma_f32_16x16x32_bf16 v[76:79], a[100:103], v[228:231], v[76:79]\n"
"v_mfma_f32_16x16x32_bf16 v[80:83], a[104:107], v[228:231], v[80:83]\n"
"ds_read_b128 v[224:227], v232 offset:24576\n"
"v_mfma_f32_16x16x32_bf16 v[84:87], a[108:111], v[228:231], v[84:87]\n"
"global_load_dwordx4 a[244:247], v239, s[58:59] offset:-3072 sc1\n"
"v_mfma_f32_16x16x32_bf16 v[88:91], a[112:115], v[228:231], v[88:91]\n"
"v_mfma_f32_16x16x32_bf16 v[92:95], a[116:119], v[228:231], v[92:95]\n"
"s_waitcnt lgkmcnt(9)\n"
"v_mfma_f32_16x16x32_bf16 v[96:99], a[96:99], v[216:219], v[96:99]\n"
"s_add_u32 s78, s64, 0x42000\n"
"s_addc_u32 s79, s65, 0\n"
"v_mfma_f32_16x16x32_bf16 v[100:103], a[100:103], v[216:219], v[100:103]\n"
"s_add_u32 m0, s74, 0xc000\n"
"s_waitcnt vmcnt(8)\n"
"ds_read_b64_tr_b16 a[216:217], v243 offset:0\n"
"v_mfma_f32_16x16x32_bf16 v[104:107], a[104:107], v[216:219], v[104:107]\n"
"ds_read_b128 v[228:231], v232 offset:28672\n"
"s_waitcnt lgkmcnt(3)\n"
"global_load_lds_dwordx4 v240, s[78:79] offset:0\n"
"v_mfma_f32_16x16x32_bf16 v[108:111], a[108:111], v[216:219], v[108:111]\n"
"ds_read_b64_tr_b16 a[218:219], v243 offset:768\n"
"v_mfma_f32_16x16x32_bf16 v[112:115], a[112:115], v[216:219], v[112:115]\n"
"ds_read_b64_tr_b16 a[220:221], v243 offset:8\n"
"v_mfma_f32_16x16x32_bf16 v[116:119], a[116:119], v[216:219], v[116:119]\n"
"ds_read_b64_tr_b16 a[222:223], v243 offset:776\n"
"v_mfma_f32_16x16x32_bf16 v[120:123], a[96:99], v[220:223], v[120:123]\n"
"global_load_lds_dwordx4 v241, s[78:79] offset:1024\n"
"ds_read_b64_tr_b16 a[224:225], v243 offset:64\n"
"v_mfma_f32_16x16x32_bf16 v[124:127], a[100:103], v[220:223], v[124:127]\n"
"ds_read_b64_tr_b16 a[226:227], v243 offset:832\n"
"v_mfma_f32_16x16x32_bf16 v[128:131], a[104:107], v[220:223], v[128:131]\n"
"ds_read_b128 v[216:219], v232 offset:32768\n"
"v_add_u32 v232, 0xffff7000, v232\n"
"v_mfma_f32_16x16x32_bf16 v[132:135], a[108:111], v[220:223], v[132:135]\n"
"ds_read_b64_tr_b16 a[228:229], v243 offset:72\n"
"v_mfma_f32_16x16x32_bf16 v[136:139], a[112:115], v[220:223], v[136:139]\n"
"global_load_lds_dwordx4 v242, s[78:79] offset:2048\n"
"ds_read_b64_tr_b16 a[230:231], v243 offset:840\n"
"v_mfma_f32_16x16x32_bf16 v[140:143], a[116:119], v[220:223], v[140:143]\n"
"ds_read_b64_tr_b16 a[232:233], v243 offset:128\n"
"s_waitcnt lgkmcnt(11)\n"
"v_mfma_f32_16x16x32_bf16 v[144:147], a[96:99], v[224:227], v[144:147]\n"
"s_add_u32 s78, s64, 0x45000\n"
"s_addc_u32 s79, s65, 0\n"
"v_mfma_f32_16x16x32_bf16 v[148:151], a[100:103], v[224:227], v[148:151]\n"
"s_add_u32 m0, s74, 0xcc00\n"
"ds_read_b64_tr_b16 a[234:235], v243 offset:896\n"
"v_mfma_f32_16x16x32_bf16 v[152:155], a[104:107], v[224:227], v[152:155]\n"
"ds_read_b128 v[220:223], v233 offset:0\n"
"global_load_lds_dwordx4 v240, s[78:79] offset:0\n"
"v_mfma_f32_16x16x32_bf16 v[156:159], a[108:111], v[224:227], v[156:159]\n"
"ds_read_b64_tr_b16 a[236:237], v243 offset:136\n"
"v_mfma_f32_16x16x32_bf16 v[160:163], a[112:115], v[224:227], v[160:163]\n"
"ds_read_b64_tr_b16 a[238:239], v243 offset:904\n"
"v_mfma_f32_16x16x32_bf16 v[164:167], a[116:119], v[224:227], v[164:167]\n"
"s_waitcnt lgkmcnt(13)\n"
"v_mfma_f32_16x16x32_bf16 v[168:171], a[96:99], v[228:231], v[168:171]\n"
"global_load_lds_dwordx4 v241, s[78:79] offset:1024\n"
"v_mfma_f32_16x16x32_bf16 v[172:175], a[100:103], v[228:231], v[172:175]\n"
"v_mfma_f32_16x16x32_bf16 v[176:179], a[104:107], v[228:231], v[176:179]\n"
"ds_read_b128 v[224:227], v233 offset:4096\n"
"v_mfma_f32_16x16x32_bf16 v[180:183], a[108:111], v[228:231], v[180:183]\n"
"v_mfma_f32_16x16x32_bf16 v[184:187], a[112:115], v[228:231], v[184:187]\n"
"global_load_lds_dwordx4 v242, s[78:79] offset:2048\n"
"v_mfma_f32_16x16x32_bf16 v[188:191], a[116:119], v[228:231], v[188:191]\n"
"s_waitcnt lgkmcnt(8)\n"
"v_mfma_f32_16x16x32_bf16 v[192:195], a[96:99], v[216:219], v[192:195]\n"
"v_mfma_f32_16x16x32_bf16 v[196:199], a[100:103], v[216:219], v[196:199]\n"
"global_load_dwordx4 a[248:251], v239, s[58:59] offset:-2048 sc1\n"
"s_waitcnt vmcnt(9)\n"
"ds_read_b64_tr_b16 a[0:1], v243 offset:24576\n"
"v_mfma_f32_16x16x32_bf16 v[200:203], a[104:107], v[216:219], v[200:203]\n"
"ds_read_b128 v[228:231], v233 offset:8192\n"
"ds_read_b64_tr_b16 a[2:3], v243 offset:25344\n"
"v_mfma_f32_16x16x32_bf16 v[204:207], a[108:111], v[216:219], v[204:207]\n"
"ds_read_b64_tr_b16 a[4:5], v243 offset:24584\n"
"v_mfma_f32_16x16x32_bf16 v[208:211], a[112:115], v[216:219], v[208:211]\n"
"ds_read_b64_tr_b16 a[6:7], v243 offset:25352\n"
"v_mfma_f32_16x16x32_bf16 v[212:215], a[116:119], v[216:219], v[212:215]\n"
"global_load_dwordx4 a[252:255], v239, s[58:59] offset:-1024 sc1\n"
"s_waitcnt lgkmcnt(8)\n"
"v_mfma_f32_16x16x32_bf16 v[0:3], a[120:123], v[220:223], v[0:3]\n"
"ds_read_b64_tr_b16 a[8:9], v243 offset:24640\n"
"v_mfma_f32_16x16x32_bf16 v[4:7], a[124:127], v[220:223], v[4:7]\n"
"ds_read_b64_tr_b16 a[10:11], v243 offset:25408\n"
"v_mfma_f32_16x16x32_bf16 v[8:11], a[128:131], v[220:223], v[8:11]\n"
"ds_read_b128 v[216:219], v233 offset:12288\n"
"ds_read_b64_tr_b16 a[12:13], v243 offset:24648\n"
"v_mfma_f32_16x16x32_bf16 v[12:15], a[132:135], v[220:223], v[12:15]\n"
"global_load_dwordx4 a[48:51], v239, s[58:59] offset:0 sc1\n"
"ds_read_b64_tr_b16 a[14:15], v243 offset:25416\n"
"v_mfma_f32_16x16x32_bf16 v[16:19], a[136:139], v[220:223], v[16:19]\n"
"ds_read_b64_tr_b16 a[16:17], v243 offset:24704\n"
"v_mfma_f32_16x16x32_bf16 v[20:23], a[140:143], v[220:223], v[20:23]\n"
"s_waitcnt lgkmcnt(11)\n"
"v_mfma_f32_16x16x32_bf16 v[24:27], a[120:123], v[224:227], v[24:27]\n"
"ds_read_b64_tr_b16 a[18:19], v243 offset:25472\n"
"v_mfma_f32_16x16x32_bf16 v[28:31], a[124:127], v[224:227], v[28:31]\n"
"global_load_dwordx4 a[52:55], v239, s[58:59] offset:1024 sc1\n"
"v_mfma_f32_16x16x32_bf16 v[32:35], a[128:131], v[224:227], v[32:35]\n"
"ds_read_b128 v[220:223], v233 offset:16384\n"
"v_mfma_f32_16x16x32_bf16 v[36:39], a[132:135], v[224:227], v[36:39]\n"
"ds_read_b64_tr_b16 a[20:21], v243 offset:24712\n"
"v_mfma_f32_16x16x32_bf16 v[40:43], a[136:139], v[224:227], v[40:43]\n"
"ds_read_b64_tr_b16 a[22:23], v243 offset:25480\n"
"v_mfma_f32_16x16x32_bf16 v[44:47], a[140:143], v[224:227], v[44:47]\n"
"s_add_u32 m0, s30, 0xfffffe00\n"
"s_waitcnt lgkmcnt(13)\n"
"v_mfma_f32_16x16x32_bf16 v[48:51], a[120:123], v[228:231], v[48:51]\n"
"global_load_lds_dwordx4 v236, s[32:33] offset:512 nt\n"
"v_mfma_f32_16x16x32_bf16 v[52:55], a[124:127], v[228:231], v[52:55]\n"
"v_mfma_f32_16x16x32_bf16 v[56:59], a[128:131], v[228:231], v[56:59]\n"
"ds_read_b128 v[224:227], v233 offset:20480\n"
"s_add_u32 m0, s30, 0xe00\n"
"v_mfma_f32_16x16x32_bf16 v[60:63], a[132:135], v[228:231], v[60:63]\n"
"global_load_lds_dwordx4 v236, s[34:35] offset:512 nt\n"
"v_mfma_f32_16x16x32_bf16 v[64:67], a[136:139], v[228:231], v[64:67]\n"
"v_mfma_f32_16x16x32_bf16 v[68:71], a[140:143], v[228:231], v[68:71]\n"
"s_add_u32 m0, s30, 0x1e00\n"
"s_waitcnt lgkmcnt(8)\n"
"v_mfma_f32_16x16x32_bf16 v[72:75], a[120:123], v[216:219], v[72:75]\n"
"global_load_lds_dwordx4 v236, s[36:37] offset:512 nt\n"
"v_mfma_f32_16x16x32_bf16 v[76:79], a[124:127], v[216:219], v[76:79]\n"
"v_mfma_f32_16x16x32_bf16 v[80:83], a[128:131], v[216:219], v[80:83]\n"
"ds_read_b128 v[228:231], v233 offset:24576\n"
"s_add_u32 m0, s30, 0x2e00\n"
"v_mfma_f32_16x16x32_bf16 v[84:87], a[132:135], v[216:219], v[84:87]\n"
"global_load_lds_dwordx4 v236, s[38:39] offset:512 nt\n"
"v_mfma_f32_16x16x32_bf16 v[88:91], a[136:139], v[216:219], v[88:91]\n"
"v_mfma_f32_16x16x32_bf16 v[92:95], a[140:143], v[216:219], v[92:95]\n"
"s_add_u32 m0, s30, 0x3e00\n"
"s_waitcnt lgkmcnt(4)\n"
"v_mfma_f32_16x16x32_bf16 v[96:99], a[120:123], v[220:223], v[96:99]\n"
"global_load_lds_dwordx4 v236, s[40:41] offset:512 nt\n"
"v_mfma_f32_16x16x32_bf16 v[100:103], a[124:127], v[220:223], v[100:103]\n"
"v_mfma_f32_16x16x32_bf16 v[104:107], a[128:131], v[220:223], v[104:107]\n"
"ds_read_b128 v[216:219], v233 offset:28672\n"
"s_add_u32 m0, s30, 0x4e00\n"
"v_mfma_f32_16x16x32_bf16 v[108:111], a[132:135], v[220:223], v[108:111]\n"
"global_load_lds_dwordx4 v236, s[42:43] offset:512 nt\n"
"s_waitcnt vmcnt(10)\n"
"ds_read_b64_tr_b16 a[24:25], v243 offset:49152\n"
"v_mfma_f32_16x16x32_bf16 v[112:115], a[136:139], v[220:223], v[112:115]\n"
"ds_read_b64_tr_b16 a[26:27], v243 offset:49920\n"
"v_mfma_f32_16x16x32_bf16 v[116:119], a[140:143], v[220:223], v[116:119]\n"
"s_add_u32 m0, s30, 0x5e00\n"
"s_waitcnt lgkmcnt(4)\n"
"v_mfma_f32_16x16x32_bf16 v[120:123], a[120:123], v[224:227], v[120:123]\n"
"global_load_lds_dwordx4 v236, s[44:45] offset:512 nt\n"
"ds_read_b64_tr_b16 a[28:29], v243 offset:49160\n"
"v_mfma_f32_16x16x32_bf16 v[124:127], a[124:127], v[224:227], v[124:127]\n"
"ds_read_b64_tr_b16 a[30:31], v243 offset:49928\n"
"v_mfma_f32_16x16x32_bf16 v[128:131], a[128:131], v[224:227], v[128:131]\n"
"ds_read_b128 v[220:223], v233 offset:32768\n"
"v_add_u32 v233, 0xffff7000, v233\n"
"s_add_u32 m0, s30, 0x6e00\n"
"v_mfma_f32_16x16x32_bf16 v[132:135], a[132:135], v[224:227], v[132:135]\n"
"global_load_lds_dwordx4 v236, s[46:47] offset:512 nt\n"
"ds_read_b64_tr_b16 a[32:33], v243 offset:49216\n"
"v_mfma_f32_16x16x32_bf16 v[136:139], a[136:139], v[224:227], v[136:139]\n"
"ds_read_b64_tr_b16 a[34:35], v243 offset:49984\n"
"v_mfma_f32_16x16x32_bf16 v[140:143], a[140:143], v[224:227], v[140:143]\n"
"s_add_u32 m0, s30, 0x7e00\n"
"s_waitcnt lgkmcnt(8)\n"
"v_mfma_f32_16x16x32_bf16 v[144:147], a[120:123], v[228:231], v[144:147]\n"
"global_load_lds_dwordx4 v236, s[48:49] offset:512 nt\n"
"ds_read_b64_tr_b16 a[36:37], v243 offset:49224\n"
"v_mfma_f32_16x16x32_bf16 v[148:151], a[124:127], v[228:231], v[148:151]\n"
"ds_read_b64_tr_b16 a[38:39], v243 offset:49992\n"
"v_mfma_f32_16x16x32_bf16 v[152:155], a[128:131], v[228:231], v[152:155]\n"
"ds_read_b128 v[224:227], v234 offset:0\n"
"ds_read_b64_tr_b16 a[40:41], v243 offset:49280\n"
"v_mfma_f32_16x16x32_bf16 v[156:159], a[132:135], v[228:231], v[156:159]\n"
"global_load_dwordx4 a[56:59], v239, s[58:59] offset:2048 sc1\n"
"ds_read_b64_tr_b16 a[42:43], v243 offset:50048\n"
"v_mfma_f32_16x16x32_bf16 v[160:163], a[136:139], v[228:231], v[160:163]\n"
"ds_read_b64_tr_b16 a[44:45], v243 offset:49288\n"
"v_mfma_f32_16x16x32_bf16 v[164:167], a[140:143], v[228:231], v[164:167]\n"
"s_waitcnt lgkmcnt(13)\n"
"v_mfma_f32_16x16x32_bf16 v[168:171], a[120:123], v[216:219], v[168:171]\n"
"ds_read_b64_tr_b16 a[46:47], v243 offset:50056\n"
"v_mfma_f32_16x16x32_bf16 v[172:175], a[124:127], v[216:219], v[172:175]\n"
"global_load_dwordx4 a[60:63], v239, s[58:59] offset:3072 sc1\n"
"v_mfma_f32_16x16x32_bf16 v[176:179], a[128:131], v[216:219], v[176:179]\n"
"ds_read_b128 v[228:231], v234 offset:4096\n"
"v_mfma_f32_16x16x32_bf16 v[180:183], a[132:135], v[216:219], v[180:183]\n"
"ds_read_b64 v[240:241], v250 offset:24\n"
"v_mfma_f32_16x16x32_bf16 v[184:187], a[136:139], v[216:219], v[184:187]\n"
"s_add_u32 s58, s24, 0x3000\n"
"s_addc_u32 s59, s25, 0\n"
"v_mfma_f32_16x16x32_bf16 v[188:191], a[140:143], v[216:219], v[188:191]\n"
"global_load_dwordx4 a[64:67], v239, s[58:59] offset:-4096 sc1\n"
"s_waitcnt lgkmcnt(11)\n"
"v_mfma_f32_16x16x32_bf16 v[192:195], a[120:123], v[220:223], v[192:195]\n"
"s_add_u32 s14, s51, 0\n"
"v_mfma_f32_16x16x32_bf16 v[196:199], a[124:127], v[220:223], v[196:199]\n"
"s_mul_i32 s15, s14, 0xaaab\n"
"v_mfma_f32_16x16x32_bf16 v[200:203], a[128:131], v[220:223], v[200:203]\n"
"ds_read_b128 v[216:219], v234 offset:8192\n"
"v_mfma_f32_16x16x32_bf16 v[204:207], a[132:135], v[220:223], v[204:207]\n"
"global_load_dwordx4 a[68:71], v239, s[58:59] offset:-3072 sc1\n"
"v_mfma_f32_16x16x32_bf16 v[208:211], a[136:139], v[220:223], v[208:211]\n"
"s_lshr_b32 s15, s15, 19\n"
"v_mfma_f32_16x16x32_bf16 v[212:215], a[140:143], v[220:223], v[212:215]\n"
"s_waitcnt lgkmcnt(7)\n"
"v_mfma_f32_16x16x32_bf16 v[0:3], a[144:147], v[224:227], v[0:3]\n"
"s_mul_i32 s15, s15, 0x48000\n"
"v_mfma_f32_16x16x32_bf16 v[4:7], a[148:151], v[224:227], v[4:7]\n"
"global_load_dwordx4 a[72:75], v239, s[58:59] offset:-2048 sc1\n"
"v_mfma_f32_16x16x32_bf16 v[8:11], a[152:155], v[224:227], v[8:11]\n"
"ds_read_b128 v[220:223], v234 offset:12288\n"
"v_mfma_f32_16x16x32_bf16 v[12:15], a[156:159], v[224:227], v[12:15]\n"
"s_mul_i32 s14, s14, 0x6000\n"
"v_mfma_f32_16x16x32_bf16 v[16:19], a[160:163], v[224:227], v[16:19]\n"
"s_add_u32 s32, s14, s15\n"
"v_mfma_f32_16x16x32_bf16 v[20:23], a[164:167], v[224:227], v[20:23]\n"
"global_load_dwordx4 a[76:79], v239, s[58:59] offset:-1024 sc1\n"
"s_waitcnt lgkmcnt(3)\n"
"v_mfma_f32_16x16x32_bf16 v[24:27], a[144:147], v[228:231], v[24:27]\n"
"s_add_u32 s33, s32, 0x48000\n"
"v_mfma_f32_16x16x32_bf16 v[28:31], a[148:151], v[228:231], v[28:31]\n"
"s_add_u32 s14, s51, 256\n"
"v_mfma_f32_16x16x32_bf16 v[32:35], a[152:155], v[228:231], v[32:35]\n"
"ds_read_b128 v[224:227], v234 offset:16384\n"
"v_mfma_f32_16x16x32_bf16 v[36:39], a[156:159], v[228:231], v[36:39]\n"
"global_load_dwordx4 a[80:83], v239, s[58:59] offset:0 sc1\n"
"v_mfma_f32_16x16x32_bf16 v[40:43], a[160:163], v[228:231], v[40:43]\n"
"s_mul_i32 s15, s14, 0xaaab\n"
"v_mfma_f32_16x16x32_bf16 v[44:47], a[164:167], v[228:231], v[44:47]\n"
"s_waitcnt lgkmcnt(2)\n"
"v_mfma_f32_16x16x32_bf16 v[48:51], a[144:147], v[216:219], v[48:51]\n"
"s_lshr_b32 s15, s15, 19\n"
"v_mfma_f32_16x16x32_bf16 v[52:55], a[148:151], v[216:219], v[52:55]\n"
"global_load_dwordx4 a[84:87], v239, s[58:59] offset:1024 sc1\n"
"v_mfma_f32_16x16x32_bf16 v[56:59], a[152:155], v[216:219], v[56:59]\n"
"ds_read_b128 v[228:231], v234 offset:20480\n"
"v_mfma_f32_16x16x32_bf16 v[60:63], a[156:159], v[216:219], v[60:63]\n"
"s_mul_i32 s15, s15, 0x48000\n"
"v_mfma_f32_16x16x32_bf16 v[64:67], a[160:163], v[216:219], v[64:67]\n"
"s_mul_i32 s14, s14, 0x6000\n"
"v_mfma_f32_16x16x32_bf16 v[68:71], a[164:167], v[216:219], v[68:71]\n"
"global_load_dwordx4 a[88:91], v239, s[58:59] offset:2048 sc1\n"
"s_waitcnt lgkmcnt(2)\n"
"v_mfma_f32_16x16x32_bf16 v[72:75], a[144:147], v[220:223], v[72:75]\n"
"s_add_u32 s34, s14, s15\n"
"v_mfma_f32_16x16x32_bf16 v[76:79], a[148:151], v[220:223], v[76:79]\n"
"s_add_u32 s35, s34, 0x48000\n"
"v_mfma_f32_16x16x32_bf16 v[80:83], a[152:155], v[220:223], v[80:83]\n"
"ds_read_b128 v[216:219], v234 offset:24576\n"
"v_mfma_f32_16x16x32_bf16 v[84:87], a[156:159], v[220:223], v[84:87]\n"
"global_load_dwordx4 a[92:95], v239, s[58:59] offset:3072 sc1\n"
"v_mfma_f32_16x16x32_bf16 v[88:91], a[160:163], v[220:223], v[88:91]\n"
"s_add_u32 s14, s51, 512\n"
"v_mfma_f32_16x16x32_bf16 v[92:95], a[164:167], v[220:223], v[92:95]\n"
"s_waitcnt lgkmcnt(2)\n"
"v_mfma_f32_16x16x32_bf16 v[96:99], a[144:147], v[224:227], v[96:99]\n"
"s_add_u32 s58, s24, 0x5000\n"
"s_addc_u32 s59, s25, 0\n"
"v_mfma_f32_16x16x32_bf16 v[100:103], a[148:151], v[224:227], v[100:103]\n"
"global_load_dwordx4 a[96:99], v239, s[58:59] offset:-4096 sc1\n"
"v_mfma_f32_16x16x32_bf16 v[104:107], a[152:155], v[224:227], v[104:107]\n"
"ds_read_b128 v[220:223], v234 offset:28672\n"
"v_mfma_f32_16x16x32_bf16 v[108:111], a[156:159], v[224:227], v[108:111]\n"
"s_mul_i32 s15, s14, 0xaaab\n"
"v_mfma_f32_16x16x32_bf16 v[112:115], a[160:163], v[224:227], v[112:115]\n"
"s_lshr_b32 s15, s15, 19\n"
"v_mfma_f32_16x16x32_bf16 v[116:119], a[164:167], v[224:227], v[116:119]\n"
"global_load_dwordx4 a[100:103], v239, s[58:59] offset:-3072 sc1\n"
"s_waitcnt lgkmcnt(2)\n"
"v_mfma_f32_16x16x32_bf16 v[120:123], a[144:147], v[228:231], v[120:123]\n"
"s_mul_i32 s15, s15, 0x48000\n"
"v_mfma_f32_16x16x32_bf16 v[124:127], a[148:151], v[228:231], v[124:127]\n"
"s_mul_i32 s14, s14, 0x6000\n"
"v_mfma_f32_16x16x32_bf16 v[128:131], a[152:155], v[228:231], v[128:131]\n"
"ds_read_b128 v[224:227], v234 offset:32768\n"
"v_add_u32 v234, 0xffff7000, v234\n"
"v_mfma_f32_16x16x32_bf16 v[132:135], a[156:159], v[228:231], v[132:135]\n"
"global_load_dwordx4 a[104:107], v239, s[58:59] offset:-2048 sc1\n"
"v_mfma_f32_16x16x32_bf16 v[136:139], a[160:163], v[228:231], v[136:139]\n"
"s_add_u32 s36, s14, s15\n"
"v_mfma_f32_16x16x32_bf16 v[140:143], a[164:167], v[228:231], v[140:143]\n"
"s_waitcnt lgkmcnt(2)\n"
"v_mfma_f32_16x16x32_bf16 v[144:147], a[144:147], v[216:219], v[144:147]\n"
"s_add_u32 s37, s36, 0x48000\n"
"v_mfma_f32_16x16x32_bf16 v[148:151], a[148:151], v[216:219], v[148:151]\n"
"global_load_dwordx4 a[108:111], v239, s[58:59] offset:-1024 sc1\n"
"v_mfma_f32_16x16x32_bf16 v[152:155], a[152:155], v[216:219], v[152:155]\n"
"ds_read_b128 v[228:231], v235 offset:0\n"
"v_mfma_f32_16x16x32_bf16 v[156:159], a[156:159], v[216:219], v[156:159]\n"
"s_add_u32 s38, s32, 0x2400000\n"
"v_mfma_f32_16x16x32_bf16 v[160:163], a[160:163], v[216:219], v[160:163]\n"
"s_add_u32 s39, s33, 0x2400000\n"
"v_mfma_f32_16x16x32_bf16 v[164:167], a[164:167], v[216:219], v[164:167]\n"
"global_load_dwordx4 a[112:115], v239, s[58:59] offset:0 sc1\n"
"s_waitcnt lgkmcnt(2)\n"
"v_mfma_f32_16x16x32_bf16 v[168:171], a[144:147], v[220:223], v[168:171]\n"
"s_add_u32 s40, s34, 0x2400000\n"
"v_mfma_f32_16x16x32_bf16 v[172:175], a[148:151], v[220:223], v[172:175]\n"
"s_add_u32 s41, s35, 0x2400000\n"
"v_mfma_f32_16x16x32_bf16 v[176:179], a[152:155], v[220:223], v[176:179]\n"
"ds_read_b128 v[216:219], v235 offset:4096\n"
"v_mfma_f32_16x16x32_bf16 v[180:183], a[156:159], v[220:223], v[180:183]\n"
"global_load_dwordx4 a[116:119], v239, s[58:59] offset:1024 sc1\n"
"v_mfma_f32_16x16x32_bf16 v[184:187], a[160:163], v[220:223], v[184:187]\n"
"s_add_u32 s42, s36, 0x2400000\n"
"v_mfma_f32_16x16x32_bf16 v[188:191], a[164:167], v[220:223], v[188:191]\n"
"s_waitcnt lgkmcnt(2)\n"
"v_mfma_f32_16x16x32_bf16 v[192:195], a[144:147], v[224:227], v[192:195]\n"
"s_add_u32 s43, s37, 0x2400000\n"
"v_mfma_f32_16x16x32_bf16 v[196:199], a[148:151], v[224:227], v[196:199]\n"
"global_load_dwordx4 a[120:123], v239, s[58:59] offset:2048 sc1\n"
"v_mfma_f32_16x16x32_bf16 v[200:203], a[152:155], v[224:227], v[200:203]\n"
"ds_read_b128 v[220:223], v235 offset:8192\n"
"v_mfma_f32_16x16x32_bf16 v[204:207], a[156:159], v[224:227], v[204:207]\n"
"s_add_u32 s44, s38, 0x2400000\n"
"v_mfma_f32_16x16x32_bf16 v[208:211], a[160:163], v[224:227], v[208:211]\n"
"s_add_u32 s45, s39, 0x2400000\n"
"v_mfma_f32_16x16x32_bf16 v[212:215], a[164:167], v[224:227], v[212:215]\n"
"global_load_dwordx4 a[124:127], v239, s[58:59] offset:3072 sc1\n"
"s_waitcnt lgkmcnt(2)\n"
"v_mfma_f32_16x16x32_bf16 v[0:3], a[168:171], v[228:231], v[0:3]\n"
"s_add_u32 s46, s40, 0x2400000\n"
"v_mfma_f32_16x16x32_bf16 v[4:7], a[172:175], v[228:231], v[4:7]\n"
"s_add_u32 s47, s41, 0x2400000\n"
"v_mfma_f32_16x16x32_bf16 v[8:11], a[176:179], v[228:231], v[8:11]\n"
"ds_read_b128 v[224:227], v235 offset:12288\n"
"s_add_u32 s58, s24, 0x7000\n"
"s_addc_u32 s59, s25, 0\n"
"v_mfma_f32_16x16x32_bf16 v[12:15], a[180:183], v[228:231], v[12:15]\n"
"global_load_dwordx4 a[128:131], v239, s[58:59] offset:-4096 sc1\n"
"v_mfma_f32_16x16x32_bf16 v[16:19], a[184:187], v[228:231], v[16:19]\n"
"s_add_u32 s48, s42, 0x2400000\n"
"v_mfma_f32_16x16x32_bf16 v[20:23], a[188:191], v[228:231], v[20:23]\n"
"s_waitcnt lgkmcnt(2)\n"
"v_mfma_f32_16x16x32_bf16 v[24:27], a[168:171], v[216:219], v[24:27]\n"
"s_add_u32 s49, s43, 0x2400000\n"
"v_mfma_f32_16x16x32_bf16 v[28:31], a[172:175], v[216:219], v[28:31]\n"
"global_load_dwordx4 a[132:135], v239, s[58:59] offset:-3072 sc1\n"
"v_mfma_f32_16x16x32_bf16 v[32:35], a[176:179], v[216:219], v[32:35]\n"
"ds_read_b128 v[228:231], v235 offset:16384\n"
"v_mfma_f32_16x16x32_bf16 v[36:39], a[180:183], v[216:219], v[36:39]\n"
"s_mov_b64 exec, s[4:5]\n"
"s_cbranch_execz LHT4\n"
"LHR4:\n"
"s_mov_b64 exec, -1\n"
"v_mfma_f32_16x16x32_bf16 v[40:43], a[184:187], v[216:219], v[40:43]\n"
"v_mfma_f32_16x16x32_bf16 v[44:47], a[188:191], v[216:219], v[44:47]\n"
"global_load_dwordx4 a[136:139], v239, s[58:59] offset:-2048 sc1\n"
"s_waitcnt lgkmcnt(2)\n"
"v_mfma_f32_16x16x32_bf16 v[48:51], a[168:171], v[220:223], v[48:51]\n"
"v_mfma_f32_16x16x32_bf16 v[52:55], a[172:175], v[220:223], v[52:55]\n"
"v_mfma_f32_16x16x32_bf16 v[56:59], a[176:179], v[220:223], v[56:59]\n"
"ds_read_b128 v[216:219], v235 offset:20480\n"
"v_mfma_f32_16x16x32_bf16 v[60:63], a[180:183], v[220:223], v[60:63]\n"
"global_load_dwordx4 a[140:143], v239, s[58:59] offset:-1024 sc1\n"
"v_mfma_f32_16x16x32_bf16 v[64:67], a[184:187], v[220:223], v[64:67]\n"
"v_mfma_f32_16x16x32_bf16 v[68:71], a[188:191], v[220:223], v[68:71]\n"
"s_waitcnt lgkmcnt(2)\n"
"v_mfma_f32_16x16x32_bf16 v[72:75], a[168:171], v[224:227], v[72:75]\n"
"v_mfma_f32_16x16x32_bf16 v[76:79], a[172:175], v[224:227], v[76:79]\n"
"global_load_dwordx4 a[144:147], v239, s[58:59] offset:0 sc1\n"
"v_mfma_f32_16x16x32_bf16 v[80:83], a[176:179], v[224:227], v[80:83]\n"
"ds_read_b128 v[220:223], v235 offset:24576\n"
"v_mfma_f32_16x16x32_bf16 v[84:87], a[180:183], v[224:227], v[84:87]\n"
"v_mfma_f32_16x16x32_bf16 v[88:91], a[184:187], v[224:227], v[88:91]\n"
"v_mfma_f32_16x16x32_bf16 v[92:95], a[188:191], v[224:227], v[92:95]\n"
"global_load_dwordx4 a[148:151], v239, s[58:59] offset:1024 sc1\n"
"s_waitcnt lgkmcnt(2)\n"
"v_mfma_f32_16x16x32_bf16 v[96:99], a[168:171], v[228:231], v[96:99]\n"
"v_mfma_f32_16x16x32_bf16 v[100:103], a[172:175], v[228:231], v[100:103]\n"
"v_mfma_f32_16x16x32_bf16 v[104:107], a[176:179], v[228:231], v[104:107]\n"
"ds_read_b128 v[224:227], v235 offset:28672\n"
"v_mfma_f32_16x16x32_bf16 v[108:111], a[180:183], v[228:231], v[108:111]\n"
"global_load_dwordx4 a[152:155], v239, s[58:59] offset:2048 sc1\n"
"v_mfma_f32_16x16x32_bf16 v[112:115], a[184:187], v[228:231], v[112:115]\n"
"v_mfma_f32_16x16x32_bf16 v[116:119], a[188:191], v[228:231], v[116:119]\n"
"s_waitcnt lgkmcnt(2)\n"
"v_mfma_f32_16x16x32_bf16 v[120:123], a[168:171], v[216:219], v[120:123]\n"
"v_mfma_f32_16x16x32_bf16 v[124:127], a[172:175], v[216:219], v[124:127]\n"
"global_load_dwordx4 a[156:159], v239, s[58:59] offset:3072 sc1\n"
"v_mfma_f32_16x16x32_bf16 v[128:131], a[176:179], v[216:219], v[128:131]\n"
"ds_read_b128 v[228:231], v235 offset:32768\n"
"v_add_u32 v235, 0xffff7000, v235\n"
"v_mfma_f32_16x16x32_bf16 v[132:135], a[180:183], v[216:219], v[132:135]\n"
"v_mfma_f32_16x16x32_bf16 v[136:139], a[184:187], v[216:219], v[136:139]\n"
"s_add_u32 s58, s24, 0x9000\n"
"s_addc_u32 s59, s25, 0\n"
"v_mfma_f32_16x16x32_bf16 v[140:143], a[188:191], v[216:219], v[140:143]\n"
"global_load_dwordx4 a[160:163], v239, s[58:59] offset:-4096 sc1\n"
"s_waitcnt lgkmcnt(2)\n"
"v_mfma_f32_16x16x32_bf16 v[144:147], a[168:171], v[220:223], v[144:147]\n"
"v_mfma_f32_16x16x32_bf16 v[148:151], a[172:175], v[220:223], v[148:151]\n"
"s_waitcnt vmcnt(27)\n"
"s_barrier\n"
"v_mfma_f32_16x16x32_bf16 v[152:155], a[176:179], v[220:223], v[152:155]\n"
"ds_read_b128 v[216:219], v232 offset:0\n"
"v_mfma_f32_16x16x32_bf16 v[156:159], a[180:183], v[220:223], v[156:159]\n"
"global_load_dwordx4 a[164:167], v239, s[58:59] offset:-3072 sc1\n"
"v_mfma_f32_16x16x32_bf16 v[160:163], a[184:187], v[220:223], v[160:163]\n"
"v_mfma_f32_16x16x32_bf16 v[164:167], a[188:191], v[220:223], v[164:167]\n"
"s_waitcnt lgkmcnt(2)\n"
"v_mfma_f32_16x16x32_bf16 v[168:171], a[168:171], v[224:227], v[168:171]\n"
"v_mfma_f32_16x16x32_bf16 v[172:175], a[172:175], v[224:227], v[172:175]\n"
"v_mfma_f32_16x16x32_bf16 v[176:179], a[176:179], v[224:227], v[176:179]\n"
"ds_read_b128 v[220:223], v232 offset:4096\n"
"v_mfma_f32_16x16x32_bf16 v[180:183], a[180:183], v[224:227], v[180:183]\n"
"v_mfma_f32_16x16x32_bf16 v[184:187], a[184:187], v[224:227], v[184:187]\n"
"v_mfma_f32_16x16x32_bf16 v[188:191], a[188:191], v[224:227], v[188:191]\n"
"s_waitcnt lgkmcnt(2)\n"
"v_mfma_f32_16x16x32_bf16 v[192:195], a[168:171], v[228:231], v[192:195]\n"
"v_mfma_f32_16x16x32_bf16 v[196:199], a[172:175], v[228:231], v[196:199]\n"
"LHT4:\n"
"s_cbranch_execz LHR4\n"
"v_mfma_f32_16x16x32_bf16 v[200:203], a[176:179], v[228:231], v[200:203]\n"
"ds_read_b128 v[224:227], v232 offset:8192\n"
"v_mfma_f32_16x16x32_bf16 v[204:207], a[180:183], v[228:231], v[204:207]\n"
"v_mfma_f32_16x16x32_bf16 v[208:211], a[184:187], v[228:231], v[208:211]\n"
"v_mfma_f32_16x16x32_bf16 v[212:215], a[188:191], v[228:231], v[212:215]\n"
"s_waitcnt lgkmcnt(2)\n"
"v_mfma_f32_16x16x32_bf16 v[0:3], a[192:195], v[216:219], v[0:3]\n"
"global_load_dwordx4 a[168:171], v239, s[58:59] offset:-2048 sc1\n"
"v_mfma_f32_16x16x32_bf16 v[4:7], a[196:199], v[216:219], v[4:7]\n"
"v_mfma_f32_16x16x32_bf16 v[8:11], a[200:203], v[216:219], v[8:11]\n"
"ds_read_b128 v[228:231], v232 offset:12288\n"
"v_mfma_f32_16x16x32_bf16 v[12:15], a[204:207], v[216:219], v[12:15]\n"
"v_mfma_f32_16x16x32_bf16 v[16:19], a[208:211], v[216:219], v[16:19]\n"
"global_load_dwordx4 a[172:175], v239, s[58:59] offset:-1024 sc1\n"
"v_mfma_f32_16x16x32_bf16 v[20:23], a[212:215], v[216:219], v[20:23]\n"
"s_waitcnt lgkmcnt(2)\n"
"v_mfma_f32_16x16x32_bf16 v[24:27], a[192:195], v[220:223], v[24:27]\n"
"v_mfma_f32_16x16x32_bf16 v[28:31], a[196:199], v[220:223], v[28:31]\n"
"v_mfma_f32_16x16x32_bf16 v[32:35], a[200:203], v[220:223], v[32:35]\n"
"ds_read_b128 v[216:219], v232 offset:16384\n"
"global_load_dwordx4 a[176:179], v239, s[58:59] offset:0 sc1\n"
"v_mfma_f32_16x16x32_bf16 v[36:39], a[204:207], v[220:223], v[36:39]\n"
"v_mfma_f32_16x16x32_bf16 v[40:43], a[208:211], v[220:223], v[40:43]\n"
"v_mfma_f32_16x16x32_bf16 v[44:47], a[212:215], v[220:223], v[44:47]\n"
"s_waitcnt lgkmcnt(2)\n"
"v_mfma_f32_16x16x32_bf16 v[48:51], a[192:195], v[224:227], v[48:51]\n"
"global_load_dwordx4 a[180:183], v239, s[58:59] offset:1024 sc1\n"
"v_mfma_f32_16x16x32_bf16 v[52:55], a[196:199], v[224:227], v[52:55]\n"
"v_mfma_f32_16x16x32_bf16 v[56:59], a[200:203], v[224:227], v[56:59]\n"
"ds_read_b128 v[220:223], v232 offset:20480\n"
"v_mfma_f32_16x16x32_bf16 v[60:63], a[204:207], v[224:227], v[60:63]\n"
"v_mfma_f32_16x16x32_bf16 v[64:67], a[208:211], v[224:227], v[64:67]\n"
"global_load_dwordx4 a[184:187], v239, s[58:59] offset:2048 sc1\n"
"v_mfma_f32_16x16x32_bf16 v[68:71], a[212:215], v[224:227], v[68:71]\n"
"s_waitcnt lgkmcnt(2)\n"
"v_mfma_f32_16x16x32_bf16 v[72:75], a[192:195], v[228:231], v[72:75]\n"
"v_mfma_f32_16x16x32_bf16 v[76:79], a[196:199], v[228:231], v[76:79]\n"
"v_mfma_f32_16x16x32_bf16 v[80:83], a[200:203], v[228:231], v[80:83]\n"
"ds_read_b128 v[224:227], v232 offset:24576\n"
"s_add_u32 s52, s20, s32\n"
"s_addc_u32 s53, s21, 0\n"
"s_add_u32 m0, s31, 0x0\n"
"v_mfma_f32_16x16x32_bf16 v[84:87], a[204:207], v[228:231], v[84:87]\n"
"global_load_lds_dwordx4 v237, s[52:53] offset:0\n"
"v_mfma_f32_16x16x32_bf16 v[88:91], a[208:211], v[228:231], v[88:91]\n"
"v_mfma_f32_16x16x32_bf16 v[92:95], a[212:215], v[228:231], v[92:95]\n"
"s_add_u32 m0, s31, 0x1f00\n"
"s_waitcnt lgkmcnt(2)\n"
"v_mfma_f32_16x16x32_bf16 v[96:99], a[192:195], v[216:219], v[96:99]\n"
"global_load_lds_dwordx4 v237, s[52:53] offset:256\n"
"v_mfma_f32_16x16x32_bf16 v[100:103], a[196:199], v[216:219], v[100:103]\n"
"v_mfma_f32_16x16x32_bf16 v[104:107], a[200:203], v[216:219], v[104:107]\n"
"ds_read_b128 v[228:231], v232 offset:28672\n"
"s_add_u32 m0, s31, 0x3e00\n"
"v_mfma_f32_16x16x32_bf16 v[108:111], a[204:207], v[216:219], v[108:111]\n"
"global_load_lds_dwordx4 v237, s[52:53] offset:512\n"
"v_mfma_f32_16x16x32_bf16 v[112:115], a[208:211], v[216:219], v[112:115]\n"
"v_mfma_f32_16x16x32_bf16 v[116:119], a[212:215], v[216:219], v[116:119]\n"
"s_add_u32 m0, s31, 0x1000\n"
"s_waitcnt lgkmcnt(2)\n"
"v_mfma_f32_16x16x32_bf16 v[120:123], a[192:195], v[220:223], v[120:123]\n"
"global_load_lds_dwordx4 v238, s[52:53] offset:0\n"
"v_mfma_f32_16x16x32_bf16 v[124:127], a[196:199], v[220:223], v[124:127]\n"
"v_mfma_f32_16x16x32_bf16 v[128:131], a[200:203], v[220:223], v[128:131]\n"
"ds_read_b128 v[216:219], v232 offset:32768\n"
"v_add_u32 v232, v249, v232\n"
"s_add_u32 m0, s31, 0x2f00\n"
"v_mfma_f32_16x16x32_bf16 v[132:135], a[204:207], v[220:223], v[132:135]\n"
"global_load_lds_dwordx4 v238, s[52:53] offset:256\n"
"v_mfma_f32_16x16x32_bf16 v[136:139], a[208:211], v[220:223], v[136:139]\n"
"v_mfma_f32_16x16x32_bf16 v[140:143], a[212:215], v[220:223], v[140:143]\n"
"s_add_u32 m0, s31, 0x4e00\n"
"s_waitcnt lgkmcnt(2)\n"
"v_mfma_f32_16x16x32_bf16 v[144:147], a[192:195], v[224:227], v[144:147]\n"
"global_load_lds_dwordx4 v238, s[52:53] offset:512\n"
"v_mfma_f32_16x16x32_bf16 v[148:151], a[196:199], v[224:227], v[148:151]\n"
"v_mfma_f32_16x16x32_bf16 v[152:155], a[200:203], v[224:227], v[152:155]\n"
"ds_read_b128 v[220:223], v233 offset:0\n"
"v_mfma_f32_16x16x32_bf16 v[156:159], a[204:207], v[224:227], v[156:159]\n"
"global_load_dwordx4 a[188:191], v239, s[58:59] offset:3072 sc1\n"
"v_mfma_f32_16x16x32_bf16 v[160:163], a[208:211], v[224:227], v[160:163]\n"
"v_mfma_f32_16x16x32_bf16 v[164:167], a[212:215], v[224:227], v[164:167]\n"
"s_waitcnt lgkmcnt(2)\n"
"v_mfma_f32_16x16x32_bf16 v[168:171], a[192:195], v[228:231], v[168:171]\n"
"v_mfma_f32_16x16x32_bf16 v[172:175], a[196:199], v[228:231], v[172:175]\n"
"v_mfma_f32_16x16x32_bf16 v[176:179], a[200:203], v[228:231], v[176:179]\n"
"ds_read_b128 v[224:227], v233 offset:4096\n"
"v_mfma_f32_16x16x32_bf16 v[180:183], a[204:207], v[228:231], v[180:183]\n"
"v_mfma_f32_16x16x32_bf16 v[184:187], a[208:211], v[228:231], v[184:187]\n"
"v_mfma_f32_16x16x32_bf16 v[188:191], a[212:215], v[228:231], v[188:191]\n"
"s_waitcnt lgkmcnt(2)\n"
"v_mfma_f32_16x16x32_bf16 v[192:195], a[192:195], v[216:219], v[192:195]\n"
"v_mfma_f32_16x16x32_bf16 v[196:199], a[196:199], v[216:219], v[196:199]\n"
"v_mfma_f32_16x16x32_bf16 v[200:203], a[200:203], v[216:219], v[200:203]\n"
"ds_read_b128 v[228:231], v233 offset:8192\n"
"v_mfma_f32_16x16x32_bf16 v[204:207], a[204:207], v[216:219], v[204:207]\n"
"v_mfma_f32_16x16x32_bf16 v[208:211], a[208:211], v[216:219], v[208:211]\n"
"v_mfma_f32_16x16x32_bf16 v[212:215], a[212:215], v[216:219], v[212:215]\n"
"s_add_u32 s58, s24, 0xb000\n"
"s_addc_u32 s59, s25, 0\n"
"s_waitcnt lgkmcnt(2)\n"
"v_mfma_f32_16x16x32_bf16 v[0:3], a[216:219], v[220:223], v[0:3]\n"
"global_load_dwordx4 a[192:195], v239, s[58:59] offset:-4096 sc1\n"
"v_mfma_f32_16x16x32_bf16 v[4:7], a[220:223], v[220:223], v[4:7]\n"
"v_mfma_f32_16x16x32_bf16 v[8:11], a[224:227], v[220:223], v[8:11]\n"
"ds_read_b128 v[216:219], v233 offset:12288\n"
"v_mfma_f32_16x16x32_bf16 v[12:15], a[228:231], v[220:223], v[12:15]\n"
"v_mfma_f32_16x16x32_bf16 v[16:19], a[232:235], v[220:223], v[16:19]\n"
"global_load_dwordx4 a[196:199], v239, s[58:59] offset:-3072 sc1\n"
"v_mfma_f32_16x16x32_bf16 v[20:23], a[236:239], v[220:223], v[20:23]\n"
"s_waitcnt lgkmcnt(2)\n"
"v_mfma_f32_16x16x32_bf16 v[24:27], a[216:219], v[224:227], v[24:27]\n"
"v_mfma_f32_16x16x32_bf16 v[28:31], a[220:223], v[224:227], v[28:31]\n"
"v_mfma_f32_16x16x32_bf16 v[32:35], a[224:227], v[224:227], v[32:35]\n"
"ds_read_b128 v[220:223], v233 offset:16384\n"
"global_load_dwordx4 a[200:203], v239, s[58:59] offset:-2048 sc1\n"
"v_mfma_f32_16x16x32_bf16 v[36:39], a[228:231], v[224:227], v[36:39]\n"
"v_mfma_f32_16x16x32_bf16 v[40:43], a[232:235], v[224:227], v[40:43]\n"
"v_mfma_f32_16x16x32_bf16 v[44:47], a[236:239], v[224:227], v[44:47]\n"
"s_waitcnt lgkmcnt(2)\n"
"v_mfma_f32_16x16x32_bf16 v[48:51], a[216:219], v[228:231], v[48:51]\n"
"global_load_dwordx4 a[204:207], v239, s[58:59] offset:-1024 sc1\n"
"v_mfma_f32_16x16x32_bf16 v[52:55], a[220:223], v[228:231], v[52:55]\n"
"v_mfma_f32_16x16x32_bf16 v[56:59], a[224:227], v[228:231], v[56:59]\n"
"ds_read_b128 v[224:227], v233 offset:20480\n"
"v_mfma_f32_16x16x32_bf16 v[60:63], a[228:231], v[228:231], v[60:63]\n"
"v_mfma_f32_16x16x32_bf16 v[64:67], a[232:235], v[228:231], v[64:67]\n"
"global_load_dwordx4 a[208:211], v239, s[58:59] offset:0 sc1\n"
"v_mfma_f32_16x16x32_bf16 v[68:71], a[236:239], v[228:231], v[68:71]\n"
"s_waitcnt lgkmcnt(2)\n"
"v_mfma_f32_16x16x32_bf16 v[72:75], a[216:219], v[216:219], v[72:75]\n"
"v_mfma_f32_16x16x32_bf16 v[76:79], a[220:223], v[216:219], v[76:79]\n"
"v_mfma_f32_16x16x32_bf16 v[80:83], a[224:227], v[216:219], v[80:83]\n"
"ds_read_b128 v[228:231], v233 offset:24576\n"
"global_load_dwordx4 a[212:215], v239, s[58:59] offset:1024 sc1\n"
"v_mfma_f32_16x16x32_bf16 v[84:87], a[228:231], v[216:219], v[84:87]\n"
"v_mfma_f32_16x16x32_bf16 v[88:91], a[232:235], v[216:219], v[88:91]\n"
"v_mfma_f32_16x16x32_bf16 v[92:95], a[236:239], v[216:219], v[92:95]\n"
"s_waitcnt lgkmcnt(2)\n"
"v_mfma_f32_16x16x32_bf16 v[96:99], a[216:219], v[220:223], v[96:99]\n"
"v_mfma_f32_16x16x32_bf16 v[100:103], a[220:223], v[220:223], v[100:103]\n"
"v_mfma_f32_16x16x32_bf16 v[104:107], a[224:227], v[220:223], v[104:107]\n"
"ds_read_b128 v[216:219], v233 offset:28672\n"
"v_mfma_f32_16x16x32_bf16 v[108:111], a[228:231], v[220:223], v[108:111]\n"
"v_mfma_f32_16x16x32_bf16 v[112:115], a[232:235], v[220:223], v[112:115]\n"
"v_mfma_f32_16x16x32_bf16 v[116:119], a[236:239], v[220:223], v[116:119]\n"
"s_add_u32 s52, s20, s33\n"
"s_addc_u32 s53, s21, 0\n"
"s_add_u32 m0, s31, 0x6000\n"
"s_waitcnt lgkmcnt(2)\n"
"v_mfma_f32_16x16x32_bf16 v[120:123], a[216:219], v[224:227], v[120:123]\n"
"global_load_lds_dwordx4 v237, s[52:53] offset:0\n"
"v_mfma_f32_16x16x32_bf16 v[124:127], a[220:223], v[224:227], v[124:127]\n"
"v_mfma_f32_16x16x32_bf16 v[128:131], a[224:227], v[224:227], v[128:131]\n"
"ds_read_b128 v[220:223], v233 offset:32768\n"
"v_add_u32 v233, v249, v233\n"
"s_add_u32 m0, s31, 0x7f00\n"
"v_mfma_f32_16x16x32_bf16 v[132:135], a[228:231], v[224:227], v[132:135]\n"
"global_load_lds_dwordx4 v237, s[52:53] offset:256\n"
"v_mfma_f32_16x16x32_bf16 v[136:139], a[232:235], v[224:227], v[136:139]\n"
"v_mfma_f32_16x16x32_bf16 v[140:143], a[236:239], v[224:227], v[140:143]\n"
"s_add_u32 m0, s31, 0x9e00\n"
"s_waitcnt lgkmcnt(2)\n"
"v_mfma_f32_16x16x32_bf16 v[144:147], a[216:219], v[228:231], v[144:147]\n"
"global_load_lds_dwordx4 v237, s[52:53] offset:512\n"
"v_mfma_f32_16x16x32_bf16 v[148:151], a[220:223], v[228:231], v[148:151]\n"
"v_mfma_f32_16x16x32_bf16 v[152:155], a[224:227], v[228:231], v[152:155]\n"
"ds_read_b128 v[224:227], v234 offset:0\n"
"s_add_u32 m0, s31, 0x7000\n"
"v_mfma_f32_16x16x32_bf16 v[156:159], a[228:231], v[228:231], v[156:159]\n"
"global_load_lds_dwordx4 v238, s[52:53] offset:0\n"
"v_mfma_f32_16x16x32_bf16 v[160:163], a[232:235], v[228:231], v[160:163]\n"
"v_mfma_f32_16x16x32_bf16 v[164:167], a[236:239], v[228:231], v[164:167]\n"
"s_add_u32 m0, s31, 0x8f00\n"
"s_waitcnt lgkmcnt(2)\n"
"v_mfma_f32_16x16x32_bf16 v[168:171], a[216:219], v[216:219], v[168:171]\n"
"global_load_lds_dwordx4 v238, s[52:53] offset:256\n"
"v_mfma_f32_16x16x32_bf16 v[172:175], a[220:223], v[216:219], v[172:175]\n"
"v_mfma_f32_16x16x32_bf16 v[176:179], a[224:227], v[216:219], v[176:179]\n"
"ds_read_b128 v[228:231], v234 offset:4096\n"
"s_add_u32 m0, s31, 0xae00\n"
"v_mfma_f32_16x16x32_bf16 v[180:183], a[228:231], v[216:219], v[180:183]\n"
"global_load_lds_dwordx4 v238, s[52:53] offset:512\n"
"v_mfma_f32_16x16x32_bf16 v[184:187], a[232:235], v[216:219], v[184:187]\n"
"v_mfma_f32_16x16x32_bf16 v[188:191], a[236:239], v[216:219], v[188:191]\n"
"s_waitcnt lgkmcnt(2)\n"
"v_mfma_f32_16x16x32_bf16 v[192:195], a[216:219], v[220:223], v[192:195]\n"
"v_mfma_f32_16x16x32_bf16 v[196:199], a[220:223], v[220:223], v[196:199]\n"
"v_mfma_f32_16x16x32_bf16 v[200:203], a[224:227], v[220:223], v[200:203]\n"
"ds_read_b128 v[216:219], v234 offset:8192\n"
"v_mfma_f32_16x16x32_bf16 v[204:207], a[228:231], v[220:223], v[204:207]\n"
"v_mfma_f32_16x16x32_bf16 v[208:211], a[232:235], v[220:223], v[208:211]\n"
"v_mfma_f32_16x16x32_bf16 v[212:215], a[236:239], v[220:223], v[212:215]\n"
"s_waitcnt lgkmcnt(2)\n"
"v_mfma_f32_16x16x32_bf16 v[0:3], a[0:3], v[224:227], v[0:3]\n"
"global_load_dwordx4 a[216:219], v239, s[58:59] offset:2048 sc1\n"
"ds_read_b128 v[244:247], v250\n"
"v_mfma_f32_16x16x32_bf16 v[4:7], a[4:7], v[224:227], v[4:7]\n"
"ds_read_b64 v[252:253], v250 offset:16\n"
"v_mfma_f32_16x16x32_bf16 v[8:11], a[8:11], v[224:227], v[8:11]\n"
"ds_read_b128 v[220:223], v234 offset:12288\n"
"v_mfma_f32_16x16x32_bf16 v[12:15], a[12:15], v[224:227], v[12:15]\n"
"v_mfma_f32_16x16x32_bf16 v[16:19], a[16:19], v[224:227], v[16:19]\n"
"global_load_dwordx4 a[220:223], v239, s[58:59] offset:3072 sc1\n"
"v_mfma_f32_16x16x32_bf16 v[20:23], a[20:23], v[224:227], v[20:23]\n"
"s_waitcnt lgkmcnt(4)\n"
"v_mfma_f32_16x16x32_bf16 v[24:27], a[0:3], v[228:231], v[24:27]\n"
"v_mfma_f32_16x16x32_bf16 v[28:31], a[4:7], v[228:231], v[28:31]\n"
"s_add_u32 s58, s24, 0xd000\n"
"s_addc_u32 s59, s25, 0\n"
"v_mfma_f32_16x16x32_bf16 v[32:35], a[8:11], v[228:231], v[32:35]\n"
"ds_read_b128 v[224:227], v234 offset:16384\n"
"global_load_dwordx4 a[224:227], v239, s[58:59] offset:-4096 sc1\n"
"v_mfma_f32_16x16x32_bf16 v[36:39], a[12:15], v[228:231], v[36:39]\n"
"v_mfma_f32_16x16x32_bf16 v[40:43], a[16:19], v[228:231], v[40:43]\n"
"v_mfma_f32_16x16x32_bf16 v[44:47], a[20:23], v[228:231], v[44:47]\n"
"s_waitcnt lgkmcnt(4)\n"
"v_mfma_f32_16x16x32_bf16 v[48:51], a[0:3], v[216:219], v[48:51]\n"
"global_load_dwordx4 a[228:231], v239, s[58:59] offset:-3072 sc1\n"
"v_mfma_f32_16x16x32_bf16 v[52:55], a[4:7], v[216:219], v[52:55]\n"
"v_mfma_f32_16x16x32_bf16 v[56:59], a[8:11], v[216:219], v[56:59]\n"
"ds_read_b128 v[228:231], v234 offset:20480\n"
"v_mfma_f32_16x16x32_bf16 v[60:63], a[12:15], v[216:219], v[60:63]\n"
"v_mfma_f32_16x16x32_bf16 v[64:67], a[16:19], v[216:219], v[64:67]\n"
"global_load_dwordx4 a[232:235], v239, s[58:59] offset:-2048 sc1\n"
"v_mfma_f32_16x16x32_bf16 v[68:71], a[20:23], v[216:219], v[68:71]\n"
"s_waitcnt lgkmcnt(2)\n"
"v_mfma_f32_16x16x32_bf16 v[72:75], a[0:3], v[220:223], v[72:75]\n"
"v_mfma_f32_16x16x32_bf16 v[76:79], a[4:7], v[220:223], v[76:79]\n"
"v_mfma_f32_16x16x32_bf16 v[80:83], a[8:11], v[220:223], v[80:83]\n"
"ds_read_b128 v[216:219], v234 offset:24576\n"
"global_load_dwordx4 a[236:239], v239, s[58:59] offset:-1024 sc1\n"
"v_mfma_f32_16x16x32_bf16 v[84:87], a[12:15], v[220:223], v[84:87]\n"
"v_mfma_f32_16x16x32_bf16 v[88:91], a[16:19], v[220:223], v[88:91]\n"
"v_mfma_f32_16x16x32_bf16 v[92:95], a[20:23], v[220:223], v[92:95]\n"
"s_waitcnt lgkmcnt(2)\n"
"v_mfma_f32_16x16x32_bf16 v[96:99], a[0:3], v[224:227], v[96:99]\n"
"v_mfma_f32_16x16x32_bf16 v[100:103], a[4:7], v[224:227], v[100:103]\n"
"v_mfma_f32_16x16x32_bf16 v[104:107], a[8:11], v[224:227], v[104:107]\n"
"ds_read_b128 v[220:223], v234 offset:28672\n"
"v_mfma_f32_16x16x32_bf16 v[108:111], a[12:15], v[224:227], v[108:111]\n"
"v_mfma_f32_16x16x32_bf16 v[112:115], a[16:19], v[224:227], v[112:115]\n"
"v_mfma_f32_16x16x32_bf16 v[116:119], a[20:23], v[224:227], v[116:119]\n"
"s_waitcnt lgkmcnt(2)\n"
"v_mfma_f32_16x16x32_bf16 v[120:123], a[0:3], v[228:231], v[120:123]\n"
"v_mfma_f32_16x16x32_bf16 v[124:127], a[4:7], v[228:231], v[124:127]\n"
"v_mfma_f32_16x16x32_bf16 v[128:131], a[8:11], v[228:231], v[128:131]\n"
"ds_read_b128 v[224:227], v234 offset:32768\n"
"v_add_u32 v234, v249, v234\n"
"v_mfma_f32_16x16x32_bf16 v[132:135], a[12:15], v[228:231], v[132:135]\n"
"v_mfma_f32_16x16x32_bf16 v[136:139], a[16:19], v[228:231], v[136:139]\n"
"v_mfma_f32_16x16x32_bf16 v[140:143], a[20:23], v[228:231], v[140:143]\n"
"s_waitcnt lgkmcnt(2)\n"
"v_mfma_f32_16x16x32_bf16 v[144:147], a[0:3], v[216:219], v[144:147]\n"
"v_mfma_f32_16x16x32_bf16 v[148:151], a[4:7], v[216:219], v[148:151]\n"
"v_mfma_f32_16x16x32_bf16 v[152:155], a[8:11], v[216:219], v[152:155]\n"
"ds_read_b128 v[228:231], v235 offset:4096\n"
"v_mfma_f32_16x16x32_bf16 v[156:159], a[12:15], v[216:219], v[156:159]\n"
"v_mfma_f32_16x16x32_bf16 v[160:163], a[16:19], v[216:219], v[160:163]\n"
"v_mfma_f32_16x16x32_bf16 v[164:167], a[20:23], v[216:219], v[164:167]\n"
"s_waitcnt lgkmcnt(2)\n"
"v_mfma_f32_16x16x32_bf16 v[168:171], a[0:3], v[220:223], v[168:171]\n"
"v_mfma_f32_16x16x32_bf16 v[172:175], a[4:7], v[220:223], v[172:175]\n"
"v_mfma_f32_16x16x32_bf16 v[176:179], a[8:11], v[220:223], v[176:179]\n"
"ds_read_b128 v[216:219], v235 offset:8192\n"
"v_mfma_f32_16x16x32_bf16 v[180:183], a[12:15], v[220:223], v[180:183]\n"
"v_mfma_f32_16x16x32_bf16 v[184:187], a[16:19], v[220:223], v[184:187]\n"
"v_mfma_f32_16x16x32_bf16 v[188:191], a[20:23], v[220:223], v[188:191]\n"
"s_waitcnt lgkmcnt(2)\n"
"v_mfma_f32_16x16x32_bf16 v[192:195], a[0:3], v[224:227], v[192:195]\n"
"v_mfma_f32_16x16x32_bf16 v[196:199], a[4:7], v[224:227], v[196:199]\n"
"v_mfma_f32_16x16x32_bf16 v[200:203], a[8:11], v[224:227], v[200:203]\n"
"ds_read_b128 v[220:223], v235 offset:12288\n"
"v_mfma_f32_16x16x32_bf16 v[204:207], a[12:15], v[224:227], v[204:207]\n"
"v_mfma_f32_16x16x32_bf16 v[208:211], a[16:19], v[224:227], v[208:211]\n"
"v_mfma_f32_16x16x32_bf16 v[212:215], a[20:23], v[224:227], v[212:215]\n"
"s_waitcnt lgkmcnt(2)\n"
"v_mfma_f32_16x16x32_bf16 v[24:27], a[24:27], v[228:231], v[24:27]\n"
"global_load_dwordx4 a[0:3], v239, s[58:59] offset:0 sc1\n"
"v_mfma_f32_16x16x32_bf16 v[28:31], a[28:31], v[228:231], v[28:31]\n"
"v_mfma_f32_16x16x32_bf16 v[32:35], a[32:35], v[228:231], v[32:35]\n"
"ds_read_b128 v[224:227], v235 offset:16384\n"
"v_mfma_f32_16x16x32_bf16 v[36:39], a[36:39], v[228:231], v[36:39]\n"
"v_mfma_f32_16x16x32_bf16 v[40:43], a[40:43], v[228:231], v[40:43]\n"
"global_load_dwordx4 a[4:7], v239, s[58:59] offset:1024 sc1\n"
"v_mfma_f32_16x16x32_bf16 v[44:47], a[44:47], v[228:231], v[44:47]\n"
"s_waitcnt lgkmcnt(2)\n"
"v_mfma_f32_16x16x32_bf16 v[48:51], a[24:27], v[216:219], v[48:51]\n"
"v_cvt_pk_bf16_f32 v24, v24, v25\n"
"v_cvt_pk_bf16_f32 v25, v26, v27\n"
"v_mfma_f32_16x16x32_bf16 v[52:55], a[28:31], v[216:219], v[52:55]\n"
"v_cvt_pk_bf16_f32 v26, v28, v29\n"
"v_cvt_pk_bf16_f32 v27, v30, v31\n"
"v_mfma_f32_16x16x32_bf16 v[56:59], a[32:35], v[216:219], v[56:59]\n"
"ds_read_b128 v[228:231], v235 offset:20480\n"
"v_cvt_pk_bf16_f32 v28, v32, v33\n"
"v_cvt_pk_bf16_f32 v29, v34, v35\n"
"global_load_dwordx4 a[8:11], v239, s[58:59] offset:2048 sc1\n"
"v_mfma_f32_16x16x32_bf16 v[60:63], a[36:39], v[216:219], v[60:63]\n"
"v_cvt_pk_bf16_f32 v30, v36, v37\n"
"v_cvt_pk_bf16_f32 v31, v38, v39\n"
"v_mfma_f32_16x16x32_bf16 v[64:67], a[40:43], v[216:219], v[64:67]\n"
"v_cvt_pk_bf16_f32 v32, v40, v41\n"
"v_cvt_pk_bf16_f32 v33, v42, v43\n"
"v_mfma_f32_16x16x32_bf16 v[68:71], a[44:47], v[216:219], v[68:71]\n"
"v_cvt_pk_bf16_f32 v34, v44, v45\n"
"v_cvt_pk_bf16_f32 v35, v46, v47\n"
"s_waitcnt lgkmcnt(2)\n"
"v_mfma_f32_16x16x32_bf16 v[72:75], a[24:27], v[220:223], v[72:75]\n"
"v_cvt_pk_bf16_f32 v48, v48, v49\n"
"v_cvt_pk_bf16_f32 v49, v50, v51\n"
"global_load_dwordx4 a[12:15], v239, s[58:59] offset:3072 sc1\n"
"v_mfma_f32_16x16x32_bf16 v[76:79], a[28:31], v[220:223], v[76:79]\n"
"v_cvt_pk_bf16_f32 v50, v52, v53\n"
"v_cvt_pk_bf16_f32 v51, v54, v55\n"
"v_mfma_f32_16x16x32_bf16 v[80:83], a[32:35], v[220:223], v[80:83]\n"
"ds_read_b128 v[216:219], v235 offset:24576\n"
"v_cvt_pk_bf16_f32 v52, v56, v57\n"
"v_cvt_pk_bf16_f32 v53, v58, v59\n"
"v_mfma_f32_16x16x32_bf16 v[84:87], a[36:39], v[220:223], v[84:87]\n"
"v_cvt_pk_bf16_f32 v54, v60, v61\n"
"v_cvt_pk_bf16_f32 v55, v62, v63\n"
"s_add_u32 s58, s24, 0xf000\n"
"s_addc_u32 s59, s25, 0\n"
"v_mfma_f32_16x16x32_bf16 v[88:91], a[40:43], v[220:223], v[88:91]\n"
"v_cvt_pk_bf16_f32 v56, v64, v65\n"
"v_cvt_pk_bf16_f32 v57, v66, v67\n"
"global_load_dwordx4 a[16:19], v239, s[58:59] offset:-4096 sc1\n"
"v_mfma_f32_16x16x32_bf16 v[92:95], a[44:47], v[220:223], v[92:95]\n"
"v_cvt_pk_bf16_f32 v58, v68, v69\n"
"v_cvt_pk_bf16_f32 v59, v70, v71\n"
"s_waitcnt lgkmcnt(2)\n"
"v_mfma_f32_16x16x32_bf16 v[96:99], a[24:27], v[224:227], v[96:99]\n"
"v_cvt_pk_bf16_f32 v72, v72, v73\n"
"v_cvt_pk_bf16_f32 v73, v74, v75\n"
"v_mfma_f32_16x16x32_bf16 v[100:103], a[28:31], v[224:227], v[100:103]\n"
"v_cvt_pk_bf16_f32 v74, v76, v77\n"
"v_cvt_pk_bf16_f32 v75, v78, v79\n"
"v_mfma_f32_16x16x32_bf16 v[104:107], a[32:35], v[224:227], v[104:107]\n"
"ds_read_b128 v[220:223], v235 offset:28672\n"
"v_cvt_pk_bf16_f32 v76, v80, v81\n"
"v_cvt_pk_bf16_f32 v77, v82, v83\n"
"global_load_dwordx4 a[20:23], v239, s[58:59] offset:-3072 sc1\n"
"v_mfma_f32_16x16x32_bf16 v[108:111], a[36:39], v[224:227], v[108:111]\n"
"v_cvt_pk_bf16_f32 v78, v84, v85\n"
"v_cvt_pk_bf16_f32 v79, v86, v87\n"
"v_mfma_f32_16x16x32_bf16 v[112:115], a[40:43], v[224:227], v[112:115]\n"
"v_cvt_pk_bf16_f32 v80, v88, v89\n"
"v_cvt_pk_bf16_f32 v81, v90, v91\n"
"v_mfma_f32_16x16x32_bf16 v[116:119], a[44:47], v[224:227], v[116:119]\n"
"v_cvt_pk_bf16_f32 v82, v92, v93\n"
"v_cvt_pk_bf16_f32 v83, v94, v95\n"
"s_waitcnt lgkmcnt(2)\n"
"v_mfma_f32_16x16x32_bf16 v[120:123], a[24:27], v[228:231], v[120:123]\n"
"s_memrealtime s[84:85]\n"
"v_cvt_pk_bf16_f32 v96, v96, v97\n"
"v_cvt_pk_bf16_f32 v97, v98, v99\n"
"v_mfma_f32_16x16x32_bf16 v[124:127], a[28:31], v[228:231], v[124:127]\n"
"v_cvt_pk_bf16_f32 v98, v100, v101\n"
"v_cvt_pk_bf16_f32 v99, v102, v103\n"
"v_mfma_f32_16x16x32_bf16 v[128:131], a[32:35], v[228:231], v[128:131]\n"
"ds_read_b128 v[224:227], v235 offset:32768\n"
"v_cvt_pk_bf16_f32 v100, v104, v105\n"
"v_cvt_pk_bf16_f32 v101, v106, v107\n"
"v_mfma_f32_16x16x32_bf16 v[132:135], a[36:39], v[228:231], v[132:135]\n"
"v_cvt_pk_bf16_f32 v102, v108, v109\n"
"v_cvt_pk_bf16_f32 v103, v110, v111\n"
"v_mfma_f32_16x16x32_bf16 v[136:139], a[40:43], v[228:231], v[136:139]\n"
"v_cvt_pk_bf16_f32 v104, v112, v113\n"
"v_cvt_pk_bf16_f32 v105, v114, v115\n"
"v_mfma_f32_16x16x32_bf16 v[140:143], a[44:47], v[228:231], v[140:143]\n"
"v_cvt_pk_bf16_f32 v106, v116, v117\n"
"v_cvt_pk_bf16_f32 v107, v118, v119\n"
"s_waitcnt lgkmcnt(2)\n"
"v_mfma_f32_16x16x32_bf16 v[144:147], a[24:27], v[216:219], v[144:147]\n"
"v_cvt_pk_bf16_f32 v120, v120, v121\n"
"v_cvt_pk_bf16_f32 v121, v122, v123\n"
"v_mfma_f32_16x16x32_bf16 v[148:151], a[28:31], v[216:219], v[148:151]\n"
"s_waitcnt lgkmcnt(0)\n"
"s_sub_u32 s86, s83, s84\n"
"v_cvt_pk_bf16_f32 v122, v124, v125\n"
"v_cvt_pk_bf16_f32 v123, v126, v127\n"
"v_mfma_f32_16x16x32_bf16 v[152:155], a[32:35], v[216:219], v[152:155]\n"
"ds_read_b128 v[228:231], v235 offset:0\n"
"v_add_u32 v235, v249, v235\n"
"s_waitcnt vmcnt(25)\n"
"s_barrier\n"
"v_cvt_pk_bf16_f32 v124, v128, v129\n"
"v_cvt_pk_bf16_f32 v125, v130, v131\n"
"v_mfma_f32_16x16x32_bf16 v[156:159], a[36:39], v[216:219], v[156:159]\n"
"ds_read_b128 v[36:39], v232 offset:0\n"
"v_cvt_pk_bf16_f32 v126, v132, v133\n"
"v_cvt_pk_bf16_f32 v127, v134, v135\n"
"v_mfma_f32_16x16x32_bf16 v[160:163], a[40:43], v[216:219], v[160:163]\n"
"v_cvt_pk_bf16_f32 v128, v136, v137\n"
"v_cvt_pk_bf16_f32 v129, v138, v139\n"
"v_mfma_f32_16x16x32_bf16 v[164:167], a[44:47], v[216:219], v[164:167]\n"
"ds_read_b128 v[40:43], v233 offset:0\n"
"v_cvt_pk_bf16_f32 v130, v140, v141\n"
"v_cvt_pk_bf16_f32 v131, v142, v143\n"
"v_mfma_f32_16x16x32_bf16 v[168:171], a[24:27], v[220:223], v[168:171]\n"
"v_cvt_pk_bf16_f32 v144, v144, v145\n"
"v_cvt_pk_bf16_f32 v145, v146, v147\n"
"global_load_dwordx4 v[140:143], v239, s[58:59] offset:-2048 sc1\n"
"v_mfma_f32_16x16x32_bf16 v[172:175], a[28:31], v[220:223], v[172:175]\n"
"ds_read_b128 v[44:47], v234 offset:0\n"
"v_cvt_pk_bf16_f32 v146, v148, v149\n"
"v_cvt_pk_bf16_f32 v147, v150, v151\n"
"v_mfma_f32_16x16x32_bf16 v[176:179], a[32:35], v[220:223], v[176:179]\n"
"v_cvt_pk_bf16_f32 v148, v152, v153\n"
"v_cvt_pk_bf16_f32 v149, v154, v155\n"
"v_mfma_f32_16x16x32_bf16 v[180:183], a[36:39], v[220:223], v[180:183]\n"
"ds_read_b128 v[60:63], v235 offset:0\n"
"v_cvt_pk_bf16_f32 v150, v156, v157\n"
"v_cvt_pk_bf16_f32 v151, v158, v159\n"
"v_mfma_f32_16x16x32_bf16 v[184:187], a[40:43], v[220:223], v[184:187]\n"
"v_cvt_pk_bf16_f32 v152, v160, v161\n"
"v_cvt_pk_bf16_f32 v153, v162, v163\n"
"v_mfma_f32_16x16x32_bf16 v[188:191], a[44:47], v[220:223], v[188:191]\n"
"ds_read_b128 v[64:67], v232 offset:8192\n"
"v_cvt_pk_bf16_f32 v154, v164, v165\n"
"v_cvt_pk_bf16_f32 v155, v166, v167\n"
"v_mfma_f32_16x16x32_bf16 v[192:195], a[24:27], v[224:227], v[192:195]\n"
"v_cvt_pk_bf16_f32 v168, v168, v169\n"
"v_cvt_pk_bf16_f32 v169, v170, v171\n"
"global_load_dwordx4 v[156:159], v239, s[58:59] offset:-1024 sc1\n"
"v_mfma_f32_16x16x32_bf16 v[196:199], a[28:31], v[224:227], v[196:199]\n"
"ds_read_b128 v[68:71], v233 offset:8192\n"
"v_cvt_pk_bf16_f32 v170, v172, v173\n"
"v_cvt_pk_bf16_f32 v171, v174, v175\n"
"v_mfma_f32_16x16x32_bf16 v[200:203], a[32:35], v[224:227], v[200:203]\n"
"v_cvt_pk_bf16_f32 v172, v176, v177\n"
"v_cvt_pk_bf16_f32 v173, v178, v179\n"
"v_mfma_f32_16x16x32_bf16 v[204:207], a[36:39], v[224:227], v[204:207]\n"
"ds_read_b128 v[84:87], v234 offset:8192\n"
"v_cvt_pk_bf16_f32 v174, v180, v181\n"
"v_cvt_pk_bf16_f32 v175, v182, v183\n"
"v_mfma_f32_16x16x32_bf16 v[208:211], a[40:43], v[224:227], v[208:211]\n"
"v_cvt_pk_bf16_f32 v176, v184, v185\n"
"v_cvt_pk_bf16_f32 v177, v186, v187\n"
"global_load_dwordx4 v[160:163], v239, s[58:59] offset:0 sc1\n"
"v_mfma_f32_16x16x32_bf16 v[212:215], a[44:47], v[224:227], v[212:215]\n"
"ds_read_b128 v[88:91], v235 offset:8192\n"
"v_cvt_pk_bf16_f32 v178, v188, v189\n"
"v_cvt_pk_bf16_f32 v179, v190, v191\n"
"s_waitcnt lgkmcnt(8)\n"
"v_mfma_f32_16x16x32_bf16 v[0:3], a[24:27], v[228:231], v[0:3]\n"
"v_cvt_pk_bf16_f32 v192, v192, v193\n"
"v_cvt_pk_bf16_f32 v193, v194, v195\n"
"v_mfma_f32_16x16x32_bf16 v[4:7], a[28:31], v[228:231], v[4:7]\n"
"ds_read_b128 v[92:95], v232 offset:16384\n"
"v_cvt_pk_bf16_f32 v194, v196, v197\n"
"v_cvt_pk_bf16_f32 v195, v198, v199\n"
"v_mfma_f32_16x16x32_bf16 v[8:11], a[32:35], v[228:231], v[8:11]\n"
"v_cvt_pk_bf16_f32 v196, v200, v201\n"
"v_cvt_pk_bf16_f32 v197, v202, v203\n"
"global_load_dwordx4 v[164:167], v239, s[58:59] offset:1024 sc1\n"
"v_mfma_f32_16x16x32_bf16 v[12:15], a[36:39], v[228:231], v[12:15]\n"
"ds_read_b128 v[108:111], v233 offset:16384\n"
"v_cvt_pk_bf16_f32 v198, v204, v205\n"
"v_cvt_pk_bf16_f32 v199, v206, v207\n"
"v_mfma_f32_16x16x32_bf16 v[16:19], a[40:43], v[228:231], v[16:19]\n"
"ds_read_b128 v[112:115], v234 offset:16384\n"
"v_cvt_pk_bf16_f32 v200, v208, v209\n"
"v_cvt_pk_bf16_f32 v201, v210, v211\n"
"v_mfma_f32_16x16x32_bf16 v[20:23], a[44:47], v[228:231], v[20:23]\n"
"ds_read_b128 v[116:119], v235 offset:16384\n"
"v_cvt_pk_bf16_f32 v202, v212, v213\n"
"v_cvt_pk_bf16_f32 v203, v214, v215\n"
"s_waitcnt lgkmcnt(8)\n"
"v_mfma_f32_16x16x32_bf16 v[132:135], a[240:243], v[36:39], v[0:3]\n"
"global_load_dwordx4 v[180:183], v239, s[58:59] offset:2048 sc1\n"
"v_mfma_f32_16x16x32_bf16 v[132:135], a[244:247], v[40:43], v[132:135]\n"
"s_mov_b64 exec, s[6:7]\n"
"s_cbranch_execz LHT5\n"
"LHR5:\n"
"s_mov_b64 exec, -1\n"
"v_mfma_f32_16x16x32_bf16 v[132:135], a[248:251], v[44:47], v[132:135]\n"
"v_mfma_f32_16x16x32_bf16 v[132:135], a[252:255], v[60:63], v[132:135]\n"
"global_load_dwordx4 v[184:187], v239, s[58:59] offset:3072 sc1\n"
"s_waitcnt lgkmcnt(4)\n"
"v_mfma_f32_16x16x32_bf16 v[132:135], a[48:51], v[64:67], v[132:135]\n"
"v_mfma_f32_16x16x32_bf16 v[132:135], a[52:55], v[68:71], v[132:135]\n"
"v_mfma_f32_16x16x32_bf16 v[132:135], a[56:59], v[84:87], v[132:135]\n"
"s_add_u32 s58, s24, 0x11000\n"
"s_addc_u32 s59, s25, 0\n"
"global_load_dwordx4 v[188:191], v239, s[58:59] offset:-4096 sc1\n"
"v_mfma_f32_16x16x32_bf16 v[132:135], a[60:63], v[88:91], v[132:135]\n"
"s_waitcnt lgkmcnt(0)\n"
"v_mfma_f32_16x16x32_bf16 v[132:135], a[64:67], v[92:95], v[132:135]\n"
"v_mfma_f32_16x16x32_bf16 v[132:135], a[68:71], v[108:111], v[132:135]\n"
"global_load_dwordx4 v[204:207], v239, s[58:59] offset:-3072 sc1\n"
"v_mfma_f32_16x16x32_bf16 v[132:135], a[72:75], v[112:115], v[132:135]\n"
"v_mfma_f32_16x16x32_bf16 v[132:135], a[76:79], v[116:119], v[132:135]\n"
"v_mfma_f32_16x16x32_bf16 v[136:139], a[80:83], v[36:39], v[4:7]\n"
"global_load_dwordx4 a[24:27], v239, s[58:59] offset:-2048 sc1\n"
"v_mfma_f32_16x16x32_bf16 v[136:139], a[84:87], v[40:43], v[136:139]\n"
"v_mfma_f32_16x16x32_bf16 v[136:139], a[88:91], v[44:47], v[136:139]\n"
"v_mfma_f32_16x16x32_bf16 v[136:139], a[92:95], v[60:63], v[136:139]\n"
"global_load_dwordx4 a[28:31], v239, s[58:59] offset:-1024 sc1\n"
"v_mfma_f32_16x16x32_bf16 v[136:139], a[96:99], v[64:67], v[136:139]\n"
"v_cvt_pk_bf16_f32 v208, v132, v133\n"
"v_mfma_f32_16x16x32_bf16 v[136:139], a[100:103], v[68:71], v[136:139]\n"
"v_cvt_pk_bf16_f32 v209, v134, v135\n"
"v_mfma_f32_16x16x32_bf16 v[136:139], a[104:107], v[84:87], v[136:139]\n"
"global_load_dwordx4 a[32:35], v239, s[58:59] offset:0 sc1\n"
"v_mfma_f32_16x16x32_bf16 v[136:139], a[108:111], v[88:91], v[136:139]\n"
"v_mfma_f32_16x16x32_bf16 v[136:139], a[112:115], v[92:95], v[136:139]\n"
"v_mfma_f32_16x16x32_bf16 v[136:139], a[116:119], v[108:111], v[136:139]\n"
"global_load_dwordx4 a[36:39], v239, s[58:59] offset:1024 sc1\n"
"v_mfma_f32_16x16x32_bf16 v[136:139], a[120:123], v[112:115], v[136:139]\n"
"ds_write_b64 v244, v[208:209] offset:12288\n"
"v_mfma_f32_16x16x32_bf16 v[136:139], a[124:127], v[116:119], v[136:139]\n"
"v_mfma_f32_16x16x32_bf16 v[132:135], a[128:131], v[36:39], v[8:11]\n"
"global_load_dwordx4 a[40:43], v239, s[58:59] offset:2048 sc1\n"
"v_mfma_f32_16x16x32_bf16 v[132:135], a[132:135], v[40:43], v[132:135]\n"
"v_mfma_f32_16x16x32_bf16 v[132:135], a[136:139], v[44:47], v[132:135]\n"
"v_mfma_f32_16x16x32_bf16 v[132:135], a[140:143], v[60:63], v[132:135]\n"
"global_load_dwordx4 a[44:47], v239, s[58:59] offset:3072 sc1\n"
"v_mfma_f32_16x16x32_bf16 v[132:135], a[144:147], v[64:67], v[132:135]\n"
"v_cvt_pk_bf16_f32 v210, v136, v137\n"
"v_mfma_f32_16x16x32_bf16 v[132:135], a[148:151], v[68:71], v[132:135]\n"
"v_cvt_pk_bf16_f32 v211, v138, v139\n"
"v_mfma_f32_16x16x32_bf16 v[132:135], a[152:155], v[84:87], v[132:135]\n"
"global_load_dwordx4 v[228:231], v248, s[28:29] offset:3072 sc1\n"
"v_mfma_f32_16x16x32_bf16 v[132:135], a[156:159], v[88:91], v[132:135]\n"
"v_mfma_f32_16x16x32_bf16 v[132:135], a[160:163], v[92:95], v[132:135]\n"
"v_mfma_f32_16x16x32_bf16 v[132:135], a[164:167], v[108:111], v[132:135]\n"
"s_add_u32 s52, s20, s34\n"
"s_addc_u32 s53, s21, 0\n"
"s_add_u32 m0, s31, 0xc000\n"
"v_mfma_f32_16x16x32_bf16 v[132:135], a[168:171], v[112:115], v[132:135]\n"
"global_load_lds_dwordx4 v237, s[52:53] offset:0\n"
"v_mfma_f32_16x16x32_bf16 v[132:135], a[172:175], v[116:119], v[132:135]\n"
"ds_write_b64 v244, v[210:211] offset:12296\n"
"s_waitcnt vmcnt(40)\n"
"v_mfma_f32_16x16x32_bf16 v[136:139], a[176:179], v[36:39], v[12:15]\n"
"v_mfma_f32_16x16x32_bf16 v[136:139], a[180:183], v[40:43], v[136:139]\n"
"v_mfma_f32_16x16x32_bf16 v[136:139], a[184:187], v[44:47], v[136:139]\n"
"v_mfma_f32_16x16x32_bf16 v[136:139], a[188:191], v[60:63], v[136:139]\n"
"s_add_u32 m0, s31, 0xdf00\n"
"s_waitcnt vmcnt(36)\n"
"v_mfma_f32_16x16x32_bf16 v[136:139], a[192:195], v[64:67], v[136:139]\n"
"global_load_lds_dwordx4 v237, s[52:53] offset:256\n"
"LHT5:\n"
"s_cbranch_execz LHR5\n"
"v_mfma_f32_16x16x32_bf16 v[136:139], a[196:199], v[68:71], v[136:139]\n"
"v_cvt_pk_bf16_f32 v212, v132, v133\n"
"v_mfma_f32_16x16x32_bf16 v[136:139], a[200:203], v[84:87], v[136:139]\n"
"v_cvt_pk_bf16_f32 v213, v134, v135\n"
"v_mfma_f32_16x16x32_bf16 v[136:139], a[204:207], v[88:91], v[136:139]\n"
"s_waitcnt vmcnt(27)\n"
"v_mfma_f32_16x16x32_bf16 v[136:139], a[208:211], v[92:95], v[136:139]\n"
"v_mfma_f32_16x16x32_bf16 v[136:139], a[212:215], v[108:111], v[136:139]\n"
"s_add_u32 m0, s31, 0xfe00\n"
"v_mfma_f32_16x16x32_bf16 v[136:139], a[216:219], v[112:115], v[136:139]\n"
"global_load_lds_dwordx4 v237, s[52:53] offset:512\n"
"v_mfma_f32_16x16x32_bf16 v[136:139], a[220:223], v[116:119], v[136:139]\n"
"ds_write_b64 v245, v[212:213] offset:12288\n"
"s_waitcnt vmcnt(24)\n"
"v_mfma_f32_16x16x32_bf16 v[132:135], a[224:227], v[36:39], v[16:19]\n"
"v_mfma_f32_16x16x32_bf16 v[132:135], a[228:231], v[40:43], v[132:135]\n"
"v_mfma_f32_16x16x32_bf16 v[132:135], a[232:235], v[44:47], v[132:135]\n"
"v_mfma_f32_16x16x32_bf16 v[132:135], a[236:239], v[60:63], v[132:135]\n"
"s_add_u32 m0, s31, 0xd000\n"
"s_waitcnt vmcnt(20)\n"
"v_mfma_f32_16x16x32_bf16 v[132:135], a[0:3], v[64:67], v[132:135]\n"
"global_load_lds_dwordx4 v238, s[52:53] offset:0\n"
"v_mfma_f32_16x16x32_bf16 v[132:135], a[4:7], v[68:71], v[132:135]\n"
"v_cvt_pk_bf16_f32 v214, v136, v137\n"
"v_mfma_f32_16x16x32_bf16 v[132:135], a[8:11], v[84:87], v[132:135]\n"
"v_cvt_pk_bf16_f32 v215, v138, v139\n"
"v_mfma_f32_16x16x32_bf16 v[132:135], a[12:15], v[88:91], v[132:135]\n"
"s_waitcnt vmcnt(17)\n"
"v_mfma_f32_16x16x32_bf16 v[132:135], a[16:19], v[92:95], v[132:135]\n"
"v_mfma_f32_16x16x32_bf16 v[132:135], a[20:23], v[108:111], v[132:135]\n"
"s_add_u32 m0, s31, 0xef00\n"
"v_mfma_f32_16x16x32_bf16 v[132:135], v[140:143], v[112:115], v[132:135]\n"
"global_load_lds_dwordx4 v238, s[52:53] offset:256\n"
"v_mfma_f32_16x16x32_bf16 v[132:135], v[156:159], v[116:119], v[132:135]\n"
"s_barrier\n"
"ds_write_b64 v245, v[214:215] offset:12296\n"
"s_waitcnt vmcnt(14)\n"
"v_mfma_f32_16x16x32_bf16 v[136:139], v[160:163], v[36:39], v[20:23]\n"
"ds_read_b128 v[36:39], v232 offset:256\n"
"v_mfma_f32_16x16x32_bf16 v[136:139], v[164:167], v[40:43], v[136:139]\n"
"ds_read_b128 v[40:43], v233 offset:256\n"
"v_mfma_f32_16x16x32_bf16 v[136:139], v[180:183], v[44:47], v[136:139]\n"
"ds_read_b128 v[44:47], v234 offset:256\n"
"v_mfma_f32_16x16x32_bf16 v[136:139], v[184:187], v[60:63], v[136:139]\n"
"ds_read_b128 v[60:63], v235 offset:256\n"
"s_add_u32 m0, s31, 0x10e00\n"
"s_waitcnt vmcnt(10)\n"
"v_mfma_f32_16x16x32_bf16 v[136:139], v[188:191], v[64:67], v[136:139]\n"
"ds_read_b128 v[64:67], v232 offset:8448\n"
"global_load_lds_dwordx4 v238, s[52:53] offset:512\n"
"v_mfma_f32_16x16x32_bf16 v[136:139], v[204:207], v[68:71], v[136:139]\n"
"ds_read_b128 v[68:71], v233 offset:8448\n"
"v_cvt_pk_bf16_f32 v208, v132, v133\n"
"v_mfma_f32_16x16x32_bf16 v[136:139], a[24:27], v[84:87], v[136:139]\n"
"ds_read_b128 v[84:87], v234 offset:8448\n"
"v_cvt_pk_bf16_f32 v209, v134, v135\n"
"v_mfma_f32_16x16x32_bf16 v[136:139], a[28:31], v[88:91], v[136:139]\n"
"ds_read_b128 v[88:91], v235 offset:8448\n"
"s_waitcnt vmcnt(7)\n"
"v_mfma_f32_16x16x32_bf16 v[136:139], a[32:35], v[92:95], v[136:139]\n"
"ds_read_b128 v[92:95], v232 offset:16640\n"
"v_mfma_f32_16x16x32_bf16 v[136:139], a[36:39], v[108:111], v[136:139]\n"
"ds_read_b128 v[108:111], v233 offset:16640\n"
"v_mfma_f32_16x16x32_bf16 v[136:139], a[40:43], v[112:115], v[136:139]\n"
"ds_read_b128 v[112:115], v234 offset:16640\n"
"v_mfma_f32_16x16x32_bf16 v[136:139], a[44:47], v[116:119], v[136:139]\n"
"ds_read_b128 v[116:119], v235 offset:16640\n"
"s_waitcnt lgkmcnt(8)\n"
"v_mfma_f32_16x16x32_bf16 v[132:135], a[240:243], v[36:39], v[0:3]\n"
"s_add_u32 s52, s20, s35\n"
"s_addc_u32 s53, s21, 0\n"
"s_add_u32 m0, s31, 0x12000\n"
"v_mfma_f32_16x16x32_bf16 v[132:135], a[244:247], v[40:43], v[132:135]\n"
"global_load_lds_dwordx4 v237, s[52:53] offset:0\n"
"v_mfma_f32_16x16x32_bf16 v[132:135], a[248:251], v[44:47], v[132:135]\n"
"ds_write_b64 v244, v[208:209] offset:12416\n"
"v_mfma_f32_16x16x32_bf16 v[132:135], a[252:255], v[60:63], v[132:135]\n"
"v_cvt_pk_bf16_f32 v210, v136, v137\n"
"s_waitcnt lgkmcnt(5)\n"
"v_mfma_f32_16x16x32_bf16 v[132:135], a[48:51], v[64:67], v[132:135]\n"
"v_cvt_pk_bf16_f32 v211, v138, v139\n"
"v_mfma_f32_16x16x32_bf16 v[132:135], a[52:55], v[68:71], v[132:135]\n"
"v_mfma_f32_16x16x32_bf16 v[132:135], a[56:59], v[84:87], v[132:135]\n"
"s_add_u32 m0, s31, 0x13f00\n"
"v_mfma_f32_16x16x32_bf16 v[132:135], a[60:63], v[88:91], v[132:135]\n"
"global_load_lds_dwordx4 v237, s[52:53] offset:256\n"
"s_waitcnt lgkmcnt(1)\n"
"v_mfma_f32_16x16x32_bf16 v[132:135], a[64:67], v[92:95], v[132:135]\n"
"v_mfma_f32_16x16x32_bf16 v[132:135], a[68:71], v[108:111], v[132:135]\n"
"ds_write_b64 v244, v[210:211] offset:12424\n"
"v_mfma_f32_16x16x32_bf16 v[132:135], a[72:75], v[112:115], v[132:135]\n"
"v_mfma_f32_16x16x32_bf16 v[132:135], a[76:79], v[116:119], v[132:135]\n"
"v_mfma_f32_16x16x32_bf16 v[136:139], a[124:127], v[116:119], v[4:7]\n"
"s_add_u32 m0, s31, 0x15e00\n"
"v_mfma_f32_16x16x32_bf16 v[136:139], a[120:123], v[112:115], v[136:139]\n"
"global_load_lds_dwordx4 v237, s[52:53] offset:512\n"
"v_mfma_f32_16x16x32_bf16 v[136:139], a[116:119], v[108:111], v[136:139]\n"
"v_mfma_f32_16x16x32_bf16 v[136:139], a[112:115], v[92:95], v[136:139]\n"
"v_cvt_pk_bf16_f32 v208, v132, v133\n"
"v_mfma_f32_16x16x32_bf16 v[136:139], a[108:111], v[88:91], v[136:139]\n"
"v_cvt_pk_bf16_f32 v209, v134, v135\n"
"v_mfma_f32_16x16x32_bf16 v[136:139], a[104:107], v[84:87], v[136:139]\n"
"v_mfma_f32_16x16x32_bf16 v[136:139], a[100:103], v[68:71], v[136:139]\n"
"s_add_u32 m0, s31, 0x13000\n"
"v_mfma_f32_16x16x32_bf16 v[136:139], a[96:99], v[64:67], v[136:139]\n"
"global_load_lds_dwordx4 v238, s[52:53] offset:0\n"
"v_mfma_f32_16x16x32_bf16 v[136:139], a[92:95], v[60:63], v[136:139]\n"
"s_waitcnt vmcnt(10)\n"
"v_mfma_f32_16x16x32_bf16 v[136:139], a[88:91], v[44:47], v[136:139]\n"
"v_cmp_ne_u64 s[58:59], s[80:81], v[228:229]\n"
"v_mfma_f32_16x16x32_bf16 v[136:139], a[84:87], v[40:43], v[136:139]\n"
"v_subrev_u32 v230, s83, v230\n"
"v_mfma_f32_16x16x32_bf16 v[136:139], a[80:83], v[36:39], v[136:139]\n"
"ds_write_b64 v244, v[208:209] offset:24576\n"
"v_mfma_f32_16x16x32_bf16 v[132:135], a[128:131], v[36:39], v[8:11]\n"
"v_add_u32_dpp v230, v230, v230 row_shr:1 row_mask:0xf bank_mask:0xf\n"
"s_add_u32 m0, s31, 0x14f00\n"
"v_mfma_f32_16x16x32_bf16 v[132:135], a[132:135], v[40:43], v[132:135]\n"
"global_load_lds_dwordx4 v238, s[52:53] offset:256\n"
"v_mfma_f32_16x16x32_bf16 v[132:135], a[136:139], v[44:47], v[132:135]\n"
"v_add_u32_dpp v230, v230, v230 row_shr:2 row_mask:0xf bank_mask:0xf\n"
"v_mfma_f32_16x16x32_bf16 v[132:135], a[140:143], v[60:63], v[132:135]\n"
"v_cvt_pk_bf16_f32 v210, v136, v137\n"
"v_mfma_f32_16x16x32_bf16 v[132:135], a[144:147], v[64:67], v[132:135]\n"
"v_add_u32_dpp v230, v230, v230 row_shr:4 row_mask:0xf bank_mask:0xf\n"
"v_mfma_f32_16x16x32_bf16 v[132:135], a[148:151], v[68:71], v[132:135]\n"
"v_cvt_pk_bf16_f32 v211, v138, v139\n"
"v_mfma_f32_16x16x32_bf16 v[132:135], a[152:155], v[84:87], v[132:135]\n"
"v_readlane_b32 s90, v230, 63\n"
"s_add_u32 m0, s31, 0x16e00\n"
"v_mfma_f32_16x16x32_bf16 v[132:135], a[156:159], v[88:91], v[132:135]\n"
"global_load_lds_dwordx4 v238, s[52:53] offset:512\n"
"v_mfma_f32_16x16x32_bf16 v[132:135], a[160:163], v[92:95], v[132:135]\n"
"s_ashr_i32 s90, s90, 3\n"
"v_mfma_f32_16x16x32_bf16 v[132:135], a[164:167], v[108:111], v[132:135]\n"
"s_add_i32 s90, s90, 2678\n"
"v_mfma_f32_16x16x32_bf16 v[132:135], a[168:171], v[112:115], v[132:135]\n"
"s_max_i32 s90, s90, 0\n"
"ds_write_b64 v244, v[210:211] offset:24584\n"
"v_mfma_f32_16x16x32_bf16 v[132:135], a[172:175], v[116:119], v[132:135]\n"
"s_min_i32 s90, s90, 120\n"
"v_mfma_f32_16x16x32_bf16 v[136:139], a[220:223], v[116:119], v[12:15]\n"
"s_mul_i32 s90, s90, 12\n"
"v_mfma_f32_16x16x32_bf16 v[136:139], a[216:219], v[112:115], v[136:139]\n"
"s_ashr_i32 s90, s90, 3\n"
"v_mfma_f32_16x16x32_bf16 v[136:139], a[212:215], v[108:111], v[136:139]\n"
"s_lshr_b32 s59, s59, 24\n"
"s_cselect_b32 s90, 0, s90\n"
"v_mfma_f32_16x16x32_bf16 v[136:139], a[208:211], v[92:95], v[136:139]\n"
"s_add_i32 s86, s86, s90\n"
"v_mfma_f32_16x16x32_bf16 v[136:139], a[204:207], v[88:91], v[136:139]\n"
"s_ashr_i32 s86, s86, 4\n"
"v_mov_b32 v242, 2\n"
"v_mfma_f32_16x16x32_bf16 v[136:139], a[200:203], v[84:87], v[136:139]\n"
"s_max_i32 s86, s86, 144\n"
"v_mfma_f32_16x16x32_bf16 v[136:139], a[196:199], v[68:71], v[136:139]\n"
"s_min_i32 s86, s86, 185\n"
"s_waitcnt lgkmcnt(2)\n"
"ds_write_b32 v240, v242\n"
"v_mfma_f32_16x16x32_bf16 v[136:139], a[192:195], v[64:67], v[136:139]\n"
"s_mov_b32 s83, s84\n"
"v_mfma_f32_16x16x32_bf16 v[136:139], a[188:191], v[60:63], v[136:139]\n"
"v_cvt_pk_bf16_f32 v212, v132, v133\n"
"v_mfma_f32_16x16x32_bf16 v[136:139], a[184:187], v[44:47], v[136:139]\n"
"v_cvt_pk_bf16_f32 v213, v134, v135\n"
"v_mfma_f32_16x16x32_bf16 v[136:139], a[180:183], v[40:43], v[136:139]\n"
"v_mfma_f32_16x16x32_bf16 v[136:139], a[176:179], v[36:39], v[136:139]\n"
"v_mfma_f32_16x16x32_bf16 v[132:135], a[224:227], v[36:39], v[16:19]\n"
"v_mfma_f32_16x16x32_bf16 v[132:135], a[228:231], v[40:43], v[132:135]\n"
"v_mfma_f32_16x16x32_bf16 v[132:135], a[232:235], v[44:47], v[132:135]\n"
"ds_read_b32 v243, v241\n"
"s_add_u32 s83, s83, s86\n"
"v_mfma_f32_16x16x32_bf16 v[132:135], a[236:239], v[60:63], v[132:135]\n"
"s_memrealtime s[84:85]\n"
"v_cvt_pk_bf16_f32 v215, v138, v139\n"
"v_mfma_f32_16x16x32_bf16 v[132:135], a[0:3], v[64:67], v[132:135]\n"
"ds_write_b64 v245, v[212:213] offset:24576\n"
"s_sub_i32 s88, s88, 6\n"
"v_mfma_f32_16x16x32_bf16 v[132:135], a[4:7], v[68:71], v[132:135]\n"
"v_cvt_pk_bf16_f32 v214, v136, v137\n"
"s_max_i32 s88, s88, 0\n"
"v_mfma_f32_16x16x32_bf16 v[132:135], a[8:11], v[84:87], v[132:135]\n"
"s_waitcnt lgkmcnt(0)\n"
"v_cmp_gt_u32 s[56:57], 2, v243\n"
"v_mfma_f32_16x16x32_bf16 v[132:135], a[12:15], v[88:91], v[132:135]\n"
"s_sub_u32 s84, s84, s83\n"
"v_mfma_f32_16x16x32_bf16 v[132:135], a[16:19], v[92:95], v[132:135]\n"
"s_cmp_lg_u64 s[56:57], 0\n"
"s_cbranch_scc1 LB2_1\n"
"LB2_1R:\n"
"v_mfma_f32_16x16x32_bf16 v[132:135], a[20:23], v[108:111], v[132:135]\n"
"s_cmp_lt_i32 s84, s88\n"
"s_cselect_b64 vcc, -1, 0\n"
"v_mfma_f32_16x16x32_bf16 v[132:135], v[140:143], v[112:115], v[132:135]\n"
"ds_write_b64 v245, v[214:215] offset:24584\n"
"v_mfma_f32_16x16x32_bf16 v[132:135], v[156:159], v[116:119], v[132:135]\n"
"v_mfma_f32_16x16x32_bf16 v[136:139], a[44:47], v[116:119], v[20:23]\n"
"ds_read_b128 v[116:119], v235 offset:40960\n"
"v_mfma_f32_16x16x32_bf16 v[136:139], a[40:43], v[112:115], v[136:139]\n"
"ds_read_b128 v[112:115], v234 offset:40960\n"
"v_mfma_f32_16x16x32_bf16 v[136:139], a[36:39], v[108:111], v[136:139]\n"
"ds_read_b128 v[108:111], v233 offset:40960\n"
"v_mfma_f32_16x16x32_bf16 v[136:139], a[32:35], v[92:95], v[136:139]\n"
"ds_read_b128 v[92:95], v232 offset:40960\n"
"v_cvt_pk_bf16_f32 v208, v132, v133\n"
"v_mfma_f32_16x16x32_bf16 v[136:139], a[28:31], v[88:91], v[136:139]\n"
"ds_read_b128 v[88:91], v235 offset:32768\n"
"v_cvt_pk_bf16_f32 v209, v134, v135\n"
"v_mfma_f32_16x16x32_bf16 v[136:139], a[24:27], v[84:87], v[136:139]\n"
"ds_read_b128 v[84:87], v234 offset:32768\n"
"v_mfma_f32_16x16x32_bf16 v[136:139], v[204:207], v[68:71], v[136:139]\n"
"ds_read_b128 v[68:71], v233 offset:32768\n"
"v_mfma_f32_16x16x32_bf16 v[136:139], v[188:191], v[64:67], v[136:139]\n"
"ds_read_b128 v[64:67], v232 offset:32768\n"
"v_mfma_f32_16x16x32_bf16 v[136:139], v[184:187], v[60:63], v[136:139]\n"
"ds_read_b128 v[60:63], v235 offset:24576\n"
"v_mfma_f32_16x16x32_bf16 v[136:139], v[180:183], v[44:47], v[136:139]\n"
"ds_read_b128 v[44:47], v234 offset:24576\n"
"v_mfma_f32_16x16x32_bf16 v[136:139], v[164:167], v[40:43], v[136:139]\n"
"ds_read_b128 v[40:43], v233 offset:24576\n"
"v_mfma_f32_16x16x32_bf16 v[136:139], v[160:163], v[36:39], v[136:139]\n"
"ds_read_b128 v[36:39], v232 offset:24576\n"
"LJOIN:\n"
"s_waitcnt lgkmcnt(8)\n"
"v_mfma_f32_16x16x32_bf16 v[132:135], a[76:79], v[116:119], v[0:3]\n"
"ds_write_b64 v244, v[208:209] offset:24704\n"
"v_mfma_f32_16x16x32_bf16 v[132:135], a[72:75], v[112:115], v[132:135]\n"
"s_add_u32 s54, s26, s32\n"
"s_addc_u32 s55, s27, 0\n"
"v_mfma_f32_16x16x32_bf16 v[132:135], a[68:71], v[108:111], v[132:135]\n"
"s_sub_i32 s84, s84, s19\n"
"v_mfma_f32_16x16x32_bf16 v[132:135], a[64:67], v[92:95], v[132:135]\n"
"v_cvt_pk_bf16_f32 v210, v136, v137\n"
"s_waitcnt lgkmcnt(5)\n"
"v_mfma_f32_16x16x32_bf16 v[132:135], a[60:63], v[88:91], v[132:135]\n"
"v_cvt_pk_bf16_f32 v211, v138, v139\n"
"v_mfma_f32_16x16x32_bf16 v[132:135], a[56:59], v[84:87], v[132:135]\n"
"s_max_i32 s88, s88, s84\n"
"v_mfma_f32_16x16x32_bf16 v[132:135], a[52:55], v[68:71], v[132:135]\n"
"ds_read_b128 v[216:219], v246 offset:12288\n"
"v_mfma_f32_16x16x32_bf16 v[132:135], a[48:51], v[64:67], v[132:135]\n"
"ds_read_b128 v[220:223], v246 offset:13312\n"
"s_waitcnt lgkmcnt(3)\n"
"v_mfma_f32_16x16x32_bf16 v[132:135], a[252:255], v[60:63], v[132:135]\n"
"ds_read_b128 v[224:227], v246 offset:14336\n"
"v_mfma_f32_16x16x32_bf16 v[132:135], a[248:251], v[44:47], v[132:135]\n"
"ds_write_b64 v244, v[210:211] offset:24712\n"
"v_mfma_f32_16x16x32_bf16 v[132:135], a[244:247], v[40:43], v[132:135]\n"
"v_mfma_f32_16x16x32_bf16 v[132:135], a[240:243], v[36:39], v[132:135]\n"
"v_mfma_f32_16x16x32_bf16 v[136:139], a[80:83], v[36:39], v[4:7]\n"
"v_mfma_f32_16x16x32_bf16 v[136:139], a[84:87], v[40:43], v[136:139]\n"
"v_mfma_f32_16x16x32_bf16 v[136:139], a[88:91], v[44:47], v[136:139]\n"
"v_mfma_f32_16x16x32_bf16 v[136:139], a[92:95], v[60:63], v[136:139]\n"
"v_cvt_pk_bf16_f32 v208, v132, v133\n"
"v_mfma_f32_16x16x32_bf16 v[136:139], a[96:99], v[64:67], v[136:139]\n"
"v_cvt_pk_bf16_f32 v209, v134, v135\n"
"v_mfma_f32_16x16x32_bf16 v[136:139], a[100:103], v[68:71], v[136:139]\n"
"v_mfma_f32_16x16x32_bf16 v[136:139], a[104:107], v[84:87], v[136:139]\n"
"v_mfma_f32_16x16x32_bf16 v[136:139], a[108:111], v[88:91], v[136:139]\n"
"s_waitcnt lgkmcnt(1)\n"
"v_mfma_f32_16x16x32_bf16 v[136:139], a[112:115], v[92:95], v[136:139]\n"
"v_mfma_f32_16x16x32_bf16 v[136:139], a[116:119], v[108:111], v[136:139]\n"
"global_store_dwordx4 v247, v[216:219], s[54:55] offset:0 sc1\n"
"ds_write_b64 v244, v[208:209] offset:0\n"
"v_mfma_f32_16x16x32_bf16 v[136:139], a[120:123], v[112:115], v[136:139]\n"
"v_mfma_f32_16x16x32_bf16 v[136:139], a[124:127], v[116:119], v[136:139]\n"
"v_mfma_f32_16x16x32_bf16 v[132:135], a[172:175], v[116:119], v[8:11]\n"
"v_mfma_f32_16x16x32_bf16 v[132:135], a[168:171], v[112:115], v[132:135]\n"
"v_mfma_f32_16x16x32_bf16 v[132:135], a[164:167], v[108:111], v[132:135]\n"
"v_mfma_f32_16x16x32_bf16 v[132:135], a[160:163], v[92:95], v[132:135]\n"
"v_cvt_pk_bf16_f32 v210, v136, v137\n"
"v_mfma_f32_16x16x32_bf16 v[132:135], a[156:159], v[88:91], v[132:135]\n"
"v_cvt_pk_bf16_f32 v211, v138, v139\n"
"v_mfma_f32_16x16x32_bf16 v[132:135], a[152:155], v[84:87], v[132:135]\n"
"v_mfma_f32_16x16x32_bf16 v[132:135], a[148:151], v[68:71], v[132:135]\n"
"v_mfma_f32_16x16x32_bf16 v[132:135], a[144:147], v[64:67], v[132:135]\n"
"v_mfma_f32_16x16x32_bf16 v[132:135], a[140:143], v[60:63], v[132:135]\n"
"v_mfma_f32_16x16x32_bf16 v[132:135], a[136:139], v[44:47], v[132:135]\n"
"global_store_dwordx4 v252, v[220:223], s[54:55] offset:0 sc1\n"
"ds_write_b64 v244, v[210:211] offset:8\n"
"v_mfma_f32_16x16x32_bf16 v[132:135], a[132:135], v[40:43], v[132:135]\n"
"s_add_u32 s52, s20, s36\n"
"s_addc_u32 s53, s21, 0\n"
"v_mfma_f32_16x16x32_bf16 v[132:135], a[128:131], v[36:39], v[132:135]\n"
"s_add_u32 m0, s31, 0x0\n"
"v_mfma_f32_16x16x32_bf16 v[136:139], a[176:179], v[36:39], v[12:15]\n"
"global_load_lds_dwordx4 v237, s[52:53] offset:0 nt\n"
"v_mfma_f32_16x16x32_bf16 v[136:139], a[180:183], v[40:43], v[136:139]\n"
"v_mfma_f32_16x16x32_bf16 v[136:139], a[184:187], v[44:47], v[136:139]\n"
"v_mfma_f32_16x16x32_bf16 v[136:139], a[188:191], v[60:63], v[136:139]\n"
"v_cvt_pk_bf16_f32 v212, v132, v133\n"
"v_mfma_f32_16x16x32_bf16 v[136:139], a[192:195], v[64:67], v[136:139]\n"
"v_mov_b32 v242, 3\n"
"v_mfma_f32_16x16x32_bf16 v[136:139], a[196:199], v[68:71], v[136:139]\n"
"v_cvt_pk_bf16_f32 v213, v134, v135\n"
"v_mfma_f32_16x16x32_bf16 v[136:139], a[200:203], v[84:87], v[136:139]\n"
"s_waitcnt lgkmcnt(2)\n"
"ds_write_b32 v240, v242\n"
"v_mfma_f32_16x16x32_bf16 v[136:139], a[204:207], v[88:91], v[136:139]\n"
"v_mfma_f32_16x16x32_bf16 v[136:139], a[208:211], v[92:95], v[136:139]\n"
"v_mfma_f32_16x16x32_bf16 v[136:139], a[212:215], v[108:111], v[136:139]\n"
"v_mfma_f32_16x16x32_bf16 v[136:139], a[216:219], v[112:115], v[136:139]\n"
"global_store_dwordx4 v253, v[224:227], s[54:55] offset:0 sc1\n"
"ds_write_b64 v245, v[212:213] offset:0\n"
"v_mfma_f32_16x16x32_bf16 v[136:139], a[220:223], v[116:119], v[136:139]\n"
"v_mfma_f32_16x16x32_bf16 v[132:135], v[156:159], v[116:119], v[16:19]\n"
"v_mfma_f32_16x16x32_bf16 v[132:135], v[140:143], v[112:115], v[132:135]\n"
"v_mfma_f32_16x16x32_bf16 v[132:135], a[20:23], v[108:111], v[132:135]\n"
"ds_read_b32 v243, v241\n"
"v_mfma_f32_16x16x32_bf16 v[132:135], a[16:19], v[92:95], v[132:135]\n"
"v_cvt_pk_bf16_f32 v214, v136, v137\n"
"v_mfma_f32_16x16x32_bf16 v[132:135], a[12:15], v[88:91], v[132:135]\n"
"v_cvt_pk_bf16_f32 v215, v138, v139\n"
"v_mfma_f32_16x16x32_bf16 v[132:135], a[8:11], v[84:87], v[132:135]\n"
"v_mfma_f32_16x16x32_bf16 v[132:135], a[4:7], v[68:71], v[132:135]\n"
"s_waitcnt lgkmcnt(0)\n"
"v_cmp_gt_u32 s[56:57], 3, v243\n"
"v_mfma_f32_16x16x32_bf16 v[132:135], a[0:3], v[64:67], v[132:135]\n"
"v_mfma_f32_16x16x32_bf16 v[132:135], a[236:239], v[60:63], v[132:135]\n"
"s_or_b64 s[56:57], s[56:57], vcc\n"
"s_cbranch_scc1 LB3\n"
"LB3R:\n"
"v_mfma_f32_16x16x32_bf16 v[132:135], a[232:235], v[44:47], v[132:135]\n"
"ds_write_b64 v245, v[214:215] offset:8\n"
"v_mfma_f32_16x16x32_bf16 v[132:135], a[228:231], v[40:43], v[132:135]\n"
"v_mfma_f32_16x16x32_bf16 v[132:135], a[224:227], v[36:39], v[132:135]\n"
"v_mfma_f32_16x16x32_bf16 v[136:139], v[160:163], v[36:39], v[20:23]\n"
"ds_read_b128 v[36:39], v232 offset:24832\n"
"v_mfma_f32_16x16x32_bf16 v[136:139], v[164:167], v[40:43], v[136:139]\n"
"ds_read_b128 v[40:43], v233 offset:24832\n"
"v_mfma_f32_16x16x32_bf16 v[136:139], v[180:183], v[44:47], v[136:139]\n"
"ds_read_b128 v[44:47], v234 offset:24832\n"
"v_mfma_f32_16x16x32_bf16 v[136:139], v[184:187], v[60:63], v[136:139]\n"
"ds_read_b128 v[60:63], v235 offset:24832\n"
"v_cvt_pk_bf16_f32 v208, v132, v133\n"
"v_mfma_f32_16x16x32_bf16 v[136:139], v[188:191], v[64:67], v[136:139]\n"
"ds_read_b128 v[64:67], v232 offset:33024\n"
"v_cvt_pk_bf16_f32 v209, v134, v135\n"
"v_mfma_f32_16x16x32_bf16 v[136:139], v[204:207], v[68:71], v[136:139]\n"
"ds_read_b128 v[68:71], v233 offset:33024\n"
"v_mfma_f32_16x16x32_bf16 v[136:139], a[24:27], v[84:87], v[136:139]\n"
"ds_read_b128 v[84:87], v234 offset:33024\n"
"v_mfma_f32_16x16x32_bf16 v[136:139], a[28:31], v[88:91], v[136:139]\n"
"ds_read_b128 v[88:91], v235 offset:33024\n"
"v_mfma_f32_16x16x32_bf16 v[136:139], a[32:35], v[92:95], v[136:139]\n"
"ds_read_b128 v[92:95], v232 offset:41216\n"
"v_mfma_f32_16x16x32_bf16 v[136:139], a[36:39], v[108:111], v[136:139]\n"
"ds_read_b128 v[108:111], v233 offset:41216\n"
"ds_write_b64 v244, v[208:209] offset:128\n"
"v_mfma_f32_16x16x32_bf16 v[136:139], a[40:43], v[112:115], v[136:139]\n"
"ds_read_b128 v[112:115], v234 offset:41216\n"
"v_mfma_f32_16x16x32_bf16 v[136:139], a[44:47], v[116:119], v[136:139]\n"
"ds_read_b128 v[116:119], v235 offset:41216\n"
"s_waitcnt lgkmcnt(9)\n"
"v_mfma_f32_16x16x32_bf16 v[132:135], a[240:243], v[36:39], v[0:3]\n"
"v_add_u32 v232, 0xc000, v232\n"
"v_mfma_f32_16x16x32_bf16 v[132:135], a[244:247], v[40:43], v[132:135]\n"
"v_add_u32 v233, 0xc000, v233\n"
"v_mfma_f32_16x16x32_bf16 v[132:135], a[248:251], v[44:47], v[132:135]\n"
"v_add_u32 v234, 0xc000, v234\n"
"v_mfma_f32_16x16x32_bf16 v[132:135], a[252:255], v[60:63], v[132:135]\n"
"v_add_u32 v235, 0xc000, v235\n"
"s_waitcnt lgkmcnt(5)\n"
"v_mfma_f32_16x16x32_bf16 v[132:135], a[48:51], v[64:67], v[132:135]\n"
"s_add_u32 m0, s31, 0x1f00\n"
"v_mfma_f32_16x16x32_bf16 v[132:135], a[52:55], v[68:71], v[132:135]\n"
"global_load_lds_dwordx4 v237, s[52:53] offset:256 nt\n"
"v_mfma_f32_16x16x32_bf16 v[132:135], a[56:59], v[84:87], v[132:135]\n"
"v_cvt_pk_bf16_f32 v210, v136, v137\n"
"v_mfma_f32_16x16x32_bf16 v[132:135], a[60:63], v[88:91], v[132:135]\n"
"v_cvt_pk_bf16_f32 v211, v138, v139\n"
"s_waitcnt lgkmcnt(0)\n"
"v_mfma_f32_16x16x32_bf16 v[132:135], a[64:67], v[92:95], v[132:135]\n"
"ds_read_b128 v[216:219], v246 offset:24576\n"
"v_mfma_f32_16x16x32_bf16 v[132:135], a[68:71], v[108:111], v[132:135]\n"
"ds_read_b128 v[220:223], v246 offset:25600\n"
"v_mfma_f32_16x16x32_bf16 v[132:135], a[72:75], v[112:115], v[132:135]\n"
"ds_read_b128 v[224:227], v246 offset:26624\n"
"v_mfma_f32_16x16x32_bf16 v[132:135], a[76:79], v[116:119], v[132:135]\n"
"s_add_u32 m0, s31, 0x3e00\n"
"v_mfma_f32_16x16x32_bf16 v[136:139], a[124:127], v[116:119], v[4:7]\n"
"ds_write_b64 v244, v[210:211] offset:136\n"
"v_mfma_f32_16x16x32_bf16 v[136:139], a[120:123], v[112:115], v[136:139]\n"
"global_load_lds_dwordx4 v237, s[52:53] offset:512 nt\n"
"v_mfma_f32_16x16x32_bf16 v[136:139], a[116:119], v[108:111], v[136:139]\n"
"v_lshlrev_b32 v0, 16, v24\n"
"v_mfma_f32_16x16x32_bf16 v[136:139], a[112:115], v[92:95], v[136:139]\n"
"v_cvt_pk_bf16_f32 v208, v132, v133\n"
"v_mfma_f32_16x16x32_bf16 v[136:139], a[108:111], v[88:91], v[136:139]\n"
"v_cvt_pk_bf16_f32 v209, v134, v135\n"
"v_mfma_f32_16x16x32_bf16 v[136:139], a[104:107], v[84:87], v[136:139]\n"
"v_and_b32 v1, 0xffff0000, v24\n"
"v_mfma_f32_16x16x32_bf16 v[136:139], a[100:103], v[68:71], v[136:139]\n"
"v_lshlrev_b32 v2, 16, v25\n"
"v_mfma_f32_16x16x32_bf16 v[136:139], a[96:99], v[64:67], v[136:139]\n"
"s_waitcnt lgkmcnt(1)\n"
"v_mfma_f32_16x16x32_bf16 v[136:139], a[92:95], v[60:63], v[136:139]\n"
"v_and_b32 v3, 0xffff0000, v25\n"
"v_mfma_f32_16x16x32_bf16 v[136:139], a[88:91], v[44:47], v[136:139]\n"
"global_store_dwordx4 v247, v[216:219], s[54:55] offset:768 sc1\n"
"ds_write_b64 v244, v[208:209] offset:12288\n"
"v_mfma_f32_16x16x32_bf16 v[136:139], a[84:87], v[40:43], v[136:139]\n"
"v_lshlrev_b32 v4, 16, v26\n"
"v_mfma_f32_16x16x32_bf16 v[136:139], a[80:83], v[36:39], v[136:139]\n"
"v_and_b32 v5, 0xffff0000, v26\n"
"v_mfma_f32_16x16x32_bf16 v[132:135], a[128:131], v[36:39], v[8:11]\n"
"v_lshlrev_b32 v6, 16, v27\n"
"v_mfma_f32_16x16x32_bf16 v[132:135], a[132:135], v[40:43], v[132:135]\n"
"v_and_b32 v7, 0xffff0000, v27\n"
"v_mfma_f32_16x16x32_bf16 v[132:135], a[136:139], v[44:47], v[132:135]\n"
"s_add_u32 s83, s83, s86\n"
"v_mfma_f32_16x16x32_bf16 v[132:135], a[140:143], v[60:63], v[132:135]\n"
"v_cvt_pk_bf16_f32 v210, v136, v137\n"
"v_mfma_f32_16x16x32_bf16 v[132:135], a[144:147], v[64:67], v[132:135]\n"
"v_cvt_pk_bf16_f32 v211, v138, v139\n"
"v_mfma_f32_16x16x32_bf16 v[132:135], a[148:151], v[68:71], v[132:135]\n"
"v_lshlrev_b32 v8, 16, v28\n"
"v_mfma_f32_16x16x32_bf16 v[132:135], a[152:155], v[84:87], v[132:135]\n"
"v_and_b32 v9, 0xffff0000, v28\n"
"v_mfma_f32_16x16x32_bf16 v[132:135], a[156:159], v[88:91], v[132:135]\n"
"v_lshlrev_b32 v10, 16, v29\n"
"v_mfma_f32_16x16x32_bf16 v[132:135], a[160:163], v[92:95], v[132:135]\n"
"v_and_b32 v11, 0xffff0000, v29\n"
"v_mfma_f32_16x16x32_bf16 v[132:135], a[164:167], v[108:111], v[132:135]\n"
"s_memrealtime s[84:85]\n"
"v_mfma_f32_16x16x32_bf16 v[132:135], a[168:171], v[112:115], v[132:135]\n"
"global_store_dwordx4 v252, v[220:223], s[54:55] offset:768 sc1\n"
"ds_write_b64 v244, v[210:211] offset:12296\n"
"v_mfma_f32_16x16x32_bf16 v[132:135], a[172:175], v[116:119], v[132:135]\n"
"s_add_u32 m0, s31, 0x1000\n"
"v_mfma_f32_16x16x32_bf16 v[136:139], a[220:223], v[116:119], v[12:15]\n"
"global_load_lds_dwordx4 v238, s[52:53] offset:0 nt\n"
"v_mfma_f32_16x16x32_bf16 v[136:139], a[216:219], v[112:115], v[136:139]\n"
"s_sub_i32 s88, s88, 6\n"
"v_mfma_f32_16x16x32_bf16 v[136:139], a[212:215], v[108:111], v[136:139]\n"
"s_max_i32 s88, s88, 0\n"
"v_mfma_f32_16x16x32_bf16 v[136:139], a[208:211], v[92:95], v[136:139]\n"
"v_cvt_pk_bf16_f32 v212, v132, v133\n"
"v_mfma_f32_16x16x32_bf16 v[136:139], a[204:207], v[88:91], v[136:139]\n"
"v_mov_b32 v242, 4\n"
"v_mfma_f32_16x16x32_bf16 v[136:139], a[200:203], v[84:87], v[136:139]\n"
"v_cvt_pk_bf16_f32 v213, v134, v135\n"
"v_mfma_f32_16x16x32_bf16 v[136:139], a[196:199], v[68:71], v[136:139]\n"
"s_waitcnt vmcnt(15) lgkmcnt(2)\n"
"ds_write_b32 v240, v242\n"
"v_mfma_f32_16x16x32_bf16 v[136:139], a[192:195], v[64:67], v[136:139]\n"
"v_lshlrev_b32 v12, 16, v30\n"
"v_mfma_f32_16x16x32_bf16 v[136:139], a[188:191], v[60:63], v[136:139]\n"
"v_and_b32 v13, 0xffff0000, v30\n"
"v_mfma_f32_16x16x32_bf16 v[136:139], a[184:187], v[44:47], v[136:139]\n"
"v_lshlrev_b32 v14, 16, v31\n"
"v_mfma_f32_16x16x32_bf16 v[136:139], a[180:183], v[40:43], v[136:139]\n"
"global_store_dwordx4 v253, v[224:227], s[54:55] offset:768 sc1\n"
"ds_write_b64 v245, v[212:213] offset:12288\n"
"v_mfma_f32_16x16x32_bf16 v[136:139], a[176:179], v[36:39], v[136:139]\n"
"v_and_b32 v15, 0xffff0000, v31\n"
"v_mfma_f32_16x16x32_bf16 v[132:135], a[224:227], v[36:39], v[16:19]\n"
"s_mov_b64 exec, s[8:9]\n"
"s_cbranch_execz LHT6\n"
"LHR6:\n"
"s_mov_b64 exec, -1\n"
"v_mfma_f32_16x16x32_bf16 v[132:135], a[228:231], v[40:43], v[132:135]\n"
"v_mfma_f32_16x16x32_bf16 v[132:135], a[232:235], v[44:47], v[132:135]\n"
"ds_read_b32 v243, v241\n"
"v_mfma_f32_16x16x32_bf16 v[132:135], a[236:239], v[60:63], v[132:135]\n"
"v_cvt_pk_bf16_f32 v214, v136, v137\n"
"v_mfma_f32_16x16x32_bf16 v[132:135], a[0:3], v[64:67], v[132:135]\n"
"v_cvt_pk_bf16_f32 v215, v138, v139\n"
"v_mfma_f32_16x16x32_bf16 v[132:135], a[4:7], v[68:71], v[132:135]\n"
"v_lshlrev_b32 v16, 16, v32\n"
"v_mfma_f32_16x16x32_bf16 v[132:135], a[8:11], v[84:87], v[132:135]\n"
"s_waitcnt lgkmcnt(0)\n"
"v_cmp_gt_u32 s[56:57], 4, v243\n"
"v_mfma_f32_16x16x32_bf16 v[132:135], a[12:15], v[88:91], v[132:135]\n"
"s_sub_u32 s84, s84, s83\n"
"v_mfma_f32_16x16x32_bf16 v[132:135], a[16:19], v[92:95], v[132:135]\n"
"s_or_b64 s[56:57], s[56:57], vcc\n"
"s_cbranch_scc1 LB4\n"
"LB4R:\n"
"v_mfma_f32_16x16x32_bf16 v[132:135], a[20:23], v[108:111], v[132:135]\n"
"s_cmp_lt_i32 s84, s88\n"
"s_cselect_b64 vcc, -1, 0\n"
"v_mfma_f32_16x16x32_bf16 v[132:135], v[140:143], v[112:115], v[132:135]\n"
"ds_write_b64 v245, v[214:215] offset:12296\n"
"v_mfma_f32_16x16x32_bf16 v[132:135], v[156:159], v[116:119], v[132:135]\n"
"v_and_b32 v17, 0xffff0000, v32\n"
"v_mfma_f32_16x16x32_bf16 v[136:139], a[44:47], v[116:119], v[20:23]\n"
"ds_read_b128 v[116:119], v235 offset:16384\n"
"v_mfma_f32_16x16x32_bf16 v[136:139], a[40:43], v[112:115], v[136:139]\n"
"ds_read_b128 v[112:115], v234 offset:16384\n"
"v_mfma_f32_16x16x32_bf16 v[136:139], a[36:39], v[108:111], v[136:139]\n"
"ds_read_b128 v[108:111], v233 offset:16384\n"
"v_mfma_f32_16x16x32_bf16 v[136:139], a[32:35], v[92:95], v[136:139]\n"
"ds_read_b128 v[92:95], v232 offset:16384\n"
"v_cvt_pk_bf16_f32 v208, v132, v133\n"
"v_mfma_f32_16x16x32_bf16 v[136:139], a[28:31], v[88:91], v[136:139]\n"
"ds_read_b128 v[88:91], v235 offset:8192\n"
"v_cvt_pk_bf16_f32 v209, v134, v135\n"
"v_mfma_f32_16x16x32_bf16 v[136:139], a[24:27], v[84:87], v[136:139]\n"
"ds_read_b128 v[84:87], v234 offset:8192\n"
"v_mfma_f32_16x16x32_bf16 v[136:139], v[204:207], v[68:71], v[136:139]\n"
"ds_read_b128 v[68:71], v233 offset:8192\n"
"v_mfma_f32_16x16x32_bf16 v[136:139], v[188:191], v[64:67], v[136:139]\n"
"ds_read_b128 v[64:67], v232 offset:8192\n"
"v_mfma_f32_16x16x32_bf16 v[136:139], v[184:187], v[60:63], v[136:139]\n"
"ds_read_b128 v[60:63], v235 offset:0\n"
"v_mfma_f32_16x16x32_bf16 v[136:139], v[180:183], v[44:47], v[136:139]\n"
"ds_read_b128 v[44:47], v234 offset:0\n"
"v_mfma_f32_16x16x32_bf16 v[136:139], v[164:167], v[40:43], v[136:139]\n"
"ds_read_b128 v[40:43], v233 offset:0\n"
"v_mfma_f32_16x16x32_bf16 v[136:139], v[160:163], v[36:39], v[136:139]\n"
"ds_read_b128 v[36:39], v232 offset:0\n"
"s_waitcnt lgkmcnt(8)\n"
"v_mfma_f32_16x16x32_bf16 v[132:135], a[76:79], v[116:119], v[0:3]\n"
"ds_write_b64 v244, v[208:209] offset:12416\n"
"v_mfma_f32_16x16x32_bf16 v[132:135], a[72:75], v[112:115], v[132:135]\n"
"s_add_u32 m0, s31, 0x2f00\n"
"v_mfma_f32_16x16x32_bf16 v[132:135], a[68:71], v[108:111], v[132:135]\n"
"global_load_lds_dwordx4 v238, s[52:53] offset:256 nt\n"
"v_mfma_f32_16x16x32_bf16 v[132:135], a[64:67], v[92:95], v[132:135]\n"
"v_lshlrev_b32 v18, 16, v33\n"
"v_cvt_pk_bf16_f32 v211, v138, v139\n"
"s_waitcnt lgkmcnt(5)\n"
"v_mfma_f32_16x16x32_bf16 v[132:135], a[60:63], v[88:91], v[132:135]\n"
"v_and_b32 v19, 0xffff0000, v33\n"
"v_mfma_f32_16x16x32_bf16 v[132:135], a[56:59], v[84:87], v[132:135]\n"
"v_lshlrev_b32 v20, 16, v34\n"
"v_mfma_f32_16x16x32_bf16 v[132:135], a[52:55], v[68:71], v[132:135]\n"
"v_and_b32 v21, 0xffff0000, v34\n"
"v_mfma_f32_16x16x32_bf16 v[132:135], a[48:51], v[64:67], v[132:135]\n"
"v_lshlrev_b32 v22, 16, v35\n"
"s_waitcnt lgkmcnt(1)\n"
"v_mfma_f32_16x16x32_bf16 v[132:135], a[252:255], v[60:63], v[132:135]\n"
"v_and_b32 v23, 0xffff0000, v35\n"
"v_mfma_f32_16x16x32_bf16 v[132:135], a[248:251], v[44:47], v[132:135]\n"
"v_cvt_pk_bf16_f32 v210, v136, v137\n"
"v_mfma_f32_16x16x32_bf16 v[132:135], a[244:247], v[40:43], v[132:135]\n"
"s_add_u32 s54, s26, s33\n"
"s_addc_u32 s55, s27, 0\n"
"v_mfma_f32_16x16x32_bf16 v[132:135], a[240:243], v[36:39], v[132:135]\n"
"ds_read_b128 v[216:219], v246 offset:0\n"
"v_mfma_f32_16x16x32_bf16 v[136:139], a[80:83], v[36:39], v[4:7]\n"
"ds_read_b128 v[220:223], v246 offset:1024\n"
"v_mfma_f32_16x16x32_bf16 v[136:139], a[84:87], v[40:43], v[136:139]\n"
"ds_read_b128 v[224:227], v246 offset:2048\n"
"v_mfma_f32_16x16x32_bf16 v[136:139], a[88:91], v[44:47], v[136:139]\n"
"ds_write_b64 v244, v[210:211] offset:12424\n"
"v_mfma_f32_16x16x32_bf16 v[136:139], a[92:95], v[60:63], v[136:139]\n"
"v_cvt_pk_bf16_f32 v208, v132, v133\n"
"v_mfma_f32_16x16x32_bf16 v[136:139], a[96:99], v[64:67], v[136:139]\n"
"v_cvt_pk_bf16_f32 v209, v134, v135\n"
"v_mfma_f32_16x16x32_bf16 v[136:139], a[100:103], v[68:71], v[136:139]\n"
"s_add_u32 m0, s31, 0x4e00\n"
"v_mfma_f32_16x16x32_bf16 v[136:139], a[104:107], v[84:87], v[136:139]\n"
"global_load_lds_dwordx4 v238, s[52:53] offset:512 nt\n"
"v_mfma_f32_16x16x32_bf16 v[136:139], a[108:111], v[88:91], v[136:139]\n"
"s_waitcnt lgkmcnt(1)\n"
"v_mfma_f32_16x16x32_bf16 v[136:139], a[112:115], v[92:95], v[136:139]\n"
"s_sub_i32 s84, s84, s19\n"
"v_mfma_f32_16x16x32_bf16 v[136:139], a[116:119], v[108:111], v[136:139]\n"
"global_store_dwordx4 v247, v[216:219], s[54:55] offset:0 sc1\n"
"ds_write_b64 v244, v[208:209] offset:24576\n"
"v_mfma_f32_16x16x32_bf16 v[136:139], a[120:123], v[112:115], v[136:139]\n"
"s_max_i32 s88, s88, s84\n"
"v_mfma_f32_16x16x32_bf16 v[136:139], a[124:127], v[116:119], v[136:139]\n"
"LHT6:\n"
"s_cbranch_execz LHR6\n"
"v_mfma_f32_16x16x32_bf16 v[132:135], a[172:175], v[116:119], v[8:11]\n"
"v_mfma_f32_16x16x32_bf16 v[132:135], a[168:171], v[112:115], v[132:135]\n"
"v_mfma_f32_16x16x32_bf16 v[132:135], a[164:167], v[108:111], v[132:135]\n"
"v_mfma_f32_16x16x32_bf16 v[132:135], a[160:163], v[92:95], v[132:135]\n"
"v_cvt_pk_bf16_f32 v210, v136, v137\n"
"v_mfma_f32_16x16x32_bf16 v[132:135], a[156:159], v[88:91], v[132:135]\n"
"v_cvt_pk_bf16_f32 v211, v138, v139\n"
"v_mfma_f32_16x16x32_bf16 v[132:135], a[152:155], v[84:87], v[132:135]\n"
"v_mfma_f32_16x16x32_bf16 v[132:135], a[148:151], v[68:71], v[132:135]\n"
"v_mfma_f32_16x16x32_bf16 v[132:135], a[144:147], v[64:67], v[132:135]\n"
"v_mfma_f32_16x16x32_bf16 v[132:135], a[140:143], v[60:63], v[132:135]\n"
"v_mfma_f32_16x16x32_bf16 v[132:135], a[136:139], v[44:47], v[132:135]\n"
"global_store_dwordx4 v252, v[220:223], s[54:55] offset:0 sc1\n"
"ds_write_b64 v244, v[210:211] offset:24584\n"
"v_mfma_f32_16x16x32_bf16 v[132:135], a[132:135], v[40:43], v[132:135]\n"
"v_mfma_f32_16x16x32_bf16 v[132:135], a[128:131], v[36:39], v[132:135]\n"
"v_mfma_f32_16x16x32_bf16 v[136:139], a[176:179], v[36:39], v[12:15]\n"
"s_add_u32 s52, s20, s37\n"
"s_addc_u32 s53, s21, 0\n"
"v_mfma_f32_16x16x32_bf16 v[136:139], a[180:183], v[40:43], v[136:139]\n"
"s_add_u32 m0, s31, 0x6000\n"
"v_mfma_f32_16x16x32_bf16 v[136:139], a[184:187], v[44:47], v[136:139]\n"
"global_load_lds_dwordx4 v237, s[52:53] offset:0 nt\n"
"v_mfma_f32_16x16x32_bf16 v[136:139], a[188:191], v[60:63], v[136:139]\n"
"v_cvt_pk_bf16_f32 v212, v132, v133\n"
"v_mfma_f32_16x16x32_bf16 v[136:139], a[192:195], v[64:67], v[136:139]\n"
"v_mov_b32 v242, 5\n"
"v_mfma_f32_16x16x32_bf16 v[136:139], a[196:199], v[68:71], v[136:139]\n"
"v_cvt_pk_bf16_f32 v213, v134, v135\n"
"v_mfma_f32_16x16x32_bf16 v[136:139], a[200:203], v[84:87], v[136:139]\n"
"s_waitcnt lgkmcnt(2)\n"
"ds_write_b32 v240, v242\n"
"v_mfma_f32_16x16x32_bf16 v[136:139], a[204:207], v[88:91], v[136:139]\n"
"v_mfma_f32_16x16x32_bf16 v[136:139], a[208:211], v[92:95], v[136:139]\n"
"v_mfma_f32_16x16x32_bf16 v[136:139], a[212:215], v[108:111], v[136:139]\n"
"v_mfma_f32_16x16x32_bf16 v[136:139], a[216:219], v[112:115], v[136:139]\n"
"global_store_dwordx4 v253, v[224:227], s[54:55] offset:0 sc1\n"
"ds_write_b64 v245, v[212:213] offset:24576\n"
"v_mfma_f32_16x16x32_bf16 v[136:139], a[220:223], v[116:119], v[136:139]\n"
"v_mfma_f32_16x16x32_bf16 v[132:135], v[156:159], v[116:119], v[16:19]\n"
"v_mfma_f32_16x16x32_bf16 v[132:135], v[140:143], v[112:115], v[132:135]\n"
"v_mfma_f32_16x16x32_bf16 v[132:135], a[20:23], v[108:111], v[132:135]\n"
"ds_read_b32 v243, v241\n"
"v_mfma_f32_16x16x32_bf16 v[132:135], a[16:19], v[92:95], v[132:135]\n"
"v_cvt_pk_bf16_f32 v214, v136, v137\n"
"v_mfma_f32_16x16x32_bf16 v[132:135], a[12:15], v[88:91], v[132:135]\n"
"v_cvt_pk_bf16_f32 v215, v138, v139\n"
"v_mfma_f32_16x16x32_bf16 v[132:135], a[8:11], v[84:87], v[132:135]\n"
"v_mfma_f32_16x16x32_bf16 v[132:135], a[4:7], v[68:71], v[132:135]\n"
"s_waitcnt lgkmcnt(0)\n"
"v_cmp_gt_u32 s[56:57], 5, v243\n"
"v_mfma_f32_16x16x32_bf16 v[132:135], a[0:3], v[64:67], v[132:135]\n"
"v_mfma_f32_16x16x32_bf16 v[132:135], a[236:239], v[60:63], v[132:135]\n"
"s_or_b64 s[56:57], s[56:57], vcc\n"
"s_cbranch_scc1 LB5\n"
"LB5R:\n"
"v_mfma_f32_16x16x32_bf16 v[132:135], a[232:235], v[44:47], v[132:135]\n"
"ds_write_b64 v245, v[214:215] offset:24584\n"
"v_mfma_f32_16x16x32_bf16 v[132:135], a[228:231], v[40:43], v[132:135]\n"
"v_mfma_f32_16x16x32_bf16 v[132:135], a[224:227], v[36:39], v[132:135]\n"
"v_mfma_f32_16x16x32_bf16 v[136:139], v[160:163], v[36:39], v[20:23]\n"
"ds_read_b128 v[36:39], v232 offset:256\n"
"v_mfma_f32_16x16x32_bf16 v[136:139], v[164:167], v[40:43], v[136:139]\n"
"ds_read_b128 v[40:43], v233 offset:256\n"
"v_mfma_f32_16x16x32_bf16 v[136:139], v[180:183], v[44:47], v[136:139]\n"
"ds_read_b128 v[44:47], v234 offset:256\n"
"v_mfma_f32_16x16x32_bf16 v[136:139], v[184:187], v[60:63], v[136:139]\n"
"ds_read_b128 v[60:63], v235 offset:256\n"
"v_cvt_pk_bf16_f32 v208, v132, v133\n"
"v_mfma_f32_16x16x32_bf16 v[136:139], v[188:191], v[64:67], v[136:139]\n"
"ds_read_b128 v[64:67], v232 offset:8448\n"
"v_cvt_pk_bf16_f32 v209, v134, v135\n"
"v_mfma_f32_16x16x32_bf16 v[136:139], v[204:207], v[68:71], v[136:139]\n"
"ds_read_b128 v[68:71], v233 offset:8448\n"
"v_mfma_f32_16x16x32_bf16 v[136:139], a[24:27], v[84:87], v[136:139]\n"
"ds_read_b128 v[84:87], v234 offset:8448\n"
"v_mfma_f32_16x16x32_bf16 v[136:139], a[28:31], v[88:91], v[136:139]\n"
"ds_read_b128 v[88:91], v235 offset:8448\n"
"v_mfma_f32_16x16x32_bf16 v[136:139], a[32:35], v[92:95], v[136:139]\n"
"ds_read_b128 v[92:95], v232 offset:16640\n"
"v_mfma_f32_16x16x32_bf16 v[136:139], a[36:39], v[108:111], v[136:139]\n"
"ds_read_b128 v[108:111], v233 offset:16640\n"
"v_mfma_f32_16x16x32_bf16 v[136:139], a[40:43], v[112:115], v[136:139]\n"
"ds_read_b128 v[112:115], v234 offset:16640\n"
"v_mfma_f32_16x16x32_bf16 v[136:139], a[44:47], v[116:119], v[136:139]\n"
"ds_read_b128 v[116:119], v235 offset:16640\n"
"s_waitcnt lgkmcnt(8)\n"
"v_mfma_f32_16x16x32_bf16 v[132:135], a[240:243], v[36:39], v[0:3]\n"
"ds_write_b64 v244, v[208:209] offset:24704\n"
"v_mfma_f32_16x16x32_bf16 v[132:135], a[244:247], v[40:43], v[132:135]\n"
"s_add_u32 m0, s31, 0x7f00\n"
"v_mfma_f32_16x16x32_bf16 v[132:135], a[248:251], v[44:47], v[132:135]\n"
"global_load_lds_dwordx4 v237, s[52:53] offset:256 nt\n"
"v_mfma_f32_16x16x32_bf16 v[132:135], a[252:255], v[60:63], v[132:135]\n"
"v_cvt_pk_bf16_f32 v210, v136, v137\n"
"s_waitcnt lgkmcnt(5)\n"
"v_mfma_f32_16x16x32_bf16 v[132:135], a[48:51], v[64:67], v[132:135]\n"
"v_cvt_pk_bf16_f32 v211, v138, v139\n"
"v_mfma_f32_16x16x32_bf16 v[132:135], a[52:55], v[68:71], v[132:135]\n"
"v_mfma_f32_16x16x32_bf16 v[132:135], a[56:59], v[84:87], v[132:135]\n"
"ds_read_b128 v[216:219], v246 offset:12288\n"
"v_mfma_f32_16x16x32_bf16 v[132:135], a[60:63], v[88:91], v[132:135]\n"
"ds_read_b128 v[220:223], v246 offset:13312\n"
"s_waitcnt lgkmcnt(3)\n"
"v_mfma_f32_16x16x32_bf16 v[132:135], a[64:67], v[92:95], v[132:135]\n"
"ds_read_b128 v[224:227], v246 offset:14336\n"
"v_mfma_f32_16x16x32_bf16 v[132:135], a[68:71], v[108:111], v[132:135]\n"
"ds_write_b64 v244, v[210:211] offset:24712\n"
"v_mfma_f32_16x16x32_bf16 v[132:135], a[72:75], v[112:115], v[132:135]\n"
"v_mfma_f32_16x16x32_bf16 v[132:135], a[76:79], v[116:119], v[132:135]\n"
"v_mfma_f32_16x16x32_bf16 v[136:139], a[124:127], v[116:119], v[4:7]\n"
"s_add_u32 s83, s83, s86\n"
"v_mfma_f32_16x16x32_bf16 v[136:139], a[120:123], v[112:115], v[136:139]\n"
"s_sub_i32 s88, s88, 6\n"
"v_mfma_f32_16x16x32_bf16 v[136:139], a[116:119], v[108:111], v[136:139]\n"
"s_max_i32 s88, s88, 0\n"
"v_mfma_f32_16x16x32_bf16 v[136:139], a[112:115], v[92:95], v[136:139]\n"
"v_cvt_pk_bf16_f32 v208, v132, v133\n"
"v_mfma_f32_16x16x32_bf16 v[136:139], a[108:111], v[88:91], v[136:139]\n"
"v_cvt_pk_bf16_f32 v209, v134, v135\n"
"v_mfma_f32_16x16x32_bf16 v[136:139], a[104:107], v[84:87], v[136:139]\n"
"s_add_u32 m0, s31, 0x9e00\n"
"v_mfma_f32_16x16x32_bf16 v[136:139], a[100:103], v[68:71], v[136:139]\n"
"global_load_lds_dwordx4 v237, s[52:53] offset:512 nt\n"
"v_mfma_f32_16x16x32_bf16 v[136:139], a[96:99], v[64:67], v[136:139]\n"
"s_waitcnt lgkmcnt(1)\n"
"v_mfma_f32_16x16x32_bf16 v[136:139], a[92:95], v[60:63], v[136:139]\n"
"v_mfma_f32_16x16x32_bf16 v[136:139], a[88:91], v[44:47], v[136:139]\n"
"global_store_dwordx4 v247, v[216:219], s[54:55] offset:768 sc1\n"
"ds_write_b64 v244, v[208:209] offset:0\n"
"v_mfma_f32_16x16x32_bf16 v[136:139], a[84:87], v[40:43], v[136:139]\n"
"v_mfma_f32_16x16x32_bf16 v[136:139], a[80:83], v[36:39], v[136:139]\n"
"v_mfma_f32_16x16x32_bf16 v[132:135], a[128:131], v[36:39], v[8:11]\n"
"v_mfma_f32_16x16x32_bf16 v[132:135], a[132:135], v[40:43], v[132:135]\n"
"v_mfma_f32_16x16x32_bf16 v[132:135], a[136:139], v[44:47], v[132:135]\n"
"v_mfma_f32_16x16x32_bf16 v[132:135], a[140:143], v[60:63], v[132:135]\n"
"v_cvt_pk_bf16_f32 v210, v136, v137\n"
"v_mfma_f32_16x16x32_bf16 v[132:135], a[144:147], v[64:67], v[132:135]\n"
"v_cvt_pk_bf16_f32 v211, v138, v139\n"
"v_mfma_f32_16x16x32_bf16 v[132:135], a[148:151], v[68:71], v[132:135]\n"
"v_mfma_f32_16x16x32_bf16 v[132:135], a[152:155], v[84:87], v[132:135]\n"
"v_mfma_f32_16x16x32_bf16 v[132:135], a[156:159], v[88:91], v[132:135]\n"
"v_mfma_f32_16x16x32_bf16 v[132:135], a[160:163], v[92:95], v[132:135]\n"
"v_mfma_f32_16x16x32_bf16 v[132:135], a[164:167], v[108:111], v[132:135]\n"
"s_memrealtime s[84:85]\n"
"v_mfma_f32_16x16x32_bf16 v[132:135], a[168:171], v[112:115], v[132:135]\n"
"global_store_dwordx4 v252, v[220:223], s[54:55] offset:768 sc1\n"
"ds_write_b64 v244, v[210:211] offset:8\n"
"v_mfma_f32_16x16x32_bf16 v[132:135], a[172:175], v[116:119], v[132:135]\n"
"v_mfma_f32_16x16x32_bf16 v[136:139], a[220:223], v[116:119], v[12:15]\n"
"v_mfma_f32_16x16x32_bf16 v[136:139], a[216:219], v[112:115], v[136:139]\n"
"v_mfma_f32_16x16x32_bf16 v[136:139], a[212:215], v[108:111], v[136:139]\n"
"v_mfma_f32_16x16x32_bf16 v[136:139], a[208:211], v[92:95], v[136:139]\n"
"v_cvt_pk_bf16_f32 v212, v132, v133\n"
"v_mfma_f32_16x16x32_bf16 v[136:139], a[204:207], v[88:91], v[136:139]\n"
"v_mov_b32 v242, 6\n"
"v_mfma_f32_16x16x32_bf16 v[136:139], a[200:203], v[84:87], v[136:139]\n"
"v_cvt_pk_bf16_f32 v213, v134, v135\n"
"v_mfma_f32_16x16x32_bf16 v[136:139], a[196:199], v[68:71], v[136:139]\n"
"s_waitcnt vmcnt(20) lgkmcnt(2)\n"
"ds_write_b32 v240, v242\n"
"v_mfma_f32_16x16x32_bf16 v[136:139], a[192:195], v[64:67], v[136:139]\n"
"s_add_u32 m0, s31, 0x7000\n"
"v_mfma_f32_16x16x32_bf16 v[136:139], a[188:191], v[60:63], v[136:139]\n"
"global_load_lds_dwordx4 v238, s[52:53] offset:0 nt\n"
"v_mfma_f32_16x16x32_bf16 v[136:139], a[184:187], v[44:47], v[136:139]\n"
"v_mfma_f32_16x16x32_bf16 v[136:139], a[180:183], v[40:43], v[136:139]\n"
"global_store_dwordx4 v253, v[224:227], s[54:55] offset:768 sc1\n"
"ds_write_b64 v245, v[212:213] offset:0\n"
"v_mfma_f32_16x16x32_bf16 v[136:139], a[176:179], v[36:39], v[136:139]\n"
"v_mfma_f32_16x16x32_bf16 v[132:135], a[224:227], v[36:39], v[16:19]\n"
"v_mfma_f32_16x16x32_bf16 v[132:135], a[228:231], v[40:43], v[132:135]\n"
"v_mfma_f32_16x16x32_bf16 v[132:135], a[232:235], v[44:47], v[132:135]\n"
"ds_read_b32 v243, v241\n"
"v_mfma_f32_16x16x32_bf16 v[132:135], a[236:239], v[60:63], v[132:135]\n"
"v_cvt_pk_bf16_f32 v214, v136, v137\n"
"v_mfma_f32_16x16x32_bf16 v[132:135], a[0:3], v[64:67], v[132:135]\n"
"v_cvt_pk_bf16_f32 v215, v138, v139\n"
"v_mfma_f32_16x16x32_bf16 v[132:135], a[4:7], v[68:71], v[132:135]\n"
"v_mfma_f32_16x16x32_bf16 v[132:135], a[8:11], v[84:87], v[132:135]\n"
"s_waitcnt lgkmcnt(0)\n"
"v_cmp_gt_u32 s[56:57], 6, v243\n"
"v_mfma_f32_16x16x32_bf16 v[132:135], a[12:15], v[88:91], v[132:135]\n"
"s_sub_u32 s84, s84, s83\n"
"v_mfma_f32_16x16x32_bf16 v[132:135], a[16:19], v[92:95], v[132:135]\n"
"s_or_b64 s[56:57], s[56:57], vcc\n"
"s_cbranch_scc1 LB6\n"
"LB6R:\n"
"v_mfma_f32_16x16x32_bf16 v[132:135], a[20:23], v[108:111], v[132:135]\n"
"s_cmp_lt_i32 s84, s88\n"
"s_cselect_b64 vcc, -1, 0\n"
"v_mfma_f32_16x16x32_bf16 v[132:135], v[140:143], v[112:115], v[132:135]\n"
"ds_write_b64 v245, v[214:215] offset:8\n"
"v_mfma_f32_16x16x32_bf16 v[132:135], v[156:159], v[116:119], v[132:135]\n"
"v_mfma_f32_16x16x32_bf16 v[136:139], a[44:47], v[116:119], v[20:23]\n"
"ds_read_b128 v[116:119], v235 offset:40960\n"
"v_mfma_f32_16x16x32_bf16 v[136:139], a[40:43], v[112:115], v[136:139]\n"
"ds_read_b128 v[112:115], v234 offset:40960\n"
"v_mfma_f32_16x16x32_bf16 v[136:139], a[36:39], v[108:111], v[136:139]\n"
"ds_read_b128 v[108:111], v233 offset:40960\n"
"v_mfma_f32_16x16x32_bf16 v[136:139], a[32:35], v[92:95], v[136:139]\n"
"ds_read_b128 v[92:95], v232 offset:40960\n"
"v_cvt_pk_bf16_f32 v208, v132, v133\n"
"v_mfma_f32_16x16x32_bf16 v[136:139], a[28:31], v[88:91], v[136:139]\n"
"ds_read_b128 v[88:91], v235 offset:32768\n"
"v_cvt_pk_bf16_f32 v209, v134, v135\n"
"v_mfma_f32_16x16x32_bf16 v[136:139], a[24:27], v[84:87], v[136:139]\n"
"ds_read_b128 v[84:87], v234 offset:32768\n"
"v_mfma_f32_16x16x32_bf16 v[136:139], v[204:207], v[68:71], v[136:139]\n"
"ds_read_b128 v[68:71], v233 offset:32768\n"
"v_mfma_f32_16x16x32_bf16 v[136:139], v[188:191], v[64:67], v[136:139]\n"
"ds_read_b128 v[64:67], v232 offset:32768\n"
"v_mfma_f32_16x16x32_bf16 v[136:139], v[184:187], v[60:63], v[136:139]\n"
"ds_read_b128 v[60:63], v235 offset:24576\n"
"v_mfma_f32_16x16x32_bf16 v[136:139], v[180:183], v[44:47], v[136:139]\n"
"ds_read_b128 v[44:47], v234 offset:24576\n"
"v_mfma_f32_16x16x32_bf16 v[136:139], v[164:167], v[40:43], v[136:139]\n"
"ds_read_b128 v[40:43], v233 offset:24576\n"
"v_mfma_f32_16x16x32_bf16 v[136:139], v[160:163], v[36:39], v[136:139]\n"
"ds_read_b128 v[36:39], v232 offset:24576\n"
"s_waitcnt lgkmcnt(8)\n"
"v_mfma_f32_16x16x32_bf16 v[132:135], a[76:79], v[116:119], v[0:3]\n"
"ds_write_b64 v244, v[208:209] offset:128\n"
"v_mfma_f32_16x16x32_bf16 v[132:135], a[72:75], v[112:115], v[132:135]\n"
"s_add_u32 m0, s31, 0x8f00\n"
"v_mfma_f32_16x16x32_bf16 v[132:135], a[68:71], v[108:111], v[132:135]\n"
"global_load_lds_dwordx4 v238, s[52:53] offset:256 nt\n"
"v_mfma_f32_16x16x32_bf16 v[132:135], a[64:67], v[92:95], v[132:135]\n"
"v_cvt_pk_bf16_f32 v210, v136, v137\n"
"s_waitcnt lgkmcnt(5)\n"
"v_mfma_f32_16x16x32_bf16 v[132:135], a[60:63], v[88:91], v[132:135]\n"
"v_cvt_pk_bf16_f32 v211, v138, v139\n"
"v_mfma_f32_16x16x32_bf16 v[132:135], a[56:59], v[84:87], v[132:135]\n"
"s_add_u32 s54, s26, s34\n"
"s_addc_u32 s55, s27, 0\n"
"v_mfma_f32_16x16x32_bf16 v[132:135], a[52:55], v[68:71], v[132:135]\n"
"ds_read_b128 v[216:219], v246 offset:24576\n"
"v_mfma_f32_16x16x32_bf16 v[132:135], a[48:51], v[64:67], v[132:135]\n"
"ds_read_b128 v[220:223], v246 offset:25600\n"
"s_waitcnt lgkmcnt(3)\n"
"v_mfma_f32_16x16x32_bf16 v[132:135], a[252:255], v[60:63], v[132:135]\n"
"ds_read_b128 v[224:227], v246 offset:26624\n"
"v_mfma_f32_16x16x32_bf16 v[132:135], a[248:251], v[44:47], v[132:135]\n"
"ds_write_b64 v244, v[210:211] offset:136\n"
"v_mfma_f32_16x16x32_bf16 v[132:135], a[244:247], v[40:43], v[132:135]\n"
"s_sub_i32 s84, s84, s19\n"
"v_mfma_f32_16x16x32_bf16 v[132:135], a[240:243], v[36:39], v[132:135]\n"
"s_max_i32 s88, s88, s84\n"
"v_mfma_f32_16x16x32_bf16 v[136:139], a[80:83], v[36:39], v[4:7]\n"
"v_mfma_f32_16x16x32_bf16 v[136:139], a[84:87], v[40:43], v[136:139]\n"
"v_mfma_f32_16x16x32_bf16 v[136:139], a[88:91], v[44:47], v[136:139]\n"
"v_mfma_f32_16x16x32_bf16 v[136:139], a[92:95], v[60:63], v[136:139]\n"
"v_cvt_pk_bf16_f32 v208, v132, v133\n"
"v_mfma_f32_16x16x32_bf16 v[136:139], a[96:99], v[64:67], v[136:139]\n"
"v_cvt_pk_bf16_f32 v209, v134, v135\n"
"v_mfma_f32_16x16x32_bf16 v[136:139], a[100:103], v[68:71], v[136:139]\n"
"v_mfma_f32_16x16x32_bf16 v[136:139], a[104:107], v[84:87], v[136:139]\n"
"v_mfma_f32_16x16x32_bf16 v[136:139], a[108:111], v[88:91], v[136:139]\n"
"s_waitcnt lgkmcnt(1)\n"
"v_mfma_f32_16x16x32_bf16 v[136:139], a[112:115], v[92:95], v[136:139]\n"
"v_mfma_f32_16x16x32_bf16 v[136:139], a[116:119], v[108:111], v[136:139]\n"
"global_store_dwordx4 v247, v[216:219], s[54:55] offset:0 sc1\n"
"ds_write_b64 v244, v[208:209] offset:12288\n"
"v_mfma_f32_16x16x32_bf16 v[136:139], a[120:123], v[112:115], v[136:139]\n"
"s_add_u32 m0, s31, 0xae00\n"
"v_mfma_f32_16x16x32_bf16 v[136:139], a[124:127], v[116:119], v[136:139]\n"
"global_load_lds_dwordx4 v238, s[52:53] offset:512 nt\n"
"v_mfma_f32_16x16x32_bf16 v[132:135], a[172:175], v[116:119], v[8:11]\n"
"v_min_u32 v28, 0x370, v239\n"
"v_mfma_f32_16x16x32_bf16 v[132:135], a[168:171], v[112:115], v[132:135]\n"
"v_mfma_f32_16x16x32_bf16 v[132:135], a[164:167], v[108:111], v[132:135]\n"
"v_mfma_f32_16x16x32_bf16 v[132:135], a[160:163], v[92:95], v[132:135]\n"
"v_cvt_pk_bf16_f32 v210, v136, v137\n"
"v_mfma_f32_16x16x32_bf16 v[132:135], a[156:159], v[88:91], v[132:135]\n"
"v_cvt_pk_bf16_f32 v211, v138, v139\n"
"v_mfma_f32_16x16x32_bf16 v[132:135], a[152:155], v[84:87], v[132:135]\n"
"v_add_u32 v28, 0x2480, v28\n"
"v_mfma_f32_16x16x32_bf16 v[132:135], a[148:151], v[68:71], v[132:135]\n"
"global_load_dwordx4 v[24:27], v28, s[28:29] sc1\n"
"v_mfma_f32_16x16x32_bf16 v[132:135], a[144:147], v[64:67], v[132:135]\n"
"v_mfma_f32_16x16x32_bf16 v[132:135], a[140:143], v[60:63], v[132:135]\n"
"v_mfma_f32_16x16x32_bf16 v[132:135], a[136:139], v[44:47], v[132:135]\n"
"global_store_dwordx4 v252, v[220:223], s[54:55] offset:0 sc1\n"
"ds_write_b64 v244, v[210:211] offset:12296\n"
"v_mfma_f32_16x16x32_bf16 v[132:135], a[132:135], v[40:43], v[132:135]\n"
"v_mfma_f32_16x16x32_bf16 v[132:135], a[128:131], v[36:39], v[132:135]\n"
"v_mfma_f32_16x16x32_bf16 v[136:139], a[176:179], v[36:39], v[12:15]\n"
"v_mfma_f32_16x16x32_bf16 v[136:139], a[180:183], v[40:43], v[136:139]\n"
"v_mfma_f32_16x16x32_bf16 v[136:139], a[184:187], v[44:47], v[136:139]\n"
"v_mfma_f32_16x16x32_bf16 v[136:139], a[188:191], v[60:63], v[136:139]\n"
"v_cvt_pk_bf16_f32 v212, v132, v133\n"
"v_mfma_f32_16x16x32_bf16 v[136:139], a[192:195], v[64:67], v[136:139]\n"
"v_mov_b32 v242, 7\n"
"v_mfma_f32_16x16x32_bf16 v[136:139], a[196:199], v[68:71], v[136:139]\n"
"v_cvt_pk_bf16_f32 v213, v134, v135\n"
"v_mfma_f32_16x16x32_bf16 v[136:139], a[200:203], v[84:87], v[136:139]\n"
"s_waitcnt lgkmcnt(2)\n"
"ds_write_b32 v240, v242\n"
"v_mfma_f32_16x16x32_bf16 v[136:139], a[204:207], v[88:91], v[136:139]\n"
"v_mfma_f32_16x16x32_bf16 v[136:139], a[208:211], v[92:95], v[136:139]\n"
"v_mfma_f32_16x16x32_bf16 v[136:139], a[212:215], v[108:111], v[136:139]\n"
"v_mfma_f32_16x16x32_bf16 v[136:139], a[216:219], v[112:115], v[136:139]\n"
"global_store_dwordx4 v253, v[224:227], s[54:55] offset:0 sc1\n"
"ds_write_b64 v245, v[212:213] offset:12288\n"
"v_mfma_f32_16x16x32_bf16 v[136:139], a[220:223], v[116:119], v[136:139]\n"
"s_add_u32 s52, s20, s38\n"
"s_addc_u32 s53, s21, 0\n"
"v_mfma_f32_16x16x32_bf16 v[132:135], v[156:159], v[116:119], v[16:19]\n"
"s_add_u32 m0, s31, 0xc000\n"
"v_mfma_f32_16x16x32_bf16 v[132:135], v[140:143], v[112:115], v[132:135]\n"
"global_load_lds_dwordx4 v237, s[52:53] offset:0 nt\n"
"v_mfma_f32_16x16x32_bf16 v[132:135], a[20:23], v[108:111], v[132:135]\n"
"ds_read_b32 v243, v241\n"
"v_mfma_f32_16x16x32_bf16 v[132:135], a[16:19], v[92:95], v[132:135]\n"
"v_cvt_pk_bf16_f32 v214, v136, v137\n"
"v_mfma_f32_16x16x32_bf16 v[132:135], a[12:15], v[88:91], v[132:135]\n"
"v_cvt_pk_bf16_f32 v215, v138, v139\n"
"v_mfma_f32_16x16x32_bf16 v[132:135], a[8:11], v[84:87], v[132:135]\n"
"v_mfma_f32_16x16x32_bf16 v[132:135], a[4:7], v[68:71], v[132:135]\n"
"s_waitcnt lgkmcnt(0)\n"
"v_cmp_gt_u32 s[56:57], 7, v243\n"
"v_mfma_f32_16x16x32_bf16 v[132:135], a[0:3], v[64:67], v[132:135]\n"
"v_mfma_f32_16x16x32_bf16 v[132:135], a[236:239], v[60:63], v[132:135]\n"
"s_or_b64 s[56:57], s[56:57], vcc\n"
"s_cbranch_scc1 LB7\n"
"LB7R:\n"
"v_mfma_f32_16x16x32_bf16 v[132:135], a[232:235], v[44:47], v[132:135]\n"
"ds_write_b64 v245, v[214:215] offset:12296\n"
"v_mfma_f32_16x16x32_bf16 v[132:135], a[228:231], v[40:43], v[132:135]\n"
"v_mfma_f32_16x16x32_bf16 v[132:135], a[224:227], v[36:39], v[132:135]\n"
"v_mfma_f32_16x16x32_bf16 v[136:139], v[160:163], v[36:39], v[20:23]\n"
"ds_read_b128 v[36:39], v232 offset:24832\n"
"v_mfma_f32_16x16x32_bf16 v[136:139], v[164:167], v[40:43], v[136:139]\n"
"ds_read_b128 v[40:43], v233 offset:24832\n"
"v_mfma_f32_16x16x32_bf16 v[136:139], v[180:183], v[44:47], v[136:139]\n"
"ds_read_b128 v[44:47], v234 offset:24832\n"
"v_mfma_f32_16x16x32_bf16 v[136:139], v[184:187], v[60:63], v[136:139]\n"
"ds_read_b128 v[60:63], v235 offset:24832\n"
"v_cvt_pk_bf16_f32 v208, v132, v133\n"
"v_mfma_f32_16x16x32_bf16 v[136:139], v[188:191], v[64:67], v[136:139]\n"
"ds_read_b128 v[64:67], v232 offset:33024\n"
"v_cvt_pk_bf16_f32 v209, v134, v135\n"
"v_mfma_f32_16x16x32_bf16 v[136:139], v[204:207], v[68:71], v[136:139]\n"
"ds_read_b128 v[68:71], v233 offset:33024\n"
"v_mfma_f32_16x16x32_bf16 v[136:139], a[24:27], v[84:87], v[136:139]\n"
"ds_read_b128 v[84:87], v234 offset:33024\n"
"v_mfma_f32_16x16x32_bf16 v[136:139], a[28:31], v[88:91], v[136:139]\n"
"ds_read_b128 v[88:91], v235 offset:33024\n"
"v_mfma_f32_16x16x32_bf16 v[136:139], a[32:35], v[92:95], v[136:139]\n"
"ds_read_b128 v[92:95], v232 offset:41216\n"
"v_mfma_f32_16x16x32_bf16 v[136:139], a[36:39], v[108:111], v[136:139]\n"
"ds_read_b128 v[108:111], v233 offset:41216\n"
"ds_write_b64 v244, v[208:209] offset:12416\n"
"v_mfma_f32_16x16x32_bf16 v[136:139], a[40:43], v[112:115], v[136:139]\n"
"ds_read_b128 v[112:115], v234 offset:41216\n"
"v_mfma_f32_16x16x32_bf16 v[136:139], a[44:47], v[116:119], v[136:139]\n"
"ds_read_b128 v[116:119], v235 offset:41216\n"
"s_waitcnt lgkmcnt(9)\n"
"v_mfma_f32_16x16x32_bf16 v[132:135], a[240:243], v[36:39], v[0:3]\n"
"v_add_u32 v232, 0xffff4000, v232\n"
"v_mfma_f32_16x16x32_bf16 v[132:135], a[244:247], v[40:43], v[132:135]\n"
"v_add_u32 v233, 0xffff4000, v233\n"
"v_mfma_f32_16x16x32_bf16 v[132:135], a[248:251], v[44:47], v[132:135]\n"
"v_add_u32 v234, 0xffff4000, v234\n"
"v_mfma_f32_16x16x32_bf16 v[132:135], a[252:255], v[60:63], v[132:135]\n"
"v_add_u32 v235, 0xffff4000, v235\n"
"s_waitcnt lgkmcnt(5)\n"
"v_mfma_f32_16x16x32_bf16 v[132:135], a[48:51], v[64:67], v[132:135]\n"
"s_add_u32 m0, s31, 0xdf00\n"
"v_mfma_f32_16x16x32_bf16 v[132:135], a[52:55], v[68:71], v[132:135]\n"
"global_load_lds_dwordx4 v237, s[52:53] offset:256 nt\n"
"v_mfma_f32_16x16x32_bf16 v[132:135], a[56:59], v[84:87], v[132:135]\n"
"v_cvt_pk_bf16_f32 v210, v136, v137\n"
"v_mfma_f32_16x16x32_bf16 v[132:135], a[60:63], v[88:91], v[132:135]\n"
"v_cvt_pk_bf16_f32 v211, v138, v139\n"
"s_waitcnt lgkmcnt(0)\n"
"v_mfma_f32_16x16x32_bf16 v[132:135], a[64:67], v[92:95], v[132:135]\n"
"ds_read_b128 v[216:219], v246 offset:0\n"
"v_mfma_f32_16x16x32_bf16 v[132:135], a[68:71], v[108:111], v[132:135]\n"
"ds_read_b128 v[220:223], v246 offset:1024\n"
"v_mfma_f32_16x16x32_bf16 v[132:135], a[72:75], v[112:115], v[132:135]\n"
"ds_read_b128 v[224:227], v246 offset:2048\n"
"v_mfma_f32_16x16x32_bf16 v[132:135], a[76:79], v[116:119], v[132:135]\n"
"v_lshlrev_b32 v0, 16, v48\n"
"v_mfma_f32_16x16x32_bf16 v[136:139], a[124:127], v[116:119], v[4:7]\n"
"ds_write_b64 v244, v[210:211] offset:12424\n"
"v_mfma_f32_16x16x32_bf16 v[136:139], a[120:123], v[112:115], v[136:139]\n"
"v_and_b32 v1, 0xffff0000, v48\n"
"v_mfma_f32_16x16x32_bf16 v[136:139], a[116:119], v[108:111], v[136:139]\n"
"v_lshlrev_b32 v2, 16, v49\n"
"v_mfma_f32_16x16x32_bf16 v[136:139], a[112:115], v[92:95], v[136:139]\n"
"v_cvt_pk_bf16_f32 v208, v132, v133\n"
"v_mfma_f32_16x16x32_bf16 v[136:139], a[108:111], v[88:91], v[136:139]\n"
"v_cvt_pk_bf16_f32 v209, v134, v135\n"
"v_mfma_f32_16x16x32_bf16 v[136:139], a[104:107], v[84:87], v[136:139]\n"
"v_and_b32 v3, 0xffff0000, v49\n"
"v_mfma_f32_16x16x32_bf16 v[136:139], a[100:103], v[68:71], v[136:139]\n"
"v_lshlrev_b32 v4, 16, v50\n"
"v_mfma_f32_16x16x32_bf16 v[136:139], a[96:99], v[64:67], v[136:139]\n"
"s_waitcnt lgkmcnt(1)\n"
"v_mfma_f32_16x16x32_bf16 v[136:139], a[92:95], v[60:63], v[136:139]\n"
"v_and_b32 v5, 0xffff0000, v50\n"
"v_mfma_f32_16x16x32_bf16 v[136:139], a[88:91], v[44:47], v[136:139]\n"
"global_store_dwordx4 v247, v[216:219], s[54:55] offset:768 sc1\n"
"ds_write_b64 v244, v[208:209] offset:24576\n"
"v_mfma_f32_16x16x32_bf16 v[136:139], a[84:87], v[40:43], v[136:139]\n"
"s_add_u32 m0, s31, 0xfe00\n"
"v_mfma_f32_16x16x32_bf16 v[136:139], a[80:83], v[36:39], v[136:139]\n"
"global_load_lds_dwordx4 v237, s[52:53] offset:512 nt\n"
"v_mfma_f32_16x16x32_bf16 v[132:135], a[128:131], v[36:39], v[8:11]\n"
"v_lshlrev_b32 v6, 16, v51\n"
"v_mfma_f32_16x16x32_bf16 v[132:135], a[132:135], v[40:43], v[132:135]\n"
"v_and_b32 v7, 0xffff0000, v51\n"
"v_mfma_f32_16x16x32_bf16 v[132:135], a[136:139], v[44:47], v[132:135]\n"
"s_add_u32 s83, s83, s86\n"
"v_mfma_f32_16x16x32_bf16 v[132:135], a[140:143], v[60:63], v[132:135]\n"
"v_cvt_pk_bf16_f32 v210, v136, v137\n"
"v_mfma_f32_16x16x32_bf16 v[132:135], a[144:147], v[64:67], v[132:135]\n"
"v_cvt_pk_bf16_f32 v211, v138, v139\n"
"v_mfma_f32_16x16x32_bf16 v[132:135], a[148:151], v[68:71], v[132:135]\n"
"v_lshlrev_b32 v8, 16, v52\n"
"v_mfma_f32_16x16x32_bf16 v[132:135], a[152:155], v[84:87], v[132:135]\n"
"v_and_b32 v9, 0xffff0000, v52\n"
"v_mfma_f32_16x16x32_bf16 v[132:135], a[156:159], v[88:91], v[132:135]\n"
"v_lshlrev_b32 v10, 16, v53\n"
"v_mfma_f32_16x16x32_bf16 v[132:135], a[160:163], v[92:95], v[132:135]\n"
"v_and_b32 v11, 0xffff0000, v53\n"
"v_mfma_f32_16x16x32_bf16 v[132:135], a[164:167], v[108:111], v[132:135]\n"
"s_memrealtime s[84:85]\n"
"v_mfma_f32_16x16x32_bf16 v[132:135], a[168:171], v[112:115], v[132:135]\n"
"global_store_dwordx4 v252, v[220:223], s[54:55] offset:768 sc1\n"
"ds_write_b64 v244, v[210:211] offset:24584\n"
"v_mfma_f32_16x16x32_bf16 v[132:135], a[172:175], v[116:119], v[132:135]\n"
"s_sub_i32 s88, s88, 6\n"
"v_mfma_f32_16x16x32_bf16 v[136:139], a[220:223], v[116:119], v[12:15]\n"
"s_max_i32 s88, s88, 0\n"
"v_mfma_f32_16x16x32_bf16 v[136:139], a[216:219], v[112:115], v[136:139]\n"
"s_mov_b64 exec, s[10:11]\n"
"s_cbranch_execz LHT7\n"
"LHR7:\n"
"s_mov_b64 exec, -1\n"
"v_mfma_f32_16x16x32_bf16 v[136:139], a[212:215], v[108:111], v[136:139]\n"
"v_mfma_f32_16x16x32_bf16 v[136:139], a[208:211], v[92:95], v[136:139]\n"
"v_cvt_pk_bf16_f32 v212, v132, v133\n"
"v_mfma_f32_16x16x32_bf16 v[136:139], a[204:207], v[88:91], v[136:139]\n"
"v_mov_b32 v242, 8\n"
"v_mfma_f32_16x16x32_bf16 v[136:139], a[200:203], v[84:87], v[136:139]\n"
"v_cvt_pk_bf16_f32 v213, v134, v135\n"
"v_mfma_f32_16x16x32_bf16 v[136:139], a[196:199], v[68:71], v[136:139]\n"
"s_waitcnt vmcnt(21) lgkmcnt(2)\n"
"ds_write_b32 v240, v242\n"
"v_mfma_f32_16x16x32_bf16 v[136:139], a[192:195], v[64:67], v[136:139]\n"
"v_lshlrev_b32 v12, 16, v54\n"
"v_mfma_f32_16x16x32_bf16 v[136:139], a[188:191], v[60:63], v[136:139]\n"
"v_and_b32 v13, 0xffff0000, v54\n"
"v_mfma_f32_16x16x32_bf16 v[136:139], a[184:187], v[44:47], v[136:139]\n"
"v_lshlrev_b32 v14, 16, v55\n"
"v_mfma_f32_16x16x32_bf16 v[136:139], a[180:183], v[40:43], v[136:139]\n"
"global_store_dwordx4 v253, v[224:227], s[54:55] offset:768 sc1\n"
"ds_write_b64 v245, v[212:213] offset:24576\n"
"v_mfma_f32_16x16x32_bf16 v[136:139], a[176:179], v[36:39], v[136:139]\n"
"s_add_u32 m0, s31, 0xd000\n"
"v_mfma_f32_16x16x32_bf16 v[132:135], a[224:227], v[36:39], v[16:19]\n"
"global_load_lds_dwordx4 v238, s[52:53] offset:0 nt\n"
"v_mfma_f32_16x16x32_bf16 v[132:135], a[228:231], v[40:43], v[132:135]\n"
"v_and_b32 v15, 0xffff0000, v55\n"
"v_mfma_f32_16x16x32_bf16 v[132:135], a[232:235], v[44:47], v[132:135]\n"
"ds_read_b32 v243, v241\n"
"v_mfma_f32_16x16x32_bf16 v[132:135], a[236:239], v[60:63], v[132:135]\n"
"v_cvt_pk_bf16_f32 v214, v136, v137\n"
"v_mfma_f32_16x16x32_bf16 v[132:135], a[0:3], v[64:67], v[132:135]\n"
"v_cvt_pk_bf16_f32 v215, v138, v139\n"
"v_mfma_f32_16x16x32_bf16 v[132:135], a[4:7], v[68:71], v[132:135]\n"
"v_lshlrev_b32 v16, 16, v56\n"
"v_mfma_f32_16x16x32_bf16 v[132:135], a[8:11], v[84:87], v[132:135]\n"
"s_waitcnt lgkmcnt(0)\n"
"v_cmp_gt_u32 s[56:57], 8, v243\n"
"v_mfma_f32_16x16x32_bf16 v[132:135], a[12:15], v[88:91], v[132:135]\n"
"s_sub_u32 s84, s84, s83\n"
"v_mfma_f32_16x16x32_bf16 v[132:135], a[16:19], v[92:95], v[132:135]\n"
"s_or_b64 s[56:57], s[56:57], vcc\n"
"s_cbranch_scc1 LB8\n"
"LB8R:\n"
"v_mfma_f32_16x16x32_bf16 v[132:135], a[20:23], v[108:111], v[132:135]\n"
"s_cmp_lt_i32 s84, s88\n"
"s_cselect_b64 vcc, -1, 0\n"
"v_mfma_f32_16x16x32_bf16 v[132:135], v[140:143], v[112:115], v[132:135]\n"
"ds_write_b64 v245, v[214:215] offset:24584\n"
"v_mfma_f32_16x16x32_bf16 v[132:135], v[156:159], v[116:119], v[132:135]\n"
"v_and_b32 v17, 0xffff0000, v56\n"
"v_mfma_f32_16x16x32_bf16 v[136:139], a[44:47], v[116:119], v[20:23]\n"
"ds_read_b128 v[116:119], v235 offset:16384\n"
"v_mfma_f32_16x16x32_bf16 v[136:139], a[40:43], v[112:115], v[136:139]\n"
"ds_read_b128 v[112:115], v234 offset:16384\n"
"v_mfma_f32_16x16x32_bf16 v[136:139], a[36:39], v[108:111], v[136:139]\n"
"ds_read_b128 v[108:111], v233 offset:16384\n"
"v_mfma_f32_16x16x32_bf16 v[136:139], a[32:35], v[92:95], v[136:139]\n"
"ds_read_b128 v[92:95], v232 offset:16384\n"
"v_cvt_pk_bf16_f32 v208, v132, v133\n"
"v_mfma_f32_16x16x32_bf16 v[136:139], a[28:31], v[88:91], v[136:139]\n"
"ds_read_b128 v[88:91], v235 offset:8192\n"
"v_cvt_pk_bf16_f32 v209, v134, v135\n"
"v_mfma_f32_16x16x32_bf16 v[136:139], a[24:27], v[84:87], v[136:139]\n"
"ds_read_b128 v[84:87], v234 offset:8192\n"
"v_mfma_f32_16x16x32_bf16 v[136:139], v[204:207], v[68:71], v[136:139]\n"
"ds_read_b128 v[68:71], v233 offset:8192\n"
"v_mfma_f32_16x16x32_bf16 v[136:139], v[188:191], v[64:67], v[136:139]\n"
"ds_read_b128 v[64:67], v232 offset:8192\n"
"v_mfma_f32_16x16x32_bf16 v[136:139], v[184:187], v[60:63], v[136:139]\n"
"ds_read_b128 v[60:63], v235 offset:0\n"
"v_mfma_f32_16x16x32_bf16 v[136:139], v[180:183], v[44:47], v[136:139]\n"
"ds_read_b128 v[44:47], v234 offset:0\n"
"v_mfma_f32_16x16x32_bf16 v[136:139], v[164:167], v[40:43], v[136:139]\n"
"ds_read_b128 v[40:43], v233 offset:0\n"
"v_mfma_f32_16x16x32_bf16 v[136:139], v[160:163], v[36:39], v[136:139]\n"
"ds_read_b128 v[36:39], v232 offset:0\n"
"s_waitcnt lgkmcnt(8)\n"
"v_mfma_f32_16x16x32_bf16 v[132:135], a[76:79], v[116:119], v[0:3]\n"
"ds_write_b64 v244, v[208:209] offset:24704\n"
"v_mfma_f32_16x16x32_bf16 v[132:135], a[72:75], v[112:115], v[132:135]\n"
"v_lshlrev_b32 v18, 16, v57\n"
"v_mfma_f32_16x16x32_bf16 v[132:135], a[68:71], v[108:111], v[132:135]\n"
"v_and_b32 v19, 0xffff0000, v57\n"
"v_mfma_f32_16x16x32_bf16 v[132:135], a[64:67], v[92:95], v[132:135]\n"
"v_lshlrev_b32 v20, 16, v58\n"
"s_waitcnt lgkmcnt(5)\n"
"v_mfma_f32_16x16x32_bf16 v[132:135], a[60:63], v[88:91], v[132:135]\n"
"v_and_b32 v21, 0xffff0000, v58\n"
"v_mfma_f32_16x16x32_bf16 v[132:135], a[56:59], v[84:87], v[132:135]\n"
"v_lshlrev_b32 v22, 16, v59\n"
"v_mfma_f32_16x16x32_bf16 v[132:135], a[52:55], v[68:71], v[132:135]\n"
"v_and_b32 v23, 0xffff0000, v59\n"
"v_mfma_f32_16x16x32_bf16 v[132:135], a[48:51], v[64:67], v[132:135]\n"
"v_cvt_pk_bf16_f32 v210, v136, v137\n"
"s_waitcnt lgkmcnt(1)\n"
"v_mfma_f32_16x16x32_bf16 v[132:135], a[252:255], v[60:63], v[132:135]\n"
"v_cvt_pk_bf16_f32 v211, v138, v139\n"
"v_mfma_f32_16x16x32_bf16 v[132:135], a[248:251], v[44:47], v[132:135]\n"
"s_add_u32 s54, s26, s35\n"
"s_addc_u32 s55, s27, 0\n"
"v_mfma_f32_16x16x32_bf16 v[132:135], a[244:247], v[40:43], v[132:135]\n"
"ds_read_b128 v[216:219], v246 offset:12288\n"
"v_mfma_f32_16x16x32_bf16 v[132:135], a[240:243], v[36:39], v[132:135]\n"
"ds_read_b128 v[220:223], v246 offset:13312\n"
"v_mfma_f32_16x16x32_bf16 v[136:139], a[80:83], v[36:39], v[4:7]\n"
"ds_read_b128 v[224:227], v246 offset:14336\n"
"v_mfma_f32_16x16x32_bf16 v[136:139], a[84:87], v[40:43], v[136:139]\n"
"ds_write_b64 v244, v[210:211] offset:24712\n"
"LHT7:\n"
"s_cbranch_execz LHR7\n"
"v_mfma_f32_16x16x32_bf16 v[136:139], a[88:91], v[44:47], v[136:139]\n"
"s_add_u32 m0, s31, 0xef00\n"
"v_mfma_f32_16x16x32_bf16 v[136:139], a[92:95], v[60:63], v[136:139]\n"
"v_cvt_pk_bf16_f32 v208, v132, v133\n"
"v_mfma_f32_16x16x32_bf16 v[136:139], a[96:99], v[64:67], v[136:139]\n"
"v_cvt_pk_bf16_f32 v209, v134, v135\n"
"v_mfma_f32_16x16x32_bf16 v[136:139], a[100:103], v[68:71], v[136:139]\n"
"global_load_lds_dwordx4 v238, s[52:53] offset:256 nt\n"
"v_mfma_f32_16x16x32_bf16 v[136:139], a[104:107], v[84:87], v[136:139]\n"
"s_sub_i32 s84, s84, s19\n"
"v_mfma_f32_16x16x32_bf16 v[136:139], a[108:111], v[88:91], v[136:139]\n"
"s_waitcnt lgkmcnt(1)\n"
"v_mfma_f32_16x16x32_bf16 v[136:139], a[112:115], v[92:95], v[136:139]\n"
"s_max_i32 s88, s88, s84\n"
"v_mfma_f32_16x16x32_bf16 v[136:139], a[116:119], v[108:111], v[136:139]\n"
"global_store_dwordx4 v247, v[216:219], s[54:55] offset:0 sc1\n"
"ds_write_b64 v244, v[208:209] offset:0\n"
"v_mfma_f32_16x16x32_bf16 v[136:139], a[120:123], v[112:115], v[136:139]\n"
"v_mfma_f32_16x16x32_bf16 v[136:139], a[124:127], v[116:119], v[136:139]\n"
"v_mfma_f32_16x16x32_bf16 v[132:135], a[172:175], v[116:119], v[8:11]\n"
"v_mfma_f32_16x16x32_bf16 v[132:135], a[168:171], v[112:115], v[132:135]\n"
"s_add_u32 m0, s31, 0x10e00\n"
"v_mfma_f32_16x16x32_bf16 v[132:135], a[164:167], v[108:111], v[132:135]\n"
"global_load_lds_dwordx4 v238, s[52:53] offset:512 nt\n"
"v_mfma_f32_16x16x32_bf16 v[132:135], a[160:163], v[92:95], v[132:135]\n"
"v_cvt_pk_bf16_f32 v210, v136, v137\n"
"v_mfma_f32_16x16x32_bf16 v[132:135], a[156:159], v[88:91], v[132:135]\n"
"v_cvt_pk_bf16_f32 v211, v138, v139\n"
"v_mfma_f32_16x16x32_bf16 v[132:135], a[152:155], v[84:87], v[132:135]\n"
"v_mfma_f32_16x16x32_bf16 v[132:135], a[148:151], v[68:71], v[132:135]\n"
"v_mfma_f32_16x16x32_bf16 v[132:135], a[144:147], v[64:67], v[132:135]\n"
"v_mfma_f32_16x16x32_bf16 v[132:135], a[140:143], v[60:63], v[132:135]\n"
"v_mfma_f32_16x16x32_bf16 v[132:135], a[136:139], v[44:47], v[132:135]\n"
"global_store_dwordx4 v252, v[220:223], s[54:55] offset:0 sc1\n"
"ds_write_b64 v244, v[210:211] offset:8\n"
"v_mfma_f32_16x16x32_bf16 v[132:135], a[132:135], v[40:43], v[132:135]\n"
"v_mfma_f32_16x16x32_bf16 v[132:135], a[128:131], v[36:39], v[132:135]\n"
"v_mfma_f32_16x16x32_bf16 v[136:139], a[176:179], v[36:39], v[12:15]\n"
"v_mfma_f32_16x16x32_bf16 v[136:139], a[180:183], v[40:43], v[136:139]\n"
"v_mfma_f32_16x16x32_bf16 v[136:139], a[184:187], v[44:47], v[136:139]\n"
"v_mfma_f32_16x16x32_bf16 v[136:139], a[188:191], v[60:63], v[136:139]\n"
"v_cvt_pk_bf16_f32 v212, v132, v133\n"
"v_mfma_f32_16x16x32_bf16 v[136:139], a[192:195], v[64:67], v[136:139]\n"
"v_mov_b32 v242, 9\n"
"v_mfma_f32_16x16x32_bf16 v[136:139], a[196:199], v[68:71], v[136:139]\n"
"v_cvt_pk_bf16_f32 v213, v134, v135\n"
"v_mfma_f32_16x16x32_bf16 v[136:139], a[200:203], v[84:87], v[136:139]\n"
"s_waitcnt lgkmcnt(2)\n"
"ds_write_b32 v240, v242\n"
"v_mfma_f32_16x16x32_bf16 v[136:139], a[204:207], v[88:91], v[136:139]\n"
"v_mfma_f32_16x16x32_bf16 v[136:139], a[208:211], v[92:95], v[136:139]\n"
"v_mfma_f32_16x16x32_bf16 v[136:139], a[212:215], v[108:111], v[136:139]\n"
"v_mfma_f32_16x16x32_bf16 v[136:139], a[216:219], v[112:115], v[136:139]\n"
"global_store_dwordx4 v253, v[224:227], s[54:55] offset:0 sc1\n"
"ds_write_b64 v245, v[212:213] offset:0\n"
"v_mfma_f32_16x16x32_bf16 v[136:139], a[220:223], v[116:119], v[136:139]\n"
"v_mfma_f32_16x16x32_bf16 v[132:135], v[156:159], v[116:119], v[16:19]\n"
"v_mfma_f32_16x16x32_bf16 v[132:135], v[140:143], v[112:115], v[132:135]\n"
"s_add_u32 s52, s20, s39\n"
"s_addc_u32 s53, s21, 0\n"
"v_mfma_f32_16x16x32_bf16 v[132:135], a[20:23], v[108:111], v[132:135]\n"
"ds_read_b32 v243, v241\n"
"v_mfma_f32_16x16x32_bf16 v[132:135], a[16:19], v[92:95], v[132:135]\n"
"v_cvt_pk_bf16_f32 v214, v136, v137\n"
"v_mfma_f32_16x16x32_bf16 v[132:135], a[12:15], v[88:91], v[132:135]\n"
"v_cvt_pk_bf16_f32 v215, v138, v139\n"
"v_mfma_f32_16x16x32_bf16 v[132:135], a[8:11], v[84:87], v[132:135]\n"
"s_add_u32 m0, s31, 0x12000\n"
"v_mfma_f32_16x16x32_bf16 v[132:135], a[4:7], v[68:71], v[132:135]\n"
"s_waitcnt lgkmcnt(0)\n"
"v_cmp_gt_u32 s[56:57], 9, v243\n"
"v_mfma_f32_16x16x32_bf16 v[132:135], a[0:3], v[64:67], v[132:135]\n"
"global_load_lds_dwordx4 v237, s[52:53] offset:0 nt\n"
"v_mfma_f32_16x16x32_bf16 v[132:135], a[236:239], v[60:63], v[132:135]\n"
"s_or_b64 s[56:57], s[56:57], vcc\n"
"s_cbranch_scc1 LB9\n"
"LB9R:\n"
"v_mfma_f32_16x16x32_bf16 v[132:135], a[232:235], v[44:47], v[132:135]\n"
"ds_write_b64 v245, v[214:215] offset:8\n"
"v_mfma_f32_16x16x32_bf16 v[132:135], a[228:231], v[40:43], v[132:135]\n"
"v_mfma_f32_16x16x32_bf16 v[132:135], a[224:227], v[36:39], v[132:135]\n"
"v_mfma_f32_16x16x32_bf16 v[136:139], v[160:163], v[36:39], v[20:23]\n"
"ds_read_b128 v[36:39], v232 offset:256\n"
"v_mfma_f32_16x16x32_bf16 v[136:139], v[164:167], v[40:43], v[136:139]\n"
"ds_read_b128 v[40:43], v233 offset:256\n"
"v_mfma_f32_16x16x32_bf16 v[136:139], v[180:183], v[44:47], v[136:139]\n"
"ds_read_b128 v[44:47], v234 offset:256\n"
"v_mfma_f32_16x16x32_bf16 v[136:139], v[184:187], v[60:63], v[136:139]\n"
"ds_read_b128 v[60:63], v235 offset:256\n"
"v_cvt_pk_bf16_f32 v208, v132, v133\n"
"v_mfma_f32_16x16x32_bf16 v[136:139], v[188:191], v[64:67], v[136:139]\n"
"ds_read_b128 v[64:67], v232 offset:8448\n"
"v_cvt_pk_bf16_f32 v209, v134, v135\n"
"v_mfma_f32_16x16x32_bf16 v[136:139], v[204:207], v[68:71], v[136:139]\n"
"ds_read_b128 v[68:71], v233 offset:8448\n"
"v_mfma_f32_16x16x32_bf16 v[136:139], a[24:27], v[84:87], v[136:139]\n"
"ds_read_b128 v[84:87], v234 offset:8448\n"
"v_mfma_f32_16x16x32_bf16 v[136:139], a[28:31], v[88:91], v[136:139]\n"
"ds_read_b128 v[88:91], v235 offset:8448\n"
"v_mfma_f32_16x16x32_bf16 v[136:139], a[32:35], v[92:95], v[136:139]\n"
"ds_read_b128 v[92:95], v232 offset:16640\n"
"v_mfma_f32_16x16x32_bf16 v[136:139], a[36:39], v[108:111], v[136:139]\n"
"ds_read_b128 v[108:111], v233 offset:16640\n"
"v_mfma_f32_16x16x32_bf16 v[136:139], a[40:43], v[112:115], v[136:139]\n"
"ds_read_b128 v[112:115], v234 offset:16640\n"
"v_mfma_f32_16x16x32_bf16 v[136:139], a[44:47], v[116:119], v[136:139]\n"
"ds_read_b128 v[116:119], v235 offset:16640\n"
"s_waitcnt lgkmcnt(8)\n"
"v_mfma_f32_16x16x32_bf16 v[132:135], a[240:243], v[36:39], v[0:3]\n"
"ds_write_b64 v244, v[208:209] offset:128\n"
"v_mfma_f32_16x16x32_bf16 v[132:135], a[244:247], v[40:43], v[132:135]\n"
"v_mfma_f32_16x16x32_bf16 v[132:135], a[248:251], v[44:47], v[132:135]\n"
"v_mfma_f32_16x16x32_bf16 v[132:135], a[252:255], v[60:63], v[132:135]\n"
"v_cvt_pk_bf16_f32 v210, v136, v137\n"
"s_waitcnt lgkmcnt(5)\n"
"v_mfma_f32_16x16x32_bf16 v[132:135], a[48:51], v[64:67], v[132:135]\n"
"v_cvt_pk_bf16_f32 v211, v138, v139\n"
"v_mfma_f32_16x16x32_bf16 v[132:135], a[52:55], v[68:71], v[132:135]\n"
"s_add_u32 m0, s31, 0x13f00\n"
"v_mfma_f32_16x16x32_bf16 v[132:135], a[56:59], v[84:87], v[132:135]\n"
"ds_read_b128 v[216:219], v246 offset:24576\n"
"v_mfma_f32_16x16x32_bf16 v[132:135], a[60:63], v[88:91], v[132:135]\n"
"ds_read_b128 v[220:223], v246 offset:25600\n"
"s_waitcnt lgkmcnt(3)\n"
"v_mfma_f32_16x16x32_bf16 v[132:135], a[64:67], v[92:95], v[132:135]\n"
"ds_read_b128 v[224:227], v246 offset:26624\n"
"v_mfma_f32_16x16x32_bf16 v[132:135], a[68:71], v[108:111], v[132:135]\n"
"ds_write_b64 v244, v[210:211] offset:136\n"
"v_mfma_f32_16x16x32_bf16 v[132:135], a[72:75], v[112:115], v[132:135]\n"
"global_load_lds_dwordx4 v237, s[52:53] offset:256 nt\n"
"v_mfma_f32_16x16x32_bf16 v[132:135], a[76:79], v[116:119], v[132:135]\n"
"v_mfma_f32_16x16x32_bf16 v[136:139], a[124:127], v[116:119], v[4:7]\n"
"s_add_u32 s83, s83, s86\n"
"v_mfma_f32_16x16x32_bf16 v[136:139], a[120:123], v[112:115], v[136:139]\n"
"s_sub_i32 s88, s88, 6\n"
"v_mfma_f32_16x16x32_bf16 v[136:139], a[116:119], v[108:111], v[136:139]\n"
"s_max_i32 s88, s88, 0\n"
"v_mfma_f32_16x16x32_bf16 v[136:139], a[112:115], v[92:95], v[136:139]\n"
"v_cvt_pk_bf16_f32 v208, v132, v133\n"
"v_mfma_f32_16x16x32_bf16 v[136:139], a[108:111], v[88:91], v[136:139]\n"
"v_cvt_pk_bf16_f32 v209, v134, v135\n"
"v_mfma_f32_16x16x32_bf16 v[136:139], a[104:107], v[84:87], v[136:139]\n"
"v_mfma_f32_16x16x32_bf16 v[136:139], a[100:103], v[68:71], v[136:139]\n"
"v_mfma_f32_16x16x32_bf16 v[136:139], a[96:99], v[64:67], v[136:139]\n"
"s_waitcnt lgkmcnt(1)\n"
"v_mfma_f32_16x16x32_bf16 v[136:139], a[92:95], v[60:63], v[136:139]\n"
"v_mfma_f32_16x16x32_bf16 v[136:139], a[88:91], v[44:47], v[136:139]\n"
"global_store_dwordx4 v247, v[216:219], s[54:55] offset:768 sc1\n"
"ds_write_b64 v244, v[208:209] offset:12288\n"
"v_mfma_f32_16x16x32_bf16 v[136:139], a[84:87], v[40:43], v[136:139]\n"
"v_mfma_f32_16x16x32_bf16 v[136:139], a[80:83], v[36:39], v[136:139]\n"
"v_mfma_f32_16x16x32_bf16 v[132:135], a[128:131], v[36:39], v[8:11]\n"
"v_mfma_f32_16x16x32_bf16 v[132:135], a[132:135], v[40:43], v[132:135]\n"
"v_mfma_f32_16x16x32_bf16 v[132:135], a[136:139], v[44:47], v[132:135]\n"
"v_mfma_f32_16x16x32_bf16 v[132:135], a[140:143], v[60:63], v[132:135]\n"
"v_cvt_pk_bf16_f32 v210, v136, v137\n"
"v_mfma_f32_16x16x32_bf16 v[132:135], a[144:147], v[64:67], v[132:135]\n"
"v_cvt_pk_bf16_f32 v211, v138, v139\n"
"v_mfma_f32_16x16x32_bf16 v[132:135], a[148:151], v[68:71], v[132:135]\n"
"s_add_u32 m0, s31, 0x15e00\n"
"v_mfma_f32_16x16x32_bf16 v[132:135], a[152:155], v[84:87], v[132:135]\n"
"global_load_lds_dwordx4 v237, s[52:53] offset:512 nt\n"
"v_mfma_f32_16x16x32_bf16 v[132:135], a[156:159], v[88:91], v[132:135]\n"
"v_mfma_f32_16x16x32_bf16 v[132:135], a[160:163], v[92:95], v[132:135]\n"
"v_mfma_f32_16x16x32_bf16 v[132:135], a[164:167], v[108:111], v[132:135]\n"
"s_memrealtime s[84:85]\n"
"v_mfma_f32_16x16x32_bf16 v[132:135], a[168:171], v[112:115], v[132:135]\n"
"global_store_dwordx4 v252, v[220:223], s[54:55] offset:768 sc1\n"
"ds_write_b64 v244, v[210:211] offset:12296\n"
"v_mfma_f32_16x16x32_bf16 v[132:135], a[172:175], v[116:119], v[132:135]\n"
"v_mfma_f32_16x16x32_bf16 v[136:139], a[220:223], v[116:119], v[12:15]\n"
"v_mfma_f32_16x16x32_bf16 v[136:139], a[216:219], v[112:115], v[136:139]\n"
"v_mfma_f32_16x16x32_bf16 v[136:139], a[212:215], v[108:111], v[136:139]\n"
"v_mfma_f32_16x16x32_bf16 v[136:139], a[208:211], v[92:95], v[136:139]\n"
"v_cvt_pk_bf16_f32 v212, v132, v133\n"
"v_mfma_f32_16x16x32_bf16 v[136:139], a[204:207], v[88:91], v[136:139]\n"
"v_mov_b32 v242, 10\n"
"v_mfma_f32_16x16x32_bf16 v[136:139], a[200:203], v[84:87], v[136:139]\n"
"v_cvt_pk_bf16_f32 v213, v134, v135\n"
"v_mfma_f32_16x16x32_bf16 v[136:139], a[196:199], v[68:71], v[136:139]\n"
"s_waitcnt vmcnt(20) lgkmcnt(2)\n"
"ds_write_b32 v240, v242\n"
"v_mfma_f32_16x16x32_bf16 v[136:139], a[192:195], v[64:67], v[136:139]\n"
"v_mfma_f32_16x16x32_bf16 v[136:139], a[188:191], v[60:63], v[136:139]\n"
"v_mfma_f32_16x16x32_bf16 v[136:139], a[184:187], v[44:47], v[136:139]\n"
"v_mfma_f32_16x16x32_bf16 v[136:139], a[180:183], v[40:43], v[136:139]\n"
"global_store_dwordx4 v253, v[224:227], s[54:55] offset:768 sc1\n"
"ds_write_b64 v245, v[212:213] offset:12288\n"
"v_mfma_f32_16x16x32_bf16 v[136:139], a[176:179], v[36:39], v[136:139]\n"
"v_mfma_f32_16x16x32_bf16 v[132:135], a[224:227], v[36:39], v[16:19]\n"
"v_mfma_f32_16x16x32_bf16 v[132:135], a[228:231], v[40:43], v[132:135]\n"
"v_mfma_f32_16x16x32_bf16 v[132:135], a[232:235], v[44:47], v[132:135]\n"
"ds_read_b32 v243, v241\n"
"v_mfma_f32_16x16x32_bf16 v[132:135], a[236:239], v[60:63], v[132:135]\n"
"v_cvt_pk_bf16_f32 v214, v136, v137\n"
"v_mfma_f32_16x16x32_bf16 v[132:135], a[0:3], v[64:67], v[132:135]\n"
"v_cvt_pk_bf16_f32 v215, v138, v139\n"
"v_mfma_f32_16x16x32_bf16 v[132:135], a[4:7], v[68:71], v[132:135]\n"
"v_mfma_f32_16x16x32_bf16 v[132:135], a[8:11], v[84:87], v[132:135]\n"
"s_waitcnt lgkmcnt(0)\n"
"v_cmp_gt_u32 s[56:57], 10, v243\n"
"v_mfma_f32_16x16x32_bf16 v[132:135], a[12:15], v[88:91], v[132:135]\n"
"s_sub_u32 s84, s84, s83\n"
"v_mfma_f32_16x16x32_bf16 v[132:135], a[16:19], v[92:95], v[132:135]\n"
"s_or_b64 s[56:57], s[56:57], vcc\n"
"s_cbranch_scc1 LB10\n"
"LB10R:\n"
"v_mfma_f32_16x16x32_bf16 v[132:135], a[20:23], v[108:111], v[132:135]\n"
"s_cmp_lt_i32 s84, s88\n"
"s_cselect_b64 vcc, -1, 0\n"
"v_mfma_f32_16x16x32_bf16 v[132:135], v[140:143], v[112:115], v[132:135]\n"
"ds_write_b64 v245, v[214:215] offset:12296\n"
"v_mfma_f32_16x16x32_bf16 v[132:135], v[156:159], v[116:119], v[132:135]\n"
"s_add_u32 m0, s31, 0x13000\n"
"v_mfma_f32_16x16x32_bf16 v[136:139], a[44:47], v[116:119], v[20:23]\n"
"ds_read_b128 v[116:119], v235 offset:40960\n"
"v_mfma_f32_16x16x32_bf16 v[136:139], a[40:43], v[112:115], v[136:139]\n"
"ds_read_b128 v[112:115], v234 offset:40960\n"
"v_mfma_f32_16x16x32_bf16 v[136:139], a[36:39], v[108:111], v[136:139]\n"
"ds_read_b128 v[108:111], v233 offset:40960\n"
"v_mfma_f32_16x16x32_bf16 v[136:139], a[32:35], v[92:95], v[136:139]\n"
"ds_read_b128 v[92:95], v232 offset:40960\n"
"v_cvt_pk_bf16_f32 v208, v132, v133\n"
"v_mfma_f32_16x16x32_bf16 v[136:139], a[28:31], v[88:91], v[136:139]\n"
"ds_read_b128 v[88:91], v235 offset:32768\n"
"v_cvt_pk_bf16_f32 v209, v134, v135\n"
"v_mfma_f32_16x16x32_bf16 v[136:139], a[24:27], v[84:87], v[136:139]\n"
"ds_read_b128 v[84:87], v234 offset:32768\n"
"v_mfma_f32_16x16x32_bf16 v[136:139], v[204:207], v[68:71], v[136:139]\n"
"ds_read_b128 v[68:71], v233 offset:32768\n"
"v_mfma_f32_16x16x32_bf16 v[136:139], v[188:191], v[64:67], v[136:139]\n"
"ds_read_b128 v[64:67], v232 offset:32768\n"
"v_mfma_f32_16x16x32_bf16 v[136:139], v[184:187], v[60:63], v[136:139]\n"
"ds_read_b128 v[60:63], v235 offset:24576\n"
"v_mfma_f32_16x16x32_bf16 v[136:139], v[180:183], v[44:47], v[136:139]\n"
"ds_read_b128 v[44:47], v234 offset:24576\n"
"v_mfma_f32_16x16x32_bf16 v[136:139], v[164:167], v[40:43], v[136:139]\n"
"ds_read_b128 v[40:43], v233 offset:24576\n"
"v_mfma_f32_16x16x32_bf16 v[136:139], v[160:163], v[36:39], v[136:139]\n"
"ds_read_b128 v[36:39], v232 offset:24576\n"
"s_waitcnt lgkmcnt(8)\n"
"v_mfma_f32_16x16x32_bf16 v[132:135], a[76:79], v[116:119], v[0:3]\n"
"ds_write_b64 v244, v[208:209] offset:12416\n"
"v_mfma_f32_16x16x32_bf16 v[132:135], a[72:75], v[112:115], v[132:135]\n"
"global_load_lds_dwordx4 v238, s[52:53] offset:0 nt\n"
"v_mfma_f32_16x16x32_bf16 v[132:135], a[68:71], v[108:111], v[132:135]\n"
"s_add_u32 s54, s26, s36\n"
"s_addc_u32 s55, s27, 0\n"
"v_mfma_f32_16x16x32_bf16 v[132:135], a[64:67], v[92:95], v[132:135]\n"
"v_cvt_pk_bf16_f32 v210, v136, v137\n"
"s_waitcnt lgkmcnt(5)\n"
"v_mfma_f32_16x16x32_bf16 v[132:135], a[60:63], v[88:91], v[132:135]\n"
"v_cvt_pk_bf16_f32 v211, v138, v139\n"
"v_mfma_f32_16x16x32_bf16 v[132:135], a[56:59], v[84:87], v[132:135]\n"
"s_sub_i32 s84, s84, s19\n"
"v_mfma_f32_16x16x32_bf16 v[132:135], a[52:55], v[68:71], v[132:135]\n"
"ds_read_b128 v[216:219], v246 offset:0\n"
"v_mfma_f32_16x16x32_bf16 v[132:135], a[48:51], v[64:67], v[132:135]\n"
"ds_read_b128 v[220:223], v246 offset:1024\n"
"s_waitcnt lgkmcnt(3)\n"
"v_mfma_f32_16x16x32_bf16 v[132:135], a[252:255], v[60:63], v[132:135]\n"
"ds_read_b128 v[224:227], v246 offset:2048\n"
"v_mfma_f32_16x16x32_bf16 v[132:135], a[248:251], v[44:47], v[132:135]\n"
"ds_write_b64 v244, v[210:211] offset:12424\n"
"v_mfma_f32_16x16x32_bf16 v[132:135], a[244:247], v[40:43], v[132:135]\n"
"s_add_u32 m0, s31, 0x14f00\n"
"v_mfma_f32_16x16x32_bf16 v[132:135], a[240:243], v[36:39], v[132:135]\n"
"global_load_lds_dwordx4 v238, s[52:53] offset:256 nt\n"
"v_mfma_f32_16x16x32_bf16 v[136:139], a[80:83], v[36:39], v[4:7]\n"
"s_max_i32 s88, s88, s84\n"
"v_mfma_f32_16x16x32_bf16 v[136:139], a[84:87], v[40:43], v[136:139]\n"
"v_mfma_f32_16x16x32_bf16 v[136:139], a[88:91], v[44:47], v[136:139]\n"
"v_mfma_f32_16x16x32_bf16 v[136:139], a[92:95], v[60:63], v[136:139]\n"
"v_cvt_pk_bf16_f32 v208, v132, v133\n"
"v_mfma_f32_16x16x32_bf16 v[136:139], a[96:99], v[64:67], v[136:139]\n"
"v_cvt_pk_bf16_f32 v209, v134, v135\n"
"v_mfma_f32_16x16x32_bf16 v[136:139], a[100:103], v[68:71], v[136:139]\n"
"v_mfma_f32_16x16x32_bf16 v[136:139], a[104:107], v[84:87], v[136:139]\n"
"v_mfma_f32_16x16x32_bf16 v[136:139], a[108:111], v[88:91], v[136:139]\n"
"s_waitcnt lgkmcnt(1)\n"
"v_mfma_f32_16x16x32_bf16 v[136:139], a[112:115], v[92:95], v[136:139]\n"
"v_mfma_f32_16x16x32_bf16 v[136:139], a[116:119], v[108:111], v[136:139]\n"
"global_store_dwordx4 v247, v[216:219], s[54:55] offset:0 sc1\n"
"ds_write_b64 v244, v[208:209] offset:24576\n"
"v_mfma_f32_16x16x32_bf16 v[136:139], a[120:123], v[112:115], v[136:139]\n"
"v_mfma_f32_16x16x32_bf16 v[136:139], a[124:127], v[116:119], v[136:139]\n"
"v_mfma_f32_16x16x32_bf16 v[132:135], a[172:175], v[116:119], v[8:11]\n"
"v_mfma_f32_16x16x32_bf16 v[132:135], a[168:171], v[112:115], v[132:135]\n"
"v_mfma_f32_16x16x32_bf16 v[132:135], a[164:167], v[108:111], v[132:135]\n"
"v_mfma_f32_16x16x32_bf16 v[132:135], a[160:163], v[92:95], v[132:135]\n"
"v_cvt_pk_bf16_f32 v210, v136, v137\n"
"v_mfma_f32_16x16x32_bf16 v[132:135], a[156:159], v[88:91], v[132:135]\n"
"v_cvt_pk_bf16_f32 v211, v138, v139\n"
"v_mfma_f32_16x16x32_bf16 v[132:135], a[152:155], v[84:87], v[132:135]\n"
"v_mfma_f32_16x16x32_bf16 v[132:135], a[148:151], v[68:71], v[132:135]\n"
"v_mfma_f32_16x16x32_bf16 v[132:135], a[144:147], v[64:67], v[132:135]\n"
"v_mfma_f32_16x16x32_bf16 v[132:135], a[140:143], v[60:63], v[132:135]\n"
"v_mfma_f32_16x16x32_bf16 v[132:135], a[136:139], v[44:47], v[132:135]\n"
"global_store_dwordx4 v252, v[220:223], s[54:55] offset:0 sc1\n"
"ds_write_b64 v244, v[210:211] offset:24584\n"
"v_mfma_f32_16x16x32_bf16 v[132:135], a[132:135], v[40:43], v[132:135]\n"
"s_add_u32 m0, s31, 0x16e00\n"
"v_mfma_f32_16x16x32_bf16 v[132:135], a[128:131], v[36:39], v[132:135]\n"
"global_load_lds_dwordx4 v238, s[52:53] offset:512 nt\n"
"v_mfma_f32_16x16x32_bf16 v[136:139], a[176:179], v[36:39], v[12:15]\n"
"v_mfma_f32_16x16x32_bf16 v[136:139], a[180:183], v[40:43], v[136:139]\n"
"v_mfma_f32_16x16x32_bf16 v[136:139], a[184:187], v[44:47], v[136:139]\n"
"v_mfma_f32_16x16x32_bf16 v[136:139], a[188:191], v[60:63], v[136:139]\n"
"v_cvt_pk_bf16_f32 v212, v132, v133\n"
"v_mfma_f32_16x16x32_bf16 v[136:139], a[192:195], v[64:67], v[136:139]\n"
"v_mov_b32 v242, 11\n"
"v_mfma_f32_16x16x32_bf16 v[136:139], a[196:199], v[68:71], v[136:139]\n"
"v_cvt_pk_bf16_f32 v213, v134, v135\n"
"v_mfma_f32_16x16x32_bf16 v[136:139], a[200:203], v[84:87], v[136:139]\n"
"s_waitcnt lgkmcnt(2)\n"
"ds_write_b32 v240, v242\n"
"v_mfma_f32_16x16x32_bf16 v[136:139], a[204:207], v[88:91], v[136:139]\n"
"v_mfma_f32_16x16x32_bf16 v[136:139], a[208:211], v[92:95], v[136:139]\n"
"v_mfma_f32_16x16x32_bf16 v[136:139], a[212:215], v[108:111], v[136:139]\n"
"v_mfma_f32_16x16x32_bf16 v[136:139], a[216:219], v[112:115], v[136:139]\n"
"global_store_dwordx4 v253, v[224:227], s[54:55] offset:0 sc1\n"
"ds_write_b64 v245, v[212:213] offset:24576\n"
"v_mfma_f32_16x16x32_bf16 v[136:139], a[220:223], v[116:119], v[136:139]\n"
"v_mfma_f32_16x16x32_bf16 v[132:135], v[156:159], v[116:119], v[16:19]\n"
"v_mfma_f32_16x16x32_bf16 v[132:135], v[140:143], v[112:115], v[132:135]\n"
"v_mfma_f32_16x16x32_bf16 v[132:135], a[20:23], v[108:111], v[132:135]\n"
"ds_read_b32 v243, v241\n"
"v_mfma_f32_16x16x32_bf16 v[132:135], a[16:19], v[92:95], v[132:135]\n"
"v_cvt_pk_bf16_f32 v214, v136, v137\n"
"v_mfma_f32_16x16x32_bf16 v[132:135], a[12:15], v[88:91], v[132:135]\n"
"v_cvt_pk_bf16_f32 v215, v138, v139\n"
"v_mfma_f32_16x16x32_bf16 v[132:135], a[8:11], v[84:87], v[132:135]\n"
"v_mfma_f32_16x16x32_bf16 v[132:135], a[4:7], v[68:71], v[132:135]\n"
"s_waitcnt lgkmcnt(0)\n"
"v_cmp_gt_u32 s[56:57], 11, v243\n"
"v_mfma_f32_16x16x32_bf16 v[132:135], a[0:3], v[64:67], v[132:135]\n"
"v_mfma_f32_16x16x32_bf16 v[132:135], a[236:239], v[60:63], v[132:135]\n"
"s_or_b64 s[56:57], s[56:57], vcc\n"
"s_cbranch_scc1 LB11\n"
"LB11R:\n"
"v_mfma_f32_16x16x32_bf16 v[132:135], a[232:235], v[44:47], v[132:135]\n"
"ds_write_b64 v245, v[214:215] offset:24584\n"
"v_mfma_f32_16x16x32_bf16 v[132:135], a[228:231], v[40:43], v[132:135]\n"
"v_mfma_f32_16x16x32_bf16 v[132:135], a[224:227], v[36:39], v[132:135]\n"
"v_mfma_f32_16x16x32_bf16 v[136:139], v[160:163], v[36:39], v[20:23]\n"
"ds_read_b128 v[36:39], v232 offset:24832\n"
"v_mfma_f32_16x16x32_bf16 v[136:139], v[164:167], v[40:43], v[136:139]\n"
"ds_read_b128 v[40:43], v233 offset:24832\n"
"v_mfma_f32_16x16x32_bf16 v[136:139], v[180:183], v[44:47], v[136:139]\n"
"ds_read_b128 v[44:47], v234 offset:24832\n"
"v_mfma_f32_16x16x32_bf16 v[136:139], v[184:187], v[60:63], v[136:139]\n"
"ds_read_b128 v[60:63], v235 offset:24832\n"
"v_cvt_pk_bf16_f32 v208, v132, v133\n"
"v_mfma_f32_16x16x32_bf16 v[136:139], v[188:191], v[64:67], v[136:139]\n"
"ds_read_b128 v[64:67], v232 offset:33024\n"
"v_cvt_pk_bf16_f32 v209, v134, v135\n"
"v_mfma_f32_16x16x32_bf16 v[136:139], v[204:207], v[68:71], v[136:139]\n"
"ds_read_b128 v[68:71], v233 offset:33024\n"
"v_mfma_f32_16x16x32_bf16 v[136:139], a[24:27], v[84:87], v[136:139]\n"
"ds_read_b128 v[84:87], v234 offset:33024\n"
"v_mfma_f32_16x16x32_bf16 v[136:139], a[28:31], v[88:91], v[136:139]\n"
"ds_read_b128 v[88:91], v235 offset:33024\n"
"v_mfma_f32_16x16x32_bf16 v[136:139], a[32:35], v[92:95], v[136:139]\n"
"ds_read_b128 v[92:95], v232 offset:41216\n"
"v_mfma_f32_16x16x32_bf16 v[136:139], a[36:39], v[108:111], v[136:139]\n"
"ds_read_b128 v[108:111], v233 offset:41216\n"
"ds_write_b64 v244, v[208:209] offset:24704\n"
"v_mfma_f32_16x16x32_bf16 v[136:139], a[40:43], v[112:115], v[136:139]\n"
"ds_read_b128 v[112:115], v234 offset:41216\n"
"v_mfma_f32_16x16x32_bf16 v[136:139], a[44:47], v[116:119], v[136:139]\n"
"ds_read_b128 v[116:119], v235 offset:41216\n"
"s_waitcnt lgkmcnt(9)\n"
"v_mfma_f32_16x16x32_bf16 v[132:135], a[240:243], v[36:39], v[0:3]\n"
"v_add_u32 v232, 0xc000, v232\n"
"v_mfma_f32_16x16x32_bf16 v[132:135], a[244:247], v[40:43], v[132:135]\n"
"v_add_u32 v233, 0xc000, v233\n"
"v_mfma_f32_16x16x32_bf16 v[132:135], a[248:251], v[44:47], v[132:135]\n"
"v_add_u32 v234, 0xc000, v234\n"
"v_mfma_f32_16x16x32_bf16 v[132:135], a[252:255], v[60:63], v[132:135]\n"
"v_add_u32 v235, 0xc000, v235\n"
"s_waitcnt lgkmcnt(5)\n"
"v_mfma_f32_16x16x32_bf16 v[132:135], a[48:51], v[64:67], v[132:135]\n"
"s_add_u32 s52, s20, s40\n"
"s_addc_u32 s53, s21, 0\n"
"v_mfma_f32_16x16x32_bf16 v[132:135], a[52:55], v[68:71], v[132:135]\n"
"s_add_u32 m0, s31, 0x0\n"
"v_mfma_f32_16x16x32_bf16 v[132:135], a[56:59], v[84:87], v[132:135]\n"
"global_load_lds_dwordx4 v237, s[52:53] offset:0 nt\n"
"v_mfma_f32_16x16x32_bf16 v[132:135], a[60:63], v[88:91], v[132:135]\n"
"v_cvt_pk_bf16_f32 v210, v136, v137\n"
"s_waitcnt lgkmcnt(0)\n"
"v_mfma_f32_16x16x32_bf16 v[132:135], a[64:67], v[92:95], v[132:135]\n"
"v_cvt_pk_bf16_f32 v211, v138, v139\n"
"v_mfma_f32_16x16x32_bf16 v[132:135], a[68:71], v[108:111], v[132:135]\n"
"ds_read_b128 v[216:219], v246 offset:12288\n"
"v_mfma_f32_16x16x32_bf16 v[132:135], a[72:75], v[112:115], v[132:135]\n"
"ds_read_b128 v[220:223], v246 offset:13312\n"
"v_mfma_f32_16x16x32_bf16 v[132:135], a[76:79], v[116:119], v[132:135]\n"
"ds_read_b128 v[224:227], v246 offset:14336\n"
"v_mfma_f32_16x16x32_bf16 v[136:139], a[124:127], v[116:119], v[4:7]\n"
"s_add_u32 m0, s31, 0x1f00\n"
"v_mfma_f32_16x16x32_bf16 v[136:139], a[120:123], v[112:115], v[136:139]\n"
"ds_write_b64 v244, v[210:211] offset:24712\n"
"v_mfma_f32_16x16x32_bf16 v[136:139], a[116:119], v[108:111], v[136:139]\n"
"global_load_lds_dwordx4 v237, s[52:53] offset:256 nt\n"
"v_mfma_f32_16x16x32_bf16 v[136:139], a[112:115], v[92:95], v[136:139]\n"
"v_cvt_pk_bf16_f32 v208, v132, v133\n"
"v_mfma_f32_16x16x32_bf16 v[136:139], a[108:111], v[88:91], v[136:139]\n"
"v_cvt_pk_bf16_f32 v209, v134, v135\n"
"v_mfma_f32_16x16x32_bf16 v[136:139], a[104:107], v[84:87], v[136:139]\n"
"v_lshlrev_b32 v0, 16, v72\n"
"v_mfma_f32_16x16x32_bf16 v[136:139], a[100:103], v[68:71], v[136:139]\n"
"v_and_b32 v1, 0xffff0000, v72\n"
"v_mfma_f32_16x16x32_bf16 v[136:139], a[96:99], v[64:67], v[136:139]\n"
"s_waitcnt lgkmcnt(1)\n"
"v_mfma_f32_16x16x32_bf16 v[136:139], a[92:95], v[60:63], v[136:139]\n"
"v_lshlrev_b32 v2, 16, v73\n"
"v_mfma_f32_16x16x32_bf16 v[136:139], a[88:91], v[44:47], v[136:139]\n"
"global_store_dwordx4 v247, v[216:219], s[54:55] offset:768 sc1\n"
"ds_write_b64 v244, v[208:209] offset:0\n"
"v_mfma_f32_16x16x32_bf16 v[136:139], a[84:87], v[40:43], v[136:139]\n"
"v_and_b32 v3, 0xffff0000, v73\n"
"v_mfma_f32_16x16x32_bf16 v[136:139], a[80:83], v[36:39], v[136:139]\n"
"v_lshlrev_b32 v4, 16, v74\n"
"v_mfma_f32_16x16x32_bf16 v[132:135], a[128:131], v[36:39], v[8:11]\n"
"v_and_b32 v5, 0xffff0000, v74\n"
"v_mfma_f32_16x16x32_bf16 v[132:135], a[132:135], v[40:43], v[132:135]\n"
"v_lshlrev_b32 v6, 16, v75\n"
"v_mfma_f32_16x16x32_bf16 v[132:135], a[136:139], v[44:47], v[132:135]\n"
"v_and_b32 v7, 0xffff0000, v75\n"
"v_mfma_f32_16x16x32_bf16 v[132:135], a[140:143], v[60:63], v[132:135]\n"
"v_cvt_pk_bf16_f32 v210, v136, v137\n"
"v_mfma_f32_16x16x32_bf16 v[132:135], a[144:147], v[64:67], v[132:135]\n"
"v_cvt_pk_bf16_f32 v211, v138, v139\n"
"v_mfma_f32_16x16x32_bf16 v[132:135], a[148:151], v[68:71], v[132:135]\n"
"v_lshlrev_b32 v8, 16, v76\n"
"v_mfma_f32_16x16x32_bf16 v[132:135], a[152:155], v[84:87], v[132:135]\n"
"v_and_b32 v9, 0xffff0000, v76\n"
"v_mfma_f32_16x16x32_bf16 v[132:135], a[156:159], v[88:91], v[132:135]\n"
"v_lshlrev_b32 v10, 16, v77\n"
"s_mov_b64 exec, s[4:5]\n"
"s_cbranch_execz LHT8\n"
"LHR8:\n"
"s_mov_b64 exec, -1\n"
"v_mfma_f32_16x16x32_bf16 v[132:135], a[160:163], v[92:95], v[132:135]\n"
"v_and_b32 v11, 0xffff0000, v77\n"
"v_mfma_f32_16x16x32_bf16 v[132:135], a[164:167], v[108:111], v[132:135]\n"
"s_memrealtime s[84:85]\n"
"v_mfma_f32_16x16x32_bf16 v[132:135], a[168:171], v[112:115], v[132:135]\n"
"global_store_dwordx4 v252, v[220:223], s[54:55] offset:768 sc1\n"
"ds_write_b64 v244, v[210:211] offset:8\n"
"v_mfma_f32_16x16x32_bf16 v[132:135], a[172:175], v[116:119], v[132:135]\n"
"s_add_u32 m0, s31, 0x3e00\n"
"v_mfma_f32_16x16x32_bf16 v[136:139], a[220:223], v[116:119], v[12:15]\n"
"global_load_lds_dwordx4 v237, s[52:53] offset:512 nt\n"
"v_mfma_f32_16x16x32_bf16 v[136:139], a[216:219], v[112:115], v[136:139]\n"
"s_add_u32 s83, s83, s86\n"
"v_mfma_f32_16x16x32_bf16 v[136:139], a[212:215], v[108:111], v[136:139]\n"
"s_sub_i32 s88, s88, 6\n"
"v_mfma_f32_16x16x32_bf16 v[136:139], a[208:211], v[92:95], v[136:139]\n"
"v_cvt_pk_bf16_f32 v212, v132, v133\n"
"v_mfma_f32_16x16x32_bf16 v[136:139], a[204:207], v[88:91], v[136:139]\n"
"v_mov_b32 v242, 12\n"
"v_mfma_f32_16x16x32_bf16 v[136:139], a[200:203], v[84:87], v[136:139]\n"
"v_cvt_pk_bf16_f32 v213, v134, v135\n"
"v_mfma_f32_16x16x32_bf16 v[136:139], a[196:199], v[68:71], v[136:139]\n"
"s_waitcnt vmcnt(19) lgkmcnt(2)\n"
"ds_write_b32 v240, v242\n"
"v_mfma_f32_16x16x32_bf16 v[136:139], a[192:195], v[64:67], v[136:139]\n"
"v_lshlrev_b32 v12, 16, v78\n"
"v_mfma_f32_16x16x32_bf16 v[136:139], a[188:191], v[60:63], v[136:139]\n"
"v_and_b32 v13, 0xffff0000, v78\n"
"v_mfma_f32_16x16x32_bf16 v[136:139], a[184:187], v[44:47], v[136:139]\n"
"v_lshlrev_b32 v14, 16, v79\n"
"v_mfma_f32_16x16x32_bf16 v[136:139], a[180:183], v[40:43], v[136:139]\n"
"global_store_dwordx4 v253, v[224:227], s[54:55] offset:768 sc1\n"
"ds_write_b64 v245, v[212:213] offset:0\n"
"v_mfma_f32_16x16x32_bf16 v[136:139], a[176:179], v[36:39], v[136:139]\n"
"v_and_b32 v15, 0xffff0000, v79\n"
"v_mfma_f32_16x16x32_bf16 v[132:135], a[224:227], v[36:39], v[16:19]\n"
"s_max_i32 s88, s88, 0\n"
"v_mfma_f32_16x16x32_bf16 v[132:135], a[228:231], v[40:43], v[132:135]\n"
"v_mfma_f32_16x16x32_bf16 v[132:135], a[232:235], v[44:47], v[132:135]\n"
"ds_read_b32 v243, v241\n"
"v_mfma_f32_16x16x32_bf16 v[132:135], a[236:239], v[60:63], v[132:135]\n"
"v_cvt_pk_bf16_f32 v214, v136, v137\n"
"v_mfma_f32_16x16x32_bf16 v[132:135], a[0:3], v[64:67], v[132:135]\n"
"v_cvt_pk_bf16_f32 v215, v138, v139\n"
"v_mfma_f32_16x16x32_bf16 v[132:135], a[4:7], v[68:71], v[132:135]\n"
"v_lshlrev_b32 v16, 16, v80\n"
"v_mfma_f32_16x16x32_bf16 v[132:135], a[8:11], v[84:87], v[132:135]\n"
"s_waitcnt lgkmcnt(0)\n"
"v_cmp_gt_u32 s[56:57], 12, v243\n"
"v_mfma_f32_16x16x32_bf16 v[132:135], a[12:15], v[88:91], v[132:135]\n"
"s_sub_u32 s84, s84, s83\n"
"v_mfma_f32_16x16x32_bf16 v[132:135], a[16:19], v[92:95], v[132:135]\n"
"s_or_b64 s[56:57], s[56:57], vcc\n"
"s_cbranch_scc1 LB12\n"
"LB12R:\n"
"v_mfma_f32_16x16x32_bf16 v[132:135], a[20:23], v[108:111], v[132:135]\n"
"s_cmp_lt_i32 s84, s88\n"
"s_cselect_b64 vcc, -1, 0\n"
"v_mfma_f32_16x16x32_bf16 v[132:135], v[140:143], v[112:115], v[132:135]\n"
"ds_write_b64 v245, v[214:215] offset:8\n"
"v_mfma_f32_16x16x32_bf16 v[132:135], v[156:159], v[116:119], v[132:135]\n"
"v_and_b32 v17, 0xffff0000, v80\n"
"v_mfma_f32_16x16x32_bf16 v[136:139], a[44:47], v[116:119], v[20:23]\n"
"ds_read_b128 v[116:119], v235 offset:16384\n"
"v_mfma_f32_16x16x32_bf16 v[136:139], a[40:43], v[112:115], v[136:139]\n"
"ds_read_b128 v[112:115], v234 offset:16384\n"
"v_mfma_f32_16x16x32_bf16 v[136:139], a[36:39], v[108:111], v[136:139]\n"
"ds_read_b128 v[108:111], v233 offset:16384\n"
"v_mfma_f32_16x16x32_bf16 v[136:139], a[32:35], v[92:95], v[136:139]\n"
"ds_read_b128 v[92:95], v232 offset:16384\n"
"v_cvt_pk_bf16_f32 v208, v132, v133\n"
"v_mfma_f32_16x16x32_bf16 v[136:139], a[28:31], v[88:91], v[136:139]\n"
"ds_read_b128 v[88:91], v235 offset:8192\n"
"v_cvt_pk_bf16_f32 v209, v134, v135\n"
"v_mfma_f32_16x16x32_bf16 v[136:139], a[24:27], v[84:87], v[136:139]\n"
"ds_read_b128 v[84:87], v234 offset:8192\n"
"v_mfma_f32_16x16x32_bf16 v[136:139], v[204:207], v[68:71], v[136:139]\n"
"ds_read_b128 v[68:71], v233 offset:8192\n"
"v_mfma_f32_16x16x32_bf16 v[136:139], v[188:191], v[64:67], v[136:139]\n"
"ds_read_b128 v[64:67], v232 offset:8192\n"
"v_mfma_f32_16x16x32_bf16 v[136:139], v[184:187], v[60:63], v[136:139]\n"
"ds_read_b128 v[60:63], v235 offset:0\n"
"v_mfma_f32_16x16x32_bf16 v[136:139], v[180:183], v[44:47], v[136:139]\n"
"ds_read_b128 v[44:47], v234 offset:0\n"
"v_mfma_f32_16x16x32_bf16 v[136:139], v[164:167], v[40:43], v[136:139]\n"
"ds_read_b128 v[40:43], v233 offset:0\n"
"v_mfma_f32_16x16x32_bf16 v[136:139], v[160:163], v[36:39], v[136:139]\n"
"ds_read_b128 v[36:39], v232 offset:0\n"
"s_waitcnt lgkmcnt(8)\n"
"v_mfma_f32_16x16x32_bf16 v[132:135], a[76:79], v[116:119], v[0:3]\n"
"ds_write_b64 v244, v[208:209] offset:128\n"
"v_mfma_f32_16x16x32_bf16 v[132:135], a[72:75], v[112:115], v[132:135]\n"
"s_add_u32 m0, s31, 0x1000\n"
"v_mfma_f32_16x16x32_bf16 v[132:135], a[68:71], v[108:111], v[132:135]\n"
"global_load_lds_dwordx4 v238, s[52:53] offset:0 nt\n"
"v_mfma_f32_16x16x32_bf16 v[132:135], a[64:67], v[92:95], v[132:135]\n"
"v_lshlrev_b32 v18, 16, v81\n"
"v_cvt_pk_bf16_f32 v211, v138, v139\n"
"s_waitcnt lgkmcnt(5)\n"
"v_mfma_f32_16x16x32_bf16 v[132:135], a[60:63], v[88:91], v[132:135]\n"
"v_and_b32 v19, 0xffff0000, v81\n"
"v_mfma_f32_16x16x32_bf16 v[132:135], a[56:59], v[84:87], v[132:135]\n"
"v_lshlrev_b32 v20, 16, v82\n"
"v_mfma_f32_16x16x32_bf16 v[132:135], a[52:55], v[68:71], v[132:135]\n"
"v_and_b32 v21, 0xffff0000, v82\n"
"v_mfma_f32_16x16x32_bf16 v[132:135], a[48:51], v[64:67], v[132:135]\n"
"v_lshlrev_b32 v22, 16, v83\n"
"s_waitcnt lgkmcnt(1)\n"
"v_mfma_f32_16x16x32_bf16 v[132:135], a[252:255], v[60:63], v[132:135]\n"
"v_and_b32 v23, 0xffff0000, v83\n"
"LHT8:\n"
"s_cbranch_execz LHR8\n"
"v_mfma_f32_16x16x32_bf16 v[132:135], a[248:251], v[44:47], v[132:135]\n"
"v_cvt_pk_bf16_f32 v210, v136, v137\n"
"v_mfma_f32_16x16x32_bf16 v[132:135], a[244:247], v[40:43], v[132:135]\n"
"s_add_u32 s54, s26, s37\n"
"s_addc_u32 s55, s27, 0\n"
"v_mfma_f32_16x16x32_bf16 v[132:135], a[240:243], v[36:39], v[132:135]\n"
"ds_read_b128 v[216:219], v246 offset:24576\n"
"v_mfma_f32_16x16x32_bf16 v[136:139], a[80:83], v[36:39], v[4:7]\n"
"ds_read_b128 v[220:223], v246 offset:25600\n"
"v_mfma_f32_16x16x32_bf16 v[136:139], a[84:87], v[40:43], v[136:139]\n"
"ds_read_b128 v[224:227], v246 offset:26624\n"
"v_mfma_f32_16x16x32_bf16 v[136:139], a[88:91], v[44:47], v[136:139]\n"
"ds_write_b64 v244, v[210:211] offset:136\n"
"v_mfma_f32_16x16x32_bf16 v[136:139], a[92:95], v[60:63], v[136:139]\n"
"v_cvt_pk_bf16_f32 v208, v132, v133\n"
"v_mfma_f32_16x16x32_bf16 v[136:139], a[96:99], v[64:67], v[136:139]\n"
"v_cvt_pk_bf16_f32 v209, v134, v135\n"
"v_mfma_f32_16x16x32_bf16 v[136:139], a[100:103], v[68:71], v[136:139]\n"
"s_add_u32 m0, s31, 0x2f00\n"
"v_mfma_f32_16x16x32_bf16 v[136:139], a[104:107], v[84:87], v[136:139]\n"
"global_load_lds_dwordx4 v238, s[52:53] offset:256 nt\n"
"v_mfma_f32_16x16x32_bf16 v[136:139], a[108:111], v[88:91], v[136:139]\n"
"s_waitcnt lgkmcnt(1)\n"
"v_mfma_f32_16x16x32_bf16 v[136:139], a[112:115], v[92:95], v[136:139]\n"
"s_sub_i32 s84, s84, s19\n"
"v_mfma_f32_16x16x32_bf16 v[136:139], a[116:119], v[108:111], v[136:139]\n"
"global_store_dwordx4 v247, v[216:219], s[54:55] offset:0 sc1\n"
"ds_write_b64 v244, v[208:209] offset:12288\n"
"v_mfma_f32_16x16x32_bf16 v[136:139], a[120:123], v[112:115], v[136:139]\n"
"s_max_i32 s88, s88, s84\n"
"v_mfma_f32_16x16x32_bf16 v[136:139], a[124:127], v[116:119], v[136:139]\n"
"v_mfma_f32_16x16x32_bf16 v[132:135], a[172:175], v[116:119], v[8:11]\n"
"v_mfma_f32_16x16x32_bf16 v[132:135], a[168:171], v[112:115], v[132:135]\n"
"v_mfma_f32_16x16x32_bf16 v[132:135], a[164:167], v[108:111], v[132:135]\n"
"v_mfma_f32_16x16x32_bf16 v[132:135], a[160:163], v[92:95], v[132:135]\n"
"v_cvt_pk_bf16_f32 v210, v136, v137\n"
"v_mfma_f32_16x16x32_bf16 v[132:135], a[156:159], v[88:91], v[132:135]\n"
"v_cvt_pk_bf16_f32 v211, v138, v139\n"
"v_mfma_f32_16x16x32_bf16 v[132:135], a[152:155], v[84:87], v[132:135]\n"
"v_mfma_f32_16x16x32_bf16 v[132:135], a[148:151], v[68:71], v[132:135]\n"
"s_mov_b32 s87, 1\n"
"s_or_b32 s56, s51, s76\n"
"s_cmp_lg_u32 s56, 0\n"
"s_cbranch_scc1 LG1\n"
"v_cmp_ne_u64 s[58:59], s[80:81], v[24:25]\n"
"v_cmp_ne_u64 s[56:57], s[80:81], v[26:27]\n"
"s_mov_b32 s87, 0\n"
"s_nop 3\n"
"s_or_b64 s[56:57], s[56:57], s[58:59]\n"
"s_cbranch_scc1 LG1\n"
"v_mov_b32 v28, 0\n"
"v_mov_b32 v30, s80\n"
"v_mov_b32 v31, s81\n"
"s_mov_b32 s87, 1\n"
"s_mov_b64 exec, 1\n"
"global_store_dwordx2 v28, v[30:31], s[28:29] sc1\n"
"s_mov_b64 exec, -1\n"
"s_nop 4\n"
"LG1:\n"
"v_mfma_f32_16x16x32_bf16 v[132:135], a[144:147], v[64:67], v[132:135]\n"
"v_mfma_f32_16x16x32_bf16 v[132:135], a[140:143], v[60:63], v[132:135]\n"
"v_mfma_f32_16x16x32_bf16 v[132:135], a[136:139], v[44:47], v[132:135]\n"
"global_store_dwordx4 v252, v[220:223], s[54:55] offset:0 sc1\n"
"ds_write_b64 v244, v[210:211] offset:12296\n"
"v_mfma_f32_16x16x32_bf16 v[132:135], a[132:135], v[40:43], v[132:135]\n"
"v_mfma_f32_16x16x32_bf16 v[132:135], a[128:131], v[36:39], v[132:135]\n"
"v_mfma_f32_16x16x32_bf16 v[136:139], a[176:179], v[36:39], v[12:15]\n"
"v_mfma_f32_16x16x32_bf16 v[136:139], a[180:183], v[40:43], v[136:139]\n"
"v_mfma_f32_16x16x32_bf16 v[136:139], a[184:187], v[44:47], v[136:139]\n"
"s_add_u32 m0, s31, 0x4e00\n"
"v_mfma_f32_16x16x32_bf16 v[136:139], a[188:191], v[60:63], v[136:139]\n"
"v_cvt_pk_bf16_f32 v212, v132, v133\n"
"v_mfma_f32_16x16x32_bf16 v[136:139], a[192:195], v[64:67], v[136:139]\n"
"v_mov_b32 v242, 13\n"
"v_mfma_f32_16x16x32_bf16 v[136:139], a[196:199], v[68:71], v[136:139]\n"
"v_cvt_pk_bf16_f32 v213, v134, v135\n"
"v_mfma_f32_16x16x32_bf16 v[136:139], a[200:203], v[84:87], v[136:139]\n"
"s_waitcnt lgkmcnt(2)\n"
"ds_write_b32 v240, v242\n"
"v_mfma_f32_16x16x32_bf16 v[136:139], a[204:207], v[88:91], v[136:139]\n"
"global_load_lds_dwordx4 v238, s[52:53] offset:512 nt\n"
"v_mfma_f32_16x16x32_bf16 v[136:139], a[208:211], v[92:95], v[136:139]\n"
"v_mfma_f32_16x16x32_bf16 v[136:139], a[212:215], v[108:111], v[136:139]\n"
"v_mfma_f32_16x16x32_bf16 v[136:139], a[216:219], v[112:115], v[136:139]\n"
"global_store_dwordx4 v253, v[224:227], s[54:55] offset:0 sc1\n"
"ds_write_b64 v245, v[212:213] offset:12288\n"
"v_mfma_f32_16x16x32_bf16 v[136:139], a[220:223], v[116:119], v[136:139]\n"
"v_mfma_f32_16x16x32_bf16 v[132:135], v[156:159], v[116:119], v[16:19]\n"
"v_mfma_f32_16x16x32_bf16 v[132:135], v[140:143], v[112:115], v[132:135]\n"
"v_mfma_f32_16x16x32_bf16 v[132:135], a[20:23], v[108:111], v[132:135]\n"
"ds_read_b32 v243, v241\n"
"v_mfma_f32_16x16x32_bf16 v[132:135], a[16:19], v[92:95], v[132:135]\n"
"v_cvt_pk_bf16_f32 v214, v136, v137\n"
"v_mfma_f32_16x16x32_bf16 v[132:135], a[12:15], v[88:91], v[132:135]\n"
"v_cvt_pk_bf16_f32 v215, v138, v139\n"
"v_mfma_f32_16x16x32_bf16 v[132:135], a[8:11], v[84:87], v[132:135]\n"
"v_mfma_f32_16x16x32_bf16 v[132:135], a[4:7], v[68:71], v[132:135]\n"
"s_waitcnt lgkmcnt(0)\n"
"v_cmp_gt_u32 s[56:57], 13, v243\n"
"v_mfma_f32_16x16x32_bf16 v[132:135], a[0:3], v[64:67], v[132:135]\n"
"v_mfma_f32_16x16x32_bf16 v[132:135], a[236:239], v[60:63], v[132:135]\n"
"s_or_b64 s[56:57], s[56:57], vcc\n"
"s_cbranch_scc1 LB13\n"
"LB13R:\n"
"v_mfma_f32_16x16x32_bf16 v[132:135], a[232:235], v[44:47], v[132:135]\n"
"ds_write_b64 v245, v[214:215] offset:12296\n"
"v_mfma_f32_16x16x32_bf16 v[132:135], a[228:231], v[40:43], v[132:135]\n"
"v_mfma_f32_16x16x32_bf16 v[132:135], a[224:227], v[36:39], v[132:135]\n"
"v_mfma_f32_16x16x32_bf16 v[136:139], v[160:163], v[36:39], v[20:23]\n"
"ds_read_b128 v[36:39], v232 offset:256\n"
"v_mfma_f32_16x16x32_bf16 v[136:139], v[164:167], v[40:43], v[136:139]\n"
"ds_read_b128 v[40:43], v233 offset:256\n"
"v_mfma_f32_16x16x32_bf16 v[136:139], v[180:183], v[44:47], v[136:139]\n"
"ds_read_b128 v[44:47], v234 offset:256\n"
"v_mfma_f32_16x16x32_bf16 v[136:139], v[184:187], v[60:63], v[136:139]\n"
"ds_read_b128 v[60:63], v235 offset:256\n"
"v_cvt_pk_bf16_f32 v208, v132, v133\n"
"v_mfma_f32_16x16x32_bf16 v[136:139], v[188:191], v[64:67], v[136:139]\n"
"ds_read_b128 v[64:67], v232 offset:8448\n"
"v_cvt_pk_bf16_f32 v209, v134, v135\n"
"v_mfma_f32_16x16x32_bf16 v[136:139], v[204:207], v[68:71], v[136:139]\n"
"ds_read_b128 v[68:71], v233 offset:8448\n"
"v_mfma_f32_16x16x32_bf16 v[136:139], a[24:27], v[84:87], v[136:139]\n"
"ds_read_b128 v[84:87], v234 offset:8448\n"
"v_mfma_f32_16x16x32_bf16 v[136:139], a[28:31], v[88:91], v[136:139]\n"
"ds_read_b128 v[88:91], v235 offset:8448\n"
"v_mfma_f32_16x16x32_bf16 v[136:139], a[32:35], v[92:95], v[136:139]\n"
"ds_read_b128 v[92:95], v232 offset:16640\n"
"v_mfma_f32_16x16x32_bf16 v[136:139], a[36:39], v[108:111], v[136:139]\n"
"ds_read_b128 v[108:111], v233 offset:16640\n"
"v_mfma_f32_16x16x32_bf16 v[136:139], a[40:43], v[112:115], v[136:139]\n"
"ds_read_b128 v[112:115], v234 offset:16640\n"
"v_mfma_f32_16x16x32_bf16 v[136:139], a[44:47], v[116:119], v[136:139]\n"
"ds_read_b128 v[116:119], v235 offset:16640\n"
"s_waitcnt lgkmcnt(8)\n"
"v_mfma_f32_16x16x32_bf16 v[132:135], a[240:243], v[36:39], v[0:3]\n"
"ds_write_b64 v244, v[208:209] offset:12416\n"
"v_mfma_f32_16x16x32_bf16 v[132:135], a[244:247], v[40:43], v[132:135]\n"
"s_add_u32 s52, s20, s41\n"
"s_addc_u32 s53, s21, 0\n"
"v_mfma_f32_16x16x32_bf16 v[132:135], a[248:251], v[44:47], v[132:135]\n"
"s_add_u32 m0, s31, 0x6000\n"
"v_mfma_f32_16x16x32_bf16 v[132:135], a[252:255], v[60:63], v[132:135]\n"
"global_load_lds_dwordx4 v237, s[52:53] offset:0 nt\n"
"s_waitcnt lgkmcnt(5)\n"
"v_mfma_f32_16x16x32_bf16 v[132:135], a[48:51], v[64:67], v[132:135]\n"
"v_cvt_pk_bf16_f32 v210, v136, v137\n"
"v_mfma_f32_16x16x32_bf16 v[132:135], a[52:55], v[68:71], v[132:135]\n"
"v_cvt_pk_bf16_f32 v211, v138, v139\n"
"v_mfma_f32_16x16x32_bf16 v[132:135], a[56:59], v[84:87], v[132:135]\n"
"ds_read_b128 v[216:219], v246 offset:0\n"
"v_mfma_f32_16x16x32_bf16 v[132:135], a[60:63], v[88:91], v[132:135]\n"
"ds_read_b128 v[220:223], v246 offset:1024\n"
"s_waitcnt lgkmcnt(3)\n"
"v_mfma_f32_16x16x32_bf16 v[132:135], a[64:67], v[92:95], v[132:135]\n"
"ds_read_b128 v[224:227], v246 offset:2048\n"
"v_mfma_f32_16x16x32_bf16 v[132:135], a[68:71], v[108:111], v[132:135]\n"
"v_mfma_f32_16x16x32_bf16 v[132:135], a[72:75], v[112:115], v[132:135]\n"
"ds_write_b64 v244, v[210:211] offset:12424\n"
"v_mfma_f32_16x16x32_bf16 v[132:135], a[76:79], v[116:119], v[132:135]\n"
"v_mfma_f32_16x16x32_bf16 v[136:139], a[124:127], v[116:119], v[4:7]\n"
"s_add_u32 s83, s83, s86\n"
"v_mfma_f32_16x16x32_bf16 v[136:139], a[120:123], v[112:115], v[136:139]\n"
"s_sub_i32 s88, s88, 6\n"
"v_mfma_f32_16x16x32_bf16 v[136:139], a[116:119], v[108:111], v[136:139]\n"
"s_max_i32 s88, s88, 0\n"
"v_mfma_f32_16x16x32_bf16 v[136:139], a[112:115], v[92:95], v[136:139]\n"
"v_cvt_pk_bf16_f32 v208, v132, v133\n"
"v_mfma_f32_16x16x32_bf16 v[136:139], a[108:111], v[88:91], v[136:139]\n"
"v_cvt_pk_bf16_f32 v209, v134, v135\n"
"v_mfma_f32_16x16x32_bf16 v[136:139], a[104:107], v[84:87], v[136:139]\n"
"v_mfma_f32_16x16x32_bf16 v[136:139], a[100:103], v[68:71], v[136:139]\n"
"v_mfma_f32_16x16x32_bf16 v[136:139], a[96:99], v[64:67], v[136:139]\n"
"s_waitcnt lgkmcnt(1)\n"
"v_mfma_f32_16x16x32_bf16 v[136:139], a[92:95], v[60:63], v[136:139]\n"
"v_mfma_f32_16x16x32_bf16 v[136:139], a[88:91], v[44:47], v[136:139]\n"
"global_store_dwordx4 v247, v[216:219], s[54:55] offset:768 sc1\n"
"ds_write_b64 v244, v[208:209] offset:24576\n"
"v_mfma_f32_16x16x32_bf16 v[136:139], a[84:87], v[40:43], v[136:139]\n"
"s_add_u32 m0, s31, 0x7f00\n"
"v_mfma_f32_16x16x32_bf16 v[136:139], a[80:83], v[36:39], v[136:139]\n"
"global_load_lds_dwordx4 v237, s[52:53] offset:256 nt\n"
"v_mfma_f32_16x16x32_bf16 v[132:135], a[128:131], v[36:39], v[8:11]\n"
"v_mfma_f32_16x16x32_bf16 v[132:135], a[132:135], v[40:43], v[132:135]\n"
"v_mfma_f32_16x16x32_bf16 v[132:135], a[136:139], v[44:47], v[132:135]\n"
"v_mfma_f32_16x16x32_bf16 v[132:135], a[140:143], v[60:63], v[132:135]\n"
"v_cvt_pk_bf16_f32 v210, v136, v137\n"
"v_mfma_f32_16x16x32_bf16 v[132:135], a[144:147], v[64:67], v[132:135]\n"
"v_cvt_pk_bf16_f32 v211, v138, v139\n"
"v_mfma_f32_16x16x32_bf16 v[132:135], a[148:151], v[68:71], v[132:135]\n"
"v_mfma_f32_16x16x32_bf16 v[132:135], a[152:155], v[84:87], v[132:135]\n"
"v_mfma_f32_16x16x32_bf16 v[132:135], a[156:159], v[88:91], v[132:135]\n"
"v_mfma_f32_16x16x32_bf16 v[132:135], a[160:163], v[92:95], v[132:135]\n"
"v_mfma_f32_16x16x32_bf16 v[132:135], a[164:167], v[108:111], v[132:135]\n"
"s_memrealtime s[84:85]\n"
"v_mfma_f32_16x16x32_bf16 v[132:135], a[168:171], v[112:115], v[132:135]\n"
"global_store_dwordx4 v252, v[220:223], s[54:55] offset:768 sc1\n"
"ds_write_b64 v244, v[210:211] offset:24584\n"
"v_mfma_f32_16x16x32_bf16 v[132:135], a[172:175], v[116:119], v[132:135]\n"
"v_mfma_f32_16x16x32_bf16 v[136:139], a[220:223], v[116:119], v[12:15]\n"
"v_mfma_f32_16x16x32_bf16 v[136:139], a[216:219], v[112:115], v[136:139]\n"
"v_mfma_f32_16x16x32_bf16 v[136:139], a[212:215], v[108:111], v[136:139]\n"
"v_mfma_f32_16x16x32_bf16 v[136:139], a[208:211], v[92:95], v[136:139]\n"
"v_cvt_pk_bf16_f32 v212, v132, v133\n"
"v_mfma_f32_16x16x32_bf16 v[136:139], a[204:207], v[88:91], v[136:139]\n"
"v_mov_b32 v242, 14\n"
"v_mfma_f32_16x16x32_bf16 v[136:139], a[200:203], v[84:87], v[136:139]\n"
"v_cvt_pk_bf16_f32 v213, v134, v135\n"
"v_mfma_f32_16x16x32_bf16 v[136:139], a[196:199], v[68:71], v[136:139]\n"
"s_waitcnt vmcnt(17) lgkmcnt(2)\n"
"ds_write_b32 v240, v242\n"
"v_mfma_f32_16x16x32_bf16 v[136:139], a[192:195], v[64:67], v[136:139]\n"
"v_mfma_f32_16x16x32_bf16 v[136:139], a[188:191], v[60:63], v[136:139]\n"
"v_mfma_f32_16x16x32_bf16 v[136:139], a[184:187], v[44:47], v[136:139]\n"
"v_mfma_f32_16x16x32_bf16 v[136:139], a[180:183], v[40:43], v[136:139]\n"
"global_store_dwordx4 v253, v[224:227], s[54:55] offset:768 sc1\n"
"ds_write_b64 v245, v[212:213] offset:24576\n"
"v_mfma_f32_16x16x32_bf16 v[136:139], a[176:179], v[36:39], v[136:139]\n"
"s_add_u32 m0, s31, 0x9e00\n"
"v_mfma_f32_16x16x32_bf16 v[132:135], a[224:227], v[36:39], v[16:19]\n"
"global_load_lds_dwordx4 v237, s[52:53] offset:512 nt\n"
"v_mfma_f32_16x16x32_bf16 v[132:135], a[228:231], v[40:43], v[132:135]\n"
"v_mfma_f32_16x16x32_bf16 v[132:135], a[232:235], v[44:47], v[132:135]\n"
"ds_read_b32 v243, v241\n"
"v_mfma_f32_16x16x32_bf16 v[132:135], a[236:239], v[60:63], v[132:135]\n"
"v_cvt_pk_bf16_f32 v214, v136, v137\n"
"v_mfma_f32_16x16x32_bf16 v[132:135], a[0:3], v[64:67], v[132:135]\n"
"v_cvt_pk_bf16_f32 v215, v138, v139\n"
"v_mfma_f32_16x16x32_bf16 v[132:135], a[4:7], v[68:71], v[132:135]\n"
"v_mfma_f32_16x16x32_bf16 v[132:135], a[8:11], v[84:87], v[132:135]\n"
"s_waitcnt lgkmcnt(0)\n"
"v_cmp_gt_u32 s[56:57], 14, v243\n"
"v_mfma_f32_16x16x32_bf16 v[132:135], a[12:15], v[88:91], v[132:135]\n"
"s_sub_u32 s84, s84, s83\n"
"v_mfma_f32_16x16x32_bf16 v[132:135], a[16:19], v[92:95], v[132:135]\n"
"s_or_b64 s[56:57], s[56:57], vcc\n"
"s_cbranch_scc1 LB14\n"
"LB14R:\n"
"v_mfma_f32_16x16x32_bf16 v[132:135], a[20:23], v[108:111], v[132:135]\n"
"s_cmp_lt_i32 s84, s88\n"
"s_cselect_b64 vcc, -1, 0\n"
"v_mfma_f32_16x16x32_bf16 v[132:135], v[140:143], v[112:115], v[132:135]\n"
"ds_write_b64 v245, v[214:215] offset:24584\n"
"v_mfma_f32_16x16x32_bf16 v[132:135], v[156:159], v[116:119], v[132:135]\n"
"v_mfma_f32_16x16x32_bf16 v[136:139], a[44:47], v[116:119], v[20:23]\n"
"ds_read_b128 v[116:119], v235 offset:40960\n"
"v_mfma_f32_16x16x32_bf16 v[136:139], a[40:43], v[112:115], v[136:139]\n"
"ds_read_b128 v[112:115], v234 offset:40960\n"
"v_mfma_f32_16x16x32_bf16 v[136:139], a[36:39], v[108:111], v[136:139]\n"
"ds_read_b128 v[108:111], v233 offset:40960\n"
"v_mfma_f32_16x16x32_bf16 v[136:139], a[32:35], v[92:95], v[136:139]\n"
"ds_read_b128 v[92:95], v232 offset:40960\n"
"v_cvt_pk_bf16_f32 v208, v132, v133\n"
"v_mfma_f32_16x16x32_bf16 v[136:139], a[28:31], v[88:91], v[136:139]\n"
"ds_read_b128 v[88:91], v235 offset:32768\n"
"v_cvt_pk_bf16_f32 v209, v134, v135\n"
"v_mfma_f32_16x16x32_bf16 v[136:139], a[24:27], v[84:87], v[136:139]\n"
"ds_read_b128 v[84:87], v234 offset:32768\n"
"v_mfma_f32_16x16x32_bf16 v[136:139], v[204:207], v[68:71], v[136:139]\n"
"ds_read_b128 v[68:71], v233 offset:32768\n"
"v_mfma_f32_16x16x32_bf16 v[136:139], v[188:191], v[64:67], v[136:139]\n"
"ds_read_b128 v[64:67], v232 offset:32768\n"
"v_mfma_f32_16x16x32_bf16 v[136:139], v[184:187], v[60:63], v[136:139]\n"
"ds_read_b128 v[60:63], v235 offset:24576\n"
"v_mfma_f32_16x16x32_bf16 v[136:139], v[180:183], v[44:47], v[136:139]\n"
"ds_read_b128 v[44:47], v234 offset:24576\n"
"v_mfma_f32_16x16x32_bf16 v[136:139], v[164:167], v[40:43], v[136:139]\n"
"ds_read_b128 v[40:43], v233 offset:24576\n"
"v_mfma_f32_16x16x32_bf16 v[136:139], v[160:163], v[36:39], v[136:139]\n"
"ds_read_b128 v[36:39], v232 offset:24576\n"
"s_waitcnt lgkmcnt(8)\n"
"v_mfma_f32_16x16x32_bf16 v[132:135], a[76:79], v[116:119], v[0:3]\n"
"ds_write_b64 v244, v[208:209] offset:24704\n"
"v_mfma_f32_16x16x32_bf16 v[132:135], a[72:75], v[112:115], v[132:135]\n"
"s_add_u32 m0, s31, 0x7000\n"
"v_mfma_f32_16x16x32_bf16 v[132:135], a[68:71], v[108:111], v[132:135]\n"
"global_load_lds_dwordx4 v238, s[52:53] offset:0 nt\n"
"v_mfma_f32_16x16x32_bf16 v[132:135], a[64:67], v[92:95], v[132:135]\n"
"v_cvt_pk_bf16_f32 v210, v136, v137\n"
"s_waitcnt lgkmcnt(5)\n"
"v_mfma_f32_16x16x32_bf16 v[132:135], a[60:63], v[88:91], v[132:135]\n"
"v_cvt_pk_bf16_f32 v211, v138, v139\n"
"v_mfma_f32_16x16x32_bf16 v[132:135], a[56:59], v[84:87], v[132:135]\n"
"s_add_u32 s54, s26, s38\n"
"s_addc_u32 s55, s27, 0\n"
"v_mfma_f32_16x16x32_bf16 v[132:135], a[52:55], v[68:71], v[132:135]\n"
"ds_read_b128 v[216:219], v246 offset:12288\n"
"v_mfma_f32_16x16x32_bf16 v[132:135], a[48:51], v[64:67], v[132:135]\n"
"ds_read_b128 v[220:223], v246 offset:13312\n"
"s_waitcnt lgkmcnt(3)\n"
"v_mfma_f32_16x16x32_bf16 v[132:135], a[252:255], v[60:63], v[132:135]\n"
"ds_read_b128 v[224:227], v246 offset:14336\n"
"v_mfma_f32_16x16x32_bf16 v[132:135], a[248:251], v[44:47], v[132:135]\n"
"ds_write_b64 v244, v[210:211] offset:24712\n"
"v_mfma_f32_16x16x32_bf16 v[132:135], a[244:247], v[40:43], v[132:135]\n"
"s_sub_i32 s84, s84, s19\n"
"v_mfma_f32_16x16x32_bf16 v[132:135], a[240:243], v[36:39], v[132:135]\n"
"s_max_i32 s88, s88, s84\n"
"v_mfma_f32_16x16x32_bf16 v[136:139], a[80:83], v[36:39], v[4:7]\n"
"v_mfma_f32_16x16x32_bf16 v[136:139], a[84:87], v[40:43], v[136:139]\n"
"v_mfma_f32_16x16x32_bf16 v[136:139], a[88:91], v[44:47], v[136:139]\n"
"v_mfma_f32_16x16x32_bf16 v[136:139], a[92:95], v[60:63], v[136:139]\n"
"v_cvt_pk_bf16_f32 v208, v132, v133\n"
"v_mfma_f32_16x16x32_bf16 v[136:139], a[96:99], v[64:67], v[136:139]\n"
"v_cvt_pk_bf16_f32 v209, v134, v135\n"
"v_mfma_f32_16x16x32_bf16 v[136:139], a[100:103], v[68:71], v[136:139]\n"
"v_mfma_f32_16x16x32_bf16 v[136:139], a[104:107], v[84:87], v[136:139]\n"
"v_mfma_f32_16x16x32_bf16 v[136:139], a[108:111], v[88:91], v[136:139]\n"
"s_waitcnt lgkmcnt(1)\n"
"v_mfma_f32_16x16x32_bf16 v[136:139], a[112:115], v[92:95], v[136:139]\n"
"v_mfma_f32_16x16x32_bf16 v[136:139], a[116:119], v[108:111], v[136:139]\n"
"global_store_dwordx4 v247, v[216:219], s[54:55] offset:0 sc1\n"
"ds_write_b64 v244, v[208:209] offset:0\n"
"v_mfma_f32_16x16x32_bf16 v[136:139], a[120:123], v[112:115], v[136:139]\n"
"s_add_u32 m0, s31, 0x8f00\n"
"v_mfma_f32_16x16x32_bf16 v[136:139], a[124:127], v[116:119], v[136:139]\n"
"global_load_lds_dwordx4 v238, s[52:53] offset:256 nt\n"
"v_mfma_f32_16x16x32_bf16 v[132:135], a[172:175], v[116:119], v[8:11]\n"
"v_mfma_f32_16x16x32_bf16 v[132:135], a[168:171], v[112:115], v[132:135]\n"
"v_mfma_f32_16x16x32_bf16 v[132:135], a[164:167], v[108:111], v[132:135]\n"
"v_mfma_f32_16x16x32_bf16 v[132:135], a[160:163], v[92:95], v[132:135]\n"
"v_cvt_pk_bf16_f32 v210, v136, v137\n"
"v_mfma_f32_16x16x32_bf16 v[132:135], a[156:159], v[88:91], v[132:135]\n"
"v_cvt_pk_bf16_f32 v211, v138, v139\n"
"v_mfma_f32_16x16x32_bf16 v[132:135], a[152:155], v[84:87], v[132:135]\n"
"v_mfma_f32_16x16x32_bf16 v[132:135], a[148:151], v[68:71], v[132:135]\n"
"v_mfma_f32_16x16x32_bf16 v[132:135], a[144:147], v[64:67], v[132:135]\n"
"v_mfma_f32_16x16x32_bf16 v[132:135], a[140:143], v[60:63], v[132:135]\n"
"v_mfma_f32_16x16x32_bf16 v[132:135], a[136:139], v[44:47], v[132:135]\n"
"global_store_dwordx4 v252, v[220:223], s[54:55] offset:0 sc1\n"
"ds_write_b64 v244, v[210:211] offset:8\n"
"v_mfma_f32_16x16x32_bf16 v[132:135], a[132:135], v[40:43], v[132:135]\n"
"v_mfma_f32_16x16x32_bf16 v[132:135], a[128:131], v[36:39], v[132:135]\n"
"v_mfma_f32_16x16x32_bf16 v[136:139], a[176:179], v[36:39], v[12:15]\n"
"v_mfma_f32_16x16x32_bf16 v[136:139], a[180:183], v[40:43], v[136:139]\n"
"v_mfma_f32_16x16x32_bf16 v[136:139], a[184:187], v[44:47], v[136:139]\n"
"v_mfma_f32_16x16x32_bf16 v[136:139], a[188:191], v[60:63], v[136:139]\n"
"v_cvt_pk_bf16_f32 v212, v132, v133\n"
"v_mfma_f32_16x16x32_bf16 v[136:139], a[192:195], v[64:67], v[136:139]\n"
"v_mov_b32 v242, 15\n"
"v_mfma_f32_16x16x32_bf16 v[136:139], a[196:199], v[68:71], v[136:139]\n"
"v_cvt_pk_bf16_f32 v213, v134, v135\n"
"v_mfma_f32_16x16x32_bf16 v[136:139], a[200:203], v[84:87], v[136:139]\n"
"s_waitcnt lgkmcnt(2)\n"
"ds_write_b32 v240, v242\n"
"v_mfma_f32_16x16x32_bf16 v[136:139], a[204:207], v[88:91], v[136:139]\n"
"v_mfma_f32_16x16x32_bf16 v[136:139], a[208:211], v[92:95], v[136:139]\n"
"v_mfma_f32_16x16x32_bf16 v[136:139], a[212:215], v[108:111], v[136:139]\n"
"v_mfma_f32_16x16x32_bf16 v[136:139], a[216:219], v[112:115], v[136:139]\n"
"global_store_dwordx4 v253, v[224:227], s[54:55] offset:0 sc1\n"
"ds_write_b64 v245, v[212:213] offset:0\n"
"v_mfma_f32_16x16x32_bf16 v[136:139], a[220:223], v[116:119], v[136:139]\n"
"s_add_u32 m0, s31, 0xae00\n"
"v_mfma_f32_16x16x32_bf16 v[132:135], v[156:159], v[116:119], v[16:19]\n"
"global_load_lds_dwordx4 v238, s[52:53] offset:512 nt\n"
"v_mfma_f32_16x16x32_bf16 v[132:135], v[140:143], v[112:115], v[132:135]\n"
"v_mfma_f32_16x16x32_bf16 v[132:135], a[20:23], v[108:111], v[132:135]\n"
"ds_read_b32 v243, v241\n"
"v_mfma_f32_16x16x32_bf16 v[132:135], a[16:19], v[92:95], v[132:135]\n"
"v_cvt_pk_bf16_f32 v214, v136, v137\n"
"v_mfma_f32_16x16x32_bf16 v[132:135], a[12:15], v[88:91], v[132:135]\n"
"v_cvt_pk_bf16_f32 v215, v138, v139\n"
"v_mfma_f32_16x16x32_bf16 v[132:135], a[8:11], v[84:87], v[132:135]\n"
"v_mfma_f32_16x16x32_bf16 v[132:135], a[4:7], v[68:71], v[132:135]\n"
"s_waitcnt lgkmcnt(0)\n"
"v_cmp_gt_u32 s[56:57], 15, v243\n"
"v_mfma_f32_16x16x32_bf16 v[132:135], a[0:3], v[64:67], v[132:135]\n"
"v_mfma_f32_16x16x32_bf16 v[132:135], a[236:239], v[60:63], v[132:135]\n"
"s_or_b64 s[56:57], s[56:57], vcc\n"
"s_cbranch_scc1 LB15\n"
"LB15R:\n"
"v_mfma_f32_16x16x32_bf16 v[132:135], a[232:235], v[44:47], v[132:135]\n"
"ds_write_b64 v245, v[214:215] offset:8\n"
"v_mfma_f32_16x16x32_bf16 v[132:135], a[228:231], v[40:43], v[132:135]\n"
"v_mfma_f32_16x16x32_bf16 v[132:135], a[224:227], v[36:39], v[132:135]\n"
"v_mfma_f32_16x16x32_bf16 v[136:139], v[160:163], v[36:39], v[20:23]\n"
"ds_read_b128 v[36:39], v232 offset:24832\n"
"v_mfma_f32_16x16x32_bf16 v[136:139], v[164:167], v[40:43], v[136:139]\n"
"ds_read_b128 v[40:43], v233 offset:24832\n"
"v_mfma_f32_16x16x32_bf16 v[136:139], v[180:183], v[44:47], v[136:139]\n"
"ds_read_b128 v[44:47], v234 offset:24832\n"
"v_mfma_f32_16x16x32_bf16 v[136:139], v[184:187], v[60:63], v[136:139]\n"
"ds_read_b128 v[60:63], v235 offset:24832\n"
"v_cvt_pk_bf16_f32 v208, v132, v133\n"
"v_mfma_f32_16x16x32_bf16 v[136:139], v[188:191], v[64:67], v[136:139]\n"
"ds_read_b128 v[64:67], v232 offset:33024\n"
"v_cvt_pk_bf16_f32 v209, v134, v135\n"
"v_mfma_f32_16x16x32_bf16 v[136:139], v[204:207], v[68:71], v[136:139]\n"
"ds_read_b128 v[68:71], v233 offset:33024\n"
"v_mfma_f32_16x16x32_bf16 v[136:139], a[24:27], v[84:87], v[136:139]\n"
"ds_read_b128 v[84:87], v234 offset:33024\n"
"v_mfma_f32_16x16x32_bf16 v[136:139], a[28:31], v[88:91], v[136:139]\n"
"ds_read_b128 v[88:91], v235 offset:33024\n"
"v_mfma_f32_16x16x32_bf16 v[136:139], a[32:35], v[92:95], v[136:139]\n"
"ds_read_b128 v[92:95], v232 offset:41216\n"
"v_mfma_f32_16x16x32_bf16 v[136:139], a[36:39], v[108:111], v[136:139]\n"
"ds_read_b128 v[108:111], v233 offset:41216\n"
"ds_write_b64 v244, v[208:209] offset:128\n"
"v_mfma_f32_16x16x32_bf16 v[136:139], a[40:43], v[112:115], v[136:139]\n"
"ds_read_b128 v[112:115], v234 offset:41216\n"
"v_mfma_f32_16x16x32_bf16 v[136:139], a[44:47], v[116:119], v[136:139]\n"
"ds_read_b128 v[116:119], v235 offset:41216\n"
"s_waitcnt lgkmcnt(9)\n"
"v_mfma_f32_16x16x32_bf16 v[132:135], a[240:243], v[36:39], v[0:3]\n"
"v_add_u32 v232, 0xffff4000, v232\n"
"v_mfma_f32_16x16x32_bf16 v[132:135], a[244:247], v[40:43], v[132:135]\n"
"v_add_u32 v233, 0xffff4000, v233\n"
"v_mfma_f32_16x16x32_bf16 v[132:135], a[248:251], v[44:47], v[132:135]\n"
"v_add_u32 v234, 0xffff4000, v234\n"
"v_mfma_f32_16x16x32_bf16 v[132:135], a[252:255], v[60:63], v[132:135]\n"
"v_add_u32 v235, 0xffff4000, v235\n"
"s_waitcnt lgkmcnt(5)\n"
"v_mfma_f32_16x16x32_bf16 v[132:135], a[48:51], v[64:67], v[132:135]\n"
"s_add_u32 s52, s20, s42\n"
"s_addc_u32 s53, s21, 0\n"
"v_mfma_f32_16x16x32_bf16 v[132:135], a[52:55], v[68:71], v[132:135]\n"
"s_add_u32 m0, s31, 0xc000\n"
"v_mfma_f32_16x16x32_bf16 v[132:135], a[56:59], v[84:87], v[132:135]\n"
"global_load_lds_dwordx4 v237, s[52:53] offset:0 nt\n"
"v_mfma_f32_16x16x32_bf16 v[132:135], a[60:63], v[88:91], v[132:135]\n"
"v_cvt_pk_bf16_f32 v210, v136, v137\n"
"s_waitcnt lgkmcnt(0)\n"
"v_mfma_f32_16x16x32_bf16 v[132:135], a[64:67], v[92:95], v[132:135]\n"
"v_cvt_pk_bf16_f32 v211, v138, v139\n"
"v_mfma_f32_16x16x32_bf16 v[132:135], a[68:71], v[108:111], v[132:135]\n"
"ds_read_b128 v[216:219], v246 offset:24576\n"
"v_mfma_f32_16x16x32_bf16 v[132:135], a[72:75], v[112:115], v[132:135]\n"
"ds_read_b128 v[220:223], v246 offset:25600\n"
"v_mfma_f32_16x16x32_bf16 v[132:135], a[76:79], v[116:119], v[132:135]\n"
"ds_read_b128 v[224:227], v246 offset:26624\n"
"v_mfma_f32_16x16x32_bf16 v[136:139], a[124:127], v[116:119], v[4:7]\n"
"v_lshlrev_b32 v0, 16, v96\n"
"v_mfma_f32_16x16x32_bf16 v[136:139], a[120:123], v[112:115], v[136:139]\n"
"ds_write_b64 v244, v[210:211] offset:136\n"
"v_mfma_f32_16x16x32_bf16 v[136:139], a[116:119], v[108:111], v[136:139]\n"
"v_and_b32 v1, 0xffff0000, v96\n"
"v_mfma_f32_16x16x32_bf16 v[136:139], a[112:115], v[92:95], v[136:139]\n"
"v_cvt_pk_bf16_f32 v208, v132, v133\n"
"v_mfma_f32_16x16x32_bf16 v[136:139], a[108:111], v[88:91], v[136:139]\n"
"v_cvt_pk_bf16_f32 v209, v134, v135\n"
"v_mfma_f32_16x16x32_bf16 v[136:139], a[104:107], v[84:87], v[136:139]\n"
"v_lshlrev_b32 v2, 16, v97\n"
"v_mfma_f32_16x16x32_bf16 v[136:139], a[100:103], v[68:71], v[136:139]\n"
"v_and_b32 v3, 0xffff0000, v97\n"
"v_mfma_f32_16x16x32_bf16 v[136:139], a[96:99], v[64:67], v[136:139]\n"
"s_waitcnt lgkmcnt(1)\n"
"v_mfma_f32_16x16x32_bf16 v[136:139], a[92:95], v[60:63], v[136:139]\n"
"v_lshlrev_b32 v4, 16, v98\n"
"s_mov_b64 exec, s[6:7]\n"
"s_cbranch_execz LHT9\n"
"LHR9:\n"
"s_mov_b64 exec, -1\n"
"v_mfma_f32_16x16x32_bf16 v[136:139], a[88:91], v[44:47], v[136:139]\n"
"global_store_dwordx4 v247, v[216:219], s[54:55] offset:768 sc1\n"
"ds_write_b64 v244, v[208:209] offset:12288\n"
"v_mfma_f32_16x16x32_bf16 v[136:139], a[84:87], v[40:43], v[136:139]\n"
"v_and_b32 v5, 0xffff0000, v98\n"
"v_mfma_f32_16x16x32_bf16 v[136:139], a[80:83], v[36:39], v[136:139]\n"
"s_add_u32 m0, s31, 0xdf00\n"
"v_mfma_f32_16x16x32_bf16 v[132:135], a[128:131], v[36:39], v[8:11]\n"
"global_load_lds_dwordx4 v237, s[52:53] offset:256 nt\n"
"v_mfma_f32_16x16x32_bf16 v[132:135], a[132:135], v[40:43], v[132:135]\n"
"v_lshlrev_b32 v6, 16, v99\n"
"v_mfma_f32_16x16x32_bf16 v[132:135], a[136:139], v[44:47], v[132:135]\n"
"v_and_b32 v7, 0xffff0000, v99\n"
"v_mfma_f32_16x16x32_bf16 v[132:135], a[140:143], v[60:63], v[132:135]\n"
"v_cvt_pk_bf16_f32 v210, v136, v137\n"
"v_mfma_f32_16x16x32_bf16 v[132:135], a[144:147], v[64:67], v[132:135]\n"
"v_cvt_pk_bf16_f32 v211, v138, v139\n"
"v_mfma_f32_16x16x32_bf16 v[132:135], a[148:151], v[68:71], v[132:135]\n"
"v_lshlrev_b32 v8, 16, v100\n"
"v_mfma_f32_16x16x32_bf16 v[132:135], a[152:155], v[84:87], v[132:135]\n"
"v_and_b32 v9, 0xffff0000, v100\n"
"v_mfma_f32_16x16x32_bf16 v[132:135], a[156:159], v[88:91], v[132:135]\n"
"v_lshlrev_b32 v10, 16, v101\n"
"v_mfma_f32_16x16x32_bf16 v[132:135], a[160:163], v[92:95], v[132:135]\n"
"v_and_b32 v11, 0xffff0000, v101\n"
"v_mfma_f32_16x16x32_bf16 v[132:135], a[164:167], v[108:111], v[132:135]\n"
"s_memrealtime s[84:85]\n"
"v_mfma_f32_16x16x32_bf16 v[132:135], a[168:171], v[112:115], v[132:135]\n"
"global_store_dwordx4 v252, v[220:223], s[54:55] offset:768 sc1\n"
"ds_write_b64 v244, v[210:211] offset:12296\n"
"v_mfma_f32_16x16x32_bf16 v[132:135], a[172:175], v[116:119], v[132:135]\n"
"s_add_u32 s83, s83, s86\n"
"v_mfma_f32_16x16x32_bf16 v[136:139], a[220:223], v[116:119], v[12:15]\n"
"s_sub_i32 s88, s88, 6\n"
"v_mfma_f32_16x16x32_bf16 v[136:139], a[216:219], v[112:115], v[136:139]\n"
"s_max_i32 s88, s88, 0\n"
"v_mfma_f32_16x16x32_bf16 v[136:139], a[212:215], v[108:111], v[136:139]\n"
"v_mfma_f32_16x16x32_bf16 v[136:139], a[208:211], v[92:95], v[136:139]\n"
"v_cvt_pk_bf16_f32 v212, v132, v133\n"
"v_mfma_f32_16x16x32_bf16 v[136:139], a[204:207], v[88:91], v[136:139]\n"
"v_mov_b32 v242, 16\n"
"v_mfma_f32_16x16x32_bf16 v[136:139], a[200:203], v[84:87], v[136:139]\n"
"v_cvt_pk_bf16_f32 v213, v134, v135\n"
"v_mfma_f32_16x16x32_bf16 v[136:139], a[196:199], v[68:71], v[136:139]\n"
"s_waitcnt vmcnt(17) lgkmcnt(2)\n"
"ds_write_b32 v240, v242\n"
"v_mfma_f32_16x16x32_bf16 v[136:139], a[192:195], v[64:67], v[136:139]\n"
"v_lshlrev_b32 v12, 16, v102\n"
"v_mfma_f32_16x16x32_bf16 v[136:139], a[188:191], v[60:63], v[136:139]\n"
"v_and_b32 v13, 0xffff0000, v102\n"
"v_mfma_f32_16x16x32_bf16 v[136:139], a[184:187], v[44:47], v[136:139]\n"
"v_lshlrev_b32 v14, 16, v103\n"
"v_mfma_f32_16x16x32_bf16 v[136:139], a[180:183], v[40:43], v[136:139]\n"
"global_store_dwordx4 v253, v[224:227], s[54:55] offset:768 sc1\n"
"ds_write_b64 v245, v[212:213] offset:12288\n"
"v_mfma_f32_16x16x32_bf16 v[136:139], a[176:179], v[36:39], v[136:139]\n"
"v_and_b32 v15, 0xffff0000, v103\n"
"v_mfma_f32_16x16x32_bf16 v[132:135], a[224:227], v[36:39], v[16:19]\n"
"s_add_u32 m0, s31, 0xfe00\n"
"v_mfma_f32_16x16x32_bf16 v[132:135], a[228:231], v[40:43], v[132:135]\n"
"global_load_lds_dwordx4 v237, s[52:53] offset:512 nt\n"
"v_mfma_f32_16x16x32_bf16 v[132:135], a[232:235], v[44:47], v[132:135]\n"
"ds_read_b32 v243, v241\n"
"v_mfma_f32_16x16x32_bf16 v[132:135], a[236:239], v[60:63], v[132:135]\n"
"v_cvt_pk_bf16_f32 v214, v136, v137\n"
"v_mfma_f32_16x16x32_bf16 v[132:135], a[0:3], v[64:67], v[132:135]\n"
"v_cvt_pk_bf16_f32 v215, v138, v139\n"
"v_mfma_f32_16x16x32_bf16 v[132:135], a[4:7], v[68:71], v[132:135]\n"
"v_lshlrev_b32 v16, 16, v104\n"
"v_mfma_f32_16x16x32_bf16 v[132:135], a[8:11], v[84:87], v[132:135]\n"
"s_waitcnt lgkmcnt(0)\n"
"v_cmp_gt_u32 s[56:57], 16, v243\n"
"v_mfma_f32_16x16x32_bf16 v[132:135], a[12:15], v[88:91], v[132:135]\n"
"s_sub_u32 s84, s84, s83\n"
"v_mfma_f32_16x16x32_bf16 v[132:135], a[16:19], v[92:95], v[132:135]\n"
"s_or_b64 s[56:57], s[56:57], vcc\n"
"s_cbranch_scc1 LB16\n"
"LB16R:\n"
"v_mfma_f32_16x16x32_bf16 v[132:135], a[20:23], v[108:111], v[132:135]\n"
"s_cmp_lt_i32 s84, s88\n"
"s_cselect_b64 vcc, -1, 0\n"
"v_mfma_f32_16x16x32_bf16 v[132:135], v[140:143], v[112:115], v[132:135]\n"
"ds_write_b64 v245, v[214:215] offset:12296\n"
"v_mfma_f32_16x16x32_bf16 v[132:135], v[156:159], v[116:119], v[132:135]\n"
"v_and_b32 v17, 0xffff0000, v104\n"
"v_mfma_f32_16x16x32_bf16 v[136:139], a[44:47], v[116:119], v[20:23]\n"
"ds_read_b128 v[116:119], v235 offset:16384\n"
"v_mfma_f32_16x16x32_bf16 v[136:139], a[40:43], v[112:115], v[136:139]\n"
"ds_read_b128 v[112:115], v234 offset:16384\n"
"v_mfma_f32_16x16x32_bf16 v[136:139], a[36:39], v[108:111], v[136:139]\n"
"ds_read_b128 v[108:111], v233 offset:16384\n"
"v_mfma_f32_16x16x32_bf16 v[136:139], a[32:35], v[92:95], v[136:139]\n"
"ds_read_b128 v[92:95], v232 offset:16384\n"
"v_cvt_pk_bf16_f32 v208, v132, v133\n"
"v_mfma_f32_16x16x32_bf16 v[136:139], a[28:31], v[88:91], v[136:139]\n"
"ds_read_b128 v[88:91], v235 offset:8192\n"
"v_cvt_pk_bf16_f32 v209, v134, v135\n"
"v_mfma_f32_16x16x32_bf16 v[136:139], a[24:27], v[84:87], v[136:139]\n"
"ds_read_b128 v[84:87], v234 offset:8192\n"
"v_mfma_f32_16x16x32_bf16 v[136:139], v[204:207], v[68:71], v[136:139]\n"
"ds_read_b128 v[68:71], v233 offset:8192\n"
"v_mfma_f32_16x16x32_bf16 v[136:139], v[188:191], v[64:67], v[136:139]\n"
"ds_read_b128 v[64:67], v232 offset:8192\n"
"v_mfma_f32_16x16x32_bf16 v[136:139], v[184:187], v[60:63], v[136:139]\n"
"ds_read_b128 v[60:63], v235 offset:0\n"
"v_mfma_f32_16x16x32_bf16 v[136:139], v[180:183], v[44:47], v[136:139]\n"
"ds_read_b128 v[44:47], v234 offset:0\n"
"LHT9:\n"
"s_cbranch_execz LHR9\n"
"v_mfma_f32_16x16x32_bf16 v[136:139], v[164:167], v[40:43], v[136:139]\n"
"ds_read_b128 v[40:43], v233 offset:0\n"
"v_mfma_f32_16x16x32_bf16 v[136:139], v[160:163], v[36:39], v[136:139]\n"
"ds_read_b128 v[36:39], v232 offset:0\n"
"s_waitcnt lgkmcnt(8)\n"
"v_mfma_f32_16x16x32_bf16 v[132:135], a[76:79], v[116:119], v[0:3]\n"
"ds_write_b64 v244, v[208:209] offset:12416\n"
"v_mfma_f32_16x16x32_bf16 v[132:135], a[72:75], v[112:115], v[132:135]\n"
"v_lshlrev_b32 v18, 16, v105\n"
"v_mfma_f32_16x16x32_bf16 v[132:135], a[68:71], v[108:111], v[132:135]\n"
"v_and_b32 v19, 0xffff0000, v105\n"
"v_mfma_f32_16x16x32_bf16 v[132:135], a[64:67], v[92:95], v[132:135]\n"
"v_lshlrev_b32 v20, 16, v106\n"
"s_waitcnt lgkmcnt(5)\n"
"v_mfma_f32_16x16x32_bf16 v[132:135], a[60:63], v[88:91], v[132:135]\n"
"v_and_b32 v21, 0xffff0000, v106\n"
"v_mfma_f32_16x16x32_bf16 v[132:135], a[56:59], v[84:87], v[132:135]\n"
"v_lshlrev_b32 v22, 16, v107\n"
"v_mfma_f32_16x16x32_bf16 v[132:135], a[52:55], v[68:71], v[132:135]\n"
"v_and_b32 v23, 0xffff0000, v107\n"
"v_mfma_f32_16x16x32_bf16 v[132:135], a[48:51], v[64:67], v[132:135]\n"
"v_cvt_pk_bf16_f32 v210, v136, v137\n"
"s_waitcnt lgkmcnt(1)\n"
"v_mfma_f32_16x16x32_bf16 v[132:135], a[252:255], v[60:63], v[132:135]\n"
"v_cvt_pk_bf16_f32 v211, v138, v139\n"
"v_mfma_f32_16x16x32_bf16 v[132:135], a[248:251], v[44:47], v[132:135]\n"
"s_add_u32 s54, s26, s39\n"
"s_addc_u32 s55, s27, 0\n"
"v_mfma_f32_16x16x32_bf16 v[132:135], a[244:247], v[40:43], v[132:135]\n"
"ds_read_b128 v[216:219], v246 offset:0\n"
"v_mfma_f32_16x16x32_bf16 v[132:135], a[240:243], v[36:39], v[132:135]\n"
"ds_read_b128 v[220:223], v246 offset:1024\n"
"v_mfma_f32_16x16x32_bf16 v[136:139], a[80:83], v[36:39], v[4:7]\n"
"ds_read_b128 v[224:227], v246 offset:2048\n"
"v_mfma_f32_16x16x32_bf16 v[136:139], a[84:87], v[40:43], v[136:139]\n"
"ds_write_b64 v244, v[210:211] offset:12424\n"
"v_mfma_f32_16x16x32_bf16 v[136:139], a[88:91], v[44:47], v[136:139]\n"
"s_add_u32 m0, s31, 0xd000\n"
"v_mfma_f32_16x16x32_bf16 v[136:139], a[92:95], v[60:63], v[136:139]\n"
"v_cvt_pk_bf16_f32 v208, v132, v133\n"
"v_mfma_f32_16x16x32_bf16 v[136:139], a[96:99], v[64:67], v[136:139]\n"
"v_cvt_pk_bf16_f32 v209, v134, v135\n"
"v_mfma_f32_16x16x32_bf16 v[136:139], a[100:103], v[68:71], v[136:139]\n"
"global_load_lds_dwordx4 v238, s[52:53] offset:0 nt\n"
"v_mfma_f32_16x16x32_bf16 v[136:139], a[104:107], v[84:87], v[136:139]\n"
"s_sub_i32 s84, s84, s19\n"
"v_mfma_f32_16x16x32_bf16 v[136:139], a[108:111], v[88:91], v[136:139]\n"
"s_waitcnt lgkmcnt(1)\n"
"v_mfma_f32_16x16x32_bf16 v[136:139], a[112:115], v[92:95], v[136:139]\n"
"s_max_i32 s88, s88, s84\n"
"v_mfma_f32_16x16x32_bf16 v[136:139], a[116:119], v[108:111], v[136:139]\n"
"global_store_dwordx4 v247, v[216:219], s[54:55] offset:0 sc1\n"
"ds_write_b64 v244, v[208:209] offset:24576\n"
"v_mfma_f32_16x16x32_bf16 v[136:139], a[120:123], v[112:115], v[136:139]\n"
"v_mfma_f32_16x16x32_bf16 v[136:139], a[124:127], v[116:119], v[136:139]\n"
"v_mfma_f32_16x16x32_bf16 v[132:135], a[172:175], v[116:119], v[8:11]\n"
"v_mfma_f32_16x16x32_bf16 v[132:135], a[168:171], v[112:115], v[132:135]\n"
"v_mfma_f32_16x16x32_bf16 v[132:135], a[164:167], v[108:111], v[132:135]\n"
"v_mfma_f32_16x16x32_bf16 v[132:135], a[160:163], v[92:95], v[132:135]\n"
"v_cvt_pk_bf16_f32 v210, v136, v137\n"
"v_mfma_f32_16x16x32_bf16 v[132:135], a[156:159], v[88:91], v[132:135]\n"
"v_cvt_pk_bf16_f32 v211, v138, v139\n"
"v_mfma_f32_16x16x32_bf16 v[132:135], a[152:155], v[84:87], v[132:135]\n"
"s_add_u32 m0, s31, 0xef00\n"
"v_mfma_f32_16x16x32_bf16 v[132:135], a[148:151], v[68:71], v[132:135]\n"
"global_load_lds_dwordx4 v238, s[52:53] offset:256 nt\n"
"v_mfma_f32_16x16x32_bf16 v[132:135], a[144:147], v[64:67], v[132:135]\n"
"v_mfma_f32_16x16x32_bf16 v[132:135], a[140:143], v[60:63], v[132:135]\n"
"v_mfma_f32_16x16x32_bf16 v[132:135], a[136:139], v[44:47], v[132:135]\n"
"global_store_dwordx4 v252, v[220:223], s[54:55] offset:0 sc1\n"
"ds_write_b64 v244, v[210:211] offset:24584\n"
"v_mfma_f32_16x16x32_bf16 v[132:135], a[132:135], v[40:43], v[132:135]\n"
"v_mfma_f32_16x16x32_bf16 v[132:135], a[128:131], v[36:39], v[132:135]\n"
"v_mfma_f32_16x16x32_bf16 v[136:139], a[176:179], v[36:39], v[12:15]\n"
"v_mfma_f32_16x16x32_bf16 v[136:139], a[180:183], v[40:43], v[136:139]\n"
"v_mfma_f32_16x16x32_bf16 v[136:139], a[184:187], v[44:47], v[136:139]\n"
"v_mfma_f32_16x16x32_bf16 v[136:139], a[188:191], v[60:63], v[136:139]\n"
"v_cvt_pk_bf16_f32 v212, v132, v133\n"
"v_mfma_f32_16x16x32_bf16 v[136:139], a[192:195], v[64:67], v[136:139]\n"
"v_mov_b32 v242, 17\n"
"v_mfma_f32_16x16x32_bf16 v[136:139], a[196:199], v[68:71], v[136:139]\n"
"v_cvt_pk_bf16_f32 v213, v134, v135\n"
"v_mfma_f32_16x16x32_bf16 v[136:139], a[200:203], v[84:87], v[136:139]\n"
"s_waitcnt lgkmcnt(2)\n"
"ds_write_b32 v240, v242\n"
"v_mfma_f32_16x16x32_bf16 v[136:139], a[204:207], v[88:91], v[136:139]\n"
"v_mfma_f32_16x16x32_bf16 v[136:139], a[208:211], v[92:95], v[136:139]\n"
"v_mfma_f32_16x16x32_bf16 v[136:139], a[212:215], v[108:111], v[136:139]\n"
"v_mfma_f32_16x16x32_bf16 v[136:139], a[216:219], v[112:115], v[136:139]\n"
"global_store_dwordx4 v253, v[224:227], s[54:55] offset:0 sc1\n"
"ds_write_b64 v245, v[212:213] offset:24576\n"
"v_mfma_f32_16x16x32_bf16 v[136:139], a[220:223], v[116:119], v[136:139]\n"
"v_mfma_f32_16x16x32_bf16 v[132:135], v[156:159], v[116:119], v[16:19]\n"
"v_mfma_f32_16x16x32_bf16 v[132:135], v[140:143], v[112:115], v[132:135]\n"
"v_mfma_f32_16x16x32_bf16 v[132:135], a[20:23], v[108:111], v[132:135]\n"
"ds_read_b32 v243, v241\n"
"v_mfma_f32_16x16x32_bf16 v[132:135], a[16:19], v[92:95], v[132:135]\n"
"v_cvt_pk_bf16_f32 v214, v136, v137\n"
"v_mfma_f32_16x16x32_bf16 v[132:135], a[12:15], v[88:91], v[132:135]\n"
"v_cvt_pk_bf16_f32 v215, v138, v139\n"
"v_mfma_f32_16x16x32_bf16 v[132:135], a[8:11], v[84:87], v[132:135]\n"
"s_add_u32 m0, s31, 0x10e00\n"
"v_mfma_f32_16x16x32_bf16 v[132:135], a[4:7], v[68:71], v[132:135]\n"
"s_waitcnt lgkmcnt(0)\n"
"v_cmp_gt_u32 s[56:57], 17, v243\n"
"v_mfma_f32_16x16x32_bf16 v[132:135], a[0:3], v[64:67], v[132:135]\n"
"global_load_lds_dwordx4 v238, s[52:53] offset:512 nt\n"
"v_mfma_f32_16x16x32_bf16 v[132:135], a[236:239], v[60:63], v[132:135]\n"
"s_or_b64 s[56:57], s[56:57], vcc\n"
"s_cbranch_scc1 LB17\n"
"LB17R:\n"
"v_mfma_f32_16x16x32_bf16 v[132:135], a[232:235], v[44:47], v[132:135]\n"
"ds_write_b64 v245, v[214:215] offset:24584\n"
"v_mfma_f32_16x16x32_bf16 v[132:135], a[228:231], v[40:43], v[132:135]\n"
"v_mfma_f32_16x16x32_bf16 v[132:135], a[224:227], v[36:39], v[132:135]\n"
"v_mfma_f32_16x16x32_bf16 v[136:139], v[160:163], v[36:39], v[20:23]\n"
"ds_read_b128 v[36:39], v232 offset:256\n"
"v_mfma_f32_16x16x32_bf16 v[136:139], v[164:167], v[40:43], v[136:139]\n"
"ds_read_b128 v[40:43], v233 offset:256\n"
"v_mfma_f32_16x16x32_bf16 v[136:139], v[180:183], v[44:47], v[136:139]\n"
"ds_read_b128 v[44:47], v234 offset:256\n"
"v_mfma_f32_16x16x32_bf16 v[136:139], v[184:187], v[60:63], v[136:139]\n"
"ds_read_b128 v[60:63], v235 offset:256\n"
"v_cvt_pk_bf16_f32 v208, v132, v133\n"
"v_mfma_f32_16x16x32_bf16 v[136:139], v[188:191], v[64:67], v[136:139]\n"
"ds_read_b128 v[64:67], v232 offset:8448\n"
"v_cvt_pk_bf16_f32 v209, v134, v135\n"
"v_mfma_f32_16x16x32_bf16 v[136:139], v[204:207], v[68:71], v[136:139]\n"
"ds_read_b128 v[68:71], v233 offset:8448\n"
"v_mfma_f32_16x16x32_bf16 v[136:139], a[24:27], v[84:87], v[136:139]\n"
"ds_read_b128 v[84:87], v234 offset:8448\n"
"v_mfma_f32_16x16x32_bf16 v[136:139], a[28:31], v[88:91], v[136:139]\n"
"ds_read_b128 v[88:91], v235 offset:8448\n"
"v_mfma_f32_16x16x32_bf16 v[136:139], a[32:35], v[92:95], v[136:139]\n"
"ds_read_b128 v[92:95], v232 offset:16640\n"
"v_mfma_f32_16x16x32_bf16 v[136:139], a[36:39], v[108:111], v[136:139]\n"
"ds_read_b128 v[108:111], v233 offset:16640\n"
"v_mfma_f32_16x16x32_bf16 v[136:139], a[40:43], v[112:115], v[136:139]\n"
"ds_read_b128 v[112:115], v234 offset:16640\n"
"v_mfma_f32_16x16x32_bf16 v[136:139], a[44:47], v[116:119], v[136:139]\n"
"ds_read_b128 v[116:119], v235 offset:16640\n"
"s_waitcnt lgkmcnt(8)\n"
"v_mfma_f32_16x16x32_bf16 v[132:135], a[240:243], v[36:39], v[0:3]\n"
"ds_write_b64 v244, v[208:209] offset:24704\n"
"v_mfma_f32_16x16x32_bf16 v[132:135], a[244:247], v[40:43], v[132:135]\n"
"v_mfma_f32_16x16x32_bf16 v[132:135], a[248:251], v[44:47], v[132:135]\n"
"v_mfma_f32_16x16x32_bf16 v[132:135], a[252:255], v[60:63], v[132:135]\n"
"v_cvt_pk_bf16_f32 v210, v136, v137\n"
"s_waitcnt lgkmcnt(5)\n"
"v_mfma_f32_16x16x32_bf16 v[132:135], a[48:51], v[64:67], v[132:135]\n"
"v_cvt_pk_bf16_f32 v211, v138, v139\n"
"v_mfma_f32_16x16x32_bf16 v[132:135], a[52:55], v[68:71], v[132:135]\n"
"s_add_u32 s52, s20, s43\n"
"s_addc_u32 s53, s21, 0\n"
"v_mfma_f32_16x16x32_bf16 v[132:135], a[56:59], v[84:87], v[132:135]\n"
"ds_read_b128 v[216:219], v246 offset:12288\n"
"v_mfma_f32_16x16x32_bf16 v[132:135], a[60:63], v[88:91], v[132:135]\n"
"ds_read_b128 v[220:223], v246 offset:13312\n"
"s_waitcnt lgkmcnt(3)\n"
"v_mfma_f32_16x16x32_bf16 v[132:135], a[64:67], v[92:95], v[132:135]\n"
"ds_read_b128 v[224:227], v246 offset:14336\n"
"v_mfma_f32_16x16x32_bf16 v[132:135], a[68:71], v[108:111], v[132:135]\n"
"ds_write_b64 v244, v[210:211] offset:24712\n"
"v_mfma_f32_16x16x32_bf16 v[132:135], a[72:75], v[112:115], v[132:135]\n"
"s_add_u32 m0, s31, 0x12000\n"
"v_mfma_f32_16x16x32_bf16 v[132:135], a[76:79], v[116:119], v[132:135]\n"
"global_load_lds_dwordx4 v237, s[52:53] offset:0 nt\n"
"v_mfma_f32_16x16x32_bf16 v[136:139], a[124:127], v[116:119], v[4:7]\n"
"s_add_u32 s83, s83, s86\n"
"v_mfma_f32_16x16x32_bf16 v[136:139], a[120:123], v[112:115], v[136:139]\n"
"s_sub_i32 s88, s88, 6\n"
"v_mfma_f32_16x16x32_bf16 v[136:139], a[116:119], v[108:111], v[136:139]\n"
"s_max_i32 s88, s88, 0\n"
"v_mfma_f32_16x16x32_bf16 v[136:139], a[112:115], v[92:95], v[136:139]\n"
"v_cvt_pk_bf16_f32 v208, v132, v133\n"
"v_mfma_f32_16x16x32_bf16 v[136:139], a[108:111], v[88:91], v[136:139]\n"
"v_cvt_pk_bf16_f32 v209, v134, v135\n"
"v_mfma_f32_16x16x32_bf16 v[136:139], a[104:107], v[84:87], v[136:139]\n"
"v_mfma_f32_16x16x32_bf16 v[136:139], a[100:103], v[68:71], v[136:139]\n"
"v_mfma_f32_16x16x32_bf16 v[136:139], a[96:99], v[64:67], v[136:139]\n"
"s_waitcnt lgkmcnt(1)\n"
"v_mfma_f32_16x16x32_bf16 v[136:139], a[92:95], v[60:63], v[136:139]\n"
"v_mfma_f32_16x16x32_bf16 v[136:139], a[88:91], v[44:47], v[136:139]\n"
"global_store_dwordx4 v247, v[216:219], s[54:55] offset:768 sc1\n"
"ds_write_b64 v244, v[208:209] offset:0\n"
"v_mfma_f32_16x16x32_bf16 v[136:139], a[84:87], v[40:43], v[136:139]\n"
"v_mfma_f32_16x16x32_bf16 v[136:139], a[80:83], v[36:39], v[136:139]\n"
"v_mfma_f32_16x16x32_bf16 v[132:135], a[128:131], v[36:39], v[8:11]\n"
"v_mfma_f32_16x16x32_bf16 v[132:135], a[132:135], v[40:43], v[132:135]\n"
"v_mfma_f32_16x16x32_bf16 v[132:135], a[136:139], v[44:47], v[132:135]\n"
"v_mfma_f32_16x16x32_bf16 v[132:135], a[140:143], v[60:63], v[132:135]\n"
"v_cvt_pk_bf16_f32 v210, v136, v137\n"
"v_mfma_f32_16x16x32_bf16 v[132:135], a[144:147], v[64:67], v[132:135]\n"
"v_cvt_pk_bf16_f32 v211, v138, v139\n"
"v_mfma_f32_16x16x32_bf16 v[132:135], a[148:151], v[68:71], v[132:135]\n"
"v_mfma_f32_16x16x32_bf16 v[132:135], a[152:155], v[84:87], v[132:135]\n"
"v_mfma_f32_16x16x32_bf16 v[132:135], a[156:159], v[88:91], v[132:135]\n"
"v_mfma_f32_16x16x32_bf16 v[132:135], a[160:163], v[92:95], v[132:135]\n"
"v_mfma_f32_16x16x32_bf16 v[132:135], a[164:167], v[108:111], v[132:135]\n"
"s_memrealtime s[84:85]\n"
"v_mfma_f32_16x16x32_bf16 v[132:135], a[168:171], v[112:115], v[132:135]\n"
"global_store_dwordx4 v252, v[220:223], s[54:55] offset:768 sc1\n"
"ds_write_b64 v244, v[210:211] offset:8\n"
"v_mfma_f32_16x16x32_bf16 v[132:135], a[172:175], v[116:119], v[132:135]\n"
"s_add_u32 m0, s31, 0x13f00\n"
"v_mfma_f32_16x16x32_bf16 v[136:139], a[220:223], v[116:119], v[12:15]\n"
"global_load_lds_dwordx4 v237, s[52:53] offset:256 nt\n"
"v_mfma_f32_16x16x32_bf16 v[136:139], a[216:219], v[112:115], v[136:139]\n"
"v_mfma_f32_16x16x32_bf16 v[136:139], a[212:215], v[108:111], v[136:139]\n"
"v_mfma_f32_16x16x32_bf16 v[136:139], a[208:211], v[92:95], v[136:139]\n"
"v_cvt_pk_bf16_f32 v212, v132, v133\n"
"v_mfma_f32_16x16x32_bf16 v[136:139], a[204:207], v[88:91], v[136:139]\n"
"v_mov_b32 v242, 18\n"
"v_mfma_f32_16x16x32_bf16 v[136:139], a[200:203], v[84:87], v[136:139]\n"
"v_cvt_pk_bf16_f32 v213, v134, v135\n"
"v_mfma_f32_16x16x32_bf16 v[136:139], a[196:199], v[68:71], v[136:139]\n"
"s_waitcnt vmcnt(16) lgkmcnt(2)\n"
"ds_write_b32 v240, v242\n"
"v_mfma_f32_16x16x32_bf16 v[136:139], a[192:195], v[64:67], v[136:139]\n"
"v_mfma_f32_16x16x32_bf16 v[136:139], a[188:191], v[60:63], v[136:139]\n"
"v_mfma_f32_16x16x32_bf16 v[136:139], a[184:187], v[44:47], v[136:139]\n"
"v_mfma_f32_16x16x32_bf16 v[136:139], a[180:183], v[40:43], v[136:139]\n"
"global_store_dwordx4 v253, v[224:227], s[54:55] offset:768 sc1\n"
"ds_write_b64 v245, v[212:213] offset:0\n"
"v_mfma_f32_16x16x32_bf16 v[136:139], a[176:179], v[36:39], v[136:139]\n"
"v_mfma_f32_16x16x32_bf16 v[132:135], a[224:227], v[36:39], v[16:19]\n"
"v_mfma_f32_16x16x32_bf16 v[132:135], a[228:231], v[40:43], v[132:135]\n"
"v_mfma_f32_16x16x32_bf16 v[132:135], a[232:235], v[44:47], v[132:135]\n"
"ds_read_b32 v243, v241\n"
"v_mfma_f32_16x16x32_bf16 v[132:135], a[236:239], v[60:63], v[132:135]\n"
"v_cvt_pk_bf16_f32 v214, v136, v137\n"
"v_mfma_f32_16x16x32_bf16 v[132:135], a[0:3], v[64:67], v[132:135]\n"
"v_cvt_pk_bf16_f32 v215, v138, v139\n"
"v_mfma_f32_16x16x32_bf16 v[132:135], a[4:7], v[68:71], v[132:135]\n"
"v_mfma_f32_16x16x32_bf16 v[132:135], a[8:11], v[84:87], v[132:135]\n"
"s_waitcnt lgkmcnt(0)\n"
"v_cmp_gt_u32 s[56:57], 18, v243\n"
"v_mfma_f32_16x16x32_bf16 v[132:135], a[12:15], v[88:91], v[132:135]\n"
"s_sub_u32 s84, s84, s83\n"
"v_mfma_f32_16x16x32_bf16 v[132:135], a[16:19], v[92:95], v[132:135]\n"
"s_or_b64 s[56:57], s[56:57], vcc\n"
"s_cbranch_scc1 LB18\n"
"LB18R:\n"
"v_mfma_f32_16x16x32_bf16 v[132:135], a[20:23], v[108:111], v[132:135]\n"
"s_cmp_lt_i32 s84, s88\n"
"s_cselect_b64 vcc, -1, 0\n"
"v_mfma_f32_16x16x32_bf16 v[132:135], v[140:143], v[112:115], v[132:135]\n"
"ds_write_b64 v245, v[214:215] offset:8\n"
"v_mfma_f32_16x16x32_bf16 v[132:135], v[156:159], v[116:119], v[132:135]\n"
"s_add_u32 m0, s31, 0x15e00\n"
"v_mfma_f32_16x16x32_bf16 v[136:139], a[44:47], v[116:119], v[20:23]\n"
"ds_read_b128 v[116:119], v235 offset:40960\n"
"v_mfma_f32_16x16x32_bf16 v[136:139], a[40:43], v[112:115], v[136:139]\n"
"ds_read_b128 v[112:115], v234 offset:40960\n"
"v_mfma_f32_16x16x32_bf16 v[136:139], a[36:39], v[108:111], v[136:139]\n"
"ds_read_b128 v[108:111], v233 offset:40960\n"
"v_mfma_f32_16x16x32_bf16 v[136:139], a[32:35], v[92:95], v[136:139]\n"
"ds_read_b128 v[92:95], v232 offset:40960\n"
"v_cvt_pk_bf16_f32 v208, v132, v133\n"
"v_mfma_f32_16x16x32_bf16 v[136:139], a[28:31], v[88:91], v[136:139]\n"
"ds_read_b128 v[88:91], v235 offset:32768\n"
"v_cvt_pk_bf16_f32 v209, v134, v135\n"
"v_mfma_f32_16x16x32_bf16 v[136:139], a[24:27], v[84:87], v[136:139]\n"
"ds_read_b128 v[84:87], v234 offset:32768\n"
"v_mfma_f32_16x16x32_bf16 v[136:139], v[204:207], v[68:71], v[136:139]\n"
"ds_read_b128 v[68:71], v233 offset:32768\n"
"v_mfma_f32_16x16x32_bf16 v[136:139], v[188:191], v[64:67], v[136:139]\n"
"ds_read_b128 v[64:67], v232 offset:32768\n"
"v_mfma_f32_16x16x32_bf16 v[136:139], v[184:187], v[60:63], v[136:139]\n"
"ds_read_b128 v[60:63], v235 offset:24576\n"
"v_mfma_f32_16x16x32_bf16 v[136:139], v[180:183], v[44:47], v[136:139]\n"
"ds_read_b128 v[44:47], v234 offset:24576\n"
"v_mfma_f32_16x16x32_bf16 v[136:139], v[164:167], v[40:43], v[136:139]\n"
"ds_read_b128 v[40:43], v233 offset:24576\n"
"v_mfma_f32_16x16x32_bf16 v[136:139], v[160:163], v[36:39], v[136:139]\n"
"ds_read_b128 v[36:39], v232 offset:24576\n"
"s_waitcnt lgkmcnt(8)\n"
"v_mfma_f32_16x16x32_bf16 v[132:135], a[76:79], v[116:119], v[0:3]\n"
"ds_write_b64 v244, v[208:209] offset:128\n"
"v_mfma_f32_16x16x32_bf16 v[132:135], a[72:75], v[112:115], v[132:135]\n"
"global_load_lds_dwordx4 v237, s[52:53] offset:512 nt\n"
"v_mfma_f32_16x16x32_bf16 v[132:135], a[68:71], v[108:111], v[132:135]\n"
"s_add_u32 s54, s26, s40\n"
"s_addc_u32 s55, s27, 0\n"
"v_mfma_f32_16x16x32_bf16 v[132:135], a[64:67], v[92:95], v[132:135]\n"
"v_cvt_pk_bf16_f32 v210, v136, v137\n"
"s_waitcnt lgkmcnt(5)\n"
"v_mfma_f32_16x16x32_bf16 v[132:135], a[60:63], v[88:91], v[132:135]\n"
"v_cvt_pk_bf16_f32 v211, v138, v139\n"
"v_mfma_f32_16x16x32_bf16 v[132:135], a[56:59], v[84:87], v[132:135]\n"
"s_sub_i32 s84, s84, s19\n"
"v_mfma_f32_16x16x32_bf16 v[132:135], a[52:55], v[68:71], v[132:135]\n"
"ds_read_b128 v[216:219], v246 offset:24576\n"
"v_mfma_f32_16x16x32_bf16 v[132:135], a[48:51], v[64:67], v[132:135]\n"
"ds_read_b128 v[220:223], v246 offset:25600\n"
"s_waitcnt lgkmcnt(3)\n"
"v_mfma_f32_16x16x32_bf16 v[132:135], a[252:255], v[60:63], v[132:135]\n"
"ds_read_b128 v[224:227], v246 offset:26624\n"
"v_mfma_f32_16x16x32_bf16 v[132:135], a[248:251], v[44:47], v[132:135]\n"
"ds_write_b64 v244, v[210:211] offset:136\n"
"v_mfma_f32_16x16x32_bf16 v[132:135], a[244:247], v[40:43], v[132:135]\n"
"s_add_u32 m0, s31, 0x13000\n"
"v_mfma_f32_16x16x32_bf16 v[132:135], a[240:243], v[36:39], v[132:135]\n"
"global_load_lds_dwordx4 v238, s[52:53] offset:0 nt\n"
"v_mfma_f32_16x16x32_bf16 v[136:139], a[80:83], v[36:39], v[4:7]\n"
"s_max_i32 s88, s88, s84\n"
"v_mfma_f32_16x16x32_bf16 v[136:139], a[84:87], v[40:43], v[136:139]\n"
"v_mfma_f32_16x16x32_bf16 v[136:139], a[88:91], v[44:47], v[136:139]\n"
"v_mfma_f32_16x16x32_bf16 v[136:139], a[92:95], v[60:63], v[136:139]\n"
"v_cvt_pk_bf16_f32 v208, v132, v133\n"
"v_mfma_f32_16x16x32_bf16 v[136:139], a[96:99], v[64:67], v[136:139]\n"
"v_cvt_pk_bf16_f32 v209, v134, v135\n"
"v_mfma_f32_16x16x32_bf16 v[136:139], a[100:103], v[68:71], v[136:139]\n"
"v_mfma_f32_16x16x32_bf16 v[136:139], a[104:107], v[84:87], v[136:139]\n"
"v_mfma_f32_16x16x32_bf16 v[136:139], a[108:111], v[88:91], v[136:139]\n"
"s_waitcnt lgkmcnt(1)\n"
"v_mfma_f32_16x16x32_bf16 v[136:139], a[112:115], v[92:95], v[136:139]\n"
"v_mfma_f32_16x16x32_bf16 v[136:139], a[116:119], v[108:111], v[136:139]\n"
"global_store_dwordx4 v247, v[216:219], s[54:55] offset:0 sc1\n"
"ds_write_b64 v244, v[208:209] offset:12288\n"
"v_mfma_f32_16x16x32_bf16 v[136:139], a[120:123], v[112:115], v[136:139]\n"
"v_mfma_f32_16x16x32_bf16 v[136:139], a[124:127], v[116:119], v[136:139]\n"
"v_mfma_f32_16x16x32_bf16 v[132:135], a[172:175], v[116:119], v[8:11]\n"
"v_mfma_f32_16x16x32_bf16 v[132:135], a[168:171], v[112:115], v[132:135]\n"
"v_mfma_f32_16x16x32_bf16 v[132:135], a[164:167], v[108:111], v[132:135]\n"
"v_mfma_f32_16x16x32_bf16 v[132:135], a[160:163], v[92:95], v[132:135]\n"
"v_cvt_pk_bf16_f32 v210, v136, v137\n"
"v_mfma_f32_16x16x32_bf16 v[132:135], a[156:159], v[88:91], v[132:135]\n"
"v_cvt_pk_bf16_f32 v211, v138, v139\n"
"v_mfma_f32_16x16x32_bf16 v[132:135], a[152:155], v[84:87], v[132:135]\n"
"v_mfma_f32_16x16x32_bf16 v[132:135], a[148:151], v[68:71], v[132:135]\n"
"v_mfma_f32_16x16x32_bf16 v[132:135], a[144:147], v[64:67], v[132:135]\n"
"v_mfma_f32_16x16x32_bf16 v[132:135], a[140:143], v[60:63], v[132:135]\n"
"v_mfma_f32_16x16x32_bf16 v[132:135], a[136:139], v[44:47], v[132:135]\n"
"global_store_dwordx4 v252, v[220:223], s[54:55] offset:0 sc1\n"
"ds_write_b64 v244, v[210:211] offset:12296\n"
"v_mfma_f32_16x16x32_bf16 v[132:135], a[132:135], v[40:43], v[132:135]\n"
"s_add_u32 m0, s31, 0x14f00\n"
"v_mfma_f32_16x16x32_bf16 v[132:135], a[128:131], v[36:39], v[132:135]\n"
"global_load_lds_dwordx4 v238, s[52:53] offset:256 nt\n"
"v_mfma_f32_16x16x32_bf16 v[136:139], a[176:179], v[36:39], v[12:15]\n"
"v_mfma_f32_16x16x32_bf16 v[136:139], a[180:183], v[40:43], v[136:139]\n"
"v_mfma_f32_16x16x32_bf16 v[136:139], a[184:187], v[44:47], v[136:139]\n"
"v_mfma_f32_16x16x32_bf16 v[136:139], a[188:191], v[60:63], v[136:139]\n"
"v_cvt_pk_bf16_f32 v212, v132, v133\n"
"v_mfma_f32_16x16x32_bf16 v[136:139], a[192:195], v[64:67], v[136:139]\n"
"v_mov_b32 v242, 19\n"
"v_mfma_f32_16x16x32_bf16 v[136:139], a[196:199], v[68:71], v[136:139]\n"
"v_cvt_pk_bf16_f32 v213, v134, v135\n"
"v_mfma_f32_16x16x32_bf16 v[136:139], a[200:203], v[84:87], v[136:139]\n"
"s_waitcnt lgkmcnt(2)\n"
"ds_write_b32 v240, v242\n"
"v_mfma_f32_16x16x32_bf16 v[136:139], a[204:207], v[88:91], v[136:139]\n"
"v_mfma_f32_16x16x32_bf16 v[136:139], a[208:211], v[92:95], v[136:139]\n"
"v_mfma_f32_16x16x32_bf16 v[136:139], a[212:215], v[108:111], v[136:139]\n"
"v_mfma_f32_16x16x32_bf16 v[136:139], a[216:219], v[112:115], v[136:139]\n"
"global_store_dwordx4 v253, v[224:227], s[54:55] offset:0 sc1\n"
"ds_write_b64 v245, v[212:213] offset:12288\n"
"v_mfma_f32_16x16x32_bf16 v[136:139], a[220:223], v[116:119], v[136:139]\n"
"v_mfma_f32_16x16x32_bf16 v[132:135], v[156:159], v[116:119], v[16:19]\n"
"v_mfma_f32_16x16x32_bf16 v[132:135], v[140:143], v[112:115], v[132:135]\n"
"v_mfma_f32_16x16x32_bf16 v[132:135], a[20:23], v[108:111], v[132:135]\n"
"ds_read_b32 v243, v241\n"
"v_mfma_f32_16x16x32_bf16 v[132:135], a[16:19], v[92:95], v[132:135]\n"
"v_cvt_pk_bf16_f32 v214, v136, v137\n"
"v_mfma_f32_16x16x32_bf16 v[132:135], a[12:15], v[88:91], v[132:135]\n"
"v_cvt_pk_bf16_f32 v215, v138, v139\n"
"v_mfma_f32_16x16x32_bf16 v[132:135], a[8:11], v[84:87], v[132:135]\n"
"v_mfma_f32_16x16x32_bf16 v[132:135], a[4:7], v[68:71], v[132:135]\n"
"s_waitcnt lgkmcnt(0)\n"
"v_cmp_gt_u32 s[56:57], 19, v243\n"
"v_mfma_f32_16x16x32_bf16 v[132:135], a[0:3], v[64:67], v[132:135]\n"
"v_mfma_f32_16x16x32_bf16 v[132:135], a[236:239], v[60:63], v[132:135]\n"
"s_or_b64 s[56:57], s[56:57], vcc\n"
"s_cbranch_scc1 LB19\n"
"LB19R:\n"
"v_mfma_f32_16x16x32_bf16 v[132:135], a[232:235], v[44:47], v[132:135]\n"
"ds_write_b64 v245, v[214:215] offset:12296\n"
"v_mfma_f32_16x16x32_bf16 v[132:135], a[228:231], v[40:43], v[132:135]\n"
"v_mfma_f32_16x16x32_bf16 v[132:135], a[224:227], v[36:39], v[132:135]\n"
"s_mov_b64 exec, s[8:9]\n"
"s_cbranch_execz LHT10\n"
"LHR10:\n"
"s_mov_b64 exec, -1\n"
"v_mfma_f32_16x16x32_bf16 v[136:139], v[160:163], v[36:39], v[20:23]\n"
"ds_read_b128 v[36:39], v232 offset:24832\n"
"v_mfma_f32_16x16x32_bf16 v[136:139], v[164:167], v[40:43], v[136:139]\n"
"ds_read_b128 v[40:43], v233 offset:24832\n"
"v_mfma_f32_16x16x32_bf16 v[136:139], v[180:183], v[44:47], v[136:139]\n"
"ds_read_b128 v[44:47], v234 offset:24832\n"
"v_mfma_f32_16x16x32_bf16 v[136:139], v[184:187], v[60:63], v[136:139]\n"
"ds_read_b128 v[60:63], v235 offset:24832\n"
"v_cvt_pk_bf16_f32 v208, v132, v133\n"
"v_mfma_f32_16x16x32_bf16 v[136:139], v[188:191], v[64:67], v[136:139]\n"
"ds_read_b128 v[64:67], v232 offset:33024\n"
"v_cvt_pk_bf16_f32 v209, v134, v135\n"
"v_mfma_f32_16x16x32_bf16 v[136:139], v[204:207], v[68:71], v[136:139]\n"
"ds_read_b128 v[68:71], v233 offset:33024\n"
"v_mfma_f32_16x16x32_bf16 v[136:139], a[24:27], v[84:87], v[136:139]\n"
"ds_read_b128 v[84:87], v234 offset:33024\n"
"v_mfma_f32_16x16x32_bf16 v[136:139], a[28:31], v[88:91], v[136:139]\n"
"ds_read_b128 v[88:91], v235 offset:33024\n"
"v_mfma_f32_16x16x32_bf16 v[136:139], a[32:35], v[92:95], v[136:139]\n"
"ds_read_b128 v[92:95], v232 offset:41216\n"
"v_mfma_f32_16x16x32_bf16 v[136:139], a[36:39], v[108:111], v[136:139]\n"
"ds_read_b128 v[108:111], v233 offset:41216\n"
"ds_write_b64 v244, v[208:209] offset:12416\n"
"v_mfma_f32_16x16x32_bf16 v[136:139], a[40:43], v[112:115], v[136:139]\n"
"ds_read_b128 v[112:115], v234 offset:41216\n"
"v_mfma_f32_16x16x32_bf16 v[136:139], a[44:47], v[116:119], v[136:139]\n"
"ds_read_b128 v[116:119], v235 offset:41216\n"
"s_waitcnt lgkmcnt(9)\n"
"v_mfma_f32_16x16x32_bf16 v[132:135], a[240:243], v[36:39], v[0:3]\n"
"v_add_u32 v232, 0xc000, v232\n"
"v_mfma_f32_16x16x32_bf16 v[132:135], a[244:247], v[40:43], v[132:135]\n"
"v_add_u32 v233, 0xc000, v233\n"
"v_mfma_f32_16x16x32_bf16 v[132:135], a[248:251], v[44:47], v[132:135]\n"
"v_add_u32 v234, 0xc000, v234\n"
"v_mfma_f32_16x16x32_bf16 v[132:135], a[252:255], v[60:63], v[132:135]\n"
"v_add_u32 v235, 0xc000, v235\n"
"s_waitcnt lgkmcnt(5)\n"
"v_mfma_f32_16x16x32_bf16 v[132:135], a[48:51], v[64:67], v[132:135]\n"
"s_add_u32 m0, s31, 0x16e00\n"
"v_mfma_f32_16x16x32_bf16 v[132:135], a[52:55], v[68:71], v[132:135]\n"
"global_load_lds_dwordx4 v238, s[52:53] offset:512 nt\n"
"v_mfma_f32_16x16x32_bf16 v[132:135], a[56:59], v[84:87], v[132:135]\n"
"v_cvt_pk_bf16_f32 v210, v136, v137\n"
"v_mfma_f32_16x16x32_bf16 v[132:135], a[60:63], v[88:91], v[132:135]\n"
"v_cvt_pk_bf16_f32 v211, v138, v139\n"
"s_waitcnt lgkmcnt(0)\n"
"v_mfma_f32_16x16x32_bf16 v[132:135], a[64:67], v[92:95], v[132:135]\n"
"ds_read_b128 v[216:219], v246 offset:0\n"
"v_mfma_f32_16x16x32_bf16 v[132:135], a[68:71], v[108:111], v[132:135]\n"
"ds_read_b128 v[220:223], v246 offset:1024\n"
"v_mfma_f32_16x16x32_bf16 v[132:135], a[72:75], v[112:115], v[132:135]\n"
"ds_read_b128 v[224:227], v246 offset:2048\n"
"v_mfma_f32_16x16x32_bf16 v[132:135], a[76:79], v[116:119], v[132:135]\n"
"s_add_u32 s52, s20, s44\n"
"s_addc_u32 s53, s21, 0\n"
"v_mfma_f32_16x16x32_bf16 v[136:139], a[124:127], v[116:119], v[4:7]\n"
"ds_write_b64 v244, v[210:211] offset:12424\n"
"v_mfma_f32_16x16x32_bf16 v[136:139], a[120:123], v[112:115], v[136:139]\n"
"s_add_u32 m0, s31, 0x0\n"
"v_mfma_f32_16x16x32_bf16 v[136:139], a[116:119], v[108:111], v[136:139]\n"
"global_load_lds_dwordx4 v237, s[52:53] offset:0 nt\n"
"v_mfma_f32_16x16x32_bf16 v[136:139], a[112:115], v[92:95], v[136:139]\n"
"v_cvt_pk_bf16_f32 v208, v132, v133\n"
"v_mfma_f32_16x16x32_bf16 v[136:139], a[108:111], v[88:91], v[136:139]\n"
"v_cvt_pk_bf16_f32 v209, v134, v135\n"
"v_mfma_f32_16x16x32_bf16 v[136:139], a[104:107], v[84:87], v[136:139]\n"
"v_lshlrev_b32 v0, 16, v120\n"
"v_mfma_f32_16x16x32_bf16 v[136:139], a[100:103], v[68:71], v[136:139]\n"
"v_and_b32 v1, 0xffff0000, v120\n"
"v_mfma_f32_16x16x32_bf16 v[136:139], a[96:99], v[64:67], v[136:139]\n"
"s_waitcnt lgkmcnt(1)\n"
"v_mfma_f32_16x16x32_bf16 v[136:139], a[92:95], v[60:63], v[136:139]\n"
"v_lshlrev_b32 v2, 16, v121\n"
"v_mfma_f32_16x16x32_bf16 v[136:139], a[88:91], v[44:47], v[136:139]\n"
"global_store_dwordx4 v247, v[216:219], s[54:55] offset:768 sc1\n"
"ds_write_b64 v244, v[208:209] offset:24576\n"
"v_mfma_f32_16x16x32_bf16 v[136:139], a[84:87], v[40:43], v[136:139]\n"
"v_and_b32 v3, 0xffff0000, v121\n"
"v_mfma_f32_16x16x32_bf16 v[136:139], a[80:83], v[36:39], v[136:139]\n"
"v_lshlrev_b32 v4, 16, v122\n"
"v_mfma_f32_16x16x32_bf16 v[132:135], a[128:131], v[36:39], v[8:11]\n"
"v_and_b32 v5, 0xffff0000, v122\n"
"v_mfma_f32_16x16x32_bf16 v[132:135], a[132:135], v[40:43], v[132:135]\n"
"v_lshlrev_b32 v6, 16, v123\n"
"v_mfma_f32_16x16x32_bf16 v[132:135], a[136:139], v[44:47], v[132:135]\n"
"v_and_b32 v7, 0xffff0000, v123\n"
"v_mfma_f32_16x16x32_bf16 v[132:135], a[140:143], v[60:63], v[132:135]\n"
"v_cvt_pk_bf16_f32 v210, v136, v137\n"
"v_mfma_f32_16x16x32_bf16 v[132:135], a[144:147], v[64:67], v[132:135]\n"
"v_cvt_pk_bf16_f32 v211, v138, v139\n"
"v_mfma_f32_16x16x32_bf16 v[132:135], a[148:151], v[68:71], v[132:135]\n"
"v_lshlrev_b32 v8, 16, v124\n"
"v_mfma_f32_16x16x32_bf16 v[132:135], a[152:155], v[84:87], v[132:135]\n"
"v_and_b32 v9, 0xffff0000, v124\n"
"v_mfma_f32_16x16x32_bf16 v[132:135], a[156:159], v[88:91], v[132:135]\n"
"v_lshlrev_b32 v10, 16, v125\n"
"v_mfma_f32_16x16x32_bf16 v[132:135], a[160:163], v[92:95], v[132:135]\n"
"v_and_b32 v11, 0xffff0000, v125\n"
"v_mfma_f32_16x16x32_bf16 v[132:135], a[164:167], v[108:111], v[132:135]\n"
"s_memrealtime s[84:85]\n"
"v_mfma_f32_16x16x32_bf16 v[132:135], a[168:171], v[112:115], v[132:135]\n"
"global_store_dwordx4 v252, v[220:223], s[54:55] offset:768 sc1\n"
"ds_write_b64 v244, v[210:211] offset:24584\n"
"v_mfma_f32_16x16x32_bf16 v[132:135], a[172:175], v[116:119], v[132:135]\n"
"s_add_u32 s83, s83, s86\n"
"v_mfma_f32_16x16x32_bf16 v[136:139], a[220:223], v[116:119], v[12:15]\n"
"s_add_u32 m0, s31, 0x1f00\n"
"v_mfma_f32_16x16x32_bf16 v[136:139], a[216:219], v[112:115], v[136:139]\n"
"global_load_lds_dwordx4 v237, s[52:53] offset:256 nt\n"
"v_mfma_f32_16x16x32_bf16 v[136:139], a[212:215], v[108:111], v[136:139]\n"
"s_sub_i32 s88, s88, 6\n"
"v_mfma_f32_16x16x32_bf16 v[136:139], a[208:211], v[92:95], v[136:139]\n"
"v_cvt_pk_bf16_f32 v212, v132, v133\n"
"v_mfma_f32_16x16x32_bf16 v[136:139], a[204:207], v[88:91], v[136:139]\n"
"v_mov_b32 v242, 20\n"
"v_mfma_f32_16x16x32_bf16 v[136:139], a[200:203], v[84:87], v[136:139]\n"
"v_cvt_pk_bf16_f32 v213, v134, v135\n"
"v_mfma_f32_16x16x32_bf16 v[136:139], a[196:199], v[68:71], v[136:139]\n"
"s_waitcnt vmcnt(16) lgkmcnt(2)\n"
"ds_write_b32 v240, v242\n"
"v_mfma_f32_16x16x32_bf16 v[136:139], a[192:195], v[64:67], v[136:139]\n"
"v_lshlrev_b32 v12, 16, v126\n"
"v_mfma_f32_16x16x32_bf16 v[136:139], a[188:191], v[60:63], v[136:139]\n"
"v_and_b32 v13, 0xffff0000, v126\n"
"v_mfma_f32_16x16x32_bf16 v[136:139], a[184:187], v[44:47], v[136:139]\n"
"v_lshlrev_b32 v14, 16, v127\n"
"v_mfma_f32_16x16x32_bf16 v[136:139], a[180:183], v[40:43], v[136:139]\n"
"global_store_dwordx4 v253, v[224:227], s[54:55] offset:768 sc1\n"
"ds_write_b64 v245, v[212:213] offset:24576\n"
"v_mfma_f32_16x16x32_bf16 v[136:139], a[176:179], v[36:39], v[136:139]\n"
"v_and_b32 v15, 0xffff0000, v127\n"
"v_mfma_f32_16x16x32_bf16 v[132:135], a[224:227], v[36:39], v[16:19]\n"
"s_max_i32 s88, s88, 0\n"
"v_mfma_f32_16x16x32_bf16 v[132:135], a[228:231], v[40:43], v[132:135]\n"
"v_mfma_f32_16x16x32_bf16 v[132:135], a[232:235], v[44:47], v[132:135]\n"
"ds_read_b32 v243, v241\n"
"v_mfma_f32_16x16x32_bf16 v[132:135], a[236:239], v[60:63], v[132:135]\n"
"v_cvt_pk_bf16_f32 v214, v136, v137\n"
"v_mfma_f32_16x16x32_bf16 v[132:135], a[0:3], v[64:67], v[132:135]\n"
"v_cvt_pk_bf16_f32 v215, v138, v139\n"
"v_mfma_f32_16x16x32_bf16 v[132:135], a[4:7], v[68:71], v[132:135]\n"
"v_lshlrev_b32 v16, 16, v128\n"
"v_mfma_f32_16x16x32_bf16 v[132:135], a[8:11], v[84:87], v[132:135]\n"
"s_waitcnt lgkmcnt(0)\n"
"v_cmp_gt_u32 s[56:57], 20, v243\n"
"v_mfma_f32_16x16x32_bf16 v[132:135], a[12:15], v[88:91], v[132:135]\n"
"s_sub_u32 s84, s84, s83\n"
"v_mfma_f32_16x16x32_bf16 v[132:135], a[16:19], v[92:95], v[132:135]\n"
"s_or_b64 s[56:57], s[56:57], vcc\n"
"s_cbranch_scc1 LB20\n"
"LB20R:\n"
"v_mfma_f32_16x16x32_bf16 v[132:135], a[20:23], v[108:111], v[132:135]\n"
"s_cmp_lt_i32 s84, s88\n"
"s_cselect_b64 vcc, -1, 0\n"
"v_mfma_f32_16x16x32_bf16 v[132:135], v[140:143], v[112:115], v[132:135]\n"
"ds_write_b64 v245, v[214:215] offset:24584\n"
"v_mfma_f32_16x16x32_bf16 v[132:135], v[156:159], v[116:119], v[132:135]\n"
"v_and_b32 v17, 0xffff0000, v128\n"
"v_mfma_f32_16x16x32_bf16 v[136:139], a[44:47], v[116:119], v[20:23]\n"
"ds_read_b128 v[116:119], v235 offset:16384\n"
"v_mfma_f32_16x16x32_bf16 v[136:139], a[40:43], v[112:115], v[136:139]\n"
"ds_read_b128 v[112:115], v234 offset:16384\n"
"v_mfma_f32_16x16x32_bf16 v[136:139], a[36:39], v[108:111], v[136:139]\n"
"ds_read_b128 v[108:111], v233 offset:16384\n"
"v_mfma_f32_16x16x32_bf16 v[136:139], a[32:35], v[92:95], v[136:139]\n"
"ds_read_b128 v[92:95], v232 offset:16384\n"
"v_cvt_pk_bf16_f32 v208, v132, v133\n"
"v_mfma_f32_16x16x32_bf16 v[136:139], a[28:31], v[88:91], v[136:139]\n"
"ds_read_b128 v[88:91], v235 offset:8192\n"
"v_cvt_pk_bf16_f32 v209, v134, v135\n"
"LHT10:\n"
"s_cbranch_execz LHR10\n"
"v_mfma_f32_16x16x32_bf16 v[136:139], a[24:27], v[84:87], v[136:139]\n"
"ds_read_b128 v[84:87], v234 offset:8192\n"
"v_mfma_f32_16x16x32_bf16 v[136:139], v[204:207], v[68:71], v[136:139]\n"
"ds_read_b128 v[68:71], v233 offset:8192\n"
"v_mfma_f32_16x16x32_bf16 v[136:139], v[188:191], v[64:67], v[136:139]\n"
"ds_read_b128 v[64:67], v232 offset:8192\n"
"v_mfma_f32_16x16x32_bf16 v[136:139], v[184:187], v[60:63], v[136:139]\n"
"ds_read_b128 v[60:63], v235 offset:0\n"
"v_mfma_f32_16x16x32_bf16 v[136:139], v[180:183], v[44:47], v[136:139]\n"
"ds_read_b128 v[44:47], v234 offset:0\n"
"v_mfma_f32_16x16x32_bf16 v[136:139], v[164:167], v[40:43], v[136:139]\n"
"ds_read_b128 v[40:43], v233 offset:0\n"
"v_mfma_f32_16x16x32_bf16 v[136:139], v[160:163], v[36:39], v[136:139]\n"
"ds_read_b128 v[36:39], v232 offset:0\n"
"s_waitcnt lgkmcnt(8)\n"
"v_mfma_f32_16x16x32_bf16 v[132:135], a[76:79], v[116:119], v[0:3]\n"
"ds_write_b64 v244, v[208:209] offset:24704\n"
"v_mfma_f32_16x16x32_bf16 v[132:135], a[72:75], v[112:115], v[132:135]\n"
"s_add_u32 m0, s31, 0x3e00\n"
"v_mfma_f32_16x16x32_bf16 v[132:135], a[68:71], v[108:111], v[132:135]\n"
"global_load_lds_dwordx4 v237, s[52:53] offset:512 nt\n"
"v_mfma_f32_16x16x32_bf16 v[132:135], a[64:67], v[92:95], v[132:135]\n"
"v_lshlrev_b32 v18, 16, v129\n"
"v_cvt_pk_bf16_f32 v211, v138, v139\n"
"s_waitcnt lgkmcnt(5)\n"
"v_mfma_f32_16x16x32_bf16 v[132:135], a[60:63], v[88:91], v[132:135]\n"
"v_and_b32 v19, 0xffff0000, v129\n"
"v_mfma_f32_16x16x32_bf16 v[132:135], a[56:59], v[84:87], v[132:135]\n"
"v_lshlrev_b32 v20, 16, v130\n"
"v_mfma_f32_16x16x32_bf16 v[132:135], a[52:55], v[68:71], v[132:135]\n"
"v_and_b32 v21, 0xffff0000, v130\n"
"v_mfma_f32_16x16x32_bf16 v[132:135], a[48:51], v[64:67], v[132:135]\n"
"v_lshlrev_b32 v22, 16, v131\n"
"s_waitcnt lgkmcnt(1)\n"
"v_mfma_f32_16x16x32_bf16 v[132:135], a[252:255], v[60:63], v[132:135]\n"
"v_and_b32 v23, 0xffff0000, v131\n"
"v_mfma_f32_16x16x32_bf16 v[132:135], a[248:251], v[44:47], v[132:135]\n"
"v_cvt_pk_bf16_f32 v210, v136, v137\n"
"v_mfma_f32_16x16x32_bf16 v[132:135], a[244:247], v[40:43], v[132:135]\n"
"s_add_u32 s54, s26, s41\n"
"s_addc_u32 s55, s27, 0\n"
"v_mfma_f32_16x16x32_bf16 v[132:135], a[240:243], v[36:39], v[132:135]\n"
"ds_read_b128 v[216:219], v246 offset:12288\n"
"v_mfma_f32_16x16x32_bf16 v[136:139], a[80:83], v[36:39], v[4:7]\n"
"ds_read_b128 v[220:223], v246 offset:13312\n"
"v_mfma_f32_16x16x32_bf16 v[136:139], a[84:87], v[40:43], v[136:139]\n"
"ds_read_b128 v[224:227], v246 offset:14336\n"
"v_mfma_f32_16x16x32_bf16 v[136:139], a[88:91], v[44:47], v[136:139]\n"
"ds_write_b64 v244, v[210:211] offset:24712\n"
"v_mfma_f32_16x16x32_bf16 v[136:139], a[92:95], v[60:63], v[136:139]\n"
"v_cvt_pk_bf16_f32 v208, v132, v133\n"
"v_mfma_f32_16x16x32_bf16 v[136:139], a[96:99], v[64:67], v[136:139]\n"
"v_cvt_pk_bf16_f32 v209, v134, v135\n"
"v_mfma_f32_16x16x32_bf16 v[136:139], a[100:103], v[68:71], v[136:139]\n"
"s_add_u32 m0, s31, 0x1000\n"
"v_mfma_f32_16x16x32_bf16 v[136:139], a[104:107], v[84:87], v[136:139]\n"
"global_load_lds_dwordx4 v238, s[52:53] offset:0 nt\n"
"v_mfma_f32_16x16x32_bf16 v[136:139], a[108:111], v[88:91], v[136:139]\n"
"s_waitcnt lgkmcnt(1)\n"
"v_mfma_f32_16x16x32_bf16 v[136:139], a[112:115], v[92:95], v[136:139]\n"
"s_sub_i32 s84, s84, s19\n"
"v_mfma_f32_16x16x32_bf16 v[136:139], a[116:119], v[108:111], v[136:139]\n"
"global_store_dwordx4 v247, v[216:219], s[54:55] offset:0 sc1\n"
"ds_write_b64 v244, v[208:209] offset:0\n"
"v_mfma_f32_16x16x32_bf16 v[136:139], a[120:123], v[112:115], v[136:139]\n"
"s_max_i32 s88, s88, s84\n"
"v_mfma_f32_16x16x32_bf16 v[136:139], a[124:127], v[116:119], v[136:139]\n"
"v_mfma_f32_16x16x32_bf16 v[132:135], a[172:175], v[116:119], v[8:11]\n"
"v_mfma_f32_16x16x32_bf16 v[132:135], a[168:171], v[112:115], v[132:135]\n"
"v_mfma_f32_16x16x32_bf16 v[132:135], a[164:167], v[108:111], v[132:135]\n"
"v_mfma_f32_16x16x32_bf16 v[132:135], a[160:163], v[92:95], v[132:135]\n"
"v_cvt_pk_bf16_f32 v210, v136, v137\n"
"v_mfma_f32_16x16x32_bf16 v[132:135], a[156:159], v[88:91], v[132:135]\n"
"v_cvt_pk_bf16_f32 v211, v138, v139\n"
"v_mfma_f32_16x16x32_bf16 v[132:135], a[152:155], v[84:87], v[132:135]\n"
"v_mfma_f32_16x16x32_bf16 v[132:135], a[148:151], v[68:71], v[132:135]\n"
"v_mfma_f32_16x16x32_bf16 v[132:135], a[144:147], v[64:67], v[132:135]\n"
"v_mfma_f32_16x16x32_bf16 v[132:135], a[140:143], v[60:63], v[132:135]\n"
"v_mfma_f32_16x16x32_bf16 v[132:135], a[136:139], v[44:47], v[132:135]\n"
"global_store_dwordx4 v252, v[220:223], s[54:55] offset:0 sc1\n"
"ds_write_b64 v244, v[210:211] offset:8\n"
"v_mfma_f32_16x16x32_bf16 v[132:135], a[132:135], v[40:43], v[132:135]\n"
"v_mfma_f32_16x16x32_bf16 v[132:135], a[128:131], v[36:39], v[132:135]\n"
"v_mfma_f32_16x16x32_bf16 v[136:139], a[176:179], v[36:39], v[12:15]\n"
"v_mfma_f32_16x16x32_bf16 v[136:139], a[180:183], v[40:43], v[136:139]\n"
"v_mfma_f32_16x16x32_bf16 v[136:139], a[184:187], v[44:47], v[136:139]\n"
"v_mfma_f32_16x16x32_bf16 v[136:139], a[188:191], v[60:63], v[136:139]\n"
"v_cvt_pk_bf16_f32 v212, v132, v133\n"
"v_mfma_f32_16x16x32_bf16 v[136:139], a[192:195], v[64:67], v[136:139]\n"
"v_mov_b32 v242, 21\n"
"v_mfma_f32_16x16x32_bf16 v[136:139], a[196:199], v[68:71], v[136:139]\n"
"v_cvt_pk_bf16_f32 v213, v134, v135\n"
"v_mfma_f32_16x16x32_bf16 v[136:139], a[200:203], v[84:87], v[136:139]\n"
"s_waitcnt lgkmcnt(2)\n"
"ds_write_b32 v240, v242\n"
"v_mfma_f32_16x16x32_bf16 v[136:139], a[204:207], v[88:91], v[136:139]\n"
"s_add_u32 m0, s31, 0x2f00\n"
"v_mfma_f32_16x16x32_bf16 v[136:139], a[208:211], v[92:95], v[136:139]\n"
"global_load_lds_dwordx4 v238, s[52:53] offset:256 nt\n"
"v_mfma_f32_16x16x32_bf16 v[136:139], a[212:215], v[108:111], v[136:139]\n"
"v_mfma_f32_16x16x32_bf16 v[136:139], a[216:219], v[112:115], v[136:139]\n"
"global_store_dwordx4 v253, v[224:227], s[54:55] offset:0 sc1\n"
"ds_write_b64 v245, v[212:213] offset:0\n"
"v_mfma_f32_16x16x32_bf16 v[136:139], a[220:223], v[116:119], v[136:139]\n"
"v_mfma_f32_16x16x32_bf16 v[132:135], v[156:159], v[116:119], v[16:19]\n"
"v_mfma_f32_16x16x32_bf16 v[132:135], v[140:143], v[112:115], v[132:135]\n"
"v_mfma_f32_16x16x32_bf16 v[132:135], a[20:23], v[108:111], v[132:135]\n"
"ds_read_b32 v243, v241\n"
"v_mfma_f32_16x16x32_bf16 v[132:135], a[16:19], v[92:95], v[132:135]\n"
"v_cvt_pk_bf16_f32 v214, v136, v137\n"
"v_mfma_f32_16x16x32_bf16 v[132:135], a[12:15], v[88:91], v[132:135]\n"
"v_cvt_pk_bf16_f32 v215, v138, v139\n"
"v_mfma_f32_16x16x32_bf16 v[132:135], a[8:11], v[84:87], v[132:135]\n"
"v_mfma_f32_16x16x32_bf16 v[132:135], a[4:7], v[68:71], v[132:135]\n"
"s_waitcnt lgkmcnt(0)\n"
"v_cmp_gt_u32 s[56:57], 21, v243\n"
"v_mfma_f32_16x16x32_bf16 v[132:135], a[0:3], v[64:67], v[132:135]\n"
"v_mfma_f32_16x16x32_bf16 v[132:135], a[236:239], v[60:63], v[132:135]\n"
"s_or_b64 s[56:57], s[56:57], vcc\n"
"s_cbranch_scc1 LB21\n"
"LB21R:\n"
"v_mfma_f32_16x16x32_bf16 v[132:135], a[232:235], v[44:47], v[132:135]\n"
"ds_write_b64 v245, v[214:215] offset:8\n"
"v_mfma_f32_16x16x32_bf16 v[132:135], a[228:231], v[40:43], v[132:135]\n"
"v_mfma_f32_16x16x32_bf16 v[132:135], a[224:227], v[36:39], v[132:135]\n"
"v_mfma_f32_16x16x32_bf16 v[136:139], v[160:163], v[36:39], v[20:23]\n"
"ds_read_b128 v[36:39], v232 offset:256\n"
"v_mfma_f32_16x16x32_bf16 v[136:139], v[164:167], v[40:43], v[136:139]\n"
"ds_read_b128 v[40:43], v233 offset:256\n"
"v_mfma_f32_16x16x32_bf16 v[136:139], v[180:183], v[44:47], v[136:139]\n"
"ds_read_b128 v[44:47], v234 offset:256\n"
"v_mfma_f32_16x16x32_bf16 v[136:139], v[184:187], v[60:63], v[136:139]\n"
"ds_read_b128 v[60:63], v235 offset:256\n"
"v_cvt_pk_bf16_f32 v208, v132, v133\n"
"v_mfma_f32_16x16x32_bf16 v[136:139], v[188:191], v[64:67], v[136:139]\n"
"ds_read_b128 v[64:67], v232 offset:8448\n"
"v_cvt_pk_bf16_f32 v209, v134, v135\n"
"v_mfma_f32_16x16x32_bf16 v[136:139], v[204:207], v[68:71], v[136:139]\n"
"ds_read_b128 v[68:71], v233 offset:8448\n"
"v_mfma_f32_16x16x32_bf16 v[136:139], a[24:27], v[84:87], v[136:139]\n"
"ds_read_b128 v[84:87], v234 offset:8448\n"
"v_mfma_f32_16x16x32_bf16 v[136:139], a[28:31], v[88:91], v[136:139]\n"
"ds_read_b128 v[88:91], v235 offset:8448\n"
"v_mfma_f32_16x16x32_bf16 v[136:139], a[32:35], v[92:95], v[136:139]\n"
"ds_read_b128 v[92:95], v232 offset:16640\n"
"v_mfma_f32_16x16x32_bf16 v[136:139], a[36:39], v[108:111], v[136:139]\n"
"ds_read_b128 v[108:111], v233 offset:16640\n"
"v_mfma_f32_16x16x32_bf16 v[136:139], a[40:43], v[112:115], v[136:139]\n"
"ds_read_b128 v[112:115], v234 offset:16640\n"
"v_mfma_f32_16x16x32_bf16 v[136:139], a[44:47], v[116:119], v[136:139]\n"
"ds_read_b128 v[116:119], v235 offset:16640\n"
"s_waitcnt lgkmcnt(8)\n"
"v_mfma_f32_16x16x32_bf16 v[132:135], a[240:243], v[36:39], v[0:3]\n"
"ds_write_b64 v244, v[208:209] offset:128\n"
"v_mfma_f32_16x16x32_bf16 v[132:135], a[244:247], v[40:43], v[132:135]\n"
"s_add_u32 m0, s31, 0x4e00\n"
"v_mfma_f32_16x16x32_bf16 v[132:135], a[248:251], v[44:47], v[132:135]\n"
"global_load_lds_dwordx4 v238, s[52:53] offset:512 nt\n"
"v_mfma_f32_16x16x32_bf16 v[132:135], a[252:255], v[60:63], v[132:135]\n"
"v_cvt_pk_bf16_f32 v210, v136, v137\n"
"s_waitcnt lgkmcnt(5)\n"
"v_mfma_f32_16x16x32_bf16 v[132:135], a[48:51], v[64:67], v[132:135]\n"
"v_cvt_pk_bf16_f32 v211, v138, v139\n"
"v_mfma_f32_16x16x32_bf16 v[132:135], a[52:55], v[68:71], v[132:135]\n"
"v_mfma_f32_16x16x32_bf16 v[132:135], a[56:59], v[84:87], v[132:135]\n"
"ds_read_b128 v[216:219], v246 offset:24576\n"
"v_mfma_f32_16x16x32_bf16 v[132:135], a[60:63], v[88:91], v[132:135]\n"
"ds_read_b128 v[220:223], v246 offset:25600\n"
"s_waitcnt lgkmcnt(3)\n"
"v_mfma_f32_16x16x32_bf16 v[132:135], a[64:67], v[92:95], v[132:135]\n"
"ds_read_b128 v[224:227], v246 offset:26624\n"
"v_mfma_f32_16x16x32_bf16 v[132:135], a[68:71], v[108:111], v[132:135]\n"
"ds_write_b64 v244, v[210:211] offset:136\n"
"v_mfma_f32_16x16x32_bf16 v[132:135], a[72:75], v[112:115], v[132:135]\n"
"v_mfma_f32_16x16x32_bf16 v[132:135], a[76:79], v[116:119], v[132:135]\n"
"v_mfma_f32_16x16x32_bf16 v[136:139], a[124:127], v[116:119], v[4:7]\n"
"s_add_u32 s83, s83, s86\n"
"v_mfma_f32_16x16x32_bf16 v[136:139], a[120:123], v[112:115], v[136:139]\n"
"s_sub_i32 s88, s88, 6\n"
"v_mfma_f32_16x16x32_bf16 v[136:139], a[116:119], v[108:111], v[136:139]\n"
"s_max_i32 s88, s88, 0\n"
"v_mfma_f32_16x16x32_bf16 v[136:139], a[112:115], v[92:95], v[136:139]\n"
"v_cvt_pk_bf16_f32 v208, v132, v133\n"
"v_mfma_f32_16x16x32_bf16 v[136:139], a[108:111], v[88:91], v[136:139]\n"
"v_cvt_pk_bf16_f32 v209, v134, v135\n"
"v_mfma_f32_16x16x32_bf16 v[136:139], a[104:107], v[84:87], v[136:139]\n"
"v_mfma_f32_16x16x32_bf16 v[136:139], a[100:103], v[68:71], v[136:139]\n"
"v_mfma_f32_16x16x32_bf16 v[136:139], a[96:99], v[64:67], v[136:139]\n"
"s_waitcnt lgkmcnt(1)\n"
"v_mfma_f32_16x16x32_bf16 v[136:139], a[92:95], v[60:63], v[136:139]\n"
"v_mfma_f32_16x16x32_bf16 v[136:139], a[88:91], v[44:47], v[136:139]\n"
"global_store_dwordx4 v247, v[216:219], s[54:55] offset:768 sc1\n"
"ds_write_b64 v244, v[208:209] offset:12288\n"
"v_mfma_f32_16x16x32_bf16 v[136:139], a[84:87], v[40:43], v[136:139]\n"
"s_add_u32 s52, s20, s45\n"
"s_addc_u32 s53, s21, 0\n"
"v_mfma_f32_16x16x32_bf16 v[136:139], a[80:83], v[36:39], v[136:139]\n"
"s_add_u32 m0, s31, 0x6000\n"
"v_mfma_f32_16x16x32_bf16 v[132:135], a[128:131], v[36:39], v[8:11]\n"
"global_load_lds_dwordx4 v237, s[52:53] offset:0 nt\n"
"v_mfma_f32_16x16x32_bf16 v[132:135], a[132:135], v[40:43], v[132:135]\n"
"v_mfma_f32_16x16x32_bf16 v[132:135], a[136:139], v[44:47], v[132:135]\n"
"v_mfma_f32_16x16x32_bf16 v[132:135], a[140:143], v[60:63], v[132:135]\n"
"v_cvt_pk_bf16_f32 v210, v136, v137\n"
"v_mfma_f32_16x16x32_bf16 v[132:135], a[144:147], v[64:67], v[132:135]\n"
"v_cvt_pk_bf16_f32 v211, v138, v139\n"
"v_mfma_f32_16x16x32_bf16 v[132:135], a[148:151], v[68:71], v[132:135]\n"
"v_mfma_f32_16x16x32_bf16 v[132:135], a[152:155], v[84:87], v[132:135]\n"
"v_mfma_f32_16x16x32_bf16 v[132:135], a[156:159], v[88:91], v[132:135]\n"
"v_mfma_f32_16x16x32_bf16 v[132:135], a[160:163], v[92:95], v[132:135]\n"
"v_mfma_f32_16x16x32_bf16 v[132:135], a[164:167], v[108:111], v[132:135]\n"
"s_memrealtime s[84:85]\n"
"v_mfma_f32_16x16x32_bf16 v[132:135], a[168:171], v[112:115], v[132:135]\n"
"global_store_dwordx4 v252, v[220:223], s[54:55] offset:768 sc1\n"
"ds_write_b64 v244, v[210:211] offset:12296\n"
"v_mfma_f32_16x16x32_bf16 v[132:135], a[172:175], v[116:119], v[132:135]\n"
"v_mfma_f32_16x16x32_bf16 v[136:139], a[220:223], v[116:119], v[12:15]\n"
"v_mfma_f32_16x16x32_bf16 v[136:139], a[216:219], v[112:115], v[136:139]\n"
"v_mfma_f32_16x16x32_bf16 v[136:139], a[212:215], v[108:111], v[136:139]\n"
"v_mfma_f32_16x16x32_bf16 v[136:139], a[208:211], v[92:95], v[136:139]\n"
"v_cvt_pk_bf16_f32 v212, v132, v133\n"
"v_mfma_f32_16x16x32_bf16 v[136:139], a[204:207], v[88:91], v[136:139]\n"
"v_mov_b32 v242, 22\n"
"v_mfma_f32_16x16x32_bf16 v[136:139], a[200:203], v[84:87], v[136:139]\n"
"v_cvt_pk_bf16_f32 v213, v134, v135\n"
"v_mfma_f32_16x16x32_bf16 v[136:139], a[196:199], v[68:71], v[136:139]\n"
"s_waitcnt vmcnt(15) lgkmcnt(2)\n"
"ds_write_b32 v240, v242\n"
"v_mfma_f32_16x16x32_bf16 v[136:139], a[192:195], v[64:67], v[136:139]\n"
"v_mfma_f32_16x16x32_bf16 v[136:139], a[188:191], v[60:63], v[136:139]\n"
"v_mfma_f32_16x16x32_bf16 v[136:139], a[184:187], v[44:47], v[136:139]\n"
"v_mfma_f32_16x16x32_bf16 v[136:139], a[180:183], v[40:43], v[136:139]\n"
"global_store_dwordx4 v253, v[224:227], s[54:55] offset:768 sc1\n"
"ds_write_b64 v245, v[212:213] offset:12288\n"
"v_mfma_f32_16x16x32_bf16 v[136:139], a[176:179], v[36:39], v[136:139]\n"
"s_add_u32 m0, s31, 0x7f00\n"
"v_mfma_f32_16x16x32_bf16 v[132:135], a[224:227], v[36:39], v[16:19]\n"
"global_load_lds_dwordx4 v237, s[52:53] offset:256 nt\n"
"v_mfma_f32_16x16x32_bf16 v[132:135], a[228:231], v[40:43], v[132:135]\n"
"v_mfma_f32_16x16x32_bf16 v[132:135], a[232:235], v[44:47], v[132:135]\n"
"ds_read_b32 v243, v241\n"
"v_mfma_f32_16x16x32_bf16 v[132:135], a[236:239], v[60:63], v[132:135]\n"
"v_cvt_pk_bf16_f32 v214, v136, v137\n"
"v_mfma_f32_16x16x32_bf16 v[132:135], a[0:3], v[64:67], v[132:135]\n"
"v_cvt_pk_bf16_f32 v215, v138, v139\n"
"v_mfma_f32_16x16x32_bf16 v[132:135], a[4:7], v[68:71], v[132:135]\n"
"v_mfma_f32_16x16x32_bf16 v[132:135], a[8:11], v[84:87], v[132:135]\n"
"s_waitcnt lgkmcnt(0)\n"
"v_cmp_gt_u32 s[56:57], 22, v243\n"
"v_mfma_f32_16x16x32_bf16 v[132:135], a[12:15], v[88:91], v[132:135]\n"
"s_sub_u32 s84, s84, s83\n"
"v_mfma_f32_16x16x32_bf16 v[132:135], a[16:19], v[92:95], v[132:135]\n"
"s_or_b64 s[56:57], s[56:57], vcc\n"
"s_cbranch_scc1 LB22\n"
"LB22R:\n"
"v_mfma_f32_16x16x32_bf16 v[132:135], a[20:23], v[108:111], v[132:135]\n"
"s_cmp_lt_i32 s84, s88\n"
"s_cselect_b64 vcc, -1, 0\n"
"v_mfma_f32_16x16x32_bf16 v[132:135], v[140:143], v[112:115], v[132:135]\n"
"ds_write_b64 v245, v[214:215] offset:12296\n"
"v_mfma_f32_16x16x32_bf16 v[132:135], v[156:159], v[116:119], v[132:135]\n"
"v_mfma_f32_16x16x32_bf16 v[136:139], a[44:47], v[116:119], v[20:23]\n"
"ds_read_b128 v[116:119], v235 offset:40960\n"
"v_mfma_f32_16x16x32_bf16 v[136:139], a[40:43], v[112:115], v[136:139]\n"
"ds_read_b128 v[112:115], v234 offset:40960\n"
"v_mfma_f32_16x16x32_bf16 v[136:139], a[36:39], v[108:111], v[136:139]\n"
"ds_read_b128 v[108:111], v233 offset:40960\n"
"v_mfma_f32_16x16x32_bf16 v[136:139], a[32:35], v[92:95], v[136:139]\n"
"ds_read_b128 v[92:95], v232 offset:40960\n"
"v_cvt_pk_bf16_f32 v208, v132, v133\n"
"v_mfma_f32_16x16x32_bf16 v[136:139], a[28:31], v[88:91], v[136:139]\n"
"ds_read_b128 v[88:91], v235 offset:32768\n"
"v_cvt_pk_bf16_f32 v209, v134, v135\n"
"v_mfma_f32_16x16x32_bf16 v[136:139], a[24:27], v[84:87], v[136:139]\n"
"ds_read_b128 v[84:87], v234 offset:32768\n"
"v_mfma_f32_16x16x32_bf16 v[136:139], v[204:207], v[68:71], v[136:139]\n"
"ds_read_b128 v[68:71], v233 offset:32768\n"
"v_mfma_f32_16x16x32_bf16 v[136:139], v[188:191], v[64:67], v[136:139]\n"
"ds_read_b128 v[64:67], v232 offset:32768\n"
"v_mfma_f32_16x16x32_bf16 v[136:139], v[184:187], v[60:63], v[136:139]\n"
"ds_read_b128 v[60:63], v235 offset:24576\n"
"v_mfma_f32_16x16x32_bf16 v[136:139], v[180:183], v[44:47], v[136:139]\n"
"ds_read_b128 v[44:47], v234 offset:24576\n"
"v_mfma_f32_16x16x32_bf16 v[136:139], v[164:167], v[40:43], v[136:139]\n"
"ds_read_b128 v[40:43], v233 offset:24576\n"
"v_mfma_f32_16x16x32_bf16 v[136:139], v[160:163], v[36:39], v[136:139]\n"
"ds_read_b128 v[36:39], v232 offset:24576\n"
"s_waitcnt lgkmcnt(8)\n"
"v_mfma_f32_16x16x32_bf16 v[132:135], a[76:79], v[116:119], v[0:3]\n"
"ds_write_b64 v244, v[208:209] offset:12416\n"
"v_mfma_f32_16x16x32_bf16 v[132:135], a[72:75], v[112:115], v[132:135]\n"
"s_add_u32 m0, s31, 0x9e00\n"
"v_mfma_f32_16x16x32_bf16 v[132:135], a[68:71], v[108:111], v[132:135]\n"
"global_load_lds_dwordx4 v237, s[52:53] offset:512 nt\n"
"v_mfma_f32_16x16x32_bf16 v[132:135], a[64:67], v[92:95], v[132:135]\n"
"v_cvt_pk_bf16_f32 v210, v136, v137\n"
"s_waitcnt lgkmcnt(5)\n"
"v_mfma_f32_16x16x32_bf16 v[132:135], a[60:63], v[88:91], v[132:135]\n"
"v_cvt_pk_bf16_f32 v211, v138, v139\n"
"v_mfma_f32_16x16x32_bf16 v[132:135], a[56:59], v[84:87], v[132:135]\n"
"s_add_u32 s54, s26, s42\n"
"s_addc_u32 s55, s27, 0\n"
"v_mfma_f32_16x16x32_bf16 v[132:135], a[52:55], v[68:71], v[132:135]\n"
"ds_read_b128 v[216:219], v246 offset:0\n"
"v_mfma_f32_16x16x32_bf16 v[132:135], a[48:51], v[64:67], v[132:135]\n"
"ds_read_b128 v[220:223], v246 offset:1024\n"
"s_waitcnt lgkmcnt(3)\n"
"v_mfma_f32_16x16x32_bf16 v[132:135], a[252:255], v[60:63], v[132:135]\n"
"ds_read_b128 v[224:227], v246 offset:2048\n"
"v_mfma_f32_16x16x32_bf16 v[132:135], a[248:251], v[44:47], v[132:135]\n"
"ds_write_b64 v244, v[210:211] offset:12424\n"
"v_mfma_f32_16x16x32_bf16 v[132:135], a[244:247], v[40:43], v[132:135]\n"
"s_sub_i32 s84, s84, s19\n"
"v_mfma_f32_16x16x32_bf16 v[132:135], a[240:243], v[36:39], v[132:135]\n"
"s_max_i32 s88, s88, s84\n"
"v_mfma_f32_16x16x32_bf16 v[136:139], a[80:83], v[36:39], v[4:7]\n"
"v_mfma_f32_16x16x32_bf16 v[136:139], a[84:87], v[40:43], v[136:139]\n"
"v_mfma_f32_16x16x32_bf16 v[136:139], a[88:91], v[44:47], v[136:139]\n"
"v_mfma_f32_16x16x32_bf16 v[136:139], a[92:95], v[60:63], v[136:139]\n"
"v_cvt_pk_bf16_f32 v208, v132, v133\n"
"v_mfma_f32_16x16x32_bf16 v[136:139], a[96:99], v[64:67], v[136:139]\n"
"v_cvt_pk_bf16_f32 v209, v134, v135\n"
"v_mfma_f32_16x16x32_bf16 v[136:139], a[100:103], v[68:71], v[136:139]\n"
"v_mfma_f32_16x16x32_bf16 v[136:139], a[104:107], v[84:87], v[136:139]\n"
"v_mfma_f32_16x16x32_bf16 v[136:139], a[108:111], v[88:91], v[136:139]\n"
"s_waitcnt lgkmcnt(1)\n"
"v_mfma_f32_16x16x32_bf16 v[136:139], a[112:115], v[92:95], v[136:139]\n"
"v_mfma_f32_16x16x32_bf16 v[136:139], a[116:119], v[108:111], v[136:139]\n"
"global_store_dwordx4 v247, v[216:219], s[54:55] offset:0 sc1\n"
"ds_write_b64 v244, v[208:209] offset:24576\n"
"v_mfma_f32_16x16x32_bf16 v[136:139], a[120:123], v[112:115], v[136:139]\n"
"s_add_u32 m0, s31, 0x7000\n"
"v_mfma_f32_16x16x32_bf16 v[136:139], a[124:127], v[116:119], v[136:139]\n"
"global_load_lds_dwordx4 v238, s[52:53] offset:0 nt\n"
"v_mfma_f32_16x16x32_bf16 v[132:135], a[172:175], v[116:119], v[8:11]\n"
"v_mfma_f32_16x16x32_bf16 v[132:135], a[168:171], v[112:115], v[132:135]\n"
"v_mfma_f32_16x16x32_bf16 v[132:135], a[164:167], v[108:111], v[132:135]\n"
"v_mfma_f32_16x16x32_bf16 v[132:135], a[160:163], v[92:95], v[132:135]\n"
"v_cvt_pk_bf16_f32 v210, v136, v137\n"
"v_mfma_f32_16x16x32_bf16 v[132:135], a[156:159], v[88:91], v[132:135]\n"
"v_cvt_pk_bf16_f32 v211, v138, v139\n"
"v_mfma_f32_16x16x32_bf16 v[132:135], a[152:155], v[84:87], v[132:135]\n"
"v_mfma_f32_16x16x32_bf16 v[132:135], a[148:151], v[68:71], v[132:135]\n"
"v_mfma_f32_16x16x32_bf16 v[132:135], a[144:147], v[64:67], v[132:135]\n"
"v_mfma_f32_16x16x32_bf16 v[132:135], a[140:143], v[60:63], v[132:135]\n"
"v_mfma_f32_16x16x32_bf16 v[132:135], a[136:139], v[44:47], v[132:135]\n"
"global_store_dwordx4 v252, v[220:223], s[54:55] offset:0 sc1\n"
"ds_write_b64 v244, v[210:211] offset:24584\n"
"v_mfma_f32_16x16x32_bf16 v[132:135], a[132:135], v[40:43], v[132:135]\n"
"v_mfma_f32_16x16x32_bf16 v[132:135], a[128:131], v[36:39], v[132:135]\n"
"v_mfma_f32_16x16x32_bf16 v[136:139], a[176:179], v[36:39], v[12:15]\n"
"v_mfma_f32_16x16x32_bf16 v[136:139], a[180:183], v[40:43], v[136:139]\n"
"v_mfma_f32_16x16x32_bf16 v[136:139], a[184:187], v[44:47], v[136:139]\n"
"v_mfma_f32_16x16x32_bf16 v[136:139], a[188:191], v[60:63], v[136:139]\n"
"v_cvt_pk_bf16_f32 v212, v132, v133\n"
"v_mfma_f32_16x16x32_bf16 v[136:139], a[192:195], v[64:67], v[136:139]\n"
"v_mov_b32 v242, 23\n"
"v_mfma_f32_16x16x32_bf16 v[136:139], a[196:199], v[68:71], v[136:139]\n"
"v_cvt_pk_bf16_f32 v213, v134, v135\n"
"v_mfma_f32_16x16x32_bf16 v[136:139], a[200:203], v[84:87], v[136:139]\n"
"s_waitcnt lgkmcnt(2)\n"
"ds_write_b32 v240, v242\n"
"v_mfma_f32_16x16x32_bf16 v[136:139], a[204:207], v[88:91], v[136:139]\n"
"v_mfma_f32_16x16x32_bf16 v[136:139], a[208:211], v[92:95], v[136:139]\n"
"v_mfma_f32_16x16x32_bf16 v[136:139], a[212:215], v[108:111], v[136:139]\n"
"v_mfma_f32_16x16x32_bf16 v[136:139], a[216:219], v[112:115], v[136:139]\n"
"global_store_dwordx4 v253, v[224:227], s[54:55] offset:0 sc1\n"
"ds_write_b64 v245, v[212:213] offset:24576\n"
"v_mfma_f32_16x16x32_bf16 v[136:139], a[220:223], v[116:119], v[136:139]\n"
"s_add_u32 m0, s31, 0x8f00\n"
"v_mfma_f32_16x16x32_bf16 v[132:135], v[156:159], v[116:119], v[16:19]\n"
"global_load_lds_dwordx4 v238, s[52:53] offset:256 nt\n"
"v_mfma_f32_16x16x32_bf16 v[132:135], v[140:143], v[112:115], v[132:135]\n"
"v_mfma_f32_16x16x32_bf16 v[132:135], a[20:23], v[108:111], v[132:135]\n"
"ds_read_b32 v243, v241\n"
"v_mfma_f32_16x16x32_bf16 v[132:135], a[16:19], v[92:95], v[132:135]\n"
"v_cvt_pk_bf16_f32 v214, v136, v137\n"
"v_mfma_f32_16x16x32_bf16 v[132:135], a[12:15], v[88:91], v[132:135]\n"
"v_cvt_pk_bf16_f32 v215, v138, v139\n"
"v_mfma_f32_16x16x32_bf16 v[132:135], a[8:11], v[84:87], v[132:135]\n"
"v_mfma_f32_16x16x32_bf16 v[132:135], a[4:7], v[68:71], v[132:135]\n"
"s_waitcnt lgkmcnt(0)\n"
"v_cmp_gt_u32 s[56:57], 23, v243\n"
"v_mfma_f32_16x16x32_bf16 v[132:135], a[0:3], v[64:67], v[132:135]\n"
"v_mfma_f32_16x16x32_bf16 v[132:135], a[236:239], v[60:63], v[132:135]\n"
"s_or_b64 s[56:57], s[56:57], vcc\n"
"s_cbranch_scc1 LB23\n"
"LB23R:\n"
"v_mfma_f32_16x16x32_bf16 v[132:135], a[232:235], v[44:47], v[132:135]\n"
"ds_write_b64 v245, v[214:215] offset:24584\n"
"v_mfma_f32_16x16x32_bf16 v[132:135], a[228:231], v[40:43], v[132:135]\n"
"v_mfma_f32_16x16x32_bf16 v[132:135], a[224:227], v[36:39], v[132:135]\n"
"s_mov_b64 exec, s[10:11]\n"
"s_cbranch_execz LHT11\n"
"LHR11:\n"
"s_mov_b64 exec, -1\n"
"v_mfma_f32_16x16x32_bf16 v[136:139], v[160:163], v[36:39], v[20:23]\n"
"ds_read_b128 v[36:39], v232 offset:24832\n"
"v_mfma_f32_16x16x32_bf16 v[136:139], v[164:167], v[40:43], v[136:139]\n"
"ds_read_b128 v[40:43], v233 offset:24832\n"
"v_mfma_f32_16x16x32_bf16 v[136:139], v[180:183], v[44:47], v[136:139]\n"
"ds_read_b128 v[44:47], v234 offset:24832\n"
"v_mfma_f32_16x16x32_bf16 v[136:139], v[184:187], v[60:63], v[136:139]\n"
"ds_read_b128 v[60:63], v235 offset:24832\n"
"v_cvt_pk_bf16_f32 v208, v132, v133\n"
"v_mfma_f32_16x16x32_bf16 v[136:139], v[188:191], v[64:67], v[136:139]\n"
"ds_read_b128 v[64:67], v232 offset:33024\n"
"v_cvt_pk_bf16_f32 v209, v134, v135\n"
"v_mfma_f32_16x16x32_bf16 v[136:139], v[204:207], v[68:71], v[136:139]\n"
"ds_read_b128 v[68:71], v233 offset:33024\n"
"v_mfma_f32_16x16x32_bf16 v[136:139], a[24:27], v[84:87], v[136:139]\n"
"ds_read_b128 v[84:87], v234 offset:33024\n"
"v_mfma_f32_16x16x32_bf16 v[136:139], a[28:31], v[88:91], v[136:139]\n"
"ds_read_b128 v[88:91], v235 offset:33024\n"
"v_mfma_f32_16x16x32_bf16 v[136:139], a[32:35], v[92:95], v[136:139]\n"
"ds_read_b128 v[92:95], v232 offset:41216\n"
"v_mfma_f32_16x16x32_bf16 v[136:139], a[36:39], v[108:111], v[136:139]\n"
"ds_read_b128 v[108:111], v233 offset:41216\n"
"ds_write_b64 v244, v[208:209] offset:24704\n"
"v_mfma_f32_16x16x32_bf16 v[136:139], a[40:43], v[112:115], v[136:139]\n"
"ds_read_b128 v[112:115], v234 offset:41216\n"
"v_mfma_f32_16x16x32_bf16 v[136:139], a[44:47], v[116:119], v[136:139]\n"
"ds_read_b128 v[116:119], v235 offset:41216\n"
"s_waitcnt lgkmcnt(9)\n"
"v_mfma_f32_16x16x32_bf16 v[132:135], a[240:243], v[36:39], v[0:3]\n"
"v_add_u32 v232, 0xffff4000, v232\n"
"v_mfma_f32_16x16x32_bf16 v[132:135], a[244:247], v[40:43], v[132:135]\n"
"v_add_u32 v233, 0xffff4000, v233\n"
"v_mfma_f32_16x16x32_bf16 v[132:135], a[248:251], v[44:47], v[132:135]\n"
"v_add_u32 v234, 0xffff4000, v234\n"
"v_mfma_f32_16x16x32_bf16 v[132:135], a[252:255], v[60:63], v[132:135]\n"
"v_add_u32 v235, 0xffff4000, v235\n"
"s_waitcnt lgkmcnt(5)\n"
"v_mfma_f32_16x16x32_bf16 v[132:135], a[48:51], v[64:67], v[132:135]\n"
"s_add_u32 m0, s31, 0xae00\n"
"v_mfma_f32_16x16x32_bf16 v[132:135], a[52:55], v[68:71], v[132:135]\n"
"global_load_lds_dwordx4 v238, s[52:53] offset:512 nt\n"
"v_mfma_f32_16x16x32_bf16 v[132:135], a[56:59], v[84:87], v[132:135]\n"
"v_cvt_pk_bf16_f32 v210, v136, v137\n"
"v_mfma_f32_16x16x32_bf16 v[132:135], a[60:63], v[88:91], v[132:135]\n"
"v_cvt_pk_bf16_f32 v211, v138, v139\n"
"s_waitcnt lgkmcnt(0)\n"
"v_mfma_f32_16x16x32_bf16 v[132:135], a[64:67], v[92:95], v[132:135]\n"
"ds_read_b128 v[216:219], v246 offset:12288\n"
"v_mfma_f32_16x16x32_bf16 v[132:135], a[68:71], v[108:111], v[132:135]\n"
"ds_read_b128 v[220:223], v246 offset:13312\n"
"v_mfma_f32_16x16x32_bf16 v[132:135], a[72:75], v[112:115], v[132:135]\n"
"ds_read_b128 v[224:227], v246 offset:14336\n"
"v_mfma_f32_16x16x32_bf16 v[132:135], a[76:79], v[116:119], v[132:135]\n"
"v_lshlrev_b32 v0, 16, v144\n"
"v_mfma_f32_16x16x32_bf16 v[136:139], a[124:127], v[116:119], v[4:7]\n"
"ds_write_b64 v244, v[210:211] offset:24712\n"
"v_mfma_f32_16x16x32_bf16 v[136:139], a[120:123], v[112:115], v[136:139]\n"
"v_and_b32 v1, 0xffff0000, v144\n"
"v_mfma_f32_16x16x32_bf16 v[136:139], a[116:119], v[108:111], v[136:139]\n"
"v_lshlrev_b32 v2, 16, v145\n"
"v_mfma_f32_16x16x32_bf16 v[136:139], a[112:115], v[92:95], v[136:139]\n"
"v_cvt_pk_bf16_f32 v208, v132, v133\n"
"v_mfma_f32_16x16x32_bf16 v[136:139], a[108:111], v[88:91], v[136:139]\n"
"v_cvt_pk_bf16_f32 v209, v134, v135\n"
"v_mfma_f32_16x16x32_bf16 v[136:139], a[104:107], v[84:87], v[136:139]\n"
"v_and_b32 v3, 0xffff0000, v145\n"
"v_mfma_f32_16x16x32_bf16 v[136:139], a[100:103], v[68:71], v[136:139]\n"
"v_lshlrev_b32 v4, 16, v146\n"
"v_mfma_f32_16x16x32_bf16 v[136:139], a[96:99], v[64:67], v[136:139]\n"
"s_waitcnt lgkmcnt(1)\n"
"v_mfma_f32_16x16x32_bf16 v[136:139], a[92:95], v[60:63], v[136:139]\n"
"v_and_b32 v5, 0xffff0000, v146\n"
"v_mfma_f32_16x16x32_bf16 v[136:139], a[88:91], v[44:47], v[136:139]\n"
"global_store_dwordx4 v247, v[216:219], s[54:55] offset:768 sc1\n"
"ds_write_b64 v244, v[208:209] offset:0\n"
"v_mfma_f32_16x16x32_bf16 v[136:139], a[84:87], v[40:43], v[136:139]\n"
"v_lshlrev_b32 v6, 16, v147\n"
"v_mfma_f32_16x16x32_bf16 v[136:139], a[80:83], v[36:39], v[136:139]\n"
"v_and_b32 v7, 0xffff0000, v147\n"
"v_mfma_f32_16x16x32_bf16 v[132:135], a[128:131], v[36:39], v[8:11]\n"
"s_add_u32 s52, s20, s46\n"
"s_addc_u32 s53, s21, 0\n"
"v_mfma_f32_16x16x32_bf16 v[132:135], a[132:135], v[40:43], v[132:135]\n"
"s_add_u32 m0, s31, 0xc000\n"
"v_mfma_f32_16x16x32_bf16 v[132:135], a[136:139], v[44:47], v[132:135]\n"
"global_load_lds_dwordx4 v237, s[52:53] offset:0 nt\n"
"v_mfma_f32_16x16x32_bf16 v[132:135], a[140:143], v[60:63], v[132:135]\n"
"v_cvt_pk_bf16_f32 v210, v136, v137\n"
"v_mfma_f32_16x16x32_bf16 v[132:135], a[144:147], v[64:67], v[132:135]\n"
"v_cvt_pk_bf16_f32 v211, v138, v139\n"
"v_mfma_f32_16x16x32_bf16 v[132:135], a[148:151], v[68:71], v[132:135]\n"
"v_lshlrev_b32 v8, 16, v148\n"
"v_mfma_f32_16x16x32_bf16 v[132:135], a[152:155], v[84:87], v[132:135]\n"
"v_and_b32 v9, 0xffff0000, v148\n"
"v_mfma_f32_16x16x32_bf16 v[132:135], a[156:159], v[88:91], v[132:135]\n"
"v_lshlrev_b32 v10, 16, v149\n"
"v_mfma_f32_16x16x32_bf16 v[132:135], a[160:163], v[92:95], v[132:135]\n"
"v_and_b32 v11, 0xffff0000, v149\n"
"v_mfma_f32_16x16x32_bf16 v[132:135], a[164:167], v[108:111], v[132:135]\n"
"s_memrealtime s[84:85]\n"
"v_mfma_f32_16x16x32_bf16 v[132:135], a[168:171], v[112:115], v[132:135]\n"
"global_store_dwordx4 v252, v[220:223], s[54:55] offset:768 sc1\n"
"ds_write_b64 v244, v[210:211] offset:8\n"
"v_mfma_f32_16x16x32_bf16 v[132:135], a[172:175], v[116:119], v[132:135]\n"
"s_add_u32 s83, s83, s86\n"
"v_mfma_f32_16x16x32_bf16 v[136:139], a[220:223], v[116:119], v[12:15]\n"
"s_sub_i32 s88, s88, 6\n"
"v_mfma_f32_16x16x32_bf16 v[136:139], a[216:219], v[112:115], v[136:139]\n"
"s_max_i32 s88, s88, 0\n"
"v_mfma_f32_16x16x32_bf16 v[136:139], a[212:215], v[108:111], v[136:139]\n"
"v_mfma_f32_16x16x32_bf16 v[136:139], a[208:211], v[92:95], v[136:139]\n"
"v_cvt_pk_bf16_f32 v212, v132, v133\n"
"v_mfma_f32_16x16x32_bf16 v[136:139], a[204:207], v[88:91], v[136:139]\n"
"v_mov_b32 v242, 24\n"
"v_mfma_f32_16x16x32_bf16 v[136:139], a[200:203], v[84:87], v[136:139]\n"
"v_cvt_pk_bf16_f32 v213, v134, v135\n"
"v_mfma_f32_16x16x32_bf16 v[136:139], a[196:199], v[68:71], v[136:139]\n"
"s_waitcnt vmcnt(15) lgkmcnt(2)\n"
"ds_write_b32 v240, v242\n"
"v_mfma_f32_16x16x32_bf16 v[136:139], a[192:195], v[64:67], v[136:139]\n"
"v_lshlrev_b32 v12, 16, v150\n"
"v_mfma_f32_16x16x32_bf16 v[136:139], a[188:191], v[60:63], v[136:139]\n"
"v_and_b32 v13, 0xffff0000, v150\n"
"v_mfma_f32_16x16x32_bf16 v[136:139], a[184:187], v[44:47], v[136:139]\n"
"v_lshlrev_b32 v14, 16, v151\n"
"v_mfma_f32_16x16x32_bf16 v[136:139], a[180:183], v[40:43], v[136:139]\n"
"global_store_dwordx4 v253, v[224:227], s[54:55] offset:768 sc1\n"
"ds_write_b64 v245, v[212:213] offset:0\n"
"v_mfma_f32_16x16x32_bf16 v[136:139], a[176:179], v[36:39], v[136:139]\n"
"v_and_b32 v15, 0xffff0000, v151\n"
"v_mfma_f32_16x16x32_bf16 v[132:135], a[224:227], v[36:39], v[16:19]\n"
"v_mfma_f32_16x16x32_bf16 v[132:135], a[228:231], v[40:43], v[132:135]\n"
"v_mfma_f32_16x16x32_bf16 v[132:135], a[232:235], v[44:47], v[132:135]\n"
"ds_read_b32 v243, v241\n"
"v_mfma_f32_16x16x32_bf16 v[132:135], a[236:239], v[60:63], v[132:135]\n"
"v_cvt_pk_bf16_f32 v214, v136, v137\n"
"v_mfma_f32_16x16x32_bf16 v[132:135], a[0:3], v[64:67], v[132:135]\n"
"v_cvt_pk_bf16_f32 v215, v138, v139\n"
"v_mfma_f32_16x16x32_bf16 v[132:135], a[4:7], v[68:71], v[132:135]\n"
"s_add_u32 m0, s31, 0xdf00\n"
"v_mfma_f32_16x16x32_bf16 v[132:135], a[8:11], v[84:87], v[132:135]\n"
"s_waitcnt lgkmcnt(0)\n"
"v_cmp_gt_u32 s[56:57], 24, v243\n"
"v_mfma_f32_16x16x32_bf16 v[132:135], a[12:15], v[88:91], v[132:135]\n"
"s_sub_u32 s84, s84, s83\n"
"v_mfma_f32_16x16x32_bf16 v[132:135], a[16:19], v[92:95], v[132:135]\n"
"s_or_b64 s[56:57], s[56:57], vcc\n"
"s_cbranch_scc1 LB24\n"
"LB24R:\n"
"v_mfma_f32_16x16x32_bf16 v[132:135], a[20:23], v[108:111], v[132:135]\n"
"s_cmp_lt_i32 s84, s88\n"
"s_cselect_b64 vcc, -1, 0\n"
"v_mfma_f32_16x16x32_bf16 v[132:135], v[140:143], v[112:115], v[132:135]\n"
"ds_write_b64 v245, v[214:215] offset:8\n"
"v_mfma_f32_16x16x32_bf16 v[132:135], v[156:159], v[116:119], v[132:135]\n"
"global_load_lds_dwordx4 v237, s[52:53] offset:256 nt\n"
"v_mfma_f32_16x16x32_bf16 v[136:139], a[44:47], v[116:119], v[20:23]\n"
"ds_read_b128 v[116:119], v235 offset:16384\n"
"LHT11:\n"
"s_cbranch_execz LHR11\n"
"v_mfma_f32_16x16x32_bf16 v[136:139], a[40:43], v[112:115], v[136:139]\n"
"ds_read_b128 v[112:115], v234 offset:16384\n"
"v_mfma_f32_16x16x32_bf16 v[136:139], a[36:39], v[108:111], v[136:139]\n"
"ds_read_b128 v[108:111], v233 offset:16384\n"
"v_mfma_f32_16x16x32_bf16 v[136:139], a[32:35], v[92:95], v[136:139]\n"
"ds_read_b128 v[92:95], v232 offset:16384\n"
"v_cvt_pk_bf16_f32 v208, v132, v133\n"
"v_mfma_f32_16x16x32_bf16 v[136:139], a[28:31], v[88:91], v[136:139]\n"
"ds_read_b128 v[88:91], v235 offset:8192\n"
"v_cvt_pk_bf16_f32 v209, v134, v135\n"
"v_mfma_f32_16x16x32_bf16 v[136:139], a[24:27], v[84:87], v[136:139]\n"
"ds_read_b128 v[84:87], v234 offset:8192\n"
"v_mfma_f32_16x16x32_bf16 v[136:139], v[204:207], v[68:71], v[136:139]\n"
"ds_read_b128 v[68:71], v233 offset:8192\n"
"v_mfma_f32_16x16x32_bf16 v[136:139], v[188:191], v[64:67], v[136:139]\n"
"ds_read_b128 v[64:67], v232 offset:8192\n"
"v_mfma_f32_16x16x32_bf16 v[136:139], v[184:187], v[60:63], v[136:139]\n"
"ds_read_b128 v[60:63], v235 offset:0\n"
"v_mfma_f32_16x16x32_bf16 v[136:139], v[180:183], v[44:47], v[136:139]\n"
"ds_read_b128 v[44:47], v234 offset:0\n"
"v_mfma_f32_16x16x32_bf16 v[136:139], v[164:167], v[40:43], v[136:139]\n"
"ds_read_b128 v[40:43], v233 offset:0\n"
"v_mfma_f32_16x16x32_bf16 v[136:139], v[160:163], v[36:39], v[136:139]\n"
"ds_read_b128 v[36:39], v232 offset:0\n"
"s_waitcnt lgkmcnt(8)\n"
"v_mfma_f32_16x16x32_bf16 v[132:135], a[76:79], v[116:119], v[0:3]\n"
"ds_write_b64 v244, v[208:209] offset:128\n"
"v_mfma_f32_16x16x32_bf16 v[132:135], a[72:75], v[112:115], v[132:135]\n"
"v_lshlrev_b32 v16, 16, v152\n"
"v_mfma_f32_16x16x32_bf16 v[132:135], a[68:71], v[108:111], v[132:135]\n"
"v_and_b32 v17, 0xffff0000, v152\n"
"v_mfma_f32_16x16x32_bf16 v[132:135], a[64:67], v[92:95], v[132:135]\n"
"v_lshlrev_b32 v18, 16, v153\n"
"v_cvt_pk_bf16_f32 v211, v138, v139\n"
"s_waitcnt lgkmcnt(5)\n"
"v_mfma_f32_16x16x32_bf16 v[132:135], a[60:63], v[88:91], v[132:135]\n"
"v_and_b32 v19, 0xffff0000, v153\n"
"v_mfma_f32_16x16x32_bf16 v[132:135], a[56:59], v[84:87], v[132:135]\n"
"v_lshlrev_b32 v20, 16, v154\n"
"v_mfma_f32_16x16x32_bf16 v[132:135], a[52:55], v[68:71], v[132:135]\n"
"v_and_b32 v21, 0xffff0000, v154\n"
"v_mfma_f32_16x16x32_bf16 v[132:135], a[48:51], v[64:67], v[132:135]\n"
"v_lshlrev_b32 v22, 16, v155\n"
"s_waitcnt lgkmcnt(1)\n"
"v_mfma_f32_16x16x32_bf16 v[132:135], a[252:255], v[60:63], v[132:135]\n"
"v_and_b32 v23, 0xffff0000, v155\n"
"v_mfma_f32_16x16x32_bf16 v[132:135], a[248:251], v[44:47], v[132:135]\n"
"v_cvt_pk_bf16_f32 v210, v136, v137\n"
"v_mfma_f32_16x16x32_bf16 v[132:135], a[244:247], v[40:43], v[132:135]\n"
"s_add_u32 s54, s26, s43\n"
"s_addc_u32 s55, s27, 0\n"
"v_mfma_f32_16x16x32_bf16 v[132:135], a[240:243], v[36:39], v[132:135]\n"
"ds_read_b128 v[216:219], v246 offset:24576\n"
"v_mfma_f32_16x16x32_bf16 v[136:139], a[80:83], v[36:39], v[4:7]\n"
"ds_read_b128 v[220:223], v246 offset:25600\n"
"v_mfma_f32_16x16x32_bf16 v[136:139], a[84:87], v[40:43], v[136:139]\n"
"ds_read_b128 v[224:227], v246 offset:26624\n"
"v_mfma_f32_16x16x32_bf16 v[136:139], a[88:91], v[44:47], v[136:139]\n"
"ds_write_b64 v244, v[210:211] offset:136\n"
"v_mfma_f32_16x16x32_bf16 v[136:139], a[92:95], v[60:63], v[136:139]\n"
"v_cvt_pk_bf16_f32 v208, v132, v133\n"
"v_mfma_f32_16x16x32_bf16 v[136:139], a[96:99], v[64:67], v[136:139]\n"
"v_cvt_pk_bf16_f32 v209, v134, v135\n"
"v_mfma_f32_16x16x32_bf16 v[136:139], a[100:103], v[68:71], v[136:139]\n"
"s_add_u32 m0, s31, 0xfe00\n"
"v_mfma_f32_16x16x32_bf16 v[136:139], a[104:107], v[84:87], v[136:139]\n"
"global_load_lds_dwordx4 v237, s[52:53] offset:512 nt\n"
"v_mfma_f32_16x16x32_bf16 v[136:139], a[108:111], v[88:91], v[136:139]\n"
"s_waitcnt lgkmcnt(1)\n"
"v_mfma_f32_16x16x32_bf16 v[136:139], a[112:115], v[92:95], v[136:139]\n"
"s_sub_i32 s84, s84, s19\n"
"v_mfma_f32_16x16x32_bf16 v[136:139], a[116:119], v[108:111], v[136:139]\n"
"global_store_dwordx4 v247, v[216:219], s[54:55] offset:0 sc1\n"
"ds_write_b64 v244, v[208:209] offset:12288\n"
"v_mfma_f32_16x16x32_bf16 v[136:139], a[120:123], v[112:115], v[136:139]\n"
"s_max_i32 s88, s88, s84\n"
"v_mfma_f32_16x16x32_bf16 v[136:139], a[124:127], v[116:119], v[136:139]\n"
"v_mfma_f32_16x16x32_bf16 v[132:135], a[172:175], v[116:119], v[8:11]\n"
"v_mfma_f32_16x16x32_bf16 v[132:135], a[168:171], v[112:115], v[132:135]\n"
"v_mfma_f32_16x16x32_bf16 v[132:135], a[164:167], v[108:111], v[132:135]\n"
"v_mfma_f32_16x16x32_bf16 v[132:135], a[160:163], v[92:95], v[132:135]\n"
"v_cvt_pk_bf16_f32 v210, v136, v137\n"
"v_mfma_f32_16x16x32_bf16 v[132:135], a[156:159], v[88:91], v[132:135]\n"
"v_cvt_pk_bf16_f32 v211, v138, v139\n"
"v_mfma_f32_16x16x32_bf16 v[132:135], a[152:155], v[84:87], v[132:135]\n"
"s_add_u32 m0, s31, 0xd000\n"
"v_mfma_f32_16x16x32_bf16 v[132:135], a[148:151], v[68:71], v[132:135]\n"
"global_load_lds_dwordx4 v238, s[52:53] offset:0 nt\n"
"v_mfma_f32_16x16x32_bf16 v[132:135], a[144:147], v[64:67], v[132:135]\n"
"v_mfma_f32_16x16x32_bf16 v[132:135], a[140:143], v[60:63], v[132:135]\n"
"v_mfma_f32_16x16x32_bf16 v[132:135], a[136:139], v[44:47], v[132:135]\n"
"global_store_dwordx4 v252, v[220:223], s[54:55] offset:0 sc1\n"
"ds_write_b64 v244, v[210:211] offset:12296\n"
"v_mfma_f32_16x16x32_bf16 v[132:135], a[132:135], v[40:43], v[132:135]\n"
"v_mfma_f32_16x16x32_bf16 v[132:135], a[128:131], v[36:39], v[132:135]\n"
"v_mfma_f32_16x16x32_bf16 v[136:139], a[176:179], v[36:39], v[12:15]\n"
"v_mfma_f32_16x16x32_bf16 v[136:139], a[180:183], v[40:43], v[136:139]\n"
"v_mfma_f32_16x16x32_bf16 v[136:139], a[184:187], v[44:47], v[136:139]\n"
"v_mfma_f32_16x16x32_bf16 v[136:139], a[188:191], v[60:63], v[136:139]\n"
"v_cvt_pk_bf16_f32 v212, v132, v133\n"
"v_mfma_f32_16x16x32_bf16 v[136:139], a[192:195], v[64:67], v[136:139]\n"
"v_mov_b32 v242, 25\n"
"v_mfma_f32_16x16x32_bf16 v[136:139], a[196:199], v[68:71], v[136:139]\n"
"v_cvt_pk_bf16_f32 v213, v134, v135\n"
"v_mfma_f32_16x16x32_bf16 v[136:139], a[200:203], v[84:87], v[136:139]\n"
"s_waitcnt lgkmcnt(2)\n"
"ds_write_b32 v240, v242\n"
"v_mfma_f32_16x16x32_bf16 v[136:139], a[204:207], v[88:91], v[136:139]\n"
"v_mfma_f32_16x16x32_bf16 v[136:139], a[208:211], v[92:95], v[136:139]\n"
"v_mfma_f32_16x16x32_bf16 v[136:139], a[212:215], v[108:111], v[136:139]\n"
"v_mfma_f32_16x16x32_bf16 v[136:139], a[216:219], v[112:115], v[136:139]\n"
"global_store_dwordx4 v253, v[224:227], s[54:55] offset:0 sc1\n"
"ds_write_b64 v245, v[212:213] offset:12288\n"
"v_mfma_f32_16x16x32_bf16 v[136:139], a[220:223], v[116:119], v[136:139]\n"
"v_mfma_f32_16x16x32_bf16 v[132:135], v[156:159], v[116:119], v[16:19]\n"
"v_mfma_f32_16x16x32_bf16 v[132:135], v[140:143], v[112:115], v[132:135]\n"
"v_mfma_f32_16x16x32_bf16 v[132:135], a[20:23], v[108:111], v[132:135]\n"
"ds_read_b32 v243, v241\n"
"v_mfma_f32_16x16x32_bf16 v[132:135], a[16:19], v[92:95], v[132:135]\n"
"v_cvt_pk_bf16_f32 v214, v136, v137\n"
"v_mfma_f32_16x16x32_bf16 v[132:135], a[12:15], v[88:91], v[132:135]\n"
"v_cvt_pk_bf16_f32 v215, v138, v139\n"
"v_mfma_f32_16x16x32_bf16 v[132:135], a[8:11], v[84:87], v[132:135]\n"
"s_add_u32 m0, s31, 0xef00\n"
"v_mfma_f32_16x16x32_bf16 v[132:135], a[4:7], v[68:71], v[132:135]\n"
"s_waitcnt lgkmcnt(0)\n"
"v_cmp_gt_u32 s[56:57], 25, v243\n"
"v_mfma_f32_16x16x32_bf16 v[132:135], a[0:3], v[64:67], v[132:135]\n"
"global_load_lds_dwordx4 v238, s[52:53] offset:256 nt\n"
"v_mfma_f32_16x16x32_bf16 v[132:135], a[236:239], v[60:63], v[132:135]\n"
"s_or_b64 s[56:57], s[56:57], vcc\n"
"s_cbranch_scc1 LB25\n"
"LB25R:\n"
"v_mfma_f32_16x16x32_bf16 v[132:135], a[232:235], v[44:47], v[132:135]\n"
"ds_write_b64 v245, v[214:215] offset:12296\n"
"v_mfma_f32_16x16x32_bf16 v[132:135], a[228:231], v[40:43], v[132:135]\n"
"v_mfma_f32_16x16x32_bf16 v[132:135], a[224:227], v[36:39], v[132:135]\n"
"v_mfma_f32_16x16x32_bf16 v[136:139], v[160:163], v[36:39], v[20:23]\n"
"ds_read_b128 v[36:39], v232 offset:256\n"
"v_mfma_f32_16x16x32_bf16 v[136:139], v[164:167], v[40:43], v[136:139]\n"
"ds_read_b128 v[40:43], v233 offset:256\n"
"v_mfma_f32_16x16x32_bf16 v[136:139], v[180:183], v[44:47], v[136:139]\n"
"ds_read_b128 v[44:47], v234 offset:256\n"
"v_mfma_f32_16x16x32_bf16 v[136:139], v[184:187], v[60:63], v[136:139]\n"
"ds_read_b128 v[60:63], v235 offset:256\n"
"v_cvt_pk_bf16_f32 v208, v132, v133\n"
"v_mfma_f32_16x16x32_bf16 v[136:139], v[188:191], v[64:67], v[136:139]\n"
"ds_read_b128 v[64:67], v232 offset:8448\n"
"v_cvt_pk_bf16_f32 v209, v134, v135\n"
"v_mfma_f32_16x16x32_bf16 v[136:139], v[204:207], v[68:71], v[136:139]\n"
"ds_read_b128 v[68:71], v233 offset:8448\n"
"v_mfma_f32_16x16x32_bf16 v[136:139], a[24:27], v[84:87], v[136:139]\n"
"ds_read_b128 v[84:87], v234 offset:8448\n"
"v_mfma_f32_16x16x32_bf16 v[136:139], a[28:31], v[88:91], v[136:139]\n"
"ds_read_b128 v[88:91], v235 offset:8448\n"
"v_mfma_f32_16x16x32_bf16 v[136:139], a[32:35], v[92:95], v[136:139]\n"
"ds_read_b128 v[92:95], v232 offset:16640\n"
"v_mfma_f32_16x16x32_bf16 v[136:139], a[36:39], v[108:111], v[136:139]\n"
"ds_read_b128 v[108:111], v233 offset:16640\n"
"v_mfma_f32_16x16x32_bf16 v[136:139], a[40:43], v[112:115], v[136:139]\n"
"ds_read_b128 v[112:115], v234 offset:16640\n"
"v_mfma_f32_16x16x32_bf16 v[136:139], a[44:47], v[116:119], v[136:139]\n"
"ds_read_b128 v[116:119], v235 offset:16640\n"
"s_waitcnt lgkmcnt(8)\n"
"v_mfma_f32_16x16x32_bf16 v[132:135], a[240:243], v[36:39], v[0:3]\n"
"ds_write_b64 v244, v[208:209] offset:12416\n"
"v_mfma_f32_16x16x32_bf16 v[132:135], a[244:247], v[40:43], v[132:135]\n"
"v_mfma_f32_16x16x32_bf16 v[132:135], a[248:251], v[44:47], v[132:135]\n"
"v_mfma_f32_16x16x32_bf16 v[132:135], a[252:255], v[60:63], v[132:135]\n"
"v_cvt_pk_bf16_f32 v210, v136, v137\n"
"s_waitcnt lgkmcnt(5)\n"
"v_mfma_f32_16x16x32_bf16 v[132:135], a[48:51], v[64:67], v[132:135]\n"
"v_cvt_pk_bf16_f32 v211, v138, v139\n"
"v_mfma_f32_16x16x32_bf16 v[132:135], a[52:55], v[68:71], v[132:135]\n"
"v_mfma_f32_16x16x32_bf16 v[132:135], a[56:59], v[84:87], v[132:135]\n"
"ds_read_b128 v[216:219], v246 offset:0\n"
"v_mfma_f32_16x16x32_bf16 v[132:135], a[60:63], v[88:91], v[132:135]\n"
"ds_read_b128 v[220:223], v246 offset:1024\n"
"s_waitcnt lgkmcnt(3)\n"
"v_mfma_f32_16x16x32_bf16 v[132:135], a[64:67], v[92:95], v[132:135]\n"
"ds_read_b128 v[224:227], v246 offset:2048\n"
"v_mfma_f32_16x16x32_bf16 v[132:135], a[68:71], v[108:111], v[132:135]\n"
"ds_write_b64 v244, v[210:211] offset:12424\n"
"v_mfma_f32_16x16x32_bf16 v[132:135], a[72:75], v[112:115], v[132:135]\n"
"s_add_u32 m0, s31, 0x10e00\n"
"v_mfma_f32_16x16x32_bf16 v[132:135], a[76:79], v[116:119], v[132:135]\n"
"global_load_lds_dwordx4 v238, s[52:53] offset:512 nt\n"
"v_mfma_f32_16x16x32_bf16 v[136:139], a[124:127], v[116:119], v[4:7]\n"
"s_add_u32 s83, s83, s86\n"
"v_mfma_f32_16x16x32_bf16 v[136:139], a[120:123], v[112:115], v[136:139]\n"
"s_sub_i32 s88, s88, 6\n"
"v_mfma_f32_16x16x32_bf16 v[136:139], a[116:119], v[108:111], v[136:139]\n"
"s_max_i32 s88, s88, 0\n"
"v_mfma_f32_16x16x32_bf16 v[136:139], a[112:115], v[92:95], v[136:139]\n"
"v_cvt_pk_bf16_f32 v208, v132, v133\n"
"v_mfma_f32_16x16x32_bf16 v[136:139], a[108:111], v[88:91], v[136:139]\n"
"v_cvt_pk_bf16_f32 v209, v134, v135\n"
"v_mfma_f32_16x16x32_bf16 v[136:139], a[104:107], v[84:87], v[136:139]\n"
"v_mfma_f32_16x16x32_bf16 v[136:139], a[100:103], v[68:71], v[136:139]\n"
"v_mfma_f32_16x16x32_bf16 v[136:139], a[96:99], v[64:67], v[136:139]\n"
"s_waitcnt lgkmcnt(1)\n"
"v_mfma_f32_16x16x32_bf16 v[136:139], a[92:95], v[60:63], v[136:139]\n"
"v_mfma_f32_16x16x32_bf16 v[136:139], a[88:91], v[44:47], v[136:139]\n"
"global_store_dwordx4 v247, v[216:219], s[54:55] offset:768 sc1\n"
"ds_write_b64 v244, v[208:209] offset:24576\n"
"v_mfma_f32_16x16x32_bf16 v[136:139], a[84:87], v[40:43], v[136:139]\n"
"v_mfma_f32_16x16x32_bf16 v[136:139], a[80:83], v[36:39], v[136:139]\n"
"v_mfma_f32_16x16x32_bf16 v[132:135], a[128:131], v[36:39], v[8:11]\n"
"v_mfma_f32_16x16x32_bf16 v[132:135], a[132:135], v[40:43], v[132:135]\n"
"v_mfma_f32_16x16x32_bf16 v[132:135], a[136:139], v[44:47], v[132:135]\n"
"v_mfma_f32_16x16x32_bf16 v[132:135], a[140:143], v[60:63], v[132:135]\n"
"v_cvt_pk_bf16_f32 v210, v136, v137\n"
"v_mfma_f32_16x16x32_bf16 v[132:135], a[144:147], v[64:67], v[132:135]\n"
"v_cvt_pk_bf16_f32 v211, v138, v139\n"
"v_mfma_f32_16x16x32_bf16 v[132:135], a[148:151], v[68:71], v[132:135]\n"
"v_mfma_f32_16x16x32_bf16 v[132:135], a[152:155], v[84:87], v[132:135]\n"
"v_mfma_f32_16x16x32_bf16 v[132:135], a[156:159], v[88:91], v[132:135]\n"
"v_mfma_f32_16x16x32_bf16 v[132:135], a[160:163], v[92:95], v[132:135]\n"
"v_mfma_f32_16x16x32_bf16 v[132:135], a[164:167], v[108:111], v[132:135]\n"
"s_memrealtime s[84:85]\n"
"v_mfma_f32_16x16x32_bf16 v[132:135], a[168:171], v[112:115], v[132:135]\n"
"global_store_dwordx4 v252, v[220:223], s[54:55] offset:768 sc1\n"
"ds_write_b64 v244, v[210:211] offset:24584\n"
"v_mfma_f32_16x16x32_bf16 v[132:135], a[172:175], v[116:119], v[132:135]\n"
"s_add_u32 s52, s20, s47\n"
"s_addc_u32 s53, s21, 0\n"
"v_mfma_f32_16x16x32_bf16 v[136:139], a[220:223], v[116:119], v[12:15]\n"
"s_add_u32 m0, s31, 0x12000\n"
"v_mfma_f32_16x16x32_bf16 v[136:139], a[216:219], v[112:115], v[136:139]\n"
"global_load_lds_dwordx4 v237, s[52:53] offset:0 nt\n"
"v_mfma_f32_16x16x32_bf16 v[136:139], a[212:215], v[108:111], v[136:139]\n"
"v_mfma_f32_16x16x32_bf16 v[136:139], a[208:211], v[92:95], v[136:139]\n"
"v_cvt_pk_bf16_f32 v212, v132, v133\n"
"v_mfma_f32_16x16x32_bf16 v[136:139], a[204:207], v[88:91], v[136:139]\n"
"v_mov_b32 v242, 26\n"
"v_mfma_f32_16x16x32_bf16 v[136:139], a[200:203], v[84:87], v[136:139]\n"
"v_cvt_pk_bf16_f32 v213, v134, v135\n"
"v_mfma_f32_16x16x32_bf16 v[136:139], a[196:199], v[68:71], v[136:139]\n"
"s_waitcnt vmcnt(15) lgkmcnt(2)\n"
"ds_write_b32 v240, v242\n"
"v_mfma_f32_16x16x32_bf16 v[136:139], a[192:195], v[64:67], v[136:139]\n"
"v_mfma_f32_16x16x32_bf16 v[136:139], a[188:191], v[60:63], v[136:139]\n"
"v_mfma_f32_16x16x32_bf16 v[136:139], a[184:187], v[44:47], v[136:139]\n"
"v_mfma_f32_16x16x32_bf16 v[136:139], a[180:183], v[40:43], v[136:139]\n"
"global_store_dwordx4 v253, v[224:227], s[54:55] offset:768 sc1\n"
"ds_write_b64 v245, v[212:213] offset:24576\n"
"v_mfma_f32_16x16x32_bf16 v[136:139], a[176:179], v[36:39], v[136:139]\n"
"v_mfma_f32_16x16x32_bf16 v[132:135], a[224:227], v[36:39], v[16:19]\n"
"v_mfma_f32_16x16x32_bf16 v[132:135], a[228:231], v[40:43], v[132:135]\n"
"v_mfma_f32_16x16x32_bf16 v[132:135], a[232:235], v[44:47], v[132:135]\n"
"ds_read_b32 v243, v241\n"
"v_mfma_f32_16x16x32_bf16 v[132:135], a[236:239], v[60:63], v[132:135]\n"
"v_cvt_pk_bf16_f32 v214, v136, v137\n"
"v_mfma_f32_16x16x32_bf16 v[132:135], a[0:3], v[64:67], v[132:135]\n"
"v_cvt_pk_bf16_f32 v215, v138, v139\n"
"v_mfma_f32_16x16x32_bf16 v[132:135], a[4:7], v[68:71], v[132:135]\n"
"v_mfma_f32_16x16x32_bf16 v[132:135], a[8:11], v[84:87], v[132:135]\n"
"s_waitcnt lgkmcnt(0)\n"
"v_cmp_gt_u32 s[56:57], 26, v243\n"
"v_mfma_f32_16x16x32_bf16 v[132:135], a[12:15], v[88:91], v[132:135]\n"
"s_sub_u32 s84, s84, s83\n"
"v_mfma_f32_16x16x32_bf16 v[132:135], a[16:19], v[92:95], v[132:135]\n"
"s_or_b64 s[56:57], s[56:57], vcc\n"
"s_cbranch_scc1 LB26\n"
"LB26R:\n"
"v_mfma_f32_16x16x32_bf16 v[132:135], a[20:23], v[108:111], v[132:135]\n"
"s_cmp_lt_i32 s84, s88\n"
"s_cselect_b64 vcc, -1, 0\n"
"v_mfma_f32_16x16x32_bf16 v[132:135], v[140:143], v[112:115], v[132:135]\n"
"ds_write_b64 v245, v[214:215] offset:24584\n"
"v_mfma_f32_16x16x32_bf16 v[132:135], v[156:159], v[116:119], v[132:135]\n"
"v_mfma_f32_16x16x32_bf16 v[136:139], a[44:47], v[116:119], v[20:23]\n"
"ds_read_b128 v[116:119], v235 offset:40960\n"
"v_mfma_f32_16x16x32_bf16 v[136:139], a[40:43], v[112:115], v[136:139]\n"
"ds_read_b128 v[112:115], v234 offset:40960\n"
"v_mfma_f32_16x16x32_bf16 v[136:139], a[36:39], v[108:111], v[136:139]\n"
"ds_read_b128 v[108:111], v233 offset:40960\n"
"v_mfma_f32_16x16x32_bf16 v[136:139], a[32:35], v[92:95], v[136:139]\n"
"ds_read_b128 v[92:95], v232 offset:40960\n"
"v_cvt_pk_bf16_f32 v208, v132, v133\n"
"v_mfma_f32_16x16x32_bf16 v[136:139], a[28:31], v[88:91], v[136:139]\n"
"ds_read_b128 v[88:91], v235 offset:32768\n"
"v_cvt_pk_bf16_f32 v209, v134, v135\n"
"v_mfma_f32_16x16x32_bf16 v[136:139], a[24:27], v[84:87], v[136:139]\n"
"ds_read_b128 v[84:87], v234 offset:32768\n"
"v_mfma_f32_16x16x32_bf16 v[136:139], v[204:207], v[68:71], v[136:139]\n"
"ds_read_b128 v[68:71], v233 offset:32768\n"
"v_mfma_f32_16x16x32_bf16 v[136:139], v[188:191], v[64:67], v[136:139]\n"
"ds_read_b128 v[64:67], v232 offset:32768\n"
"v_mfma_f32_16x16x32_bf16 v[136:139], v[184:187], v[60:63], v[136:139]\n"
"ds_read_b128 v[60:63], v235 offset:24576\n"
"v_mfma_f32_16x16x32_bf16 v[136:139], v[180:183], v[44:47], v[136:139]\n"
"ds_read_b128 v[44:47], v234 offset:24576\n"
"v_mfma_f32_16x16x32_bf16 v[136:139], v[164:167], v[40:43], v[136:139]\n"
"ds_read_b128 v[40:43], v233 offset:24576\n"
"v_mfma_f32_16x16x32_bf16 v[136:139], v[160:163], v[36:39], v[136:139]\n"
"ds_read_b128 v[36:39], v232 offset:24576\n"
"s_waitcnt lgkmcnt(8)\n"
"v_mfma_f32_16x16x32_bf16 v[132:135], a[76:79], v[116:119], v[0:3]\n"
"ds_write_b64 v244, v[208:209] offset:24704\n"
"v_mfma_f32_16x16x32_bf16 v[132:135], a[72:75], v[112:115], v[132:135]\n"
"s_add_u32 m0, s31, 0x13f00\n"
"v_mfma_f32_16x16x32_bf16 v[132:135], a[68:71], v[108:111], v[132:135]\n"
"global_load_lds_dwordx4 v237, s[52:53] offset:256 nt\n"
"v_mfma_f32_16x16x32_bf16 v[132:135], a[64:67], v[92:95], v[132:135]\n"
"v_cvt_pk_bf16_f32 v210, v136, v137\n"
"s_waitcnt lgkmcnt(5)\n"
"v_mfma_f32_16x16x32_bf16 v[132:135], a[60:63], v[88:91], v[132:135]\n"
"v_cvt_pk_bf16_f32 v211, v138, v139\n"
"v_mfma_f32_16x16x32_bf16 v[132:135], a[56:59], v[84:87], v[132:135]\n"
"s_add_u32 s54, s26, s44\n"
"s_addc_u32 s55, s27, 0\n"
"v_mfma_f32_16x16x32_bf16 v[132:135], a[52:55], v[68:71], v[132:135]\n"
"ds_read_b128 v[216:219], v246 offset:12288\n"
"v_mfma_f32_16x16x32_bf16 v[132:135], a[48:51], v[64:67], v[132:135]\n"
"ds_read_b128 v[220:223], v246 offset:13312\n"
"s_waitcnt lgkmcnt(3)\n"
"v_mfma_f32_16x16x32_bf16 v[132:135], a[252:255], v[60:63], v[132:135]\n"
"ds_read_b128 v[224:227], v246 offset:14336\n"
"v_mfma_f32_16x16x32_bf16 v[132:135], a[248:251], v[44:47], v[132:135]\n"
"ds_write_b64 v244, v[210:211] offset:24712\n"
"v_mfma_f32_16x16x32_bf16 v[132:135], a[244:247], v[40:43], v[132:135]\n"
"s_add_u32 m0, s31, 0x15e00\n"
"v_mfma_f32_16x16x32_bf16 v[132:135], a[240:243], v[36:39], v[132:135]\n"
"global_load_lds_dwordx4 v237, s[52:53] offset:512 nt\n"
"v_mfma_f32_16x16x32_bf16 v[136:139], a[80:83], v[36:39], v[4:7]\n"
"s_sub_i32 s84, s84, s19\n"
"v_mfma_f32_16x16x32_bf16 v[136:139], a[84:87], v[40:43], v[136:139]\n"
"s_max_i32 s88, s88, s84\n"
"v_mfma_f32_16x16x32_bf16 v[136:139], a[88:91], v[44:47], v[136:139]\n"
"v_mfma_f32_16x16x32_bf16 v[136:139], a[92:95], v[60:63], v[136:139]\n"
"v_cvt_pk_bf16_f32 v208, v132, v133\n"
"v_mfma_f32_16x16x32_bf16 v[136:139], a[96:99], v[64:67], v[136:139]\n"
"v_cvt_pk_bf16_f32 v209, v134, v135\n"
"v_mfma_f32_16x16x32_bf16 v[136:139], a[100:103], v[68:71], v[136:139]\n"
"v_mfma_f32_16x16x32_bf16 v[136:139], a[104:107], v[84:87], v[136:139]\n"
"v_mfma_f32_16x16x32_bf16 v[136:139], a[108:111], v[88:91], v[136:139]\n"
"s_waitcnt lgkmcnt(1)\n"
"v_mfma_f32_16x16x32_bf16 v[136:139], a[112:115], v[92:95], v[136:139]\n"
"v_mfma_f32_16x16x32_bf16 v[136:139], a[116:119], v[108:111], v[136:139]\n"
"global_store_dwordx4 v247, v[216:219], s[54:55] offset:0 sc1\n"
"ds_write_b64 v244, v[208:209] offset:0\n"
"v_mfma_f32_16x16x32_bf16 v[136:139], a[120:123], v[112:115], v[136:139]\n"
"v_mfma_f32_16x16x32_bf16 v[136:139], a[124:127], v[116:119], v[136:139]\n"
"v_mfma_f32_16x16x32_bf16 v[132:135], a[172:175], v[116:119], v[8:11]\n"
"v_mfma_f32_16x16x32_bf16 v[132:135], a[168:171], v[112:115], v[132:135]\n"
"v_mfma_f32_16x16x32_bf16 v[132:135], a[164:167], v[108:111], v[132:135]\n"
"v_mfma_f32_16x16x32_bf16 v[132:135], a[160:163], v[92:95], v[132:135]\n"
"v_cvt_pk_bf16_f32 v210, v136, v137\n"
"v_mfma_f32_16x16x32_bf16 v[132:135], a[156:159], v[88:91], v[132:135]\n"
"v_cvt_pk_bf16_f32 v211, v138, v139\n"
"v_mfma_f32_16x16x32_bf16 v[132:135], a[152:155], v[84:87], v[132:135]\n"
"v_mfma_f32_16x16x32_bf16 v[132:135], a[148:151], v[68:71], v[132:135]\n"
"v_mfma_f32_16x16x32_bf16 v[132:135], a[144:147], v[64:67], v[132:135]\n"
"v_mfma_f32_16x16x32_bf16 v[132:135], a[140:143], v[60:63], v[132:135]\n"
"v_mfma_f32_16x16x32_bf16 v[132:135], a[136:139], v[44:47], v[132:135]\n"
"global_store_dwordx4 v252, v[220:223], s[54:55] offset:0 sc1\n"
"ds_write_b64 v244, v[210:211] offset:8\n"
"v_mfma_f32_16x16x32_bf16 v[132:135], a[132:135], v[40:43], v[132:135]\n"
"s_add_u32 m0, s31, 0x13000\n"
"v_mfma_f32_16x16x32_bf16 v[132:135], a[128:131], v[36:39], v[132:135]\n"
"global_load_lds_dwordx4 v238, s[52:53] offset:0 nt\n"
"v_mfma_f32_16x16x32_bf16 v[136:139], a[176:179], v[36:39], v[12:15]\n"
"v_mfma_f32_16x16x32_bf16 v[136:139], a[180:183], v[40:43], v[136:139]\n"
"v_mfma_f32_16x16x32_bf16 v[136:139], a[184:187], v[44:47], v[136:139]\n"
"v_mfma_f32_16x16x32_bf16 v[136:139], a[188:191], v[60:63], v[136:139]\n"
"v_cvt_pk_bf16_f32 v212, v132, v133\n"
"v_mfma_f32_16x16x32_bf16 v[136:139], a[192:195], v[64:67], v[136:139]\n"
"v_mov_b32 v242, 27\n"
"v_mfma_f32_16x16x32_bf16 v[136:139], a[196:199], v[68:71], v[136:139]\n"
"v_cvt_pk_bf16_f32 v213, v134, v135\n"
"v_mfma_f32_16x16x32_bf16 v[136:139], a[200:203], v[84:87], v[136:139]\n"
"s_waitcnt lgkmcnt(2)\n"
"ds_write_b32 v240, v242\n"
"v_mfma_f32_16x16x32_bf16 v[136:139], a[204:207], v[88:91], v[136:139]\n"
"v_mfma_f32_16x16x32_bf16 v[136:139], a[208:211], v[92:95], v[136:139]\n"
"v_mfma_f32_16x16x32_bf16 v[136:139], a[212:215], v[108:111], v[136:139]\n"
"v_mfma_f32_16x16x32_bf16 v[136:139], a[216:219], v[112:115], v[136:139]\n"
"global_store_dwordx4 v253, v[224:227], s[54:55] offset:0 sc1\n"
"ds_write_b64 v245, v[212:213] offset:0\n"
"v_mfma_f32_16x16x32_bf16 v[136:139], a[220:223], v[116:119], v[136:139]\n"
"v_mfma_f32_16x16x32_bf16 v[132:135], v[156:159], v[116:119], v[16:19]\n"
"v_mfma_f32_16x16x32_bf16 v[132:135], v[140:143], v[112:115], v[132:135]\n"
"v_mfma_f32_16x16x32_bf16 v[132:135], a[20:23], v[108:111], v[132:135]\n"
"ds_read_b32 v243, v241\n"
"v_mfma_f32_16x16x32_bf16 v[132:135], a[16:19], v[92:95], v[132:135]\n"
"v_cvt_pk_bf16_f32 v214, v136, v137\n"
"v_mfma_f32_16x16x32_bf16 v[132:135], a[12:15], v[88:91], v[132:135]\n"
"v_cvt_pk_bf16_f32 v215, v138, v139\n"
"v_mfma_f32_16x16x32_bf16 v[132:135], a[8:11], v[84:87], v[132:135]\n"
"v_mfma_f32_16x16x32_bf16 v[132:135], a[4:7], v[68:71], v[132:135]\n"
"s_waitcnt lgkmcnt(0)\n"
"v_cmp_gt_u32 s[56:57], 27, v243\n"
"v_mfma_f32_16x16x32_bf16 v[132:135], a[0:3], v[64:67], v[132:135]\n"
"v_mfma_f32_16x16x32_bf16 v[132:135], a[236:239], v[60:63], v[132:135]\n"
"s_or_b64 s[56:57], s[56:57], vcc\n"
"s_cbranch_scc1 LB27\n"
"LB27R:\n"
"v_mfma_f32_16x16x32_bf16 v[132:135], a[232:235], v[44:47], v[132:135]\n"
"ds_write_b64 v245, v[214:215] offset:8\n"
"v_mfma_f32_16x16x32_bf16 v[132:135], a[228:231], v[40:43], v[132:135]\n"
"v_mfma_f32_16x16x32_bf16 v[132:135], a[224:227], v[36:39], v[132:135]\n"
"s_mov_b64 exec, s[4:5]\n"
"s_cbranch_execz LHT12\n"
"LHR12:\n"
"s_mov_b64 exec, -1\n"
"v_mfma_f32_16x16x32_bf16 v[136:139], v[160:163], v[36:39], v[20:23]\n"
"ds_read_b128 v[36:39], v232 offset:24832\n"
"v_mfma_f32_16x16x32_bf16 v[136:139], v[164:167], v[40:43], v[136:139]\n"
"ds_read_b128 v[40:43], v233 offset:24832\n"
"v_mfma_f32_16x16x32_bf16 v[136:139], v[180:183], v[44:47], v[136:139]\n"
"ds_read_b128 v[44:47], v234 offset:24832\n"
"v_mfma_f32_16x16x32_bf16 v[136:139], v[184:187], v[60:63], v[136:139]\n"
"ds_read_b128 v[60:63], v235 offset:24832\n"
"v_cvt_pk_bf16_f32 v208, v132, v133\n"
"v_mfma_f32_16x16x32_bf16 v[136:139], v[188:191], v[64:67], v[136:139]\n"
"ds_read_b128 v[64:67], v232 offset:33024\n"
"v_cvt_pk_bf16_f32 v209, v134, v135\n"
"v_mfma_f32_16x16x32_bf16 v[136:139], v[204:207], v[68:71], v[136:139]\n"
"ds_read_b128 v[68:71], v233 offset:33024\n"
"v_mfma_f32_16x16x32_bf16 v[136:139], a[24:27], v[84:87], v[136:139]\n"
"ds_read_b128 v[84:87], v234 offset:33024\n"
"v_mfma_f32_16x16x32_bf16 v[136:139], a[28:31], v[88:91], v[136:139]\n"
"ds_read_b128 v[88:91], v235 offset:33024\n"
"v_mfma_f32_16x16x32_bf16 v[136:139], a[32:35], v[92:95], v[136:139]\n"
"ds_read_b128 v[92:95], v232 offset:41216\n"
"v_mfma_f32_16x16x32_bf16 v[136:139], a[36:39], v[108:111], v[136:139]\n"
"ds_read_b128 v[108:111], v233 offset:41216\n"
"ds_write_b64 v244, v[208:209] offset:128\n"
"v_mfma_f32_16x16x32_bf16 v[136:139], a[40:43], v[112:115], v[136:139]\n"
"ds_read_b128 v[112:115], v234 offset:41216\n"
"v_mfma_f32_16x16x32_bf16 v[136:139], a[44:47], v[116:119], v[136:139]\n"
"ds_read_b128 v[116:119], v235 offset:41216\n"
"s_waitcnt lgkmcnt(9)\n"
"v_mfma_f32_16x16x32_bf16 v[132:135], a[240:243], v[36:39], v[0:3]\n"
"v_add_u32 v232, 0xc000, v232\n"
"v_mfma_f32_16x16x32_bf16 v[132:135], a[244:247], v[40:43], v[132:135]\n"
"v_add_u32 v233, 0xc000, v233\n"
"v_mfma_f32_16x16x32_bf16 v[132:135], a[248:251], v[44:47], v[132:135]\n"
"v_add_u32 v234, 0xc000, v234\n"
"v_mfma_f32_16x16x32_bf16 v[132:135], a[252:255], v[60:63], v[132:135]\n"
"v_add_u32 v235, 0xc000, v235\n"
"s_waitcnt lgkmcnt(5)\n"
"v_mfma_f32_16x16x32_bf16 v[132:135], a[48:51], v[64:67], v[132:135]\n"
"s_add_u32 m0, s31, 0x14f00\n"
"v_mfma_f32_16x16x32_bf16 v[132:135], a[52:55], v[68:71], v[132:135]\n"
"global_load_lds_dwordx4 v238, s[52:53] offset:256 nt\n"
"v_mfma_f32_16x16x32_bf16 v[132:135], a[56:59], v[84:87], v[132:135]\n"
"v_cvt_pk_bf16_f32 v210, v136, v137\n"
"v_mfma_f32_16x16x32_bf16 v[132:135], a[60:63], v[88:91], v[132:135]\n"
"v_cvt_pk_bf16_f32 v211, v138, v139\n"
"s_waitcnt lgkmcnt(0)\n"
"v_mfma_f32_16x16x32_bf16 v[132:135], a[64:67], v[92:95], v[132:135]\n"
"ds_read_b128 v[216:219], v246 offset:24576\n"
"v_mfma_f32_16x16x32_bf16 v[132:135], a[68:71], v[108:111], v[132:135]\n"
"ds_read_b128 v[220:223], v246 offset:25600\n"
"v_mfma_f32_16x16x32_bf16 v[132:135], a[72:75], v[112:115], v[132:135]\n"
"ds_read_b128 v[224:227], v246 offset:26624\n"
"v_mfma_f32_16x16x32_bf16 v[132:135], a[76:79], v[116:119], v[132:135]\n"
"v_lshlrev_b32 v0, 16, v168\n"
"v_mfma_f32_16x16x32_bf16 v[136:139], a[124:127], v[116:119], v[4:7]\n"
"ds_write_b64 v244, v[210:211] offset:136\n"
"v_mfma_f32_16x16x32_bf16 v[136:139], a[120:123], v[112:115], v[136:139]\n"
"s_add_u32 m0, s31, 0x16e00\n"
"v_mfma_f32_16x16x32_bf16 v[136:139], a[116:119], v[108:111], v[136:139]\n"
"global_load_lds_dwordx4 v238, s[52:53] offset:512 nt\n"
"v_mfma_f32_16x16x32_bf16 v[136:139], a[112:115], v[92:95], v[136:139]\n"
"v_cvt_pk_bf16_f32 v208, v132, v133\n"
"v_mfma_f32_16x16x32_bf16 v[136:139], a[108:111], v[88:91], v[136:139]\n"
"v_cvt_pk_bf16_f32 v209, v134, v135\n"
"v_mfma_f32_16x16x32_bf16 v[136:139], a[104:107], v[84:87], v[136:139]\n"
"v_and_b32 v1, 0xffff0000, v168\n"
"v_mfma_f32_16x16x32_bf16 v[136:139], a[100:103], v[68:71], v[136:139]\n"
"v_lshlrev_b32 v2, 16, v169\n"
"v_mfma_f32_16x16x32_bf16 v[136:139], a[96:99], v[64:67], v[136:139]\n"
"s_waitcnt lgkmcnt(1)\n"
"v_mfma_f32_16x16x32_bf16 v[136:139], a[92:95], v[60:63], v[136:139]\n"
"v_and_b32 v3, 0xffff0000, v169\n"
"v_mfma_f32_16x16x32_bf16 v[136:139], a[88:91], v[44:47], v[136:139]\n"
"global_store_dwordx4 v247, v[216:219], s[54:55] offset:768 sc1\n"
"ds_write_b64 v244, v[208:209] offset:12288\n"
"v_mfma_f32_16x16x32_bf16 v[136:139], a[84:87], v[40:43], v[136:139]\n"
"v_lshlrev_b32 v4, 16, v170\n"
"v_mfma_f32_16x16x32_bf16 v[136:139], a[80:83], v[36:39], v[136:139]\n"
"v_and_b32 v5, 0xffff0000, v170\n"
"v_mfma_f32_16x16x32_bf16 v[132:135], a[128:131], v[36:39], v[8:11]\n"
"v_lshlrev_b32 v6, 16, v171\n"
"v_mfma_f32_16x16x32_bf16 v[132:135], a[132:135], v[40:43], v[132:135]\n"
"v_and_b32 v7, 0xffff0000, v171\n"
"v_mfma_f32_16x16x32_bf16 v[132:135], a[136:139], v[44:47], v[132:135]\n"
"s_add_u32 s83, s83, s86\n"
"v_mfma_f32_16x16x32_bf16 v[132:135], a[140:143], v[60:63], v[132:135]\n"
"v_cvt_pk_bf16_f32 v210, v136, v137\n"
"v_mfma_f32_16x16x32_bf16 v[132:135], a[144:147], v[64:67], v[132:135]\n"
"v_cvt_pk_bf16_f32 v211, v138, v139\n"
"v_mfma_f32_16x16x32_bf16 v[132:135], a[148:151], v[68:71], v[132:135]\n"
"v_lshlrev_b32 v8, 16, v172\n"
"v_mfma_f32_16x16x32_bf16 v[132:135], a[152:155], v[84:87], v[132:135]\n"
"v_and_b32 v9, 0xffff0000, v172\n"
"v_mfma_f32_16x16x32_bf16 v[132:135], a[156:159], v[88:91], v[132:135]\n"
"v_lshlrev_b32 v10, 16, v173\n"
"v_mfma_f32_16x16x32_bf16 v[132:135], a[160:163], v[92:95], v[132:135]\n"
"v_and_b32 v11, 0xffff0000, v173\n"
"v_mfma_f32_16x16x32_bf16 v[132:135], a[164:167], v[108:111], v[132:135]\n"
"s_memrealtime s[84:85]\n"
"v_mfma_f32_16x16x32_bf16 v[132:135], a[168:171], v[112:115], v[132:135]\n"
"global_store_dwordx4 v252, v[220:223], s[54:55] offset:768 sc1\n"
"ds_write_b64 v244, v[210:211] offset:12296\n"
"v_mfma_f32_16x16x32_bf16 v[132:135], a[172:175], v[116:119], v[132:135]\n"
"s_sub_i32 s88, s88, 6\n"
"v_mfma_f32_16x16x32_bf16 v[136:139], a[220:223], v[116:119], v[12:15]\n"
"s_max_i32 s88, s88, 0\n"
"v_mfma_f32_16x16x32_bf16 v[136:139], a[216:219], v[112:115], v[136:139]\n"
"s_add_u32 s52, s20, s48\n"
"s_addc_u32 s53, s21, 0\n"
"v_mfma_f32_16x16x32_bf16 v[136:139], a[212:215], v[108:111], v[136:139]\n"
"s_add_u32 m0, s31, 0x0\n"
"v_mfma_f32_16x16x32_bf16 v[136:139], a[208:211], v[92:95], v[136:139]\n"
"v_cvt_pk_bf16_f32 v212, v132, v133\n"
"v_mfma_f32_16x16x32_bf16 v[136:139], a[204:207], v[88:91], v[136:139]\n"
"v_mov_b32 v242, 28\n"
"v_mfma_f32_16x16x32_bf16 v[136:139], a[200:203], v[84:87], v[136:139]\n"
"v_cvt_pk_bf16_f32 v213, v134, v135\n"
"v_mfma_f32_16x16x32_bf16 v[136:139], a[196:199], v[68:71], v[136:139]\n"
"s_waitcnt vmcnt(14) lgkmcnt(2)\n"
"ds_write_b32 v240, v242\n"
"v_mfma_f32_16x16x32_bf16 v[136:139], a[192:195], v[64:67], v[136:139]\n"
"global_load_lds_dwordx4 v237, s[52:53] offset:0 nt\n"
"v_mfma_f32_16x16x32_bf16 v[136:139], a[188:191], v[60:63], v[136:139]\n"
"v_lshlrev_b32 v12, 16, v174\n"
"v_mfma_f32_16x16x32_bf16 v[136:139], a[184:187], v[44:47], v[136:139]\n"
"v_and_b32 v13, 0xffff0000, v174\n"
"v_mfma_f32_16x16x32_bf16 v[136:139], a[180:183], v[40:43], v[136:139]\n"
"global_store_dwordx4 v253, v[224:227], s[54:55] offset:768 sc1\n"
"ds_write_b64 v245, v[212:213] offset:12288\n"
"v_mfma_f32_16x16x32_bf16 v[136:139], a[176:179], v[36:39], v[136:139]\n"
"v_lshlrev_b32 v14, 16, v175\n"
"v_mfma_f32_16x16x32_bf16 v[132:135], a[224:227], v[36:39], v[16:19]\n"
"v_and_b32 v15, 0xffff0000, v175\n"
"v_mfma_f32_16x16x32_bf16 v[132:135], a[228:231], v[40:43], v[132:135]\n"
"v_mfma_f32_16x16x32_bf16 v[132:135], a[232:235], v[44:47], v[132:135]\n"
"ds_read_b32 v243, v241\n"
"v_mfma_f32_16x16x32_bf16 v[132:135], a[236:239], v[60:63], v[132:135]\n"
"v_cvt_pk_bf16_f32 v214, v136, v137\n"
"v_mfma_f32_16x16x32_bf16 v[132:135], a[0:3], v[64:67], v[132:135]\n"
"v_cvt_pk_bf16_f32 v215, v138, v139\n"
"v_mfma_f32_16x16x32_bf16 v[132:135], a[4:7], v[68:71], v[132:135]\n"
"v_lshlrev_b32 v16, 16, v176\n"
"v_mfma_f32_16x16x32_bf16 v[132:135], a[8:11], v[84:87], v[132:135]\n"
"s_waitcnt lgkmcnt(0)\n"
"v_cmp_gt_u32 s[56:57], 28, v243\n"
"LHT12:\n"
"s_cbranch_execz LHR12\n"
"v_mfma_f32_16x16x32_bf16 v[132:135], a[12:15], v[88:91], v[132:135]\n"
"s_sub_u32 s84, s84, s83\n"
"v_mfma_f32_16x16x32_bf16 v[132:135], a[16:19], v[92:95], v[132:135]\n"
"s_or_b64 s[56:57], s[56:57], vcc\n"
"s_cbranch_scc1 LB28\n"
"LB28R:\n"
"v_mfma_f32_16x16x32_bf16 v[132:135], a[20:23], v[108:111], v[132:135]\n"
"s_cmp_lt_i32 s84, s88\n"
"s_cselect_b64 vcc, -1, 0\n"
"v_mfma_f32_16x16x32_bf16 v[132:135], v[140:143], v[112:115], v[132:135]\n"
"ds_write_b64 v245, v[214:215] offset:12296\n"
"v_mfma_f32_16x16x32_bf16 v[132:135], v[156:159], v[116:119], v[132:135]\n"
"v_and_b32 v17, 0xffff0000, v176\n"
"v_mfma_f32_16x16x32_bf16 v[136:139], a[44:47], v[116:119], v[20:23]\n"
"ds_read_b128 v[116:119], v235 offset:16384\n"
"v_mfma_f32_16x16x32_bf16 v[136:139], a[40:43], v[112:115], v[136:139]\n"
"ds_read_b128 v[112:115], v234 offset:16384\n"
"v_mfma_f32_16x16x32_bf16 v[136:139], a[36:39], v[108:111], v[136:139]\n"
"ds_read_b128 v[108:111], v233 offset:16384\n"
"v_mfma_f32_16x16x32_bf16 v[136:139], a[32:35], v[92:95], v[136:139]\n"
"ds_read_b128 v[92:95], v232 offset:16384\n"
"s_cmp_gt_i32 s84, 38\n"
"s_cbranch_scc1 LEARLY\n"
"v_mfma_f32_16x16x32_bf16 v[136:139], a[28:31], v[88:91], v[136:139]\n"
"ds_read_b128 v[88:91], v235 offset:8192\n"
"v_cvt_pk_bf16_f32 v208, v132, v133\n"
"v_mfma_f32_16x16x32_bf16 v[136:139], a[24:27], v[84:87], v[136:139]\n"
"ds_read_b128 v[84:87], v234 offset:8192\n"
"v_cvt_pk_bf16_f32 v209, v134, v135\n"
"v_mfma_f32_16x16x32_bf16 v[136:139], v[204:207], v[68:71], v[136:139]\n"
"ds_read_b128 v[68:71], v233 offset:8192\n"
"v_mfma_f32_16x16x32_bf16 v[136:139], v[188:191], v[64:67], v[136:139]\n"
"ds_read_b128 v[64:67], v232 offset:8192\n"
"v_mfma_f32_16x16x32_bf16 v[136:139], v[184:187], v[60:63], v[136:139]\n"
"ds_read_b128 v[60:63], v235 offset:0\n"
"v_mfma_f32_16x16x32_bf16 v[136:139], v[180:183], v[44:47], v[136:139]\n"
"ds_read_b128 v[44:47], v234 offset:0\n"
"v_mfma_f32_16x16x32_bf16 v[136:139], v[164:167], v[40:43], v[136:139]\n"
"ds_read_b128 v[40:43], v233 offset:0\n"
"v_mfma_f32_16x16x32_bf16 v[136:139], v[160:163], v[36:39], v[136:139]\n"
"ds_read_b128 v[36:39], v232 offset:0\n"
"s_waitcnt lgkmcnt(8)\n"
"v_mfma_f32_16x16x32_bf16 v[132:135], a[76:79], v[116:119], v[0:3]\n"
"ds_write_b64 v244, v[208:209] offset:12416\n"
"v_mfma_f32_16x16x32_bf16 v[132:135], a[72:75], v[112:115], v[132:135]\n"
"s_add_u32 m0, s31, 0x1f00\n"
"v_mfma_f32_16x16x32_bf16 v[132:135], a[68:71], v[108:111], v[132:135]\n"
"global_load_lds_dwordx4 v237, s[52:53] offset:256 nt\n"
"v_mfma_f32_16x16x32_bf16 v[132:135], a[64:67], v[92:95], v[132:135]\n"
"v_lshlrev_b32 v18, 16, v177\n"
"v_cvt_pk_bf16_f32 v211, v138, v139\n"
"s_waitcnt lgkmcnt(5)\n"
"v_mfma_f32_16x16x32_bf16 v[132:135], a[60:63], v[88:91], v[132:135]\n"
"v_and_b32 v19, 0xffff0000, v177\n"
"v_mfma_f32_16x16x32_bf16 v[132:135], a[56:59], v[84:87], v[132:135]\n"
"v_lshlrev_b32 v20, 16, v178\n"
"v_mfma_f32_16x16x32_bf16 v[132:135], a[52:55], v[68:71], v[132:135]\n"
"v_and_b32 v21, 0xffff0000, v178\n"
"v_mfma_f32_16x16x32_bf16 v[132:135], a[48:51], v[64:67], v[132:135]\n"
"v_lshlrev_b32 v22, 16, v179\n"
"s_waitcnt lgkmcnt(1)\n"
"v_mfma_f32_16x16x32_bf16 v[132:135], a[252:255], v[60:63], v[132:135]\n"
"v_and_b32 v23, 0xffff0000, v179\n"
"v_mfma_f32_16x16x32_bf16 v[132:135], a[248:251], v[44:47], v[132:135]\n"
"v_cvt_pk_bf16_f32 v210, v136, v137\n"
"v_mfma_f32_16x16x32_bf16 v[132:135], a[244:247], v[40:43], v[132:135]\n"
"s_add_u32 s54, s26, s45\n"
"s_addc_u32 s55, s27, 0\n"
"v_mfma_f32_16x16x32_bf16 v[132:135], a[240:243], v[36:39], v[132:135]\n"
"ds_read_b128 v[216:219], v246 offset:0\n"
"v_mfma_f32_16x16x32_bf16 v[136:139], a[80:83], v[36:39], v[4:7]\n"
"ds_read_b128 v[220:223], v246 offset:1024\n"
"v_mfma_f32_16x16x32_bf16 v[136:139], a[84:87], v[40:43], v[136:139]\n"
"ds_read_b128 v[224:227], v246 offset:2048\n"
"v_mfma_f32_16x16x32_bf16 v[136:139], a[88:91], v[44:47], v[136:139]\n"
"ds_write_b64 v244, v[210:211] offset:12424\n"
"v_mfma_f32_16x16x32_bf16 v[136:139], a[92:95], v[60:63], v[136:139]\n"
"v_cvt_pk_bf16_f32 v208, v132, v133\n"
"v_mfma_f32_16x16x32_bf16 v[136:139], a[96:99], v[64:67], v[136:139]\n"
"v_cvt_pk_bf16_f32 v209, v134, v135\n"
"v_mfma_f32_16x16x32_bf16 v[136:139], a[100:103], v[68:71], v[136:139]\n"
"s_add_u32 m0, s31, 0x3e00\n"
"v_mfma_f32_16x16x32_bf16 v[136:139], a[104:107], v[84:87], v[136:139]\n"
"global_load_lds_dwordx4 v237, s[52:53] offset:512 nt\n"
"v_mfma_f32_16x16x32_bf16 v[136:139], a[108:111], v[88:91], v[136:139]\n"
"s_waitcnt lgkmcnt(1)\n"
"v_mfma_f32_16x16x32_bf16 v[136:139], a[112:115], v[92:95], v[136:139]\n"
"s_sub_i32 s84, s84, s19\n"
"v_mfma_f32_16x16x32_bf16 v[136:139], a[116:119], v[108:111], v[136:139]\n"
"global_store_dwordx4 v247, v[216:219], s[54:55] offset:0 sc1\n"
"ds_write_b64 v244, v[208:209] offset:24576\n"
"v_mfma_f32_16x16x32_bf16 v[136:139], a[120:123], v[112:115], v[136:139]\n"
"s_max_i32 s88, s88, s84\n"
"v_mfma_f32_16x16x32_bf16 v[136:139], a[124:127], v[116:119], v[136:139]\n"
"v_mfma_f32_16x16x32_bf16 v[132:135], a[172:175], v[116:119], v[8:11]\n"
"v_mfma_f32_16x16x32_bf16 v[132:135], a[168:171], v[112:115], v[132:135]\n"
"v_mfma_f32_16x16x32_bf16 v[132:135], a[164:167], v[108:111], v[132:135]\n"
"v_mfma_f32_16x16x32_bf16 v[132:135], a[160:163], v[92:95], v[132:135]\n"
"v_cvt_pk_bf16_f32 v210, v136, v137\n"
"v_mfma_f32_16x16x32_bf16 v[132:135], a[156:159], v[88:91], v[132:135]\n"
"v_cvt_pk_bf16_f32 v211, v138, v139\n"
"v_mfma_f32_16x16x32_bf16 v[132:135], a[152:155], v[84:87], v[132:135]\n"
"v_mfma_f32_16x16x32_bf16 v[132:135], a[148:151], v[68:71], v[132:135]\n"
"v_mfma_f32_16x16x32_bf16 v[132:135], a[144:147], v[64:67], v[132:135]\n"
"v_mfma_f32_16x16x32_bf16 v[132:135], a[140:143], v[60:63], v[132:135]\n"
"v_mfma_f32_16x16x32_bf16 v[132:135], a[136:139], v[44:47], v[132:135]\n"
"global_store_dwordx4 v252, v[220:223], s[54:55] offset:0 sc1\n"
"ds_write_b64 v244, v[210:211] offset:24584\n"
"v_mfma_f32_16x16x32_bf16 v[132:135], a[132:135], v[40:43], v[132:135]\n"
"v_mfma_f32_16x16x32_bf16 v[132:135], a[128:131], v[36:39], v[132:135]\n"
"v_mfma_f32_16x16x32_bf16 v[136:139], a[176:179], v[36:39], v[12:15]\n"
"v_mfma_f32_16x16x32_bf16 v[136:139], a[180:183], v[40:43], v[136:139]\n"
"v_mfma_f32_16x16x32_bf16 v[136:139], a[184:187], v[44:47], v[136:139]\n"
"v_mfma_f32_16x16x32_bf16 v[136:139], a[188:191], v[60:63], v[136:139]\n"
"v_cvt_pk_bf16_f32 v212, v132, v133\n"
"v_mfma_f32_16x16x32_bf16 v[136:139], a[192:195], v[64:67], v[136:139]\n"
"v_mov_b32 v242, 29\n"
"v_mfma_f32_16x16x32_bf16 v[136:139], a[196:199], v[68:71], v[136:139]\n"
"v_cvt_pk_bf16_f32 v213, v134, v135\n"
"v_mfma_f32_16x16x32_bf16 v[136:139], a[200:203], v[84:87], v[136:139]\n"
"s_waitcnt lgkmcnt(2)\n"
"ds_write_b32 v240, v242\n"
"v_mfma_f32_16x16x32_bf16 v[136:139], a[204:207], v[88:91], v[136:139]\n"
"s_add_u32 m0, s31, 0x1000\n"
"v_mfma_f32_16x16x32_bf16 v[136:139], a[208:211], v[92:95], v[136:139]\n"
"global_load_lds_dwordx4 v238, s[52:53] offset:0 nt\n"
"v_mfma_f32_16x16x32_bf16 v[136:139], a[212:215], v[108:111], v[136:139]\n"
"v_mfma_f32_16x16x32_bf16 v[136:139], a[216:219], v[112:115], v[136:139]\n"
"global_store_dwordx4 v253, v[224:227], s[54:55] offset:0 sc1\n"
"ds_write_b64 v245, v[212:213] offset:24576\n"
"v_mfma_f32_16x16x32_bf16 v[136:139], a[220:223], v[116:119], v[136:139]\n"
"v_mfma_f32_16x16x32_bf16 v[132:135], v[156:159], v[116:119], v[16:19]\n"
"v_mfma_f32_16x16x32_bf16 v[132:135], v[140:143], v[112:115], v[132:135]\n"
"v_mfma_f32_16x16x32_bf16 v[132:135], a[20:23], v[108:111], v[132:135]\n"
"ds_read_b32 v243, v241\n"
"v_mfma_f32_16x16x32_bf16 v[132:135], a[16:19], v[92:95], v[132:135]\n"
"v_cvt_pk_bf16_f32 v214, v136, v137\n"
"v_mfma_f32_16x16x32_bf16 v[132:135], a[12:15], v[88:91], v[132:135]\n"
"v_cvt_pk_bf16_f32 v215, v138, v139\n"
"v_mfma_f32_16x16x32_bf16 v[132:135], a[8:11], v[84:87], v[132:135]\n"
"v_mfma_f32_16x16x32_bf16 v[132:135], a[4:7], v[68:71], v[132:135]\n"
"s_waitcnt lgkmcnt(0)\n"
"v_cmp_gt_u32 s[56:57], 29, v243\n"
"v_mfma_f32_16x16x32_bf16 v[132:135], a[0:3], v[64:67], v[132:135]\n"
"v_mfma_f32_16x16x32_bf16 v[132:135], a[236:239], v[60:63], v[132:135]\n"
"s_or_b64 s[56:57], s[56:57], vcc\n"
"s_cbranch_scc1 LB29\n"
"LB29R:\n"
"v_mfma_f32_16x16x32_bf16 v[132:135], a[232:235], v[44:47], v[132:135]\n"
"ds_write_b64 v245, v[214:215] offset:24584\n"
"v_mfma_f32_16x16x32_bf16 v[132:135], a[228:231], v[40:43], v[132:135]\n"
"v_mfma_f32_16x16x32_bf16 v[132:135], a[224:227], v[36:39], v[132:135]\n"
"v_mfma_f32_16x16x32_bf16 v[136:139], v[160:163], v[36:39], v[20:23]\n"
"ds_read_b128 v[36:39], v232 offset:256\n"
"v_mfma_f32_16x16x32_bf16 v[136:139], v[164:167], v[40:43], v[136:139]\n"
"ds_read_b128 v[40:43], v233 offset:256\n"
"v_mfma_f32_16x16x32_bf16 v[136:139], v[180:183], v[44:47], v[136:139]\n"
"ds_read_b128 v[44:47], v234 offset:256\n"
"v_mfma_f32_16x16x32_bf16 v[136:139], v[184:187], v[60:63], v[136:139]\n"
"ds_read_b128 v[60:63], v235 offset:256\n"
"v_cvt_pk_bf16_f32 v208, v132, v133\n"
"v_mfma_f32_16x16x32_bf16 v[136:139], v[188:191], v[64:67], v[136:139]\n"
"ds_read_b128 v[64:67], v232 offset:8448\n"
"v_cvt_pk_bf16_f32 v209, v134, v135\n"
"v_mfma_f32_16x16x32_bf16 v[136:139], v[204:207], v[68:71], v[136:139]\n"
"ds_read_b128 v[68:71], v233 offset:8448\n"
"v_mfma_f32_16x16x32_bf16 v[136:139], a[24:27], v[84:87], v[136:139]\n"
"ds_read_b128 v[84:87], v234 offset:8448\n"
"v_mfma_f32_16x16x32_bf16 v[136:139], a[28:31], v[88:91], v[136:139]\n"
"ds_read_b128 v[88:91], v235 offset:8448\n"
"v_mfma_f32_16x16x32_bf16 v[136:139], a[32:35], v[92:95], v[136:139]\n"
"ds_read_b128 v[92:95], v232 offset:16640\n"
"v_mfma_f32_16x16x32_bf16 v[136:139], a[36:39], v[108:111], v[136:139]\n"
"ds_read_b128 v[108:111], v233 offset:16640\n"
"v_mfma_f32_16x16x32_bf16 v[136:139], a[40:43], v[112:115], v[136:139]\n"
"ds_read_b128 v[112:115], v234 offset:16640\n"
"v_mfma_f32_16x16x32_bf16 v[136:139], a[44:47], v[116:119], v[136:139]\n"
"ds_read_b128 v[116:119], v235 offset:16640\n"
"s_waitcnt lgkmcnt(8)\n"
"v_mfma_f32_16x16x32_bf16 v[132:135], a[240:243], v[36:39], v[0:3]\n"
"ds_write_b64 v244, v[208:209] offset:24704\n"
"v_mfma_f32_16x16x32_bf16 v[132:135], a[244:247], v[40:43], v[132:135]\n"
"s_add_u32 m0, s31, 0x2f00\n"
"v_mfma_f32_16x16x32_bf16 v[132:135], a[248:251], v[44:47], v[132:135]\n"
"global_load_lds_dwordx4 v238, s[52:53] offset:256 nt\n"
"v_mfma_f32_16x16x32_bf16 v[132:135], a[252:255], v[60:63], v[132:135]\n"
"v_cvt_pk_bf16_f32 v210, v136, v137\n"
"s_waitcnt lgkmcnt(5)\n"
"v_mfma_f32_16x16x32_bf16 v[132:135], a[48:51], v[64:67], v[132:135]\n"
"v_cvt_pk_bf16_f32 v211, v138, v139\n"
"v_mfma_f32_16x16x32_bf16 v[132:135], a[52:55], v[68:71], v[132:135]\n"
"v_mfma_f32_16x16x32_bf16 v[132:135], a[56:59], v[84:87], v[132:135]\n"
"ds_read_b128 v[216:219], v246 offset:12288\n"
"v_mfma_f32_16x16x32_bf16 v[132:135], a[60:63], v[88:91], v[132:135]\n"
"ds_read_b128 v[220:223], v246 offset:13312\n"
"s_waitcnt lgkmcnt(3)\n"
"v_mfma_f32_16x16x32_bf16 v[132:135], a[64:67], v[92:95], v[132:135]\n"
"ds_read_b128 v[224:227], v246 offset:14336\n"
"v_mfma_f32_16x16x32_bf16 v[132:135], a[68:71], v[108:111], v[132:135]\n"
"ds_write_b64 v244, v[210:211] offset:24712\n"
"v_mfma_f32_16x16x32_bf16 v[132:135], a[72:75], v[112:115], v[132:135]\n"
"v_mfma_f32_16x16x32_bf16 v[132:135], a[76:79], v[116:119], v[132:135]\n"
"v_mfma_f32_16x16x32_bf16 v[136:139], a[124:127], v[116:119], v[4:7]\n"
"s_add_u32 s83, s83, s86\n"
"v_mfma_f32_16x16x32_bf16 v[136:139], a[120:123], v[112:115], v[136:139]\n"
"s_sub_i32 s88, s88, 6\n"
"v_mfma_f32_16x16x32_bf16 v[136:139], a[116:119], v[108:111], v[136:139]\n"
"s_max_i32 s88, s88, 0\n"
"v_mfma_f32_16x16x32_bf16 v[136:139], a[112:115], v[92:95], v[136:139]\n"
"v_cvt_pk_bf16_f32 v208, v132, v133\n"
"v_mfma_f32_16x16x32_bf16 v[136:139], a[108:111], v[88:91], v[136:139]\n"
"v_cvt_pk_bf16_f32 v209, v134, v135\n"
"v_mfma_f32_16x16x32_bf16 v[136:139], a[104:107], v[84:87], v[136:139]\n"
"v_mfma_f32_16x16x32_bf16 v[136:139], a[100:103], v[68:71], v[136:139]\n"
"v_mfma_f32_16x16x32_bf16 v[136:139], a[96:99], v[64:67], v[136:139]\n"
"s_waitcnt lgkmcnt(1)\n"
"v_mfma_f32_16x16x32_bf16 v[136:139], a[92:95], v[60:63], v[136:139]\n"
"v_mfma_f32_16x16x32_bf16 v[136:139], a[88:91], v[44:47], v[136:139]\n"
"global_store_dwordx4 v247, v[216:219], s[54:55] offset:768 sc1\n"
"ds_write_b64 v244, v[208:209] offset:0\n"
"v_mfma_f32_16x16x32_bf16 v[136:139], a[84:87], v[40:43], v[136:139]\n"
"s_add_u32 m0, s31, 0x4e00\n"
"v_mfma_f32_16x16x32_bf16 v[136:139], a[80:83], v[36:39], v[136:139]\n"
"global_load_lds_dwordx4 v238, s[52:53] offset:512 nt\n"
"v_mfma_f32_16x16x32_bf16 v[132:135], a[128:131], v[36:39], v[8:11]\n"
"v_mfma_f32_16x16x32_bf16 v[132:135], a[132:135], v[40:43], v[132:135]\n"
"v_mfma_f32_16x16x32_bf16 v[132:135], a[136:139], v[44:47], v[132:135]\n"
"v_mfma_f32_16x16x32_bf16 v[132:135], a[140:143], v[60:63], v[132:135]\n"
"v_cvt_pk_bf16_f32 v210, v136, v137\n"
"v_mfma_f32_16x16x32_bf16 v[132:135], a[144:147], v[64:67], v[132:135]\n"
"v_cvt_pk_bf16_f32 v211, v138, v139\n"
"v_mfma_f32_16x16x32_bf16 v[132:135], a[148:151], v[68:71], v[132:135]\n"
"v_mfma_f32_16x16x32_bf16 v[132:135], a[152:155], v[84:87], v[132:135]\n"
"v_mfma_f32_16x16x32_bf16 v[132:135], a[156:159], v[88:91], v[132:135]\n"
"v_mfma_f32_16x16x32_bf16 v[132:135], a[160:163], v[92:95], v[132:135]\n"
"v_mfma_f32_16x16x32_bf16 v[132:135], a[164:167], v[108:111], v[132:135]\n"
"s_memrealtime s[84:85]\n"
"v_mfma_f32_16x16x32_bf16 v[132:135], a[168:171], v[112:115], v[132:135]\n"
"global_store_dwordx4 v252, v[220:223], s[54:55] offset:768 sc1\n"
"ds_write_b64 v244, v[210:211] offset:8\n"
"v_mfma_f32_16x16x32_bf16 v[132:135], a[172:175], v[116:119], v[132:135]\n"
"v_mfma_f32_16x16x32_bf16 v[136:139], a[220:223], v[116:119], v[12:15]\n"
"v_mfma_f32_16x16x32_bf16 v[136:139], a[216:219], v[112:115], v[136:139]\n"
"v_mfma_f32_16x16x32_bf16 v[136:139], a[212:215], v[108:111], v[136:139]\n"
"v_mfma_f32_16x16x32_bf16 v[136:139], a[208:211], v[92:95], v[136:139]\n"
"v_cvt_pk_bf16_f32 v212, v132, v133\n"
"v_mfma_f32_16x16x32_bf16 v[136:139], a[204:207], v[88:91], v[136:139]\n"
"v_mov_b32 v242, 30\n"
"v_mfma_f32_16x16x32_bf16 v[136:139], a[200:203], v[84:87], v[136:139]\n"
"v_cvt_pk_bf16_f32 v213, v134, v135\n"
"v_mfma_f32_16x16x32_bf16 v[136:139], a[196:199], v[68:71], v[136:139]\n"
"s_waitcnt vmcnt(14) lgkmcnt(2)\n"
"ds_write_b32 v240, v242\n"
"v_mfma_f32_16x16x32_bf16 v[136:139], a[192:195], v[64:67], v[136:139]\n"
"v_mfma_f32_16x16x32_bf16 v[136:139], a[188:191], v[60:63], v[136:139]\n"
"v_mfma_f32_16x16x32_bf16 v[136:139], a[184:187], v[44:47], v[136:139]\n"
"v_mfma_f32_16x16x32_bf16 v[136:139], a[180:183], v[40:43], v[136:139]\n"
"global_store_dwordx4 v253, v[224:227], s[54:55] offset:768 sc1\n"
"ds_write_b64 v245, v[212:213] offset:0\n"
"v_mfma_f32_16x16x32_bf16 v[136:139], a[176:179], v[36:39], v[136:139]\n"
"s_add_u32 s52, s20, s49\n"
"s_addc_u32 s53, s21, 0\n"
"v_mfma_f32_16x16x32_bf16 v[132:135], a[224:227], v[36:39], v[16:19]\n"
"s_add_u32 m0, s31, 0x6000\n"
"v_mfma_f32_16x16x32_bf16 v[132:135], a[228:231], v[40:43], v[132:135]\n"
"global_load_lds_dwordx4 v237, s[52:53] offset:0 nt\n"
"v_mfma_f32_16x16x32_bf16 v[132:135], a[232:235], v[44:47], v[132:135]\n"
"ds_read_b32 v243, v241\n"
"v_mfma_f32_16x16x32_bf16 v[132:135], a[236:239], v[60:63], v[132:135]\n"
"v_cvt_pk_bf16_f32 v214, v136, v137\n"
"v_mfma_f32_16x16x32_bf16 v[132:135], a[0:3], v[64:67], v[132:135]\n"
"v_cvt_pk_bf16_f32 v215, v138, v139\n"
"v_mfma_f32_16x16x32_bf16 v[132:135], a[4:7], v[68:71], v[132:135]\n"
"v_mfma_f32_16x16x32_bf16 v[132:135], a[8:11], v[84:87], v[132:135]\n"
"s_waitcnt lgkmcnt(0)\n"
"v_cmp_gt_u32 s[56:57], 30, v243\n"
"v_mfma_f32_16x16x32_bf16 v[132:135], a[12:15], v[88:91], v[132:135]\n"
"s_sub_u32 s84, s84, s83\n"
"v_mfma_f32_16x16x32_bf16 v[132:135], a[16:19], v[92:95], v[132:135]\n"
"s_or_b64 s[56:57], s[56:57], vcc\n"
"s_cbranch_scc1 LB30\n"
"LB30R:\n"
"v_mfma_f32_16x16x32_bf16 v[132:135], a[20:23], v[108:111], v[132:135]\n"
"s_cmp_lt_i32 s84, s88\n"
"s_cselect_b64 vcc, -1, 0\n"
"v_mfma_f32_16x16x32_bf16 v[132:135], v[140:143], v[112:115], v[132:135]\n"
"ds_write_b64 v245, v[214:215] offset:8\n"
"v_mfma_f32_16x16x32_bf16 v[132:135], v[156:159], v[116:119], v[132:135]\n"
"v_mfma_f32_16x16x32_bf16 v[136:139], a[44:47], v[116:119], v[20:23]\n"
"ds_read_b128 v[116:119], v235 offset:40960\n"
"v_mfma_f32_16x16x32_bf16 v[136:139], a[40:43], v[112:115], v[136:139]\n"
"ds_read_b128 v[112:115], v234 offset:40960\n"
"v_mfma_f32_16x16x32_bf16 v[136:139], a[36:39], v[108:111], v[136:139]\n"
"ds_read_b128 v[108:111], v233 offset:40960\n"
"v_mfma_f32_16x16x32_bf16 v[136:139], a[32:35], v[92:95], v[136:139]\n"
"ds_read_b128 v[92:95], v232 offset:40960\n"
"v_cvt_pk_bf16_f32 v208, v132, v133\n"
"v_mfma_f32_16x16x32_bf16 v[136:139], a[28:31], v[88:91], v[136:139]\n"
"ds_read_b128 v[88:91], v235 offset:32768\n"
"v_cvt_pk_bf16_f32 v209, v134, v135\n"
"v_mfma_f32_16x16x32_bf16 v[136:139], a[24:27], v[84:87], v[136:139]\n"
"ds_read_b128 v[84:87], v234 offset:32768\n"
"v_mfma_f32_16x16x32_bf16 v[136:139], v[204:207], v[68:71], v[136:139]\n"
"ds_read_b128 v[68:71], v233 offset:32768\n"
"v_mfma_f32_16x16x32_bf16 v[136:139], v[188:191], v[64:67], v[136:139]\n"
"ds_read_b128 v[64:67], v232 offset:32768\n"
"v_mfma_f32_16x16x32_bf16 v[136:139], v[184:187], v[60:63], v[136:139]\n"
"ds_read_b128 v[60:63], v235 offset:24576\n"
"v_mfma_f32_16x16x32_bf16 v[136:139], v[180:183], v[44:47], v[136:139]\n"
"ds_read_b128 v[44:47], v234 offset:24576\n"
"v_mfma_f32_16x16x32_bf16 v[136:139], v[164:167], v[40:43], v[136:139]\n"
"ds_read_b128 v[40:43], v233 offset:24576\n"
"v_mfma_f32_16x16x32_bf16 v[136:139], v[160:163], v[36:39], v[136:139]\n"
"ds_read_b128 v[36:39], v232 offset:24576\n"
"s_waitcnt lgkmcnt(8)\n"
"v_mfma_f32_16x16x32_bf16 v[132:135], a[76:79], v[116:119], v[0:3]\n"
"ds_write_b64 v244, v[208:209] offset:128\n"
"v_mfma_f32_16x16x32_bf16 v[132:135], a[72:75], v[112:115], v[132:135]\n"
"s_add_u32 m0, s31, 0x7f00\n"
"v_mfma_f32_16x16x32_bf16 v[132:135], a[68:71], v[108:111], v[132:135]\n"
"global_load_lds_dwordx4 v237, s[52:53] offset:256 nt\n"
"v_mfma_f32_16x16x32_bf16 v[132:135], a[64:67], v[92:95], v[132:135]\n"
"v_cvt_pk_bf16_f32 v210, v136, v137\n"
"s_waitcnt lgkmcnt(5)\n"
"v_mfma_f32_16x16x32_bf16 v[132:135], a[60:63], v[88:91], v[132:135]\n"
"v_cvt_pk_bf16_f32 v211, v138, v139\n"
"v_mfma_f32_16x16x32_bf16 v[132:135], a[56:59], v[84:87], v[132:135]\n"
"s_add_u32 s54, s26, s46\n"
"s_addc_u32 s55, s27, 0\n"
"v_mfma_f32_16x16x32_bf16 v[132:135], a[52:55], v[68:71], v[132:135]\n"
"ds_read_b128 v[216:219], v246 offset:24576\n"
"v_mfma_f32_16x16x32_bf16 v[132:135], a[48:51], v[64:67], v[132:135]\n"
"ds_read_b128 v[220:223], v246 offset:25600\n"
"s_waitcnt lgkmcnt(3)\n"
"v_mfma_f32_16x16x32_bf16 v[132:135], a[252:255], v[60:63], v[132:135]\n"
"ds_read_b128 v[224:227], v246 offset:26624\n"
"v_mfma_f32_16x16x32_bf16 v[132:135], a[248:251], v[44:47], v[132:135]\n"
"ds_write_b64 v244, v[210:211] offset:136\n"
"v_mfma_f32_16x16x32_bf16 v[132:135], a[244:247], v[40:43], v[132:135]\n"
"s_sub_i32 s84, s84, s19\n"
"v_mfma_f32_16x16x32_bf16 v[132:135], a[240:243], v[36:39], v[132:135]\n"
"s_max_i32 s88, s88, s84\n"
"v_mfma_f32_16x16x32_bf16 v[136:139], a[80:83], v[36:39], v[4:7]\n"
"v_mfma_f32_16x16x32_bf16 v[136:139], a[84:87], v[40:43], v[136:139]\n"
"v_mfma_f32_16x16x32_bf16 v[136:139], a[88:91], v[44:47], v[136:139]\n"
"v_mfma_f32_16x16x32_bf16 v[136:139], a[92:95], v[60:63], v[136:139]\n"
"v_cvt_pk_bf16_f32 v208, v132, v133\n"
"v_mfma_f32_16x16x32_bf16 v[136:139], a[96:99], v[64:67], v[136:139]\n"
"v_cvt_pk_bf16_f32 v209, v134, v135\n"
"v_mfma_f32_16x16x32_bf16 v[136:139], a[100:103], v[68:71], v[136:139]\n"
"v_mfma_f32_16x16x32_bf16 v[136:139], a[104:107], v[84:87], v[136:139]\n"
"v_mfma_f32_16x16x32_bf16 v[136:139], a[108:111], v[88:91], v[136:139]\n"
"s_waitcnt lgkmcnt(1)\n"
"v_mfma_f32_16x16x32_bf16 v[136:139], a[112:115], v[92:95], v[136:139]\n"
"v_mfma_f32_16x16x32_bf16 v[136:139], a[116:119], v[108:111], v[136:139]\n"
"global_store_dwordx4 v247, v[216:219], s[54:55] offset:0 sc1\n"
"ds_write_b64 v244, v[208:209] offset:12288\n"
"v_mfma_f32_16x16x32_bf16 v[136:139], a[120:123], v[112:115], v[136:139]\n"
"v_mfma_f32_16x16x32_bf16 v[136:139], a[124:127], v[116:119], v[136:139]\n"
"s_add_u32 m0, s31, 0x9e00\n"
"v_mfma_f32_16x16x32_bf16 v[132:135], a[172:175], v[116:119], v[8:11]\n"
"global_load_lds_dwordx4 v237, s[52:53] offset:512 nt\n"
"v_mfma_f32_16x16x32_bf16 v[132:135], a[168:171], v[112:115], v[132:135]\n"
"v_mfma_f32_16x16x32_bf16 v[132:135], a[164:167], v[108:111], v[132:135]\n"
"v_mfma_f32_16x16x32_bf16 v[132:135], a[160:163], v[92:95], v[132:135]\n"
"v_cvt_pk_bf16_f32 v210, v136, v137\n"
"v_mfma_f32_16x16x32_bf16 v[132:135], a[156:159], v[88:91], v[132:135]\n"
"v_cvt_pk_bf16_f32 v211, v138, v139\n"
"v_mfma_f32_16x16x32_bf16 v[132:135], a[152:155], v[84:87], v[132:135]\n"
"v_mfma_f32_16x16x32_bf16 v[132:135], a[148:151], v[68:71], v[132:135]\n"
"v_mfma_f32_16x16x32_bf16 v[132:135], a[144:147], v[64:67], v[132:135]\n"
"v_mfma_f32_16x16x32_bf16 v[132:135], a[140:143], v[60:63], v[132:135]\n"
"v_mfma_f32_16x16x32_bf16 v[132:135], a[136:139], v[44:47], v[132:135]\n"
"global_store_dwordx4 v252, v[220:223], s[54:55] offset:0 sc1\n"
"ds_write_b64 v244, v[210:211] offset:12296\n"
"v_mfma_f32_16x16x32_bf16 v[132:135], a[132:135], v[40:43], v[132:135]\n"
"v_mfma_f32_16x16x32_bf16 v[132:135], a[128:131], v[36:39], v[132:135]\n"
"v_mfma_f32_16x16x32_bf16 v[136:139], a[176:179], v[36:39], v[12:15]\n"
"v_mfma_f32_16x16x32_bf16 v[136:139], a[180:183], v[40:43], v[136:139]\n"
"v_mfma_f32_16x16x32_bf16 v[136:139], a[184:187], v[44:47], v[136:139]\n"
"v_mfma_f32_16x16x32_bf16 v[136:139], a[188:191], v[60:63], v[136:139]\n"
"v_cvt_pk_bf16_f32 v212, v132, v133\n"
"v_mfma_f32_16x16x32_bf16 v[136:139], a[192:195], v[64:67], v[136:139]\n"
"v_mov_b32 v242, 31\n"
"v_mfma_f32_16x16x32_bf16 v[136:139], a[196:199], v[68:71], v[136:139]\n"
"v_cvt_pk_bf16_f32 v213, v134, v135\n"
"v_mfma_f32_16x16x32_bf16 v[136:139], a[200:203], v[84:87], v[136:139]\n"
"s_waitcnt lgkmcnt(2)\n"
"ds_write_b32 v240, v242\n"
"v_mfma_f32_16x16x32_bf16 v[136:139], a[204:207], v[88:91], v[136:139]\n"
"v_mfma_f32_16x16x32_bf16 v[136:139], a[208:211], v[92:95], v[136:139]\n"
"v_mfma_f32_16x16x32_bf16 v[136:139], a[212:215], v[108:111], v[136:139]\n"
"v_mfma_f32_16x16x32_bf16 v[136:139], a[216:219], v[112:115], v[136:139]\n"
"global_store_dwordx4 v253, v[224:227], s[54:55] offset:0 sc1\n"
"ds_write_b64 v245, v[212:213] offset:12288\n"
"v_mfma_f32_16x16x32_bf16 v[136:139], a[220:223], v[116:119], v[136:139]\n"
"v_mfma_f32_16x16x32_bf16 v[132:135], v[156:159], v[116:119], v[16:19]\n"
"s_add_u32 m0, s31, 0x7000\n"
"v_mfma_f32_16x16x32_bf16 v[132:135], v[140:143], v[112:115], v[132:135]\n"
"global_load_lds_dwordx4 v238, s[52:53] offset:0 nt\n"
"v_mfma_f32_16x16x32_bf16 v[132:135], a[20:23], v[108:111], v[132:135]\n"
"ds_read_b32 v243, v241\n"
"v_mfma_f32_16x16x32_bf16 v[132:135], a[16:19], v[92:95], v[132:135]\n"
"v_cvt_pk_bf16_f32 v214, v136, v137\n"
"v_mfma_f32_16x16x32_bf16 v[132:135], a[12:15], v[88:91], v[132:135]\n"
"v_cvt_pk_bf16_f32 v215, v138, v139\n"
"v_mfma_f32_16x16x32_bf16 v[132:135], a[8:11], v[84:87], v[132:135]\n"
"v_mfma_f32_16x16x32_bf16 v[132:135], a[4:7], v[68:71], v[132:135]\n"
"s_waitcnt lgkmcnt(0)\n"
"v_cmp_gt_u32 s[56:57], 31, v243\n"
"v_mfma_f32_16x16x32_bf16 v[132:135], a[0:3], v[64:67], v[132:135]\n"
"v_mfma_f32_16x16x32_bf16 v[132:135], a[236:239], v[60:63], v[132:135]\n"
"s_or_b64 s[56:57], s[56:57], vcc\n"
"s_cbranch_scc1 LB31\n"
"LB31R:\n"
"v_mfma_f32_16x16x32_bf16 v[132:135], a[232:235], v[44:47], v[132:135]\n"
"ds_write_b64 v245, v[214:215] offset:12296\n"
"v_mfma_f32_16x16x32_bf16 v[132:135], a[228:231], v[40:43], v[132:135]\n"
"v_mfma_f32_16x16x32_bf16 v[132:135], a[224:227], v[36:39], v[132:135]\n"
"s_mov_b64 exec, s[6:7]\n"
"s_cbranch_execz LHT13\n"
"LHR13:\n"
"s_mov_b64 exec, -1\n"
"v_mfma_f32_16x16x32_bf16 v[136:139], v[160:163], v[36:39], v[20:23]\n"
"ds_read_b128 v[36:39], v232 offset:24832\n"
"v_mfma_f32_16x16x32_bf16 v[136:139], v[164:167], v[40:43], v[136:139]\n"
"ds_read_b128 v[40:43], v233 offset:24832\n"
"v_mfma_f32_16x16x32_bf16 v[136:139], v[180:183], v[44:47], v[136:139]\n"
"ds_read_b128 v[44:47], v234 offset:24832\n"
"v_mfma_f32_16x16x32_bf16 v[136:139], v[184:187], v[60:63], v[136:139]\n"
"ds_read_b128 v[60:63], v235 offset:24832\n"
"v_cvt_pk_bf16_f32 v208, v132, v133\n"
"v_mfma_f32_16x16x32_bf16 v[136:139], v[188:191], v[64:67], v[136:139]\n"
"ds_read_b128 v[64:67], v232 offset:33024\n"
"v_cvt_pk_bf16_f32 v209, v134, v135\n"
"v_mfma_f32_16x16x32_bf16 v[136:139], v[204:207], v[68:71], v[136:139]\n"
"ds_read_b128 v[68:71], v233 offset:33024\n"
"v_mfma_f32_16x16x32_bf16 v[136:139], a[24:27], v[84:87], v[136:139]\n"
"ds_read_b128 v[84:87], v234 offset:33024\n"
"v_mfma_f32_16x16x32_bf16 v[136:139], a[28:31], v[88:91], v[136:139]\n"
"ds_read_b128 v[88:91], v235 offset:33024\n"
"v_mfma_f32_16x16x32_bf16 v[136:139], a[32:35], v[92:95], v[136:139]\n"
"ds_read_b128 v[92:95], v232 offset:41216\n"
"v_mfma_f32_16x16x32_bf16 v[136:139], a[36:39], v[108:111], v[136:139]\n"
"ds_read_b128 v[108:111], v233 offset:41216\n"
"ds_write_b64 v244, v[208:209] offset:12416\n"
"v_mfma_f32_16x16x32_bf16 v[136:139], a[40:43], v[112:115], v[136:139]\n"
"ds_read_b128 v[112:115], v234 offset:41216\n"
"v_mfma_f32_16x16x32_bf16 v[136:139], a[44:47], v[116:119], v[136:139]\n"
"ds_read_b128 v[116:119], v235 offset:41216\n"
"s_waitcnt lgkmcnt(9)\n"
"v_mfma_f32_16x16x32_bf16 v[132:135], a[240:243], v[36:39], v[0:3]\n"
"v_add_u32 v232, 0xffff4000, v232\n"
"v_mfma_f32_16x16x32_bf16 v[132:135], a[244:247], v[40:43], v[132:135]\n"
"v_add_u32 v233, 0xffff4000, v233\n"
"v_mfma_f32_16x16x32_bf16 v[132:135], a[248:251], v[44:47], v[132:135]\n"
"v_add_u32 v234, 0xffff4000, v234\n"
"v_mfma_f32_16x16x32_bf16 v[132:135], a[252:255], v[60:63], v[132:135]\n"
"v_add_u32 v235, 0xffff4000, v235\n"
"s_waitcnt lgkmcnt(5)\n"
"v_mfma_f32_16x16x32_bf16 v[132:135], a[48:51], v[64:67], v[132:135]\n"
"v_cvt_pk_bf16_f32 v210, v136, v137\n"
"v_mfma_f32_16x16x32_bf16 v[132:135], a[52:55], v[68:71], v[132:135]\n"
"v_cvt_pk_bf16_f32 v211, v138, v139\n"
"v_mfma_f32_16x16x32_bf16 v[132:135], a[56:59], v[84:87], v[132:135]\n"
"ds_read_b128 v[216:219], v246 offset:0\n"
"v_mfma_f32_16x16x32_bf16 v[132:135], a[60:63], v[88:91], v[132:135]\n"
"ds_read_b128 v[220:223], v246 offset:1024\n"
"s_waitcnt lgkmcnt(2)\n"
"v_mfma_f32_16x16x32_bf16 v[132:135], a[64:67], v[92:95], v[132:135]\n"
"ds_read_b128 v[224:227], v246 offset:2048\n"
"v_mfma_f32_16x16x32_bf16 v[132:135], a[68:71], v[108:111], v[132:135]\n"
"s_add_u32 m0, s31, 0x8f00\n"
"v_mfma_f32_16x16x32_bf16 v[132:135], a[72:75], v[112:115], v[132:135]\n"
"ds_write_b64 v244, v[210:211] offset:12424\n"
"v_mfma_f32_16x16x32_bf16 v[132:135], a[76:79], v[116:119], v[132:135]\n"
"global_load_lds_dwordx4 v238, s[52:53] offset:256 nt\n"
"v_mfma_f32_16x16x32_bf16 v[136:139], a[124:127], v[116:119], v[4:7]\n"
"v_lshlrev_b32 v0, 16, v192\n"
"v_mfma_f32_16x16x32_bf16 v[136:139], a[120:123], v[112:115], v[136:139]\n"
"v_and_b32 v1, 0xffff0000, v192\n"
"v_mfma_f32_16x16x32_bf16 v[136:139], a[116:119], v[108:111], v[136:139]\n"
"v_lshlrev_b32 v2, 16, v193\n"
"v_mfma_f32_16x16x32_bf16 v[136:139], a[112:115], v[92:95], v[136:139]\n"
"v_cvt_pk_bf16_f32 v208, v132, v133\n"
"v_mfma_f32_16x16x32_bf16 v[136:139], a[108:111], v[88:91], v[136:139]\n"
"v_cvt_pk_bf16_f32 v209, v134, v135\n"
"v_mfma_f32_16x16x32_bf16 v[136:139], a[104:107], v[84:87], v[136:139]\n"
"v_and_b32 v3, 0xffff0000, v193\n"
"v_mfma_f32_16x16x32_bf16 v[136:139], a[100:103], v[68:71], v[136:139]\n"
"v_lshlrev_b32 v4, 16, v194\n"
"v_mfma_f32_16x16x32_bf16 v[136:139], a[96:99], v[64:67], v[136:139]\n"
"s_waitcnt lgkmcnt(1)\n"
"v_mfma_f32_16x16x32_bf16 v[136:139], a[92:95], v[60:63], v[136:139]\n"
"v_and_b32 v5, 0xffff0000, v194\n"
"v_mfma_f32_16x16x32_bf16 v[136:139], a[88:91], v[44:47], v[136:139]\n"
"global_store_dwordx4 v247, v[216:219], s[54:55] offset:768 sc1\n"
"ds_write_b64 v244, v[208:209] offset:24576\n"
"v_mfma_f32_16x16x32_bf16 v[136:139], a[84:87], v[40:43], v[136:139]\n"
"v_lshlrev_b32 v6, 16, v195\n"
"v_mfma_f32_16x16x32_bf16 v[136:139], a[80:83], v[36:39], v[136:139]\n"
"v_and_b32 v7, 0xffff0000, v195\n"
"v_mfma_f32_16x16x32_bf16 v[132:135], a[128:131], v[36:39], v[8:11]\n"
"s_add_u32 s83, s83, s86\n"
"v_mfma_f32_16x16x32_bf16 v[132:135], a[132:135], v[40:43], v[132:135]\n"
"s_sub_i32 s88, s88, 6\n"
"v_mfma_f32_16x16x32_bf16 v[132:135], a[136:139], v[44:47], v[132:135]\n"
"s_add_u32 m0, s31, 0xae00\n"
"v_mfma_f32_16x16x32_bf16 v[132:135], a[140:143], v[60:63], v[132:135]\n"
"v_cvt_pk_bf16_f32 v210, v136, v137\n"
"v_mfma_f32_16x16x32_bf16 v[132:135], a[144:147], v[64:67], v[132:135]\n"
"v_cvt_pk_bf16_f32 v211, v138, v139\n"
"v_mfma_f32_16x16x32_bf16 v[132:135], a[148:151], v[68:71], v[132:135]\n"
"global_load_lds_dwordx4 v238, s[52:53] offset:512 nt\n"
"v_mfma_f32_16x16x32_bf16 v[132:135], a[152:155], v[84:87], v[132:135]\n"
"v_lshlrev_b32 v8, 16, v196\n"
"v_mfma_f32_16x16x32_bf16 v[132:135], a[156:159], v[88:91], v[132:135]\n"
"v_and_b32 v9, 0xffff0000, v196\n"
"v_mfma_f32_16x16x32_bf16 v[132:135], a[160:163], v[92:95], v[132:135]\n"
"v_lshlrev_b32 v10, 16, v197\n"
"v_mfma_f32_16x16x32_bf16 v[132:135], a[164:167], v[108:111], v[132:135]\n"
"s_memrealtime s[84:85]\n"
"v_mfma_f32_16x16x32_bf16 v[132:135], a[168:171], v[112:115], v[132:135]\n"
"global_store_dwordx4 v252, v[220:223], s[54:55] offset:768 sc1\n"
"ds_write_b64 v244, v[210:211] offset:24584\n"
"v_mfma_f32_16x16x32_bf16 v[132:135], a[172:175], v[116:119], v[132:135]\n"
"v_and_b32 v11, 0xffff0000, v197\n"
"v_mfma_f32_16x16x32_bf16 v[136:139], a[220:223], v[116:119], v[12:15]\n"
"s_max_i32 s88, s88, 0\n"
"v_mfma_f32_16x16x32_bf16 v[136:139], a[216:219], v[112:115], v[136:139]\n"
"v_mfma_f32_16x16x32_bf16 v[136:139], a[212:215], v[108:111], v[136:139]\n"
"v_mfma_f32_16x16x32_bf16 v[136:139], a[208:211], v[92:95], v[136:139]\n"
"v_cvt_pk_bf16_f32 v212, v132, v133\n"
"v_mfma_f32_16x16x32_bf16 v[136:139], a[204:207], v[88:91], v[136:139]\n"
"v_mov_b32 v242, 32\n"
"v_mfma_f32_16x16x32_bf16 v[136:139], a[200:203], v[84:87], v[136:139]\n"
"v_cvt_pk_bf16_f32 v213, v134, v135\n"
"v_mfma_f32_16x16x32_bf16 v[136:139], a[196:199], v[68:71], v[136:139]\n"
"s_waitcnt vmcnt(13) lgkmcnt(2)\n"
"ds_write_b32 v240, v242\n"
"v_mfma_f32_16x16x32_bf16 v[136:139], a[192:195], v[64:67], v[136:139]\n"
"v_lshlrev_b32 v12, 16, v198\n"
"v_mfma_f32_16x16x32_bf16 v[136:139], a[188:191], v[60:63], v[136:139]\n"
"v_and_b32 v13, 0xffff0000, v198\n"
"v_mfma_f32_16x16x32_bf16 v[136:139], a[184:187], v[44:47], v[136:139]\n"
"v_lshlrev_b32 v14, 16, v199\n"
"v_mfma_f32_16x16x32_bf16 v[136:139], a[180:183], v[40:43], v[136:139]\n"
"global_store_dwordx4 v253, v[224:227], s[54:55] offset:768 sc1\n"
"ds_write_b64 v245, v[212:213] offset:24576\n"
"v_mfma_f32_16x16x32_bf16 v[136:139], a[176:179], v[36:39], v[136:139]\n"
"v_and_b32 v15, 0xffff0000, v199\n"
"v_mfma_f32_16x16x32_bf16 v[132:135], a[224:227], v[36:39], v[16:19]\n"
"v_mfma_f32_16x16x32_bf16 v[132:135], a[228:231], v[40:43], v[132:135]\n"
"LHT13:\n"
"s_cbranch_execz LHR13\n"
"v_mfma_f32_16x16x32_bf16 v[132:135], a[232:235], v[44:47], v[132:135]\n"
"ds_read_b32 v243, v241\n"
"v_mfma_f32_16x16x32_bf16 v[132:135], a[236:239], v[60:63], v[132:135]\n"
"v_cvt_pk_bf16_f32 v214, v136, v137\n"
"v_mfma_f32_16x16x32_bf16 v[132:135], a[0:3], v[64:67], v[132:135]\n"
"v_cvt_pk_bf16_f32 v215, v138, v139\n"
"v_mfma_f32_16x16x32_bf16 v[132:135], a[4:7], v[68:71], v[132:135]\n"
"v_lshlrev_b32 v16, 16, v200\n"
"v_mfma_f32_16x16x32_bf16 v[132:135], a[8:11], v[84:87], v[132:135]\n"
"s_waitcnt lgkmcnt(0)\n"
"v_cmp_gt_u32 s[56:57], 32, v243\n"
"v_mfma_f32_16x16x32_bf16 v[132:135], a[12:15], v[88:91], v[132:135]\n"
"s_sub_u32 s84, s84, s83\n"
"v_mfma_f32_16x16x32_bf16 v[132:135], a[16:19], v[92:95], v[132:135]\n"
"s_or_b64 s[56:57], s[56:57], vcc\n"
"s_cbranch_scc1 LB32\n"
"LB32R:\n"
"v_mfma_f32_16x16x32_bf16 v[132:135], a[20:23], v[108:111], v[132:135]\n"
"s_cmp_lt_i32 s84, s88\n"
"s_cselect_b64 vcc, -1, 0\n"
"v_mfma_f32_16x16x32_bf16 v[132:135], v[140:143], v[112:115], v[132:135]\n"
"ds_write_b64 v245, v[214:215] offset:24584\n"
"v_mfma_f32_16x16x32_bf16 v[132:135], v[156:159], v[116:119], v[132:135]\n"
"v_and_b32 v17, 0xffff0000, v200\n"
"v_mfma_f32_16x16x32_bf16 v[136:139], a[44:47], v[116:119], v[20:23]\n"
"ds_read_b128 v[116:119], v235 offset:16384\n"
"v_mfma_f32_16x16x32_bf16 v[136:139], a[40:43], v[112:115], v[136:139]\n"
"ds_read_b128 v[112:115], v234 offset:16384\n"
"v_mfma_f32_16x16x32_bf16 v[136:139], a[36:39], v[108:111], v[136:139]\n"
"ds_read_b128 v[108:111], v233 offset:16384\n"
"v_mfma_f32_16x16x32_bf16 v[136:139], a[32:35], v[92:95], v[136:139]\n"
"ds_read_b128 v[92:95], v232 offset:16384\n"
"v_cvt_pk_bf16_f32 v208, v132, v133\n"
"v_mfma_f32_16x16x32_bf16 v[136:139], a[28:31], v[88:91], v[136:139]\n"
"ds_read_b128 v[88:91], v235 offset:8192\n"
"v_cvt_pk_bf16_f32 v209, v134, v135\n"
"v_mfma_f32_16x16x32_bf16 v[136:139], a[24:27], v[84:87], v[136:139]\n"
"ds_read_b128 v[84:87], v234 offset:8192\n"
"v_mfma_f32_16x16x32_bf16 v[136:139], v[204:207], v[68:71], v[136:139]\n"
"ds_read_b128 v[68:71], v233 offset:8192\n"
"v_mfma_f32_16x16x32_bf16 v[136:139], v[188:191], v[64:67], v[136:139]\n"
"ds_read_b128 v[64:67], v232 offset:8192\n"
"v_mfma_f32_16x16x32_bf16 v[136:139], v[184:187], v[60:63], v[136:139]\n"
"ds_read_b128 v[60:63], v235 offset:0\n"
"v_mfma_f32_16x16x32_bf16 v[136:139], v[180:183], v[44:47], v[136:139]\n"
"ds_read_b128 v[44:47], v234 offset:0\n"
"v_mfma_f32_16x16x32_bf16 v[136:139], v[164:167], v[40:43], v[136:139]\n"
"ds_read_b128 v[40:43], v233 offset:0\n"
"v_mfma_f32_16x16x32_bf16 v[136:139], v[160:163], v[36:39], v[136:139]\n"
"ds_read_b128 v[36:39], v232 offset:0\n"
"s_waitcnt lgkmcnt(8)\n"
"v_mfma_f32_16x16x32_bf16 v[132:135], a[76:79], v[116:119], v[0:3]\n"
"ds_write_b64 v244, v[208:209] offset:24704\n"
"v_mfma_f32_16x16x32_bf16 v[132:135], a[72:75], v[112:115], v[132:135]\n"
"v_lshlrev_b32 v18, 16, v201\n"
"v_mfma_f32_16x16x32_bf16 v[132:135], a[68:71], v[108:111], v[132:135]\n"
"v_and_b32 v19, 0xffff0000, v201\n"
"v_mfma_f32_16x16x32_bf16 v[132:135], a[64:67], v[92:95], v[132:135]\n"
"v_lshlrev_b32 v20, 16, v202\n"
"s_waitcnt lgkmcnt(5)\n"
"v_mfma_f32_16x16x32_bf16 v[132:135], a[60:63], v[88:91], v[132:135]\n"
"v_and_b32 v21, 0xffff0000, v202\n"
"v_mfma_f32_16x16x32_bf16 v[132:135], a[56:59], v[84:87], v[132:135]\n"
"v_lshlrev_b32 v22, 16, v203\n"
"v_mfma_f32_16x16x32_bf16 v[132:135], a[52:55], v[68:71], v[132:135]\n"
"v_and_b32 v23, 0xffff0000, v203\n"
"v_mfma_f32_16x16x32_bf16 v[132:135], a[48:51], v[64:67], v[132:135]\n"
"v_cvt_pk_bf16_f32 v210, v136, v137\n"
"s_waitcnt lgkmcnt(1)\n"
"v_mfma_f32_16x16x32_bf16 v[132:135], a[252:255], v[60:63], v[132:135]\n"
"v_cvt_pk_bf16_f32 v211, v138, v139\n"
"v_mfma_f32_16x16x32_bf16 v[132:135], a[248:251], v[44:47], v[132:135]\n"
"s_add_u32 s54, s26, s47\n"
"s_addc_u32 s55, s27, 0\n"
"v_mfma_f32_16x16x32_bf16 v[132:135], a[244:247], v[40:43], v[132:135]\n"
"ds_read_b128 v[216:219], v246 offset:12288\n"
"v_mfma_f32_16x16x32_bf16 v[132:135], a[240:243], v[36:39], v[132:135]\n"
"ds_read_b128 v[220:223], v246 offset:13312\n"
"v_mfma_f32_16x16x32_bf16 v[136:139], a[80:83], v[36:39], v[4:7]\n"
"ds_read_b128 v[224:227], v246 offset:14336\n"
"v_mfma_f32_16x16x32_bf16 v[136:139], a[84:87], v[40:43], v[136:139]\n"
"ds_write_b64 v244, v[210:211] offset:24712\n"
"v_mfma_f32_16x16x32_bf16 v[136:139], a[88:91], v[44:47], v[136:139]\n"
"s_sub_i32 s84, s84, s19\n"
"v_mfma_f32_16x16x32_bf16 v[136:139], a[92:95], v[60:63], v[136:139]\n"
"v_cvt_pk_bf16_f32 v208, v132, v133\n"
"v_mfma_f32_16x16x32_bf16 v[136:139], a[96:99], v[64:67], v[136:139]\n"
"v_cvt_pk_bf16_f32 v209, v134, v135\n"
"v_mfma_f32_16x16x32_bf16 v[136:139], a[100:103], v[68:71], v[136:139]\n"
"s_max_i32 s88, s88, s84\n"
"v_mfma_f32_16x16x32_bf16 v[136:139], a[104:107], v[84:87], v[136:139]\n"
"v_mfma_f32_16x16x32_bf16 v[136:139], a[108:111], v[88:91], v[136:139]\n"
"s_waitcnt lgkmcnt(1)\n"
"v_mfma_f32_16x16x32_bf16 v[136:139], a[112:115], v[92:95], v[136:139]\n"
"v_mfma_f32_16x16x32_bf16 v[136:139], a[116:119], v[108:111], v[136:139]\n"
"global_store_dwordx4 v247, v[216:219], s[54:55] offset:0 sc1\n"
"ds_write_b64 v244, v[208:209] offset:0\n"
"v_mfma_f32_16x16x32_bf16 v[136:139], a[120:123], v[112:115], v[136:139]\n"
"v_mfma_f32_16x16x32_bf16 v[136:139], a[124:127], v[116:119], v[136:139]\n"
"v_mfma_f32_16x16x32_bf16 v[132:135], a[172:175], v[116:119], v[8:11]\n"
"v_mfma_f32_16x16x32_bf16 v[132:135], a[168:171], v[112:115], v[132:135]\n"
"v_mfma_f32_16x16x32_bf16 v[132:135], a[164:167], v[108:111], v[132:135]\n"
"v_mfma_f32_16x16x32_bf16 v[132:135], a[160:163], v[92:95], v[132:135]\n"
"v_cvt_pk_bf16_f32 v210, v136, v137\n"
"v_mfma_f32_16x16x32_bf16 v[132:135], a[156:159], v[88:91], v[132:135]\n"
"v_cvt_pk_bf16_f32 v211, v138, v139\n"
"v_mfma_f32_16x16x32_bf16 v[132:135], a[152:155], v[84:87], v[132:135]\n"
"v_mfma_f32_16x16x32_bf16 v[132:135], a[148:151], v[68:71], v[132:135]\n"
"v_mfma_f32_16x16x32_bf16 v[132:135], a[144:147], v[64:67], v[132:135]\n"
"v_mfma_f32_16x16x32_bf16 v[132:135], a[140:143], v[60:63], v[132:135]\n"
"v_mfma_f32_16x16x32_bf16 v[132:135], a[136:139], v[44:47], v[132:135]\n"
"global_store_dwordx4 v252, v[220:223], s[54:55] offset:0 sc1\n"
"ds_write_b64 v244, v[210:211] offset:8\n"
"v_mfma_f32_16x16x32_bf16 v[132:135], a[132:135], v[40:43], v[132:135]\n"
"v_mfma_f32_16x16x32_bf16 v[132:135], a[128:131], v[36:39], v[132:135]\n"
"v_mfma_f32_16x16x32_bf16 v[136:139], a[176:179], v[36:39], v[12:15]\n"
"v_mfma_f32_16x16x32_bf16 v[136:139], a[180:183], v[40:43], v[136:139]\n"
"v_mfma_f32_16x16x32_bf16 v[136:139], a[184:187], v[44:47], v[136:139]\n"
"v_mfma_f32_16x16x32_bf16 v[136:139], a[188:191], v[60:63], v[136:139]\n"
"v_cvt_pk_bf16_f32 v212, v132, v133\n"
"v_mfma_f32_16x16x32_bf16 v[136:139], a[192:195], v[64:67], v[136:139]\n"
"v_mov_b32 v242, 33\n"
"v_mfma_f32_16x16x32_bf16 v[136:139], a[196:199], v[68:71], v[136:139]\n"
"v_cvt_pk_bf16_f32 v213, v134, v135\n"
"v_mfma_f32_16x16x32_bf16 v[136:139], a[200:203], v[84:87], v[136:139]\n"
"s_waitcnt lgkmcnt(2)\n"
"ds_write_b32 v240, v242\n"
"v_mfma_f32_16x16x32_bf16 v[136:139], a[204:207], v[88:91], v[136:139]\n"
"v_mfma_f32_16x16x32_bf16 v[136:139], a[208:211], v[92:95], v[136:139]\n"
"v_mfma_f32_16x16x32_bf16 v[136:139], a[212:215], v[108:111], v[136:139]\n"
"v_mfma_f32_16x16x32_bf16 v[136:139], a[216:219], v[112:115], v[136:139]\n"
"global_store_dwordx4 v253, v[224:227], s[54:55] offset:0 sc1\n"
"ds_write_b64 v245, v[212:213] offset:0\n"
"v_mfma_f32_16x16x32_bf16 v[136:139], a[220:223], v[116:119], v[136:139]\n"
"v_mfma_f32_16x16x32_bf16 v[132:135], v[156:159], v[116:119], v[16:19]\n"
"v_mfma_f32_16x16x32_bf16 v[132:135], v[140:143], v[112:115], v[132:135]\n"
"v_mfma_f32_16x16x32_bf16 v[132:135], a[20:23], v[108:111], v[132:135]\n"
"ds_read_b32 v243, v241\n"
"v_mfma_f32_16x16x32_bf16 v[132:135], a[16:19], v[92:95], v[132:135]\n"
"v_cvt_pk_bf16_f32 v214, v136, v137\n"
"v_mfma_f32_16x16x32_bf16 v[132:135], a[12:15], v[88:91], v[132:135]\n"
"v_cvt_pk_bf16_f32 v215, v138, v139\n"
"v_mfma_f32_16x16x32_bf16 v[132:135], a[8:11], v[84:87], v[132:135]\n"
"v_mfma_f32_16x16x32_bf16 v[132:135], a[4:7], v[68:71], v[132:135]\n"
"s_waitcnt lgkmcnt(0)\n"
"v_cmp_gt_u32 s[56:57], 33, v243\n"
"v_mfma_f32_16x16x32_bf16 v[132:135], a[0:3], v[64:67], v[132:135]\n"
"v_mfma_f32_16x16x32_bf16 v[132:135], a[236:239], v[60:63], v[132:135]\n"
"s_or_b64 s[56:57], s[56:57], vcc\n"
"s_cbranch_scc1 LB33\n"
"LB33R:\n"
"v_mfma_f32_16x16x32_bf16 v[132:135], a[232:235], v[44:47], v[132:135]\n"
"ds_write_b64 v245, v[214:215] offset:8\n"
"v_mfma_f32_16x16x32_bf16 v[132:135], a[228:231], v[40:43], v[132:135]\n"
"v_mfma_f32_16x16x32_bf16 v[132:135], a[224:227], v[36:39], v[132:135]\n"
"v_mfma_f32_16x16x32_bf16 v[136:139], v[160:163], v[36:39], v[20:23]\n"
"ds_read_b128 v[36:39], v232 offset:256\n"
"v_mfma_f32_16x16x32_bf16 v[136:139], v[164:167], v[40:43], v[136:139]\n"
"ds_read_b128 v[40:43], v233 offset:256\n"
"v_mfma_f32_16x16x32_bf16 v[136:139], v[180:183], v[44:47], v[136:139]\n"
"ds_read_b128 v[44:47], v234 offset:256\n"
"v_mfma_f32_16x16x32_bf16 v[136:139], v[184:187], v[60:63], v[136:139]\n"
"ds_read_b128 v[60:63], v235 offset:256\n"
"v_cvt_pk_bf16_f32 v208, v132, v133\n"
"v_mfma_f32_16x16x32_bf16 v[136:139], v[188:191], v[64:67], v[136:139]\n"
"ds_read_b128 v[64:67], v232 offset:8448\n"
"v_cvt_pk_bf16_f32 v209, v134, v135\n"
"v_mfma_f32_16x16x32_bf16 v[136:139], v[204:207], v[68:71], v[136:139]\n"
"ds_read_b128 v[68:71], v233 offset:8448\n"
"v_mfma_f32_16x16x32_bf16 v[136:139], a[24:27], v[84:87], v[136:139]\n"
"ds_read_b128 v[84:87], v234 offset:8448\n"
"v_mfma_f32_16x16x32_bf16 v[136:139], a[28:31], v[88:91], v[136:139]\n"
"ds_read_b128 v[88:91], v235 offset:8448\n"
"v_mfma_f32_16x16x32_bf16 v[136:139], a[32:35], v[92:95], v[136:139]\n"
"ds_read_b128 v[92:95], v232 offset:16640\n"
"v_mfma_f32_16x16x32_bf16 v[136:139], a[36:39], v[108:111], v[136:139]\n"
"ds_read_b128 v[108:111], v233 offset:16640\n"
"v_mfma_f32_16x16x32_bf16 v[136:139], a[40:43], v[112:115], v[136:139]\n"
"ds_read_b128 v[112:115], v234 offset:16640\n"
"v_mfma_f32_16x16x32_bf16 v[136:139], a[44:47], v[116:119], v[136:139]\n"
"ds_read_b128 v[116:119], v235 offset:16640\n"
"s_waitcnt lgkmcnt(8)\n"
"v_mfma_f32_16x16x32_bf16 v[132:135], a[240:243], v[36:39], v[0:3]\n"
"ds_write_b64 v244, v[208:209] offset:128\n"
"v_mfma_f32_16x16x32_bf16 v[132:135], a[244:247], v[40:43], v[132:135]\n"
"v_mfma_f32_16x16x32_bf16 v[132:135], a[248:251], v[44:47], v[132:135]\n"
"v_mfma_f32_16x16x32_bf16 v[132:135], a[252:255], v[60:63], v[132:135]\n"
"v_cvt_pk_bf16_f32 v210, v136, v137\n"
"s_waitcnt lgkmcnt(5)\n"
"v_mfma_f32_16x16x32_bf16 v[132:135], a[48:51], v[64:67], v[132:135]\n"
"v_cvt_pk_bf16_f32 v211, v138, v139\n"
"v_mfma_f32_16x16x32_bf16 v[132:135], a[52:55], v[68:71], v[132:135]\n"
"v_mfma_f32_16x16x32_bf16 v[132:135], a[56:59], v[84:87], v[132:135]\n"
"ds_read_b128 v[216:219], v246 offset:24576\n"
"v_mfma_f32_16x16x32_bf16 v[132:135], a[60:63], v[88:91], v[132:135]\n"
"ds_read_b128 v[220:223], v246 offset:25600\n"
"s_waitcnt lgkmcnt(3)\n"
"v_mfma_f32_16x16x32_bf16 v[132:135], a[64:67], v[92:95], v[132:135]\n"
"ds_read_b128 v[224:227], v246 offset:26624\n"
"v_mfma_f32_16x16x32_bf16 v[132:135], a[68:71], v[108:111], v[132:135]\n"
"ds_write_b64 v244, v[210:211] offset:136\n"
"v_mfma_f32_16x16x32_bf16 v[132:135], a[72:75], v[112:115], v[132:135]\n"
"v_mfma_f32_16x16x32_bf16 v[132:135], a[76:79], v[116:119], v[132:135]\n"
"v_mfma_f32_16x16x32_bf16 v[136:139], a[124:127], v[116:119], v[4:7]\n"
"v_mfma_f32_16x16x32_bf16 v[136:139], a[120:123], v[112:115], v[136:139]\n"
"v_mfma_f32_16x16x32_bf16 v[136:139], a[116:119], v[108:111], v[136:139]\n"
"v_mfma_f32_16x16x32_bf16 v[136:139], a[112:115], v[92:95], v[136:139]\n"
"v_cvt_pk_bf16_f32 v208, v132, v133\n"
"v_mfma_f32_16x16x32_bf16 v[136:139], a[108:111], v[88:91], v[136:139]\n"
"v_cvt_pk_bf16_f32 v209, v134, v135\n"
"v_mfma_f32_16x16x32_bf16 v[136:139], a[104:107], v[84:87], v[136:139]\n"
"v_mfma_f32_16x16x32_bf16 v[136:139], a[100:103], v[68:71], v[136:139]\n"
"v_mfma_f32_16x16x32_bf16 v[136:139], a[96:99], v[64:67], v[136:139]\n"
"s_waitcnt lgkmcnt(1)\n"
"v_mfma_f32_16x16x32_bf16 v[136:139], a[92:95], v[60:63], v[136:139]\n"
"v_mfma_f32_16x16x32_bf16 v[136:139], a[88:91], v[44:47], v[136:139]\n"
"global_store_dwordx4 v247, v[216:219], s[54:55] offset:768 sc1\n"
"ds_write_b64 v244, v[208:209] offset:12288\n"
"v_mfma_f32_16x16x32_bf16 v[136:139], a[84:87], v[40:43], v[136:139]\n"
"v_mfma_f32_16x16x32_bf16 v[136:139], a[80:83], v[36:39], v[136:139]\n"
"v_mfma_f32_16x16x32_bf16 v[132:135], a[128:131], v[36:39], v[8:11]\n"
"v_mfma_f32_16x16x32_bf16 v[132:135], a[132:135], v[40:43], v[132:135]\n"
"v_mfma_f32_16x16x32_bf16 v[132:135], a[136:139], v[44:47], v[132:135]\n"
"v_mfma_f32_16x16x32_bf16 v[132:135], a[140:143], v[60:63], v[132:135]\n"
"v_cvt_pk_bf16_f32 v210, v136, v137\n"
"v_mfma_f32_16x16x32_bf16 v[132:135], a[144:147], v[64:67], v[132:135]\n"
"v_cvt_pk_bf16_f32 v211, v138, v139\n"
"v_mfma_f32_16x16x32_bf16 v[132:135], a[148:151], v[68:71], v[132:135]\n"
"v_mfma_f32_16x16x32_bf16 v[132:135], a[152:155], v[84:87], v[132:135]\n"
"v_mfma_f32_16x16x32_bf16 v[132:135], a[156:159], v[88:91], v[132:135]\n"
"v_mfma_f32_16x16x32_bf16 v[132:135], a[160:163], v[92:95], v[132:135]\n"
"v_mfma_f32_16x16x32_bf16 v[132:135], a[164:167], v[108:111], v[132:135]\n"
"global_store_dwordx4 v252, v[220:223], s[54:55] offset:768 sc1\n"
"ds_write_b64 v244, v[210:211] offset:12296\n"
"v_mfma_f32_16x16x32_bf16 v[132:135], a[168:171], v[112:115], v[132:135]\n"
"v_mfma_f32_16x16x32_bf16 v[132:135], a[172:175], v[116:119], v[132:135]\n"
"v_mfma_f32_16x16x32_bf16 v[136:139], a[220:223], v[116:119], v[12:15]\n"
"v_mfma_f32_16x16x32_bf16 v[136:139], a[216:219], v[112:115], v[136:139]\n"
"v_mfma_f32_16x16x32_bf16 v[136:139], a[212:215], v[108:111], v[136:139]\n"
"v_mfma_f32_16x16x32_bf16 v[136:139], a[208:211], v[92:95], v[136:139]\n"
"v_cvt_pk_bf16_f32 v212, v132, v133\n"
"v_mfma_f32_16x16x32_bf16 v[136:139], a[204:207], v[88:91], v[136:139]\n"
"v_mov_b32 v242, 34\n"
"v_mfma_f32_16x16x32_bf16 v[136:139], a[200:203], v[84:87], v[136:139]\n"
"v_cvt_pk_bf16_f32 v213, v134, v135\n"
"v_mfma_f32_16x16x32_bf16 v[136:139], a[196:199], v[68:71], v[136:139]\n"
"s_waitcnt vmcnt(7) lgkmcnt(2)\n"
"ds_write_b32 v240, v242\n"
"v_mfma_f32_16x16x32_bf16 v[136:139], a[192:195], v[64:67], v[136:139]\n"
"v_mfma_f32_16x16x32_bf16 v[136:139], a[188:191], v[60:63], v[136:139]\n"
"v_mfma_f32_16x16x32_bf16 v[136:139], a[184:187], v[44:47], v[136:139]\n"
"v_mfma_f32_16x16x32_bf16 v[136:139], a[180:183], v[40:43], v[136:139]\n"
"global_store_dwordx4 v253, v[224:227], s[54:55] offset:768 sc1\n"
"ds_write_b64 v245, v[212:213] offset:12288\n"
"v_mfma_f32_16x16x32_bf16 v[136:139], a[176:179], v[36:39], v[136:139]\n"
"v_mfma_f32_16x16x32_bf16 v[132:135], a[224:227], v[36:39], v[16:19]\n"
"v_mfma_f32_16x16x32_bf16 v[132:135], a[228:231], v[40:43], v[132:135]\n"
"v_mfma_f32_16x16x32_bf16 v[132:135], a[232:235], v[44:47], v[132:135]\n"
"ds_read_b32 v243, v241\n"
"v_mfma_f32_16x16x32_bf16 v[132:135], a[236:239], v[60:63], v[132:135]\n"
"v_cvt_pk_bf16_f32 v214, v136, v137\n"
"v_mfma_f32_16x16x32_bf16 v[132:135], a[0:3], v[64:67], v[132:135]\n"
"v_cvt_pk_bf16_f32 v215, v138, v139\n"
"v_mfma_f32_16x16x32_bf16 v[132:135], a[4:7], v[68:71], v[132:135]\n"
"v_mfma_f32_16x16x32_bf16 v[132:135], a[8:11], v[84:87], v[132:135]\n"
"s_waitcnt lgkmcnt(0)\n"
"v_cmp_gt_u32 s[56:57], 34, v243\n"
"v_mfma_f32_16x16x32_bf16 v[132:135], a[12:15], v[88:91], v[132:135]\n"
"v_mfma_f32_16x16x32_bf16 v[132:135], a[16:19], v[92:95], v[132:135]\n"
"s_or_b64 s[56:57], s[56:57], vcc\n"
"s_cbranch_scc1 LB34\n"
"LB34R:\n"
"v_mfma_f32_16x16x32_bf16 v[132:135], a[20:23], v[108:111], v[132:135]\n"
"ds_write_b64 v245, v[214:215] offset:12296\n"
"v_mfma_f32_16x16x32_bf16 v[132:135], v[140:143], v[112:115], v[132:135]\n"
"v_mfma_f32_16x16x32_bf16 v[132:135], v[156:159], v[116:119], v[132:135]\n"
"v_mfma_f32_16x16x32_bf16 v[136:139], a[44:47], v[116:119], v[20:23]\n"
"ds_read_b128 v[116:119], v235 offset:40960\n"
"v_mfma_f32_16x16x32_bf16 v[136:139], a[40:43], v[112:115], v[136:139]\n"
"ds_read_b128 v[112:115], v234 offset:40960\n"
"v_mfma_f32_16x16x32_bf16 v[136:139], a[36:39], v[108:111], v[136:139]\n"
"ds_read_b128 v[108:111], v233 offset:40960\n"
"v_mfma_f32_16x16x32_bf16 v[136:139], a[32:35], v[92:95], v[136:139]\n"
"ds_read_b128 v[92:95], v232 offset:40960\n"
"v_cvt_pk_bf16_f32 v208, v132, v133\n"
"v_mfma_f32_16x16x32_bf16 v[136:139], a[28:31], v[88:91], v[136:139]\n"
"ds_read_b128 v[88:91], v235 offset:32768\n"
"v_cvt_pk_bf16_f32 v209, v134, v135\n"
"v_mfma_f32_16x16x32_bf16 v[136:139], a[24:27], v[84:87], v[136:139]\n"
"ds_read_b128 v[84:87], v234 offset:32768\n"
"v_mfma_f32_16x16x32_bf16 v[136:139], v[204:207], v[68:71], v[136:139]\n"
"ds_read_b128 v[68:71], v233 offset:32768\n"
"v_mfma_f32_16x16x32_bf16 v[136:139], v[188:191], v[64:67], v[136:139]\n"
"ds_read_b128 v[64:67], v232 offset:32768\n"
"v_mfma_f32_16x16x32_bf16 v[136:139], v[184:187], v[60:63], v[136:139]\n"
"ds_read_b128 v[60:63], v235 offset:24576\n"
"v_mfma_f32_16x16x32_bf16 v[136:139], v[180:183], v[44:47], v[136:139]\n"
"ds_read_b128 v[44:47], v234 offset:24576\n"
"v_mfma_f32_16x16x32_bf16 v[136:139], v[164:167], v[40:43], v[136:139]\n"
"ds_read_b128 v[40:43], v233 offset:24576\n"
"v_mfma_f32_16x16x32_bf16 v[136:139], v[160:163], v[36:39], v[136:139]\n"
"ds_read_b128 v[36:39], v232 offset:24576\n"
"s_waitcnt lgkmcnt(8)\n"
"v_mfma_f32_16x16x32_bf16 v[132:135], a[76:79], v[116:119], v[0:3]\n"
"ds_write_b64 v244, v[208:209] offset:12416\n"
"v_mfma_f32_16x16x32_bf16 v[132:135], a[72:75], v[112:115], v[132:135]\n"
"s_add_u32 s54, s26, s48\n"
"s_addc_u32 s55, s27, 0\n"
"v_mfma_f32_16x16x32_bf16 v[132:135], a[68:71], v[108:111], v[132:135]\n"
"v_mfma_f32_16x16x32_bf16 v[132:135], a[64:67], v[92:95], v[132:135]\n"
"v_cvt_pk_bf16_f32 v210, v136, v137\n"
"s_waitcnt lgkmcnt(5)\n"
"v_mfma_f32_16x16x32_bf16 v[132:135], a[60:63], v[88:91], v[132:135]\n"
"v_cvt_pk_bf16_f32 v211, v138, v139\n"
"v_mfma_f32_16x16x32_bf16 v[132:135], a[56:59], v[84:87], v[132:135]\n"
"v_mfma_f32_16x16x32_bf16 v[132:135], a[52:55], v[68:71], v[132:135]\n"
"ds_read_b128 v[216:219], v246 offset:0\n"
"v_mfma_f32_16x16x32_bf16 v[132:135], a[48:51], v[64:67], v[132:135]\n"
"ds_read_b128 v[220:223], v246 offset:1024\n"
"s_waitcnt lgkmcnt(3)\n"
"v_mfma_f32_16x16x32_bf16 v[132:135], a[252:255], v[60:63], v[132:135]\n"
"ds_read_b128 v[224:227], v246 offset:2048\n"
"v_mfma_f32_16x16x32_bf16 v[132:135], a[248:251], v[44:47], v[132:135]\n"
"ds_write_b64 v244, v[210:211] offset:12424\n"
"v_mfma_f32_16x16x32_bf16 v[132:135], a[244:247], v[40:43], v[132:135]\n"
"v_mfma_f32_16x16x32_bf16 v[132:135], a[240:243], v[36:39], v[132:135]\n"
"v_mfma_f32_16x16x32_bf16 v[136:139], a[80:83], v[36:39], v[4:7]\n"
"v_mfma_f32_16x16x32_bf16 v[136:139], a[84:87], v[40:43], v[136:139]\n"
"v_mfma_f32_16x16x32_bf16 v[136:139], a[88:91], v[44:47], v[136:139]\n"
"v_mfma_f32_16x16x32_bf16 v[136:139], a[92:95], v[60:63], v[136:139]\n"
"v_cvt_pk_bf16_f32 v208, v132, v133\n"
"v_mfma_f32_16x16x32_bf16 v[136:139], a[96:99], v[64:67], v[136:139]\n"
"v_cvt_pk_bf16_f32 v209, v134, v135\n"
"v_mfma_f32_16x16x32_bf16 v[136:139], a[100:103], v[68:71], v[136:139]\n"
"v_mfma_f32_16x16x32_bf16 v[136:139], a[104:107], v[84:87], v[136:139]\n"
"v_mfma_f32_16x16x32_bf16 v[136:139], a[108:111], v[88:91], v[136:139]\n"
"s_waitcnt lgkmcnt(1)\n"
"v_mfma_f32_16x16x32_bf16 v[136:139], a[112:115], v[92:95], v[136:139]\n"
"v_mfma_f32_16x16x32_bf16 v[136:139], a[116:119], v[108:111], v[136:139]\n"
"global_store_dwordx4 v247, v[216:219], s[54:55] offset:0 sc1\n"
"ds_write_b64 v244, v[208:209] offset:24576\n"
"v_mfma_f32_16x16x32_bf16 v[136:139], a[120:123], v[112:115], v[136:139]\n"
"v_mfma_f32_16x16x32_bf16 v[136:139], a[124:127], v[116:119], v[136:139]\n"
"v_mfma_f32_16x16x32_bf16 v[132:135], a[172:175], v[116:119], v[8:11]\n"
"v_mfma_f32_16x16x32_bf16 v[132:135], a[168:171], v[112:115], v[132:135]\n"
"v_mfma_f32_16x16x32_bf16 v[132:135], a[164:167], v[108:111], v[132:135]\n"
"v_mfma_f32_16x16x32_bf16 v[132:135], a[160:163], v[92:95], v[132:135]\n"
"v_cvt_pk_bf16_f32 v210, v136, v137\n"
"v_mfma_f32_16x16x32_bf16 v[132:135], a[156:159], v[88:91], v[132:135]\n"
"v_cvt_pk_bf16_f32 v211, v138, v139\n"
"v_mfma_f32_16x16x32_bf16 v[132:135], a[152:155], v[84:87], v[132:135]\n"
"v_mfma_f32_16x16x32_bf16 v[132:135], a[148:151], v[68:71], v[132:135]\n"
"v_mfma_f32_16x16x32_bf16 v[132:135], a[144:147], v[64:67], v[132:135]\n"
"v_mfma_f32_16x16x32_bf16 v[132:135], a[140:143], v[60:63], v[132:135]\n"
"v_mfma_f32_16x16x32_bf16 v[132:135], a[136:139], v[44:47], v[132:135]\n"
"global_store_dwordx4 v252, v[220:223], s[54:55] offset:0 sc1\n"
"ds_write_b64 v244, v[210:211] offset:24584\n"
"v_mfma_f32_16x16x32_bf16 v[132:135], a[132:135], v[40:43], v[132:135]\n"
"v_mfma_f32_16x16x32_bf16 v[132:135], a[128:131], v[36:39], v[132:135]\n"
"v_mfma_f32_16x16x32_bf16 v[136:139], a[176:179], v[36:39], v[12:15]\n"
"v_mfma_f32_16x16x32_bf16 v[136:139], a[180:183], v[40:43], v[136:139]\n"
"v_mfma_f32_16x16x32_bf16 v[136:139], a[184:187], v[44:47], v[136:139]\n"
"v_mfma_f32_16x16x32_bf16 v[136:139], a[188:191], v[60:63], v[136:139]\n"
"v_cvt_pk_bf16_f32 v212, v132, v133\n"
"v_mfma_f32_16x16x32_bf16 v[136:139], a[192:195], v[64:67], v[136:139]\n"
"v_mov_b32 v242, 35\n"
"v_mfma_f32_16x16x32_bf16 v[136:139], a[196:199], v[68:71], v[136:139]\n"
"v_cvt_pk_bf16_f32 v213, v134, v135\n"
"v_mfma_f32_16x16x32_bf16 v[136:139], a[200:203], v[84:87], v[136:139]\n"
"s_waitcnt lgkmcnt(2)\n"
"ds_write_b32 v240, v242\n"
"v_mfma_f32_16x16x32_bf16 v[136:139], a[204:207], v[88:91], v[136:139]\n"
"v_mfma_f32_16x16x32_bf16 v[136:139], a[208:211], v[92:95], v[136:139]\n"
"v_mfma_f32_16x16x32_bf16 v[136:139], a[212:215], v[108:111], v[136:139]\n"
"v_mfma_f32_16x16x32_bf16 v[136:139], a[216:219], v[112:115], v[136:139]\n"
"global_store_dwordx4 v253, v[224:227], s[54:55] offset:0 sc1\n"
"ds_write_b64 v245, v[212:213] offset:24576\n"
"v_mfma_f32_16x16x32_bf16 v[136:139], a[220:223], v[116:119], v[136:139]\n"
"v_mfma_f32_16x16x32_bf16 v[132:135], v[156:159], v[116:119], v[16:19]\n"
"v_mfma_f32_16x16x32_bf16 v[132:135], v[140:143], v[112:115], v[132:135]\n"
"v_mfma_f32_16x16x32_bf16 v[132:135], a[20:23], v[108:111], v[132:135]\n"
"ds_read_b32 v243, v241\n"
"v_mfma_f32_16x16x32_bf16 v[132:135], a[16:19], v[92:95], v[132:135]\n"
"v_cvt_pk_bf16_f32 v214, v136, v137\n"
"v_mfma_f32_16x16x32_bf16 v[132:135], a[12:15], v[88:91], v[132:135]\n"
"v_cvt_pk_bf16_f32 v215, v138, v139\n"
"v_mfma_f32_16x16x32_bf16 v[132:135], a[8:11], v[84:87], v[132:135]\n"
"v_mfma_f32_16x16x32_bf16 v[132:135], a[4:7], v[68:71], v[132:135]\n"
"s_waitcnt lgkmcnt(0)\n"
"v_cmp_gt_u32 s[56:57], 35, v243\n"
"v_mfma_f32_16x16x32_bf16 v[132:135], a[0:3], v[64:67], v[132:135]\n"
"v_mfma_f32_16x16x32_bf16 v[132:135], a[236:239], v[60:63], v[132:135]\n"
"s_cmp_lg_u64 s[56:57], 0\n"
"s_cbranch_scc1 LB35\n"
"LB35R:\n"
"v_mfma_f32_16x16x32_bf16 v[132:135], a[232:235], v[44:47], v[132:135]\n"
"ds_write_b64 v245, v[214:215] offset:24584\n"
"v_mfma_f32_16x16x32_bf16 v[132:135], a[228:231], v[40:43], v[132:135]\n"
"v_mfma_f32_16x16x32_bf16 v[132:135], a[224:227], v[36:39], v[132:135]\n"
"v_mfma_f32_16x16x32_bf16 v[136:139], v[160:163], v[36:39], v[20:23]\n"
"ds_read_b128 v[36:39], v232 offset:24832\n"
"v_mfma_f32_16x16x32_bf16 v[136:139], v[164:167], v[40:43], v[136:139]\n"
"ds_read_b128 v[40:43], v233 offset:24832\n"
"v_mfma_f32_16x16x32_bf16 v[136:139], v[180:183], v[44:47], v[136:139]\n"
"ds_read_b128 v[44:47], v234 offset:24832\n"
"v_mfma_f32_16x16x32_bf16 v[136:139], v[184:187], v[60:63], v[136:139]\n"
"ds_read_b128 v[60:63], v235 offset:24832\n"
"v_cvt_pk_bf16_f32 v208, v132, v133\n"
"v_mfma_f32_16x16x32_bf16 v[136:139], v[188:191], v[64:67], v[136:139]\n"
"ds_read_b128 v[64:67], v232 offset:33024\n"
"v_cvt_pk_bf16_f32 v209, v134, v135\n"
"v_mfma_f32_16x16x32_bf16 v[136:139], v[204:207], v[68:71], v[136:139]\n"
"ds_read_b128 v[68:71], v233 offset:33024\n"
"v_mfma_f32_16x16x32_bf16 v[136:139], a[24:27], v[84:87], v[136:139]\n"
"ds_read_b128 v[84:87], v234 offset:33024\n"
"v_mfma_f32_16x16x32_bf16 v[136:139], a[28:31], v[88:91], v[136:139]\n"
"ds_read_b128 v[88:91], v235 offset:33024\n"
"v_mfma_f32_16x16x32_bf16 v[136:139], a[32:35], v[92:95], v[136:139]\n"
"ds_read_b128 v[92:95], v232 offset:41216\n"
"v_mfma_f32_16x16x32_bf16 v[136:139], a[36:39], v[108:111], v[136:139]\n"
"ds_read_b128 v[108:111], v233 offset:41216\n"
"v_mfma_f32_16x16x32_bf16 v[136:139], a[40:43], v[112:115], v[136:139]\n"
"ds_read_b128 v[112:115], v234 offset:41216\n"
"v_mfma_f32_16x16x32_bf16 v[136:139], a[44:47], v[116:119], v[136:139]\n"
"ds_read_b128 v[116:119], v235 offset:41216\n"
"s_waitcnt lgkmcnt(8)\n"
"v_mfma_f32_16x16x32_bf16 v[132:135], a[240:243], v[36:39], v[0:3]\n"
"ds_write_b64 v244, v[208:209] offset:24704\n"
"v_mfma_f32_16x16x32_bf16 v[132:135], a[244:247], v[40:43], v[132:135]\n"
"v_mfma_f32_16x16x32_bf16 v[132:135], a[248:251], v[44:47], v[132:135]\n"
"v_mfma_f32_16x16x32_bf16 v[132:135], a[252:255], v[60:63], v[132:135]\n"
"v_cvt_pk_bf16_f32 v210, v136, v137\n"
"s_waitcnt lgkmcnt(5)\n"
"v_mfma_f32_16x16x32_bf16 v[132:135], a[48:51], v[64:67], v[132:135]\n"
"v_cvt_pk_bf16_f32 v211, v138, v139\n"
"v_mfma_f32_16x16x32_bf16 v[132:135], a[52:55], v[68:71], v[132:135]\n"
"v_mfma_f32_16x16x32_bf16 v[132:135], a[56:59], v[84:87], v[132:135]\n"
"ds_read_b128 v[216:219], v246 offset:12288\n"
"v_mfma_f32_16x16x32_bf16 v[132:135], a[60:63], v[88:91], v[132:135]\n"
"ds_read_b128 v[220:223], v246 offset:13312\n"
"s_waitcnt lgkmcnt(3)\n"
"v_mfma_f32_16x16x32_bf16 v[132:135], a[64:67], v[92:95], v[132:135]\n"
"ds_read_b128 v[224:227], v246 offset:14336\n"
"v_mfma_f32_16x16x32_bf16 v[132:135], a[68:71], v[108:111], v[132:135]\n"
"ds_write_b64 v244, v[210:211] offset:24712\n"
"v_mfma_f32_16x16x32_bf16 v[132:135], a[72:75], v[112:115], v[132:135]\n"
"v_mfma_f32_16x16x32_bf16 v[132:135], a[76:79], v[116:119], v[132:135]\n"
"v_mfma_f32_16x16x32_bf16 v[136:139], a[124:127], v[116:119], v[4:7]\n"
"v_mfma_f32_16x16x32_bf16 v[136:139], a[120:123], v[112:115], v[136:139]\n"
"v_mfma_f32_16x16x32_bf16 v[136:139], a[116:119], v[108:111], v[136:139]\n"
"v_mfma_f32_16x16x32_bf16 v[136:139], a[112:115], v[92:95], v[136:139]\n"
"v_cvt_pk_bf16_f32 v208, v132, v133\n"
"v_mfma_f32_16x16x32_bf16 v[136:139], a[108:111], v[88:91], v[136:139]\n"
"v_cvt_pk_bf16_f32 v209, v134, v135\n"
"v_mfma_f32_16x16x32_bf16 v[136:139], a[104:107], v[84:87], v[136:139]\n"
"v_mfma_f32_16x16x32_bf16 v[136:139], a[100:103], v[68:71], v[136:139]\n"
"v_mfma_f32_16x16x32_bf16 v[136:139], a[96:99], v[64:67], v[136:139]\n"
"s_waitcnt lgkmcnt(1)\n"
"v_mfma_f32_16x16x32_bf16 v[136:139], a[92:95], v[60:63], v[136:139]\n"
"v_mfma_f32_16x16x32_bf16 v[136:139], a[88:91], v[44:47], v[136:139]\n"
"global_store_dwordx4 v247, v[216:219], s[54:55] offset:768 sc1\n"
"ds_write_b64 v244, v[208:209] offset:0\n"
"v_mfma_f32_16x16x32_bf16 v[136:139], a[84:87], v[40:43], v[136:139]\n"
"v_mfma_f32_16x16x32_bf16 v[136:139], a[80:83], v[36:39], v[136:139]\n"
"v_mfma_f32_16x16x32_bf16 v[132:135], a[128:131], v[36:39], v[8:11]\n"
"v_mfma_f32_16x16x32_bf16 v[132:135], a[132:135], v[40:43], v[132:135]\n"
"v_mfma_f32_16x16x32_bf16 v[132:135], a[136:139], v[44:47], v[132:135]\n"
"v_mfma_f32_16x16x32_bf16 v[132:135], a[140:143], v[60:63], v[132:135]\n"
"v_cvt_pk_bf16_f32 v210, v136, v137\n"
"v_mfma_f32_16x16x32_bf16 v[132:135], a[144:147], v[64:67], v[132:135]\n"
"v_cvt_pk_bf16_f32 v211, v138, v139\n"
"v_mfma_f32_16x16x32_bf16 v[132:135], a[148:151], v[68:71], v[132:135]\n"
"v_mfma_f32_16x16x32_bf16 v[132:135], a[152:155], v[84:87], v[132:135]\n"
"v_mfma_f32_16x16x32_bf16 v[132:135], a[156:159], v[88:91], v[132:135]\n"
"v_mfma_f32_16x16x32_bf16 v[132:135], a[160:163], v[92:95], v[132:135]\n"
"v_mfma_f32_16x16x32_bf16 v[132:135], a[164:167], v[108:111], v[132:135]\n"
"global_store_dwordx4 v252, v[220:223], s[54:55] offset:768 sc1\n"
"ds_write_b64 v244, v[210:211] offset:8\n"
"v_mfma_f32_16x16x32_bf16 v[132:135], a[168:171], v[112:115], v[132:135]\n"
"v_mfma_f32_16x16x32_bf16 v[132:135], a[172:175], v[116:119], v[132:135]\n"
"v_mfma_f32_16x16x32_bf16 v[136:139], a[220:223], v[116:119], v[12:15]\n"
"v_mfma_f32_16x16x32_bf16 v[136:139], a[216:219], v[112:115], v[136:139]\n"
"v_mfma_f32_16x16x32_bf16 v[136:139], a[212:215], v[108:111], v[136:139]\n"
"v_mfma_f32_16x16x32_bf16 v[136:139], a[208:211], v[92:95], v[136:139]\n"
"v_cvt_pk_bf16_f32 v212, v132, v133\n"
"v_mfma_f32_16x16x32_bf16 v[136:139], a[204:207], v[88:91], v[136:139]\n"
"v_mov_b32 v242, 36\n"
"v_mfma_f32_16x16x32_bf16 v[136:139], a[200:203], v[84:87], v[136:139]\n"
"v_cvt_pk_bf16_f32 v213, v134, v135\n"
"v_mfma_f32_16x16x32_bf16 v[136:139], a[196:199], v[68:71], v[136:139]\n"
"s_waitcnt lgkmcnt(2)\n"
"ds_write_b32 v240, v242\n"
"v_mfma_f32_16x16x32_bf16 v[136:139], a[192:195], v[64:67], v[136:139]\n"
"v_mfma_f32_16x16x32_bf16 v[136:139], a[188:191], v[60:63], v[136:139]\n"
"v_mfma_f32_16x16x32_bf16 v[136:139], a[184:187], v[44:47], v[136:139]\n"
"v_mfma_f32_16x16x32_bf16 v[136:139], a[180:183], v[40:43], v[136:139]\n"
"global_store_dwordx4 v253, v[224:227], s[54:55] offset:768 sc1\n"
"ds_write_b64 v245, v[212:213] offset:0\n"
"v_mfma_f32_16x16x32_bf16 v[136:139], a[176:179], v[36:39], v[136:139]\n"
"v_mfma_f32_16x16x32_bf16 v[132:135], a[224:227], v[36:39], v[16:19]\n"
"v_mfma_f32_16x16x32_bf16 v[132:135], a[228:231], v[40:43], v[132:135]\n"
"v_mfma_f32_16x16x32_bf16 v[132:135], a[232:235], v[44:47], v[132:135]\n"
"ds_read_b32 v243, v241\n"
"v_mfma_f32_16x16x32_bf16 v[132:135], a[236:239], v[60:63], v[132:135]\n"
"v_cvt_pk_bf16_f32 v214, v136, v137\n"
"v_mfma_f32_16x16x32_bf16 v[132:135], a[0:3], v[64:67], v[132:135]\n"
"v_cvt_pk_bf16_f32 v215, v138, v139\n"
"v_mfma_f32_16x16x32_bf16 v[132:135], a[4:7], v[68:71], v[132:135]\n"
"v_mfma_f32_16x16x32_bf16 v[132:135], a[8:11], v[84:87], v[132:135]\n"
"s_waitcnt lgkmcnt(0)\n"
"v_cmp_gt_u32 s[56:57], 36, v243\n"
"v_mfma_f32_16x16x32_bf16 v[132:135], a[12:15], v[88:91], v[132:135]\n"
"v_mfma_f32_16x16x32_bf16 v[132:135], a[16:19], v[92:95], v[132:135]\n"
"s_cmp_lg_u64 s[56:57], 0\n"
"s_cbranch_scc1 LB36\n"
"LB36R:\n"
"v_mfma_f32_16x16x32_bf16 v[132:135], a[20:23], v[108:111], v[132:135]\n"
"ds_write_b64 v245, v[214:215] offset:8\n"
"v_mfma_f32_16x16x32_bf16 v[132:135], v[140:143], v[112:115], v[132:135]\n"
"v_mfma_f32_16x16x32_bf16 v[132:135], v[156:159], v[116:119], v[132:135]\n"
"v_mfma_f32_16x16x32_bf16 v[136:139], a[44:47], v[116:119], v[20:23]\n"
"v_mfma_f32_16x16x32_bf16 v[136:139], a[40:43], v[112:115], v[136:139]\n"
"v_mfma_f32_16x16x32_bf16 v[136:139], a[36:39], v[108:111], v[136:139]\n"
"v_mfma_f32_16x16x32_bf16 v[136:139], a[32:35], v[92:95], v[136:139]\n"
"v_cvt_pk_bf16_f32 v208, v132, v133\n"
"v_mfma_f32_16x16x32_bf16 v[136:139], a[28:31], v[88:91], v[136:139]\n"
"v_cvt_pk_bf16_f32 v209, v134, v135\n"
"v_mfma_f32_16x16x32_bf16 v[136:139], a[24:27], v[84:87], v[136:139]\n"
"v_mfma_f32_16x16x32_bf16 v[136:139], v[204:207], v[68:71], v[136:139]\n"
"v_mfma_f32_16x16x32_bf16 v[136:139], v[188:191], v[64:67], v[136:139]\n"
"v_mfma_f32_16x16x32_bf16 v[136:139], v[184:187], v[60:63], v[136:139]\n"
"v_mfma_f32_16x16x32_bf16 v[136:139], v[180:183], v[44:47], v[136:139]\n"
"ds_write_b64 v244, v[208:209] offset:128\n"
"v_mfma_f32_16x16x32_bf16 v[136:139], v[164:167], v[40:43], v[136:139]\n"
"v_mfma_f32_16x16x32_bf16 v[136:139], v[160:163], v[36:39], v[136:139]\n"
"s_add_u32 s54, s26, s49\n"
"s_addc_u32 s55, s27, 0\n"
"ds_read_b128 v[216:219], v246 offset:24576\n"
"ds_read_b128 v[220:223], v246 offset:25600\n"
"ds_read_b128 v[224:227], v246 offset:26624\n"
"s_nop 3\n"
"v_cvt_pk_bf16_f32 v210, v136, v137\n"
"v_cvt_pk_bf16_f32 v211, v138, v139\n"
"ds_write_b128 v244, v[208:211] offset:128\n"
"s_waitcnt lgkmcnt(3)\n"
"global_store_dwordx4 v247, v[216:219], s[54:55] offset:0 sc1\n"
"s_waitcnt lgkmcnt(2)\n"
"global_store_dwordx4 v252, v[220:223], s[54:55] offset:0 sc1\n"
"s_waitcnt lgkmcnt(1)\n"
"global_store_dwordx4 v253, v[224:227], s[54:55] offset:0 sc1\n"
"s_waitcnt lgkmcnt(0)\n"
"s_barrier\n"
"ds_read_b128 v[216:219], v246 offset:0\n"
"ds_read_b128 v[220:223], v246 offset:1024\n"
"ds_read_b128 v[224:227], v246 offset:2048\n"
"s_waitcnt lgkmcnt(2)\n"
"global_store_dwordx4 v247, v[216:219], s[54:55] offset:768 sc1\n"
"s_waitcnt lgkmcnt(1)\n"
"global_store_dwordx4 v252, v[220:223], s[54:55] offset:768 sc1\n"
"s_waitcnt lgkmcnt(0)\n"
"global_store_dwordx4 v253, v[224:227], s[54:55] offset:768 sc1\n"
"LFIN:\n"
"s_cmp_lg_u32 s87, 0\n"
"s_cbranch_scc1 LE1\n"
"v_min_u32 v0, 0x370, v239\n"
"v_add_u32 v0, 0x2480, v0\n"
"s_mov_b32 s82, 0x40000\n"
"LE0:\n"
"global_load_dwordx4 v[4:7], v0, s[28:29] sc1\n"
"s_sub_u32 s82, s82, 1\n"
"s_cbranch_scc1 LE1\n"
"s_waitcnt vmcnt(0)\n"
"v_cmp_ne_u64 vcc, s[80:81], v[4:5]\n"
"v_cmp_ne_u64 s[56:57], s[80:81], v[6:7]\n"
"s_nop 3\n"
"s_or_b64 vcc, vcc, s[56:57]\n"
"s_cbranch_scc1 LE0\n"
"v_mov_b32 v0, 0\n"
"v_mov_b32 v2, s80\n"
"v_mov_b32 v3, s81\n"
"s_mov_b64 exec, 1\n"
"global_store_dwordx2 v0, v[2:3], s[28:29] sc1\n"
"s_mov_b64 exec, -1\n"
"LE1:\n"
"s_waitcnt vmcnt(0)\n"
"s_branch LCEND\n"
"LEARLY:\n"
"v_mfma_f32_16x16x32_bf16 v[136:139], a[28:31], v[88:91], v[136:139]\n"
"ds_read_b128 v[88:91], v235 offset:8192\n"
"v_cvt_pk_bf16_f32 v208, v132, v133\n"
"v_mfma_f32_16x16x32_bf16 v[136:139], a[24:27], v[84:87], v[136:139]\n"
"ds_read_b128 v[84:87], v234 offset:8192\n"
"v_cvt_pk_bf16_f32 v209, v134, v135\n"
"v_mfma_f32_16x16x32_bf16 v[136:139], v[204:207], v[68:71], v[136:139]\n"
"ds_read_b128 v[68:71], v233 offset:8192\n"
"v_mfma_f32_16x16x32_bf16 v[136:139], v[188:191], v[64:67], v[136:139]\n"
"ds_read_b128 v[64:67], v232 offset:8192\n"
"v_mfma_f32_16x16x32_bf16 v[136:139], v[184:187], v[60:63], v[136:139]\n"
"ds_read_b128 v[60:63], v235 offset:0\n"
"v_mfma_f32_16x16x32_bf16 v[136:139], v[180:183], v[44:47], v[136:139]\n"
"ds_read_b128 v[44:47], v234 offset:0\n"
"v_mfma_f32_16x16x32_bf16 v[136:139], v[164:167], v[40:43], v[136:139]\n"
"ds_read_b128 v[40:43], v233 offset:0\n"
"v_mfma_f32_16x16x32_bf16 v[136:139], v[160:163], v[36:39], v[136:139]\n"
"ds_read_b128 v[36:39], v232 offset:0\n"
"s_waitcnt lgkmcnt(8)\n"
"v_mfma_f32_16x16x32_bf16 v[132:135], a[76:79], v[116:119], v[0:3]\n"
"ds_write_b64 v244, v[208:209] offset:12416\n"
"v_mfma_f32_16x16x32_bf16 v[132:135], a[72:75], v[112:115], v[132:135]\n"
"s_add_u32 m0, s31, 0x1f00\n"
"v_mfma_f32_16x16x32_bf16 v[132:135], a[68:71], v[108:111], v[132:135]\n"
"global_load_lds_dwordx4 v237, s[52:53] offset:256 nt\n"
"v_mfma_f32_16x16x32_bf16 v[132:135], a[64:67], v[92:95], v[132:135]\n"
"v_lshlrev_b32 v18, 16, v177\n"
"v_cvt_pk_bf16_f32 v211, v138, v139\n"
"s_waitcnt lgkmcnt(5)\n"
"v_mfma_f32_16x16x32_bf16 v[132:135], a[60:63], v[88:91], v[132:135]\n"
"v_and_b32 v19, 0xffff0000, v177\n"
"v_mfma_f32_16x16x32_bf16 v[132:135], a[56:59], v[84:87], v[132:135]\n"
"v_lshlrev_b32 v20, 16, v178\n"
"v_mfma_f32_16x16x32_bf16 v[132:135], a[52:55], v[68:71], v[132:135]\n"
"v_and_b32 v21, 0xffff0000, v178\n"
"v_mfma_f32_16x16x32_bf16 v[132:135], a[48:51], v[64:67], v[132:135]\n"
"v_lshlrev_b32 v22, 16, v179\n"
"s_waitcnt lgkmcnt(1)\n"
"v_mfma_f32_16x16x32_bf16 v[132:135], a[252:255], v[60:63], v[132:135]\n"
"v_and_b32 v23, 0xffff0000, v179\n"
"v_mfma_f32_16x16x32_bf16 v[132:135], a[248:251], v[44:47], v[132:135]\n"
"v_cvt_pk_bf16_f32 v210, v136, v137\n"
"v_mfma_f32_16x16x32_bf16 v[132:135], a[244:247], v[40:43], v[132:135]\n"
"s_add_u32 s54, s26, s45\n"
"s_addc_u32 s55, s27, 0\n"
"v_mfma_f32_16x16x32_bf16 v[132:135], a[240:243], v[36:39], v[132:135]\n"
"ds_read_b128 v[216:219], v246 offset:0\n"
"v_mfma_f32_16x16x32_bf16 v[136:139], a[80:83], v[36:39], v[4:7]\n"
"ds_read_b128 v[220:223], v246 offset:1024\n"
"v_mfma_f32_16x16x32_bf16 v[136:139], a[84:87], v[40:43], v[136:139]\n"
"ds_read_b128 v[224:227], v246 offset:2048\n"
"v_mfma_f32_16x16x32_bf16 v[136:139], a[88:91], v[44:47], v[136:139]\n"
"ds_write_b64 v244, v[210:211] offset:12424\n"
"v_mfma_f32_16x16x32_bf16 v[136:139], a[92:95], v[60:63], v[136:139]\n"
"v_cvt_pk_bf16_f32 v208, v132, v133\n"
"v_mfma_f32_16x16x32_bf16 v[136:139], a[96:99], v[64:67], v[136:139]\n"
"v_cvt_pk_bf16_f32 v209, v134, v135\n"
"v_mfma_f32_16x16x32_bf16 v[136:139], a[100:103], v[68:71], v[136:139]\n"
"s_add_u32 m0, s31, 0x3e00\n"
"v_mfma_f32_16x16x32_bf16 v[136:139], a[104:107], v[84:87], v[136:139]\n"
"global_load_lds_dwordx4 v237, s[52:53] offset:512 nt\n"
"v_mfma_f32_16x16x32_bf16 v[136:139], a[108:111], v[88:91], v[136:139]\n"
"s_waitcnt lgkmcnt(1)\n"
"v_mfma_f32_16x16x32_bf16 v[136:139], a[112:115], v[92:95], v[136:139]\n"
"s_sub_i32 s84, s84, s19\n"
"v_mfma_f32_16x16x32_bf16 v[136:139], a[116:119], v[108:111], v[136:139]\n"
"global_store_dwordx4 v247, v[216:219], s[54:55] offset:0 sc1\n"
"ds_write_b64 v244, v[208:209] offset:24576\n"
"v_mfma_f32_16x16x32_bf16 v[136:139], a[120:123], v[112:115], v[136:139]\n"
"s_add_u32 m0, s31, 0x1000\n"
"v_mfma_f32_16x16x32_bf16 v[136:139], a[124:127], v[116:119], v[136:139]\n"
"global_load_lds_dwordx4 v238, s[52:53] offset:0 nt\n"
"v_mfma_f32_16x16x32_bf16 v[132:135], a[172:175], v[116:119], v[8:11]\n"
"s_max_i32 s88, s88, s84\n"
"v_mfma_f32_16x16x32_bf16 v[132:135], a[168:171], v[112:115], v[132:135]\n"
"v_mfma_f32_16x16x32_bf16 v[132:135], a[164:167], v[108:111], v[132:135]\n"
"v_mfma_f32_16x16x32_bf16 v[132:135], a[160:163], v[92:95], v[132:135]\n"
"v_cvt_pk_bf16_f32 v210, v136, v137\n"
"v_mfma_f32_16x16x32_bf16 v[132:135], a[156:159], v[88:91], v[132:135]\n"
"v_cvt_pk_bf16_f32 v211, v138, v139\n"
"v_mfma_f32_16x16x32_bf16 v[132:135], a[152:155], v[84:87], v[132:135]\n"
"v_mfma_f32_16x16x32_bf16 v[132:135], a[148:151], v[68:71], v[132:135]\n"
"v_mfma_f32_16x16x32_bf16 v[132:135], a[144:147], v[64:67], v[132:135]\n"
"v_mfma_f32_16x16x32_bf16 v[132:135], a[140:143], v[60:63], v[132:135]\n"
"v_mfma_f32_16x16x32_bf16 v[132:135], a[136:139], v[44:47], v[132:135]\n"
"global_store_dwordx4 v252, v[220:223], s[54:55] offset:0 sc1\n"
"ds_write_b64 v244, v[210:211] offset:24584\n"
"v_mfma_f32_16x16x32_bf16 v[132:135], a[132:135], v[40:43], v[132:135]\n"
"s_add_u32 m0, s31, 0x2f00\n"
"v_mfma_f32_16x16x32_bf16 v[132:135], a[128:131], v[36:39], v[132:135]\n"
"global_load_lds_dwordx4 v238, s[52:53] offset:256 nt\n"
"v_mfma_f32_16x16x32_bf16 v[136:139], a[176:179], v[36:39], v[12:15]\n"
"v_mfma_f32_16x16x32_bf16 v[136:139], a[180:183], v[40:43], v[136:139]\n"
"v_mfma_f32_16x16x32_bf16 v[136:139], a[184:187], v[44:47], v[136:139]\n"
"v_mfma_f32_16x16x32_bf16 v[136:139], a[188:191], v[60:63], v[136:139]\n"
"v_cvt_pk_bf16_f32 v212, v132, v133\n"
"v_mfma_f32_16x16x32_bf16 v[136:139], a[192:195], v[64:67], v[136:139]\n"
"v_mov_b32 v242, 29\n"
"v_mfma_f32_16x16x32_bf16 v[136:139], a[196:199], v[68:71], v[136:139]\n"
"v_cvt_pk_bf16_f32 v213, v134, v135\n"
"v_mfma_f32_16x16x32_bf16 v[136:139], a[200:203], v[84:87], v[136:139]\n"
"s_waitcnt lgkmcnt(2)\n"
"ds_write_b32 v240, v242\n"
"v_mfma_f32_16x16x32_bf16 v[136:139], a[204:207], v[88:91], v[136:139]\n"
"v_mfma_f32_16x16x32_bf16 v[136:139], a[208:211], v[92:95], v[136:139]\n"
"v_mfma_f32_16x16x32_bf16 v[136:139], a[212:215], v[108:111], v[136:139]\n"
"v_mfma_f32_16x16x32_bf16 v[136:139], a[216:219], v[112:115], v[136:139]\n"
"global_store_dwordx4 v253, v[224:227], s[54:55] offset:0 sc1\n"
"ds_write_b64 v245, v[212:213] offset:24576\n"
"v_mfma_f32_16x16x32_bf16 v[136:139], a[220:223], v[116:119], v[136:139]\n"
"s_add_u32 m0, s31, 0x4e00\n"
"v_mfma_f32_16x16x32_bf16 v[132:135], v[156:159], v[116:119], v[16:19]\n"
"global_load_lds_dwordx4 v238, s[52:53] offset:512 nt\n"
"v_mfma_f32_16x16x32_bf16 v[132:135], v[140:143], v[112:115], v[132:135]\n"
"v_mfma_f32_16x16x32_bf16 v[132:135], a[20:23], v[108:111], v[132:135]\n"
"ds_read_b32 v243, v241\n"
"v_mfma_f32_16x16x32_bf16 v[132:135], a[16:19], v[92:95], v[132:135]\n"
"v_cvt_pk_bf16_f32 v214, v136, v137\n"
"v_mfma_f32_16x16x32_bf16 v[132:135], a[12:15], v[88:91], v[132:135]\n"
"v_cvt_pk_bf16_f32 v215, v138, v139\n"
"v_mfma_f32_16x16x32_bf16 v[132:135], a[8:11], v[84:87], v[132:135]\n"
"v_mfma_f32_16x16x32_bf16 v[132:135], a[4:7], v[68:71], v[132:135]\n"
"s_waitcnt lgkmcnt(0)\n"
"v_cmp_gt_u32 s[56:57], 29, v243\n"
"v_mfma_f32_16x16x32_bf16 v[132:135], a[0:3], v[64:67], v[132:135]\n"
"v_mfma_f32_16x16x32_bf16 v[132:135], a[236:239], v[60:63], v[132:135]\n"
"s_or_b64 s[56:57], s[56:57], vcc\n"
"s_cbranch_scc1 LB29E\n"
"LB29ER:\n"
"v_mfma_f32_16x16x32_bf16 v[132:135], a[232:235], v[44:47], v[132:135]\n"
"ds_write_b64 v245, v[214:215] offset:24584\n"
"v_mfma_f32_16x16x32_bf16 v[132:135], a[228:231], v[40:43], v[132:135]\n"
"v_mfma_f32_16x16x32_bf16 v[132:135], a[224:227], v[36:39], v[132:135]\n"
"v_mfma_f32_16x16x32_bf16 v[136:139], v[160:163], v[36:39], v[20:23]\n"
"ds_read_b128 v[36:39], v232 offset:256\n"
"v_mfma_f32_16x16x32_bf16 v[136:139], v[164:167], v[40:43], v[136:139]\n"
"ds_read_b128 v[40:43], v233 offset:256\n"
"v_mfma_f32_16x16x32_bf16 v[136:139], v[180:183], v[44:47], v[136:139]\n"
"ds_read_b128 v[44:47], v234 offset:256\n"
"v_mfma_f32_16x16x32_bf16 v[136:139], v[184:187], v[60:63], v[136:139]\n"
"ds_read_b128 v[60:63], v235 offset:256\n"
"v_cvt_pk_bf16_f32 v208, v132, v133\n"
"v_mfma_f32_16x16x32_bf16 v[136:139], v[188:191], v[64:67], v[136:139]\n"
"ds_read_b128 v[64:67], v232 offset:8448\n"
"v_cvt_pk_bf16_f32 v209, v134, v135\n"
"v_mfma_f32_16x16x32_bf16 v[136:139], v[204:207], v[68:71], v[136:139]\n"
"ds_read_b128 v[68:71], v233 offset:8448\n"
"v_mfma_f32_16x16x32_bf16 v[136:139], a[24:27], v[84:87], v[136:139]\n"
"ds_read_b128 v[84:87], v234 offset:8448\n"
"v_mfma_f32_16x16x32_bf16 v[136:139], a[28:31], v[88:91], v[136:139]\n"
"ds_read_b128 v[88:91], v235 offset:8448\n"
"v_mfma_f32_16x16x32_bf16 v[136:139], a[32:35], v[92:95], v[136:139]\n"
"ds_read_b128 v[92:95], v232 offset:16640\n"
"v_mfma_f32_16x16x32_bf16 v[136:139], a[36:39], v[108:111], v[136:139]\n"
"ds_read_b128 v[108:111], v233 offset:16640\n"
"v_mfma_f32_16x16x32_bf16 v[136:139], a[40:43], v[112:115], v[136:139]\n"
"ds_read_b128 v[112:115], v234 offset:16640\n"
"v_mfma_f32_16x16x32_bf16 v[136:139], a[44:47], v[116:119], v[136:139]\n"
"ds_read_b128 v[116:119], v235 offset:16640\n"
"s_waitcnt lgkmcnt(8)\n"
"v_mfma_f32_16x16x32_bf16 v[132:135], a[240:243], v[36:39], v[0:3]\n"
"ds_write_b64 v244, v[208:209] offset:24704\n"
"v_mfma_f32_16x16x32_bf16 v[132:135], a[244:247], v[40:43], v[132:135]\n"
"s_add_u32 s52, s20, s49\n"
"s_addc_u32 s53, s21, 0\n"
"v_mfma_f32_16x16x32_bf16 v[132:135], a[248:251], v[44:47], v[132:135]\n"
"s_add_u32 m0, s31, 0x6000\n"
"v_mfma_f32_16x16x32_bf16 v[132:135], a[252:255], v[60:63], v[132:135]\n"
"global_load_lds_dwordx4 v237, s[52:53] offset:0 nt\n"
"s_waitcnt lgkmcnt(5)\n"
"v_mfma_f32_16x16x32_bf16 v[132:135], a[48:51], v[64:67], v[132:135]\n"
"v_cvt_pk_bf16_f32 v210, v136, v137\n"
"v_mfma_f32_16x16x32_bf16 v[132:135], a[52:55], v[68:71], v[132:135]\n"
"v_cvt_pk_bf16_f32 v211, v138, v139\n"
"v_mfma_f32_16x16x32_bf16 v[132:135], a[56:59], v[84:87], v[132:135]\n"
"ds_read_b128 v[216:219], v246 offset:12288\n"
"v_mfma_f32_16x16x32_bf16 v[132:135], a[60:63], v[88:91], v[132:135]\n"
"ds_read_b128 v[220:223], v246 offset:13312\n"
"s_waitcnt lgkmcnt(3)\n"
"v_mfma_f32_16x16x32_bf16 v[132:135], a[64:67], v[92:95], v[132:135]\n"
"ds_read_b128 v[224:227], v246 offset:14336\n"
"v_mfma_f32_16x16x32_bf16 v[132:135], a[68:71], v[108:111], v[132:135]\n"
"s_add_u32 m0, s31, 0x7f00\n"
"v_mfma_f32_16x16x32_bf16 v[132:135], a[72:75], v[112:115], v[132:135]\n"
"ds_write_b64 v244, v[210:211] offset:24712\n"
"v_mfma_f32_16x16x32_bf16 v[132:135], a[76:79], v[116:119], v[132:135]\n"
"global_load_lds_dwordx4 v237, s[52:53] offset:256 nt\n"
"v_mfma_f32_16x16x32_bf16 v[136:139], a[124:127], v[116:119], v[4:7]\n"
"s_add_u32 s83, s83, s86\n"
"v_mfma_f32_16x16x32_bf16 v[136:139], a[120:123], v[112:115], v[136:139]\n"
"s_sub_i32 s88, s88, 6\n"
"v_mfma_f32_16x16x32_bf16 v[136:139], a[116:119], v[108:111], v[136:139]\n"
"s_max_i32 s88, s88, 0\n"
"v_mfma_f32_16x16x32_bf16 v[136:139], a[112:115], v[92:95], v[136:139]\n"
"v_cvt_pk_bf16_f32 v208, v132, v133\n"
"v_mfma_f32_16x16x32_bf16 v[136:139], a[108:111], v[88:91], v[136:139]\n"
"v_cvt_pk_bf16_f32 v209, v134, v135\n"
"v_mfma_f32_16x16x32_bf16 v[136:139], a[104:107], v[84:87], v[136:139]\n"
"v_mfma_f32_16x16x32_bf16 v[136:139], a[100:103], v[68:71], v[136:139]\n"
"v_mfma_f32_16x16x32_bf16 v[136:139], a[96:99], v[64:67], v[136:139]\n"
"s_waitcnt lgkmcnt(1)\n"
"v_mfma_f32_16x16x32_bf16 v[136:139], a[92:95], v[60:63], v[136:139]\n"
"v_mfma_f32_16x16x32_bf16 v[136:139], a[88:91], v[44:47], v[136:139]\n"
"global_store_dwordx4 v247, v[216:219], s[54:55] offset:768 sc1\n"
"ds_write_b64 v244, v[208:209] offset:0\n"
"v_mfma_f32_16x16x32_bf16 v[136:139], a[84:87], v[40:43], v[136:139]\n"
"s_add_u32 m0, s31, 0x9e00\n"
"v_mfma_f32_16x16x32_bf16 v[136:139], a[80:83], v[36:39], v[136:139]\n"
"global_load_lds_dwordx4 v237, s[52:53] offset:512 nt\n"
"v_mfma_f32_16x16x32_bf16 v[132:135], a[128:131], v[36:39], v[8:11]\n"
"v_mfma_f32_16x16x32_bf16 v[132:135], a[132:135], v[40:43], v[132:135]\n"
"v_mfma_f32_16x16x32_bf16 v[132:135], a[136:139], v[44:47], v[132:135]\n"
"v_mfma_f32_16x16x32_bf16 v[132:135], a[140:143], v[60:63], v[132:135]\n"
"v_cvt_pk_bf16_f32 v210, v136, v137\n"
"v_mfma_f32_16x16x32_bf16 v[132:135], a[144:147], v[64:67], v[132:135]\n"
"v_cvt_pk_bf16_f32 v211, v138, v139\n"
"v_mfma_f32_16x16x32_bf16 v[132:135], a[148:151], v[68:71], v[132:135]\n"
"v_mfma_f32_16x16x32_bf16 v[132:135], a[152:155], v[84:87], v[132:135]\n"
"v_mfma_f32_16x16x32_bf16 v[132:135], a[156:159], v[88:91], v[132:135]\n"
"v_mfma_f32_16x16x32_bf16 v[132:135], a[160:163], v[92:95], v[132:135]\n"
"v_mfma_f32_16x16x32_bf16 v[132:135], a[164:167], v[108:111], v[132:135]\n"
"s_memrealtime s[84:85]\n"
"v_mfma_f32_16x16x32_bf16 v[132:135], a[168:171], v[112:115], v[132:135]\n"
"global_store_dwordx4 v252, v[220:223], s[54:55] offset:768 sc1\n"
"ds_write_b64 v244, v[210:211] offset:8\n"
"v_mfma_f32_16x16x32_bf16 v[132:135], a[172:175], v[116:119], v[132:135]\n"
"s_add_u32 m0, s31, 0x7000\n"
"v_mfma_f32_16x16x32_bf16 v[136:139], a[220:223], v[116:119], v[12:15]\n"
"global_load_lds_dwordx4 v238, s[52:53] offset:0 nt\n"
"v_mfma_f32_16x16x32_bf16 v[136:139], a[216:219], v[112:115], v[136:139]\n"
"v_mfma_f32_16x16x32_bf16 v[136:139], a[212:215], v[108:111], v[136:139]\n"
"v_mfma_f32_16x16x32_bf16 v[136:139], a[208:211], v[92:95], v[136:139]\n"
"v_cvt_pk_bf16_f32 v212, v132, v133\n"
"v_mfma_f32_16x16x32_bf16 v[136:139], a[204:207], v[88:91], v[136:139]\n"
"v_mov_b32 v242, 30\n"
"v_mfma_f32_16x16x32_bf16 v[136:139], a[200:203], v[84:87], v[136:139]\n"
"v_cvt_pk_bf16_f32 v213, v134, v135\n"
"v_mfma_f32_16x16x32_bf16 v[136:139], a[196:199], v[68:71], v[136:139]\n"
"s_waitcnt vmcnt(18) lgkmcnt(2)\n"
"ds_write_b32 v240, v242\n"
"v_mfma_f32_16x16x32_bf16 v[136:139], a[192:195], v[64:67], v[136:139]\n"
"v_mfma_f32_16x16x32_bf16 v[136:139], a[188:191], v[60:63], v[136:139]\n"
"v_mfma_f32_16x16x32_bf16 v[136:139], a[184:187], v[44:47], v[136:139]\n"
"v_mfma_f32_16x16x32_bf16 v[136:139], a[180:183], v[40:43], v[136:139]\n"
"global_store_dwordx4 v253, v[224:227], s[54:55] offset:768 sc1\n"
"ds_write_b64 v245, v[212:213] offset:0\n"
"v_mfma_f32_16x16x32_bf16 v[136:139], a[176:179], v[36:39], v[136:139]\n"
"s_add_u32 m0, s31, 0x8f00\n"
"v_mfma_f32_16x16x32_bf16 v[132:135], a[224:227], v[36:39], v[16:19]\n"
"global_load_lds_dwordx4 v238, s[52:53] offset:256 nt\n"
"v_mfma_f32_16x16x32_bf16 v[132:135], a[228:231], v[40:43], v[132:135]\n"
"v_mfma_f32_16x16x32_bf16 v[132:135], a[232:235], v[44:47], v[132:135]\n"
"ds_read_b32 v243, v241\n"
"v_mfma_f32_16x16x32_bf16 v[132:135], a[236:239], v[60:63], v[132:135]\n"
"v_cvt_pk_bf16_f32 v214, v136, v137\n"
"v_mfma_f32_16x16x32_bf16 v[132:135], a[0:3], v[64:67], v[132:135]\n"
"v_cvt_pk_bf16_f32 v215, v138, v139\n"
"v_mfma_f32_16x16x32_bf16 v[132:135], a[4:7], v[68:71], v[132:135]\n"
"v_mfma_f32_16x16x32_bf16 v[132:135], a[8:11], v[84:87], v[132:135]\n"
"s_waitcnt lgkmcnt(0)\n"
"v_cmp_gt_u32 s[56:57], 30, v243\n"
"v_mfma_f32_16x16x32_bf16 v[132:135], a[12:15], v[88:91], v[132:135]\n"
"s_sub_u32 s84, s84, s83\n"
"v_mfma_f32_16x16x32_bf16 v[132:135], a[16:19], v[92:95], v[132:135]\n"
"s_or_b64 s[56:57], s[56:57], vcc\n"
"s_cbranch_scc1 LB30E\n"
"LB30ER:\n"
"v_mfma_f32_16x16x32_bf16 v[132:135], a[20:23], v[108:111], v[132:135]\n"
"s_cmp_lt_i32 s84, s88\n"
"s_cselect_b64 vcc, -1, 0\n"
"v_mfma_f32_16x16x32_bf16 v[132:135], v[140:143], v[112:115], v[132:135]\n"
"ds_write_b64 v245, v[214:215] offset:8\n"
"v_mfma_f32_16x16x32_bf16 v[132:135], v[156:159], v[116:119], v[132:135]\n"
"v_mfma_f32_16x16x32_bf16 v[136:139], a[44:47], v[116:119], v[20:23]\n"
"ds_read_b128 v[116:119], v235 offset:40960\n"
"v_mfma_f32_16x16x32_bf16 v[136:139], a[40:43], v[112:115], v[136:139]\n"
"ds_read_b128 v[112:115], v234 offset:40960\n"
"v_mfma_f32_16x16x32_bf16 v[136:139], a[36:39], v[108:111], v[136:139]\n"
"ds_read_b128 v[108:111], v233 offset:40960\n"
"v_mfma_f32_16x16x32_bf16 v[136:139], a[32:35], v[92:95], v[136:139]\n"
"ds_read_b128 v[92:95], v232 offset:40960\n"
"v_cvt_pk_bf16_f32 v208, v132, v133\n"
"v_mfma_f32_16x16x32_bf16 v[136:139], a[28:31], v[88:91], v[136:139]\n"
"ds_read_b128 v[88:91], v235 offset:32768\n"
"v_cvt_pk_bf16_f32 v209, v134, v135\n"
"v_mfma_f32_16x16x32_bf16 v[136:139], a[24:27], v[84:87], v[136:139]\n"
"ds_read_b128 v[84:87], v234 offset:32768\n"
"v_mfma_f32_16x16x32_bf16 v[136:139], v[204:207], v[68:71], v[136:139]\n"
"ds_read_b128 v[68:71], v233 offset:32768\n"
"v_mfma_f32_16x16x32_bf16 v[136:139], v[188:191], v[64:67], v[136:139]\n"
"ds_read_b128 v[64:67], v232 offset:32768\n"
"v_mfma_f32_16x16x32_bf16 v[136:139], v[184:187], v[60:63], v[136:139]\n"
"ds_read_b128 v[60:63], v235 offset:24576\n"
"v_mfma_f32_16x16x32_bf16 v[136:139], v[180:183], v[44:47], v[136:139]\n"
"ds_read_b128 v[44:47], v234 offset:24576\n"
"v_mfma_f32_16x16x32_bf16 v[136:139], v[164:167], v[40:43], v[136:139]\n"
"ds_read_b128 v[40:43], v233 offset:24576\n"
"v_mfma_f32_16x16x32_bf16 v[136:139], v[160:163], v[36:39], v[136:139]\n"
"ds_read_b128 v[36:39], v232 offset:24576\n"
"s_waitcnt lgkmcnt(8)\n"
"v_mfma_f32_16x16x32_bf16 v[132:135], a[76:79], v[116:119], v[0:3]\n"
"ds_write_b64 v244, v[208:209] offset:128\n"
"v_mfma_f32_16x16x32_bf16 v[132:135], a[72:75], v[112:115], v[132:135]\n"
"s_add_u32 m0, s31, 0xae00\n"
"v_mfma_f32_16x16x32_bf16 v[132:135], a[68:71], v[108:111], v[132:135]\n"
"global_load_lds_dwordx4 v238, s[52:53] offset:512 nt\n"
"v_mfma_f32_16x16x32_bf16 v[132:135], a[64:67], v[92:95], v[132:135]\n"
"v_cvt_pk_bf16_f32 v210, v136, v137\n"
"s_waitcnt lgkmcnt(5)\n"
"v_mfma_f32_16x16x32_bf16 v[132:135], a[60:63], v[88:91], v[132:135]\n"
"v_cvt_pk_bf16_f32 v211, v138, v139\n"
"v_mfma_f32_16x16x32_bf16 v[132:135], a[56:59], v[84:87], v[132:135]\n"
"s_add_u32 s54, s26, s46\n"
"s_addc_u32 s55, s27, 0\n"
"v_mfma_f32_16x16x32_bf16 v[132:135], a[52:55], v[68:71], v[132:135]\n"
"ds_read_b128 v[216:219], v246 offset:24576\n"
"v_mfma_f32_16x16x32_bf16 v[132:135], a[48:51], v[64:67], v[132:135]\n"
"ds_read_b128 v[220:223], v246 offset:25600\n"
"s_waitcnt lgkmcnt(3)\n"
"v_mfma_f32_16x16x32_bf16 v[132:135], a[252:255], v[60:63], v[132:135]\n"
"ds_read_b128 v[224:227], v246 offset:26624\n"
"v_mfma_f32_16x16x32_bf16 v[132:135], a[248:251], v[44:47], v[132:135]\n"
"ds_write_b64 v244, v[210:211] offset:136\n"
"v_mfma_f32_16x16x32_bf16 v[132:135], a[244:247], v[40:43], v[132:135]\n"
"s_sub_i32 s84, s84, s19\n"
"v_mfma_f32_16x16x32_bf16 v[132:135], a[240:243], v[36:39], v[132:135]\n"
"s_max_i32 s88, s88, s84\n"
"v_mfma_f32_16x16x32_bf16 v[136:139], a[80:83], v[36:39], v[4:7]\n"
"v_mfma_f32_16x16x32_bf16 v[136:139], a[84:87], v[40:43], v[136:139]\n"
"v_mfma_f32_16x16x32_bf16 v[136:139], a[88:91], v[44:47], v[136:139]\n"
"v_mfma_f32_16x16x32_bf16 v[136:139], a[92:95], v[60:63], v[136:139]\n"
"v_cvt_pk_bf16_f32 v208, v132, v133\n"
"v_mfma_f32_16x16x32_bf16 v[136:139], a[96:99], v[64:67], v[136:139]\n"
"v_cvt_pk_bf16_f32 v209, v134, v135\n"
"v_mfma_f32_16x16x32_bf16 v[136:139], a[100:103], v[68:71], v[136:139]\n"
"v_mfma_f32_16x16x32_bf16 v[136:139], a[104:107], v[84:87], v[136:139]\n"
"v_mfma_f32_16x16x32_bf16 v[136:139], a[108:111], v[88:91], v[136:139]\n"
"s_waitcnt lgkmcnt(1)\n"
"v_mfma_f32_16x16x32_bf16 v[136:139], a[112:115], v[92:95], v[136:139]\n"
"v_mfma_f32_16x16x32_bf16 v[136:139], a[116:119], v[108:111], v[136:139]\n"
"global_store_dwordx4 v247, v[216:219], s[54:55] offset:0 sc1\n"
"ds_write_b64 v244, v[208:209] offset:12288\n"
"v_mfma_f32_16x16x32_bf16 v[136:139], a[120:123], v[112:115], v[136:139]\n"
"v_mfma_f32_16x16x32_bf16 v[136:139], a[124:127], v[116:119], v[136:139]\n"
"v_mfma_f32_16x16x32_bf16 v[132:135], a[172:175], v[116:119], v[8:11]\n"
"v_mfma_f32_16x16x32_bf16 v[132:135], a[168:171], v[112:115], v[132:135]\n"
"v_mfma_f32_16x16x32_bf16 v[132:135], a[164:167], v[108:111], v[132:135]\n"
"v_mfma_f32_16x16x32_bf16 v[132:135], a[160:163], v[92:95], v[132:135]\n"
"v_cvt_pk_bf16_f32 v210, v136, v137\n"
"v_mfma_f32_16x16x32_bf16 v[132:135], a[156:159], v[88:91], v[132:135]\n"
"v_cvt_pk_bf16_f32 v211, v138, v139\n"
"v_mfma_f32_16x16x32_bf16 v[132:135], a[152:155], v[84:87], v[132:135]\n"
"v_mfma_f32_16x16x32_bf16 v[132:135], a[148:151], v[68:71], v[132:135]\n"
"v_mfma_f32_16x16x32_bf16 v[132:135], a[144:147], v[64:67], v[132:135]\n"
"v_mfma_f32_16x16x32_bf16 v[132:135], a[140:143], v[60:63], v[132:135]\n"
"v_mfma_f32_16x16x32_bf16 v[132:135], a[136:139], v[44:47], v[132:135]\n"
"global_store_dwordx4 v252, v[220:223], s[54:55] offset:0 sc1\n"
"ds_write_b64 v244, v[210:211] offset:12296\n"
"v_mfma_f32_16x16x32_bf16 v[132:135], a[132:135], v[40:43], v[132:135]\n"
"v_mfma_f32_16x16x32_bf16 v[132:135], a[128:131], v[36:39], v[132:135]\n"
"v_mfma_f32_16x16x32_bf16 v[136:139], a[176:179], v[36:39], v[12:15]\n"
"v_mfma_f32_16x16x32_bf16 v[136:139], a[180:183], v[40:43], v[136:139]\n"
"v_mfma_f32_16x16x32_bf16 v[136:139], a[184:187], v[44:47], v[136:139]\n"
"v_mfma_f32_16x16x32_bf16 v[136:139], a[188:191], v[60:63], v[136:139]\n"
"v_cvt_pk_bf16_f32 v212, v132, v133\n"
"v_mfma_f32_16x16x32_bf16 v[136:139], a[192:195], v[64:67], v[136:139]\n"
"v_mov_b32 v242, 31\n"
"v_mfma_f32_16x16x32_bf16 v[136:139], a[196:199], v[68:71], v[136:139]\n"
"v_cvt_pk_bf16_f32 v213, v134, v135\n"
"v_mfma_f32_16x16x32_bf16 v[136:139], a[200:203], v[84:87], v[136:139]\n"
"s_waitcnt lgkmcnt(2)\n"
"ds_write_b32 v240, v242\n"
"v_mfma_f32_16x16x32_bf16 v[136:139], a[204:207], v[88:91], v[136:139]\n"
"v_mfma_f32_16x16x32_bf16 v[136:139], a[208:211], v[92:95], v[136:139]\n"
"v_mfma_f32_16x16x32_bf16 v[136:139], a[212:215], v[108:111], v[136:139]\n"
"v_mfma_f32_16x16x32_bf16 v[136:139], a[216:219], v[112:115], v[136:139]\n"
"global_store_dwordx4 v253, v[224:227], s[54:55] offset:0 sc1\n"
"ds_write_b64 v245, v[212:213] offset:12288\n"
"v_mfma_f32_16x16x32_bf16 v[136:139], a[220:223], v[116:119], v[136:139]\n"
"v_mfma_f32_16x16x32_bf16 v[132:135], v[156:159], v[116:119], v[16:19]\n"
"v_mfma_f32_16x16x32_bf16 v[132:135], v[140:143], v[112:115], v[132:135]\n"
"v_mfma_f32_16x16x32_bf16 v[132:135], a[20:23], v[108:111], v[132:135]\n"
"ds_read_b32 v243, v241\n"
"v_mfma_f32_16x16x32_bf16 v[132:135], a[16:19], v[92:95], v[132:135]\n"
"v_cvt_pk_bf16_f32 v214, v136, v137\n"
"v_mfma_f32_16x16x32_bf16 v[132:135], a[12:15], v[88:91], v[132:135]\n"
"v_cvt_pk_bf16_f32 v215, v138, v139\n"
"v_mfma_f32_16x16x32_bf16 v[132:135], a[8:11], v[84:87], v[132:135]\n"
"v_mfma_f32_16x16x32_bf16 v[132:135], a[4:7], v[68:71], v[132:135]\n"
"s_waitcnt lgkmcnt(0)\n"
"v_cmp_gt_u32 s[56:57], 31, v243\n"
"v_mfma_f32_16x16x32_bf16 v[132:135], a[0:3], v[64:67], v[132:135]\n"
"v_mfma_f32_16x16x32_bf16 v[132:135], a[236:239], v[60:63], v[132:135]\n"
"s_or_b64 s[56:57], s[56:57], vcc\n"
"s_cbranch_scc1 LB31E\n"
"LB31ER:\n"
"v_mfma_f32_16x16x32_bf16 v[132:135], a[232:235], v[44:47], v[132:135]\n"
"ds_write_b64 v245, v[214:215] offset:12296\n"
"v_mfma_f32_16x16x32_bf16 v[132:135], a[228:231], v[40:43], v[132:135]\n"
"v_mfma_f32_16x16x32_bf16 v[132:135], a[224:227], v[36:39], v[132:135]\n"
"s_mov_b64 exec, s[8:9]\n"
"s_cbranch_execz LHT14\n"
"LHR14:\n"
"s_mov_b64 exec, -1\n"
"v_mfma_f32_16x16x32_bf16 v[136:139], v[160:163], v[36:39], v[20:23]\n"
"ds_read_b128 v[36:39], v232 offset:24832\n"
"v_mfma_f32_16x16x32_bf16 v[136:139], v[164:167], v[40:43], v[136:139]\n"
"ds_read_b128 v[40:43], v233 offset:24832\n"
"v_mfma_f32_16x16x32_bf16 v[136:139], v[180:183], v[44:47], v[136:139]\n"
"ds_read_b128 v[44:47], v234 offset:24832\n"
"v_mfma_f32_16x16x32_bf16 v[136:139], v[184:187], v[60:63], v[136:139]\n"
"ds_read_b128 v[60:63], v235 offset:24832\n"
"v_cvt_pk_bf16_f32 v208, v132, v133\n"
"v_mfma_f32_16x16x32_bf16 v[136:139], v[188:191], v[64:67], v[136:139]\n"
"ds_read_b128 v[64:67], v232 offset:33024\n"
"v_cvt_pk_bf16_f32 v209, v134, v135\n"
"v_mfma_f32_16x16x32_bf16 v[136:139], v[204:207], v[68:71], v[136:139]\n"
"ds_read_b128 v[68:71], v233 offset:33024\n"
"v_mfma_f32_16x16x32_bf16 v[136:139], a[24:27], v[84:87], v[136:139]\n"
"ds_read_b128 v[84:87], v234 offset:33024\n"
"v_mfma_f32_16x16x32_bf16 v[136:139], a[28:31], v[88:91], v[136:139]\n"
"ds_read_b128 v[88:91], v235 offset:33024\n"
"v_mfma_f32_16x16x32_bf16 v[136:139], a[32:35], v[92:95], v[136:139]\n"
"ds_read_b128 v[92:95], v232 offset:41216\n"
"v_mfma_f32_16x16x32_bf16 v[136:139], a[36:39], v[108:111], v[136:139]\n"
"ds_read_b128 v[108:111], v233 offset:41216\n"
"ds_write_b64 v244, v[208:209] offset:12416\n"
"v_mfma_f32_16x16x32_bf16 v[136:139], a[40:43], v[112:115], v[136:139]\n"
"ds_read_b128 v[112:115], v234 offset:41216\n"
"v_mfma_f32_16x16x32_bf16 v[136:139], a[44:47], v[116:119], v[136:139]\n"
"ds_read_b128 v[116:119], v235 offset:41216\n"
"s_waitcnt lgkmcnt(9)\n"
"v_mfma_f32_16x16x32_bf16 v[132:135], a[240:243], v[36:39], v[0:3]\n"
"v_add_u32 v232, 0xffff4000, v232\n"
"v_mfma_f32_16x16x32_bf16 v[132:135], a[244:247], v[40:43], v[132:135]\n"
"v_add_u32 v233, 0xffff4000, v233\n"
"v_mfma_f32_16x16x32_bf16 v[132:135], a[248:251], v[44:47], v[132:135]\n"
"v_add_u32 v234, 0xffff4000, v234\n"
"v_mfma_f32_16x16x32_bf16 v[132:135], a[252:255], v[60:63], v[132:135]\n"
"v_add_u32 v235, 0xffff4000, v235\n"
"s_waitcnt lgkmcnt(5)\n"
"v_mfma_f32_16x16x32_bf16 v[132:135], a[48:51], v[64:67], v[132:135]\n"
"v_cvt_pk_bf16_f32 v210, v136, v137\n"
"v_mfma_f32_16x16x32_bf16 v[132:135], a[52:55], v[68:71], v[132:135]\n"
"v_cvt_pk_bf16_f32 v211, v138, v139\n"
"v_mfma_f32_16x16x32_bf16 v[132:135], a[56:59], v[84:87], v[132:135]\n"
"ds_read_b128 v[216:219], v246 offset:0\n"
"v_mfma_f32_16x16x32_bf16 v[132:135], a[60:63], v[88:91], v[132:135]\n"
"ds_read_b128 v[220:223], v246 offset:1024\n"
"s_waitcnt lgkmcnt(2)\n"
"v_mfma_f32_16x16x32_bf16 v[132:135], a[64:67], v[92:95], v[132:135]\n"
"ds_read_b128 v[224:227], v246 offset:2048\n"
"v_mfma_f32_16x16x32_bf16 v[132:135], a[68:71], v[108:111], v[132:135]\n"
"v_lshlrev_b32 v0, 16, v192\n"
"v_mfma_f32_16x16x32_bf16 v[132:135], a[72:75], v[112:115], v[132:135]\n"
"ds_write_b64 v244, v[210:211] offset:12424\n"
"v_mfma_f32_16x16x32_bf16 v[132:135], a[76:79], v[116:119], v[132:135]\n"
"v_and_b32 v1, 0xffff0000, v192\n"
"v_mfma_f32_16x16x32_bf16 v[136:139], a[124:127], v[116:119], v[4:7]\n"
"v_lshlrev_b32 v2, 16, v193\n"
"v_mfma_f32_16x16x32_bf16 v[136:139], a[120:123], v[112:115], v[136:139]\n"
"v_and_b32 v3, 0xffff0000, v193\n"
"v_mfma_f32_16x16x32_bf16 v[136:139], a[116:119], v[108:111], v[136:139]\n"
"s_add_u32 s83, s83, s86\n"
"v_mfma_f32_16x16x32_bf16 v[136:139], a[112:115], v[92:95], v[136:139]\n"
"v_cvt_pk_bf16_f32 v208, v132, v133\n"
"v_mfma_f32_16x16x32_bf16 v[136:139], a[108:111], v[88:91], v[136:139]\n"
"v_cvt_pk_bf16_f32 v209, v134, v135\n"
"v_mfma_f32_16x16x32_bf16 v[136:139], a[104:107], v[84:87], v[136:139]\n"
"v_lshlrev_b32 v4, 16, v194\n"
"v_mfma_f32_16x16x32_bf16 v[136:139], a[100:103], v[68:71], v[136:139]\n"
"v_and_b32 v5, 0xffff0000, v194\n"
"v_mfma_f32_16x16x32_bf16 v[136:139], a[96:99], v[64:67], v[136:139]\n"
"s_waitcnt lgkmcnt(1)\n"
"v_mfma_f32_16x16x32_bf16 v[136:139], a[92:95], v[60:63], v[136:139]\n"
"v_lshlrev_b32 v6, 16, v195\n"
"v_mfma_f32_16x16x32_bf16 v[136:139], a[88:91], v[44:47], v[136:139]\n"
"global_store_dwordx4 v247, v[216:219], s[54:55] offset:768 sc1\n"
"ds_write_b64 v244, v[208:209] offset:24576\n"
"v_mfma_f32_16x16x32_bf16 v[136:139], a[84:87], v[40:43], v[136:139]\n"
"v_and_b32 v7, 0xffff0000, v195\n"
"v_mfma_f32_16x16x32_bf16 v[136:139], a[80:83], v[36:39], v[136:139]\n"
"s_sub_i32 s88, s88, 6\n"
"v_mfma_f32_16x16x32_bf16 v[132:135], a[128:131], v[36:39], v[8:11]\n"
"s_max_i32 s88, s88, 0\n"
"v_mfma_f32_16x16x32_bf16 v[132:135], a[132:135], v[40:43], v[132:135]\n"
"v_mfma_f32_16x16x32_bf16 v[132:135], a[136:139], v[44:47], v[132:135]\n"
"v_mfma_f32_16x16x32_bf16 v[132:135], a[140:143], v[60:63], v[132:135]\n"
"v_cvt_pk_bf16_f32 v210, v136, v137\n"
"v_mfma_f32_16x16x32_bf16 v[132:135], a[144:147], v[64:67], v[132:135]\n"
"v_cvt_pk_bf16_f32 v211, v138, v139\n"
"v_mfma_f32_16x16x32_bf16 v[132:135], a[148:151], v[68:71], v[132:135]\n"
"v_lshlrev_b32 v8, 16, v196\n"
"v_mfma_f32_16x16x32_bf16 v[132:135], a[152:155], v[84:87], v[132:135]\n"
"v_and_b32 v9, 0xffff0000, v196\n"
"v_mfma_f32_16x16x32_bf16 v[132:135], a[156:159], v[88:91], v[132:135]\n"
"v_lshlrev_b32 v10, 16, v197\n"
"v_mfma_f32_16x16x32_bf16 v[132:135], a[160:163], v[92:95], v[132:135]\n"
"v_and_b32 v11, 0xffff0000, v197\n"
"v_mfma_f32_16x16x32_bf16 v[132:135], a[164:167], v[108:111], v[132:135]\n"
"s_memrealtime s[84:85]\n"
"v_mfma_f32_16x16x32_bf16 v[132:135], a[168:171], v[112:115], v[132:135]\n"
"global_store_dwordx4 v252, v[220:223], s[54:55] offset:768 sc1\n"
"ds_write_b64 v244, v[210:211] offset:24584\n"
"v_mfma_f32_16x16x32_bf16 v[132:135], a[172:175], v[116:119], v[132:135]\n"
"v_mfma_f32_16x16x32_bf16 v[136:139], a[220:223], v[116:119], v[12:15]\n"
"v_mfma_f32_16x16x32_bf16 v[136:139], a[216:219], v[112:115], v[136:139]\n"
"v_mfma_f32_16x16x32_bf16 v[136:139], a[212:215], v[108:111], v[136:139]\n"
"v_mfma_f32_16x16x32_bf16 v[136:139], a[208:211], v[92:95], v[136:139]\n"
"v_cvt_pk_bf16_f32 v212, v132, v133\n"
"v_mfma_f32_16x16x32_bf16 v[136:139], a[204:207], v[88:91], v[136:139]\n"
"v_mov_b32 v242, 32\n"
"v_mfma_f32_16x16x32_bf16 v[136:139], a[200:203], v[84:87], v[136:139]\n"
"v_cvt_pk_bf16_f32 v213, v134, v135\n"
"v_mfma_f32_16x16x32_bf16 v[136:139], a[196:199], v[68:71], v[136:139]\n"
"s_waitcnt vmcnt(14) lgkmcnt(2)\n"
"ds_write_b32 v240, v242\n"
"v_mfma_f32_16x16x32_bf16 v[136:139], a[192:195], v[64:67], v[136:139]\n"
"v_lshlrev_b32 v12, 16, v198\n"
"v_mfma_f32_16x16x32_bf16 v[136:139], a[188:191], v[60:63], v[136:139]\n"
"v_and_b32 v13, 0xffff0000, v198\n"
"LHT14:\n"
"s_cbranch_execz LHR14\n"
"v_mfma_f32_16x16x32_bf16 v[136:139], a[184:187], v[44:47], v[136:139]\n"
"v_lshlrev_b32 v14, 16, v199\n"
"v_mfma_f32_16x16x32_bf16 v[136:139], a[180:183], v[40:43], v[136:139]\n"
"global_store_dwordx4 v253, v[224:227], s[54:55] offset:768 sc1\n"
"ds_write_b64 v245, v[212:213] offset:24576\n"
"v_mfma_f32_16x16x32_bf16 v[136:139], a[176:179], v[36:39], v[136:139]\n"
"v_and_b32 v15, 0xffff0000, v199\n"
"v_mfma_f32_16x16x32_bf16 v[132:135], a[224:227], v[36:39], v[16:19]\n"
"v_mfma_f32_16x16x32_bf16 v[132:135], a[228:231], v[40:43], v[132:135]\n"
"v_mfma_f32_16x16x32_bf16 v[132:135], a[232:235], v[44:47], v[132:135]\n"
"ds_read_b32 v243, v241\n"
"v_mfma_f32_16x16x32_bf16 v[132:135], a[236:239], v[60:63], v[132:135]\n"
"v_cvt_pk_bf16_f32 v214, v136, v137\n"
"v_mfma_f32_16x16x32_bf16 v[132:135], a[0:3], v[64:67], v[132:135]\n"
"v_cvt_pk_bf16_f32 v215, v138, v139\n"
"v_mfma_f32_16x16x32_bf16 v[132:135], a[4:7], v[68:71], v[132:135]\n"
"v_lshlrev_b32 v16, 16, v200\n"
"v_mfma_f32_16x16x32_bf16 v[132:135], a[8:11], v[84:87], v[132:135]\n"
"s_waitcnt lgkmcnt(0)\n"
"v_cmp_gt_u32 s[56:57], 32, v243\n"
"v_mfma_f32_16x16x32_bf16 v[132:135], a[12:15], v[88:91], v[132:135]\n"
"s_sub_u32 s84, s84, s83\n"
"v_mfma_f32_16x16x32_bf16 v[132:135], a[16:19], v[92:95], v[132:135]\n"
"s_or_b64 s[56:57], s[56:57], vcc\n"
"s_cbranch_scc1 LB32E\n"
"LB32ER:\n"
"v_mfma_f32_16x16x32_bf16 v[132:135], a[20:23], v[108:111], v[132:135]\n"
"s_cmp_lt_i32 s84, s88\n"
"s_cselect_b64 vcc, -1, 0\n"
"v_mfma_f32_16x16x32_bf16 v[132:135], v[140:143], v[112:115], v[132:135]\n"
"ds_write_b64 v245, v[214:215] offset:24584\n"
"v_mfma_f32_16x16x32_bf16 v[132:135], v[156:159], v[116:119], v[132:135]\n"
"v_and_b32 v17, 0xffff0000, v200\n"
"v_mfma_f32_16x16x32_bf16 v[136:139], a[44:47], v[116:119], v[20:23]\n"
"ds_read_b128 v[116:119], v235 offset:16384\n"
"v_mfma_f32_16x16x32_bf16 v[136:139], a[40:43], v[112:115], v[136:139]\n"
"ds_read_b128 v[112:115], v234 offset:16384\n"
"v_mfma_f32_16x16x32_bf16 v[136:139], a[36:39], v[108:111], v[136:139]\n"
"ds_read_b128 v[108:111], v233 offset:16384\n"
"v_mfma_f32_16x16x32_bf16 v[136:139], a[32:35], v[92:95], v[136:139]\n"
"ds_read_b128 v[92:95], v232 offset:16384\n"
"v_cvt_pk_bf16_f32 v208, v132, v133\n"
"v_mfma_f32_16x16x32_bf16 v[136:139], a[28:31], v[88:91], v[136:139]\n"
"ds_read_b128 v[88:91], v235 offset:8192\n"
"v_cvt_pk_bf16_f32 v209, v134, v135\n"
"v_mfma_f32_16x16x32_bf16 v[136:139], a[24:27], v[84:87], v[136:139]\n"
"ds_read_b128 v[84:87], v234 offset:8192\n"
"v_mfma_f32_16x16x32_bf16 v[136:139], v[204:207], v[68:71], v[136:139]\n"
"ds_read_b128 v[68:71], v233 offset:8192\n"
"v_mfma_f32_16x16x32_bf16 v[136:139], v[188:191], v[64:67], v[136:139]\n"
"ds_read_b128 v[64:67], v232 offset:8192\n"
"v_mfma_f32_16x16x32_bf16 v[136:139], v[184:187], v[60:63], v[136:139]\n"
"ds_read_b128 v[60:63], v235 offset:0\n"
"v_mfma_f32_16x16x32_bf16 v[136:139], v[180:183], v[44:47], v[136:139]\n"
"ds_read_b128 v[44:47], v234 offset:0\n"
"v_mfma_f32_16x16x32_bf16 v[136:139], v[164:167], v[40:43], v[136:139]\n"
"ds_read_b128 v[40:43], v233 offset:0\n"
"v_mfma_f32_16x16x32_bf16 v[136:139], v[160:163], v[36:39], v[136:139]\n"
"ds_read_b128 v[36:39], v232 offset:0\n"
"s_waitcnt lgkmcnt(8)\n"
"v_mfma_f32_16x16x32_bf16 v[132:135], a[76:79], v[116:119], v[0:3]\n"
"ds_write_b64 v244, v[208:209] offset:24704\n"
"v_mfma_f32_16x16x32_bf16 v[132:135], a[72:75], v[112:115], v[132:135]\n"
"v_lshlrev_b32 v18, 16, v201\n"
"v_mfma_f32_16x16x32_bf16 v[132:135], a[68:71], v[108:111], v[132:135]\n"
"v_and_b32 v19, 0xffff0000, v201\n"
"v_mfma_f32_16x16x32_bf16 v[132:135], a[64:67], v[92:95], v[132:135]\n"
"v_lshlrev_b32 v20, 16, v202\n"
"s_waitcnt lgkmcnt(5)\n"
"v_mfma_f32_16x16x32_bf16 v[132:135], a[60:63], v[88:91], v[132:135]\n"
"v_and_b32 v21, 0xffff0000, v202\n"
"v_mfma_f32_16x16x32_bf16 v[132:135], a[56:59], v[84:87], v[132:135]\n"
"v_lshlrev_b32 v22, 16, v203\n"
"v_mfma_f32_16x16x32_bf16 v[132:135], a[52:55], v[68:71], v[132:135]\n"
"v_and_b32 v23, 0xffff0000, v203\n"
"v_mfma_f32_16x16x32_bf16 v[132:135], a[48:51], v[64:67], v[132:135]\n"
"v_cvt_pk_bf16_f32 v210, v136, v137\n"
"s_waitcnt lgkmcnt(1)\n"
"v_mfma_f32_16x16x32_bf16 v[132:135], a[252:255], v[60:63], v[132:135]\n"
"v_cvt_pk_bf16_f32 v211, v138, v139\n"
"v_mfma_f32_16x16x32_bf16 v[132:135], a[248:251], v[44:47], v[132:135]\n"
"s_add_u32 s54, s26, s47\n"
"s_addc_u32 s55, s27, 0\n"
"v_mfma_f32_16x16x32_bf16 v[132:135], a[244:247], v[40:43], v[132:135]\n"
"ds_read_b128 v[216:219], v246 offset:12288\n"
"v_mfma_f32_16x16x32_bf16 v[132:135], a[240:243], v[36:39], v[132:135]\n"
"ds_read_b128 v[220:223], v246 offset:13312\n"
"v_mfma_f32_16x16x32_bf16 v[136:139], a[80:83], v[36:39], v[4:7]\n"
"ds_read_b128 v[224:227], v246 offset:14336\n"
"v_mfma_f32_16x16x32_bf16 v[136:139], a[84:87], v[40:43], v[136:139]\n"
"ds_write_b64 v244, v[210:211] offset:24712\n"
"v_mfma_f32_16x16x32_bf16 v[136:139], a[88:91], v[44:47], v[136:139]\n"
"s_sub_i32 s84, s84, s19\n"
"v_mfma_f32_16x16x32_bf16 v[136:139], a[92:95], v[60:63], v[136:139]\n"
"v_cvt_pk_bf16_f32 v208, v132, v133\n"
"v_mfma_f32_16x16x32_bf16 v[136:139], a[96:99], v[64:67], v[136:139]\n"
"v_cvt_pk_bf16_f32 v209, v134, v135\n"
"v_mfma_f32_16x16x32_bf16 v[136:139], a[100:103], v[68:71], v[136:139]\n"
"s_max_i32 s88, s88, s84\n"
"v_mfma_f32_16x16x32_bf16 v[136:139], a[104:107], v[84:87], v[136:139]\n"
"v_mfma_f32_16x16x32_bf16 v[136:139], a[108:111], v[88:91], v[136:139]\n"
"s_waitcnt lgkmcnt(1)\n"
"v_mfma_f32_16x16x32_bf16 v[136:139], a[112:115], v[92:95], v[136:139]\n"
"v_mfma_f32_16x16x32_bf16 v[136:139], a[116:119], v[108:111], v[136:139]\n"
"global_store_dwordx4 v247, v[216:219], s[54:55] offset:0 sc1\n"
"ds_write_b64 v244, v[208:209] offset:0\n"
"v_mfma_f32_16x16x32_bf16 v[136:139], a[120:123], v[112:115], v[136:139]\n"
"v_mfma_f32_16x16x32_bf16 v[136:139], a[124:127], v[116:119], v[136:139]\n"
"v_mfma_f32_16x16x32_bf16 v[132:135], a[172:175], v[116:119], v[8:11]\n"
"v_mfma_f32_16x16x32_bf16 v[132:135], a[168:171], v[112:115], v[132:135]\n"
"v_mfma_f32_16x16x32_bf16 v[132:135], a[164:167], v[108:111], v[132:135]\n"
"v_mfma_f32_16x16x32_bf16 v[132:135], a[160:163], v[92:95], v[132:135]\n"
"v_cvt_pk_bf16_f32 v210, v136, v137\n"
"v_mfma_f32_16x16x32_bf16 v[132:135], a[156:159], v[88:91], v[132:135]\n"
"v_cvt_pk_bf16_f32 v211, v138, v139\n"
"v_mfma_f32_16x16x32_bf16 v[132:135], a[152:155], v[84:87], v[132:135]\n"
"v_mfma_f32_16x16x32_bf16 v[132:135], a[148:151], v[68:71], v[132:135]\n"
"v_mfma_f32_16x16x32_bf16 v[132:135], a[144:147], v[64:67], v[132:135]\n"
"v_mfma_f32_16x16x32_bf16 v[132:135], a[140:143], v[60:63], v[132:135]\n"
"v_mfma_f32_16x16x32_bf16 v[132:135], a[136:139], v[44:47], v[132:135]\n"
"global_store_dwordx4 v252, v[220:223], s[54:55] offset:0 sc1\n"
"ds_write_b64 v244, v[210:211] offset:8\n"
"v_mfma_f32_16x16x32_bf16 v[132:135], a[132:135], v[40:43], v[132:135]\n"
"v_mfma_f32_16x16x32_bf16 v[132:135], a[128:131], v[36:39], v[132:135]\n"
"v_mfma_f32_16x16x32_bf16 v[136:139], a[176:179], v[36:39], v[12:15]\n"
"v_mfma_f32_16x16x32_bf16 v[136:139], a[180:183], v[40:43], v[136:139]\n"
"v_mfma_f32_16x16x32_bf16 v[136:139], a[184:187], v[44:47], v[136:139]\n"
"v_mfma_f32_16x16x32_bf16 v[136:139], a[188:191], v[60:63], v[136:139]\n"
"v_cvt_pk_bf16_f32 v212, v132, v133\n"
"v_mfma_f32_16x16x32_bf16 v[136:139], a[192:195], v[64:67], v[136:139]\n"
"v_mov_b32 v242, 33\n"
"v_mfma_f32_16x16x32_bf16 v[136:139], a[196:199], v[68:71], v[136:139]\n"
"v_cvt_pk_bf16_f32 v213, v134, v135\n"
"v_mfma_f32_16x16x32_bf16 v[136:139], a[200:203], v[84:87], v[136:139]\n"
"s_waitcnt lgkmcnt(2)\n"
"ds_write_b32 v240, v242\n"
"v_mfma_f32_16x16x32_bf16 v[136:139], a[204:207], v[88:91], v[136:139]\n"
"v_mfma_f32_16x16x32_bf16 v[136:139], a[208:211], v[92:95], v[136:139]\n"
"v_mfma_f32_16x16x32_bf16 v[136:139], a[212:215], v[108:111], v[136:139]\n"
"v_mfma_f32_16x16x32_bf16 v[136:139], a[216:219], v[112:115], v[136:139]\n"
"global_store_dwordx4 v253, v[224:227], s[54:55] offset:0 sc1\n"
"ds_write_b64 v245, v[212:213] offset:0\n"
"v_mfma_f32_16x16x32_bf16 v[136:139], a[220:223], v[116:119], v[136:139]\n"
"v_mfma_f32_16x16x32_bf16 v[132:135], v[156:159], v[116:119], v[16:19]\n"
"v_mfma_f32_16x16x32_bf16 v[132:135], v[140:143], v[112:115], v[132:135]\n"
"v_mfma_f32_16x16x32_bf16 v[132:135], a[20:23], v[108:111], v[132:135]\n"
"ds_read_b32 v243, v241\n"
"v_mfma_f32_16x16x32_bf16 v[132:135], a[16:19], v[92:95], v[132:135]\n"
"v_cvt_pk_bf16_f32 v214, v136, v137\n"
"v_mfma_f32_16x16x32_bf16 v[132:135], a[12:15], v[88:91], v[132:135]\n"
"v_cvt_pk_bf16_f32 v215, v138, v139\n"
"v_mfma_f32_16x16x32_bf16 v[132:135], a[8:11], v[84:87], v[132:135]\n"
"v_mfma_f32_16x16x32_bf16 v[132:135], a[4:7], v[68:71], v[132:135]\n"
"s_waitcnt lgkmcnt(0)\n"
"v_cmp_gt_u32 s[56:57], 33, v243\n"
"v_mfma_f32_16x16x32_bf16 v[132:135], a[0:3], v[64:67], v[132:135]\n"
"v_mfma_f32_16x16x32_bf16 v[132:135], a[236:239], v[60:63], v[132:135]\n"
"s_or_b64 s[56:57], s[56:57], vcc\n"
"s_cbranch_scc1 LB33E\n"
"LB33ER:\n"
"v_mfma_f32_16x16x32_bf16 v[132:135], a[232:235], v[44:47], v[132:135]\n"
"ds_write_b64 v245, v[214:215] offset:8\n"
"v_mfma_f32_16x16x32_bf16 v[132:135], a[228:231], v[40:43], v[132:135]\n"
"v_mfma_f32_16x16x32_bf16 v[132:135], a[224:227], v[36:39], v[132:135]\n"
"v_mfma_f32_16x16x32_bf16 v[136:139], v[160:163], v[36:39], v[20:23]\n"
"ds_read_b128 v[36:39], v232 offset:256\n"
"v_mfma_f32_16x16x32_bf16 v[136:139], v[164:167], v[40:43], v[136:139]\n"
"ds_read_b128 v[40:43], v233 offset:256\n"
"v_mfma_f32_16x16x32_bf16 v[136:139], v[180:183], v[44:47], v[136:139]\n"
"ds_read_b128 v[44:47], v234 offset:256\n"
"v_mfma_f32_16x16x32_bf16 v[136:139], v[184:187], v[60:63], v[136:139]\n"
"ds_read_b128 v[60:63], v235 offset:256\n"
"v_cvt_pk_bf16_f32 v208, v132, v133\n"
"v_mfma_f32_16x16x32_bf16 v[136:139], v[188:191], v[64:67], v[136:139]\n"
"ds_read_b128 v[64:67], v232 offset:8448\n"
"v_cvt_pk_bf16_f32 v209, v134, v135\n"
"v_mfma_f32_16x16x32_bf16 v[136:139], v[204:207], v[68:71], v[136:139]\n"
"ds_read_b128 v[68:71], v233 offset:8448\n"
"v_mfma_f32_16x16x32_bf16 v[136:139], a[24:27], v[84:87], v[136:139]\n"
"ds_read_b128 v[84:87], v234 offset:8448\n"
"v_mfma_f32_16x16x32_bf16 v[136:139], a[28:31], v[88:91], v[136:139]\n"
"ds_read_b128 v[88:91], v235 offset:8448\n"
"v_mfma_f32_16x16x32_bf16 v[136:139], a[32:35], v[92:95], v[136:139]\n"
"ds_read_b128 v[92:95], v232 offset:16640\n"
"v_mfma_f32_16x16x32_bf16 v[136:139], a[36:39], v[108:111], v[136:139]\n"
"ds_read_b128 v[108:111], v233 offset:16640\n"
"v_mfma_f32_16x16x32_bf16 v[136:139], a[40:43], v[112:115], v[136:139]\n"
"ds_read_b128 v[112:115], v234 offset:16640\n"
"v_mfma_f32_16x16x32_bf16 v[136:139], a[44:47], v[116:119], v[136:139]\n"
"ds_read_b128 v[116:119], v235 offset:16640\n"
"s_waitcnt lgkmcnt(8)\n"
"v_mfma_f32_16x16x32_bf16 v[132:135], a[240:243], v[36:39], v[0:3]\n"
"ds_write_b64 v244, v[208:209] offset:128\n"
"v_mfma_f32_16x16x32_bf16 v[132:135], a[244:247], v[40:43], v[132:135]\n"
"v_mfma_f32_16x16x32_bf16 v[132:135], a[248:251], v[44:47], v[132:135]\n"
"v_mfma_f32_16x16x32_bf16 v[132:135], a[252:255], v[60:63], v[132:135]\n"
"v_cvt_pk_bf16_f32 v210, v136, v137\n"
"s_waitcnt lgkmcnt(5)\n"
"v_mfma_f32_16x16x32_bf16 v[132:135], a[48:51], v[64:67], v[132:135]\n"
"v_cvt_pk_bf16_f32 v211, v138, v139\n"
"v_mfma_f32_16x16x32_bf16 v[132:135], a[52:55], v[68:71], v[132:135]\n"
"v_mfma_f32_16x16x32_bf16 v[132:135], a[56:59], v[84:87], v[132:135]\n"
"ds_read_b128 v[216:219], v246 offset:24576\n"
"v_mfma_f32_16x16x32_bf16 v[132:135], a[60:63], v[88:91], v[132:135]\n"
"ds_read_b128 v[220:223], v246 offset:25600\n"
"s_waitcnt lgkmcnt(3)\n"
"v_mfma_f32_16x16x32_bf16 v[132:135], a[64:67], v[92:95], v[132:135]\n"
"ds_read_b128 v[224:227], v246 offset:26624\n"
"v_mfma_f32_16x16x32_bf16 v[132:135], a[68:71], v[108:111], v[132:135]\n"
"ds_write_b64 v244, v[210:211] offset:136\n"
"v_mfma_f32_16x16x32_bf16 v[132:135], a[72:75], v[112:115], v[132:135]\n"
"v_mfma_f32_16x16x32_bf16 v[132:135], a[76:79], v[116:119], v[132:135]\n"
"v_mfma_f32_16x16x32_bf16 v[136:139], a[124:127], v[116:119], v[4:7]\n"
"v_mfma_f32_16x16x32_bf16 v[136:139], a[120:123], v[112:115], v[136:139]\n"
"v_mfma_f32_16x16x32_bf16 v[136:139], a[116:119], v[108:111], v[136:139]\n"
"v_mfma_f32_16x16x32_bf16 v[136:139], a[112:115], v[92:95], v[136:139]\n"
"v_cvt_pk_bf16_f32 v208, v132, v133\n"
"v_mfma_f32_16x16x32_bf16 v[136:139], a[108:111], v[88:91], v[136:139]\n"
"v_cvt_pk_bf16_f32 v209, v134, v135\n"
"v_mfma_f32_16x16x32_bf16 v[136:139], a[104:107], v[84:87], v[136:139]\n"
"v_mfma_f32_16x16x32_bf16 v[136:139], a[100:103], v[68:71], v[136:139]\n"
"v_mfma_f32_16x16x32_bf16 v[136:139], a[96:99], v[64:67], v[136:139]\n"
"s_waitcnt lgkmcnt(1)\n"
"v_mfma_f32_16x16x32_bf16 v[136:139], a[92:95], v[60:63], v[136:139]\n"
"v_mfma_f32_16x16x32_bf16 v[136:139], a[88:91], v[44:47], v[136:139]\n"
"global_store_dwordx4 v247, v[216:219], s[54:55] offset:768 sc1\n"
"ds_write_b64 v244, v[208:209] offset:12288\n"
"v_mfma_f32_16x16x32_bf16 v[136:139], a[84:87], v[40:43], v[136:139]\n"
"v_mfma_f32_16x16x32_bf16 v[136:139], a[80:83], v[36:39], v[136:139]\n"
"v_mfma_f32_16x16x32_bf16 v[132:135], a[128:131], v[36:39], v[8:11]\n"
"v_mfma_f32_16x16x32_bf16 v[132:135], a[132:135], v[40:43], v[132:135]\n"
"v_mfma_f32_16x16x32_bf16 v[132:135], a[136:139], v[44:47], v[132:135]\n"
"v_mfma_f32_16x16x32_bf16 v[132:135], a[140:143], v[60:63], v[132:135]\n"
"v_cvt_pk_bf16_f32 v210, v136, v137\n"
"v_mfma_f32_16x16x32_bf16 v[132:135], a[144:147], v[64:67], v[132:135]\n"
"v_cvt_pk_bf16_f32 v211, v138, v139\n"
"v_mfma_f32_16x16x32_bf16 v[132:135], a[148:151], v[68:71], v[132:135]\n"
"v_mfma_f32_16x16x32_bf16 v[132:135], a[152:155], v[84:87], v[132:135]\n"
"v_mfma_f32_16x16x32_bf16 v[132:135], a[156:159], v[88:91], v[132:135]\n"
"v_mfma_f32_16x16x32_bf16 v[132:135], a[160:163], v[92:95], v[132:135]\n"
"v_mfma_f32_16x16x32_bf16 v[132:135], a[164:167], v[108:111], v[132:135]\n"
"global_store_dwordx4 v252, v[220:223], s[54:55] offset:768 sc1\n"
"ds_write_b64 v244, v[210:211] offset:12296\n"
"v_mfma_f32_16x16x32_bf16 v[132:135], a[168:171], v[112:115], v[132:135]\n"
"v_mfma_f32_16x16x32_bf16 v[132:135], a[172:175], v[116:119], v[132:135]\n"
"v_mfma_f32_16x16x32_bf16 v[136:139], a[220:223], v[116:119], v[12:15]\n"
"v_mfma_f32_16x16x32_bf16 v[136:139], a[216:219], v[112:115], v[136:139]\n"
"v_mfma_f32_16x16x32_bf16 v[136:139], a[212:215], v[108:111], v[136:139]\n"
"v_mfma_f32_16x16x32_bf16 v[136:139], a[208:211], v[92:95], v[136:139]\n"
"v_cvt_pk_bf16_f32 v212, v132, v133\n"
"v_mfma_f32_16x16x32_bf16 v[136:139], a[204:207], v[88:91], v[136:139]\n"
"v_mov_b32 v242, 34\n"
"v_mfma_f32_16x16x32_bf16 v[136:139], a[200:203], v[84:87], v[136:139]\n"
"v_cvt_pk_bf16_f32 v213, v134, v135\n"
"v_mfma_f32_16x16x32_bf16 v[136:139], a[196:199], v[68:71], v[136:139]\n"
"s_waitcnt vmcnt(11) lgkmcnt(2)\n"
"ds_write_b32 v240, v242\n"
"v_mfma_f32_16x16x32_bf16 v[136:139], a[192:195], v[64:67], v[136:139]\n"
"v_mfma_f32_16x16x32_bf16 v[136:139], a[188:191], v[60:63], v[136:139]\n"
"v_mfma_f32_16x16x32_bf16 v[136:139], a[184:187], v[44:47], v[136:139]\n"
"v_mfma_f32_16x16x32_bf16 v[136:139], a[180:183], v[40:43], v[136:139]\n"
"global_store_dwordx4 v253, v[224:227], s[54:55] offset:768 sc1\n"
"ds_write_b64 v245, v[212:213] offset:12288\n"
"v_mfma_f32_16x16x32_bf16 v[136:139], a[176:179], v[36:39], v[136:139]\n"
"v_mfma_f32_16x16x32_bf16 v[132:135], a[224:227], v[36:39], v[16:19]\n"
"v_mfma_f32_16x16x32_bf16 v[132:135], a[228:231], v[40:43], v[132:135]\n"
"v_mfma_f32_16x16x32_bf16 v[132:135], a[232:235], v[44:47], v[132:135]\n"
"ds_read_b32 v243, v241\n"
"v_mfma_f32_16x16x32_bf16 v[132:135], a[236:239], v[60:63], v[132:135]\n"
"v_cvt_pk_bf16_f32 v214, v136, v137\n"
"v_mfma_f32_16x16x32_bf16 v[132:135], a[0:3], v[64:67], v[132:135]\n"
"v_cvt_pk_bf16_f32 v215, v138, v139\n"
"v_mfma_f32_16x16x32_bf16 v[132:135], a[4:7], v[68:71], v[132:135]\n"
"v_mfma_f32_16x16x32_bf16 v[132:135], a[8:11], v[84:87], v[132:135]\n"
"s_waitcnt lgkmcnt(0)\n"
"v_cmp_gt_u32 s[56:57], 34, v243\n"
"v_mfma_f32_16x16x32_bf16 v[132:135], a[12:15], v[88:91], v[132:135]\n"
"v_mfma_f32_16x16x32_bf16 v[132:135], a[16:19], v[92:95], v[132:135]\n"
"s_or_b64 s[56:57], s[56:57], vcc\n"
"s_cbranch_scc1 LB34E\n"
"LB34ER:\n"
"v_mfma_f32_16x16x32_bf16 v[132:135], a[20:23], v[108:111], v[132:135]\n"
"ds_write_b64 v245, v[214:215] offset:12296\n"
"v_mfma_f32_16x16x32_bf16 v[132:135], v[140:143], v[112:115], v[132:135]\n"
"v_mfma_f32_16x16x32_bf16 v[132:135], v[156:159], v[116:119], v[132:135]\n"
"v_mfma_f32_16x16x32_bf16 v[136:139], a[44:47], v[116:119], v[20:23]\n"
"ds_read_b128 v[116:119], v235 offset:40960\n"
"v_mfma_f32_16x16x32_bf16 v[136:139], a[40:43], v[112:115], v[136:139]\n"
"ds_read_b128 v[112:115], v234 offset:40960\n"
"v_mfma_f32_16x16x32_bf16 v[136:139], a[36:39], v[108:111], v[136:139]\n"
"ds_read_b128 v[108:111], v233 offset:40960\n"
"v_mfma_f32_16x16x32_bf16 v[136:139], a[32:35], v[92:95], v[136:139]\n"
"ds_read_b128 v[92:95], v232 offset:40960\n"
"v_cvt_pk_bf16_f32 v208, v132, v133\n"
"v_mfma_f32_16x16x32_bf16 v[136:139], a[28:31], v[88:91], v[136:139]\n"
"ds_read_b128 v[88:91], v235 offset:32768\n"
"v_cvt_pk_bf16_f32 v209, v134, v135\n"
"v_mfma_f32_16x16x32_bf16 v[136:139], a[24:27], v[84:87], v[136:139]\n"
"ds_read_b128 v[84:87], v234 offset:32768\n"
"v_mfma_f32_16x16x32_bf16 v[136:139], v[204:207], v[68:71], v[136:139]\n"
"ds_read_b128 v[68:71], v233 offset:32768\n"
"v_mfma_f32_16x16x32_bf16 v[136:139], v[188:191], v[64:67], v[136:139]\n"
"ds_read_b128 v[64:67], v232 offset:32768\n"
"v_mfma_f32_16x16x32_bf16 v[136:139], v[184:187], v[60:63], v[136:139]\n"
"ds_read_b128 v[60:63], v235 offset:24576\n"
"v_mfma_f32_16x16x32_bf16 v[136:139], v[180:183], v[44:47], v[136:139]\n"
"ds_read_b128 v[44:47], v234 offset:24576\n"
"v_mfma_f32_16x16x32_bf16 v[136:139], v[164:167], v[40:43], v[136:139]\n"
"ds_read_b128 v[40:43], v233 offset:24576\n"
"v_mfma_f32_16x16x32_bf16 v[136:139], v[160:163], v[36:39], v[136:139]\n"
"ds_read_b128 v[36:39], v232 offset:24576\n"
"s_waitcnt lgkmcnt(8)\n"
"v_mfma_f32_16x16x32_bf16 v[132:135], a[76:79], v[116:119], v[0:3]\n"
"ds_write_b64 v244, v[208:209] offset:12416\n"
"v_mfma_f32_16x16x32_bf16 v[132:135], a[72:75], v[112:115], v[132:135]\n"
"s_add_u32 s54, s26, s48\n"
"s_addc_u32 s55, s27, 0\n"
"v_mfma_f32_16x16x32_bf16 v[132:135], a[68:71], v[108:111], v[132:135]\n"
"v_mfma_f32_16x16x32_bf16 v[132:135], a[64:67], v[92:95], v[132:135]\n"
"v_cvt_pk_bf16_f32 v210, v136, v137\n"
"s_waitcnt lgkmcnt(5)\n"
"v_mfma_f32_16x16x32_bf16 v[132:135], a[60:63], v[88:91], v[132:135]\n"
"v_cvt_pk_bf16_f32 v211, v138, v139\n"
"v_mfma_f32_16x16x32_bf16 v[132:135], a[56:59], v[84:87], v[132:135]\n"
"v_mfma_f32_16x16x32_bf16 v[132:135], a[52:55], v[68:71], v[132:135]\n"
"ds_read_b128 v[216:219], v246 offset:0\n"
"v_mfma_f32_16x16x32_bf16 v[132:135], a[48:51], v[64:67], v[132:135]\n"
"ds_read_b128 v[220:223], v246 offset:1024\n"
"s_waitcnt lgkmcnt(3)\n"
"v_mfma_f32_16x16x32_bf16 v[132:135], a[252:255], v[60:63], v[132:135]\n"
"ds_read_b128 v[224:227], v246 offset:2048\n"
"v_mfma_f32_16x16x32_bf16 v[132:135], a[248:251], v[44:47], v[132:135]\n"
"ds_write_b64 v244, v[210:211] offset:12424\n"
"v_mfma_f32_16x16x32_bf16 v[132:135], a[244:247], v[40:43], v[132:135]\n"
"v_mfma_f32_16x16x32_bf16 v[132:135], a[240:243], v[36:39], v[132:135]\n"
"v_mfma_f32_16x16x32_bf16 v[136:139], a[80:83], v[36:39], v[4:7]\n"
"v_mfma_f32_16x16x32_bf16 v[136:139], a[84:87], v[40:43], v[136:139]\n"
"v_mfma_f32_16x16x32_bf16 v[136:139], a[88:91], v[44:47], v[136:139]\n"
"v_mfma_f32_16x16x32_bf16 v[136:139], a[92:95], v[60:63], v[136:139]\n"
"v_cvt_pk_bf16_f32 v208, v132, v133\n"
"v_mfma_f32_16x16x32_bf16 v[136:139], a[96:99], v[64:67], v[136:139]\n"
"v_cvt_pk_bf16_f32 v209, v134, v135\n"
"v_mfma_f32_16x16x32_bf16 v[136:139], a[100:103], v[68:71], v[136:139]\n"
"v_mfma_f32_16x16x32_bf16 v[136:139], a[104:107], v[84:87], v[136:139]\n"
"v_mfma_f32_16x16x32_bf16 v[136:139], a[108:111], v[88:91], v[136:139]\n"
"s_waitcnt lgkmcnt(1)\n"
"v_mfma_f32_16x16x32_bf16 v[136:139], a[112:115], v[92:95], v[136:139]\n"
"v_mfma_f32_16x16x32_bf16 v[136:139], a[116:119], v[108:111], v[136:139]\n"
"global_store_dwordx4 v247, v[216:219], s[54:55] offset:0 sc1\n"
"ds_write_b64 v244, v[208:209] offset:24576\n"
"v_mfma_f32_16x16x32_bf16 v[136:139], a[120:123], v[112:115], v[136:139]\n"
"v_mfma_f32_16x16x32_bf16 v[136:139], a[124:127], v[116:119], v[136:139]\n"
"v_mfma_f32_16x16x32_bf16 v[132:135], a[172:175], v[116:119], v[8:11]\n"
"v_mfma_f32_16x16x32_bf16 v[132:135], a[168:171], v[112:115], v[132:135]\n"
"v_mfma_f32_16x16x32_bf16 v[132:135], a[164:167], v[108:111], v[132:135]\n"
"v_mfma_f32_16x16x32_bf16 v[132:135], a[160:163], v[92:95], v[132:135]\n"
"v_cvt_pk_bf16_f32 v210, v136, v137\n"
"v_mfma_f32_16x16x32_bf16 v[132:135], a[156:159], v[88:91], v[132:135]\n"
"v_cvt_pk_bf16_f32 v211, v138, v139\n"
"v_mfma_f32_16x16x32_bf16 v[132:135], a[152:155], v[84:87], v[132:135]\n"
"v_mfma_f32_16x16x32_bf16 v[132:135], a[148:151], v[68:71], v[132:135]\n"
"v_mfma_f32_16x16x32_bf16 v[132:135], a[144:147], v[64:67], v[132:135]\n"
"v_mfma_f32_16x16x32_bf16 v[132:135], a[140:143], v[60:63], v[132:135]\n"
"v_mfma_f32_16x16x32_bf16 v[132:135], a[136:139], v[44:47], v[132:135]\n"
"global_store_dwordx4 v252, v[220:223], s[54:55] offset:0 sc1\n"
"ds_write_b64 v244, v[210:211] offset:24584\n"
"v_mfma_f32_16x16x32_bf16 v[132:135], a[132:135], v[40:43], v[132:135]\n"
"v_mfma_f32_16x16x32_bf16 v[132:135], a[128:131], v[36:39], v[132:135]\n"
"v_mfma_f32_16x16x32_bf16 v[136:139], a[176:179], v[36:39], v[12:15]\n"
"v_mfma_f32_16x16x32_bf16 v[136:139], a[180:183], v[40:43], v[136:139]\n"
"v_mfma_f32_16x16x32_bf16 v[136:139], a[184:187], v[44:47], v[136:139]\n"
"v_mfma_f32_16x16x32_bf16 v[136:139], a[188:191], v[60:63], v[136:139]\n"
"v_cvt_pk_bf16_f32 v212, v132, v133\n"
"v_mfma_f32_16x16x32_bf16 v[136:139], a[192:195], v[64:67], v[136:139]\n"
"v_mov_b32 v242, 35\n"
"v_mfma_f32_16x16x32_bf16 v[136:139], a[196:199], v[68:71], v[136:139]\n"
"v_cvt_pk_bf16_f32 v213, v134, v135\n"
"v_mfma_f32_16x16x32_bf16 v[136:139], a[200:203], v[84:87], v[136:139]\n"
"s_waitcnt lgkmcnt(2)\n"
"ds_write_b32 v240, v242\n"
"v_mfma_f32_16x16x32_bf16 v[136:139], a[204:207], v[88:91], v[136:139]\n"
"v_mfma_f32_16x16x32_bf16 v[136:139], a[208:211], v[92:95], v[136:139]\n"
"v_mfma_f32_16x16x32_bf16 v[136:139], a[212:215], v[108:111], v[136:139]\n"
"v_mfma_f32_16x16x32_bf16 v[136:139], a[216:219], v[112:115], v[136:139]\n"
"global_store_dwordx4 v253, v[224:227], s[54:55] offset:0 sc1\n"
"ds_write_b64 v245, v[212:213] offset:24576\n"
"v_mfma_f32_16x16x32_bf16 v[136:139], a[220:223], v[116:119], v[136:139]\n"
"v_mfma_f32_16x16x32_bf16 v[132:135], v[156:159], v[116:119], v[16:19]\n"
"v_mfma_f32_16x16x32_bf16 v[132:135], v[140:143], v[112:115], v[132:135]\n"
"v_mfma_f32_16x16x32_bf16 v[132:135], a[20:23], v[108:111], v[132:135]\n"
"ds_read_b32 v243, v241\n"
"v_mfma_f32_16x16x32_bf16 v[132:135], a[16:19], v[92:95], v[132:135]\n"
"v_cvt_pk_bf16_f32 v214, v136, v137\n"
"v_mfma_f32_16x16x32_bf16 v[132:135], a[12:15], v[88:91], v[132:135]\n"
"v_cvt_pk_bf16_f32 v215, v138, v139\n"
"v_mfma_f32_16x16x32_bf16 v[132:135], a[8:11], v[84:87], v[132:135]\n"
"v_mfma_f32_16x16x32_bf16 v[132:135], a[4:7], v[68:71], v[132:135]\n"
"s_waitcnt lgkmcnt(0)\n"
"v_cmp_gt_u32 s[56:57], 35, v243\n"
"v_mfma_f32_16x16x32_bf16 v[132:135], a[0:3], v[64:67], v[132:135]\n"
"v_mfma_f32_16x16x32_bf16 v[132:135], a[236:239], v[60:63], v[132:135]\n"
"s_cmp_lg_u64 s[56:57], 0\n"
"s_cbranch_scc1 LB35E\n"
"LB35ER:\n"
"v_mfma_f32_16x16x32_bf16 v[132:135], a[232:235], v[44:47], v[132:135]\n"
"ds_write_b64 v245, v[214:215] offset:24584\n"
"v_mfma_f32_16x16x32_bf16 v[132:135], a[228:231], v[40:43], v[132:135]\n"
"v_mfma_f32_16x16x32_bf16 v[132:135], a[224:227], v[36:39], v[132:135]\n"
"v_mfma_f32_16x16x32_bf16 v[136:139], v[160:163], v[36:39], v[20:23]\n"
"ds_read_b128 v[36:39], v232 offset:24832\n"
"v_mfma_f32_16x16x32_bf16 v[136:139], v[164:167], v[40:43], v[136:139]\n"
"ds_read_b128 v[40:43], v233 offset:24832\n"
"v_mfma_f32_16x16x32_bf16 v[136:139], v[180:183], v[44:47], v[136:139]\n"
"ds_read_b128 v[44:47], v234 offset:24832\n"
"v_mfma_f32_16x16x32_bf16 v[136:139], v[184:187], v[60:63], v[136:139]\n"
"ds_read_b128 v[60:63], v235 offset:24832\n"
"v_cvt_pk_bf16_f32 v208, v132, v133\n"
"v_mfma_f32_16x16x32_bf16 v[136:139], v[188:191], v[64:67], v[136:139]\n"
"ds_read_b128 v[64:67], v232 offset:33024\n"
"v_cvt_pk_bf16_f32 v209, v134, v135\n"
"v_mfma_f32_16x16x32_bf16 v[136:139], v[204:207], v[68:71], v[136:139]\n"
"ds_read_b128 v[68:71], v233 offset:33024\n"
"v_mfma_f32_16x16x32_bf16 v[136:139], a[24:27], v[84:87], v[136:139]\n"
"ds_read_b128 v[84:87], v234 offset:33024\n"
"v_mfma_f32_16x16x32_bf16 v[136:139], a[28:31], v[88:91], v[136:139]\n"
"ds_read_b128 v[88:91], v235 offset:33024\n"
"v_mfma_f32_16x16x32_bf16 v[136:139], a[32:35], v[92:95], v[136:139]\n"
"ds_read_b128 v[92:95], v232 offset:41216\n"
"v_mfma_f32_16x16x32_bf16 v[136:139], a[36:39], v[108:111], v[136:139]\n"
"ds_read_b128 v[108:111], v233 offset:41216\n"
"v_mfma_f32_16x16x32_bf16 v[136:139], a[40:43], v[112:115], v[136:139]\n"
"ds_read_b128 v[112:115], v234 offset:41216\n"
"v_mfma_f32_16x16x32_bf16 v[136:139], a[44:47], v[116:119], v[136:139]\n"
"ds_read_b128 v[116:119], v235 offset:41216\n"
"s_waitcnt lgkmcnt(8)\n"
"v_mfma_f32_16x16x32_bf16 v[132:135], a[240:243], v[36:39], v[0:3]\n"
"ds_write_b64 v244, v[208:209] offset:24704\n"
"v_mfma_f32_16x16x32_bf16 v[132:135], a[244:247], v[40:43], v[132:135]\n"
"v_mfma_f32_16x16x32_bf16 v[132:135], a[248:251], v[44:47], v[132:135]\n"
"v_mfma_f32_16x16x32_bf16 v[132:135], a[252:255], v[60:63], v[132:135]\n"
"v_cvt_pk_bf16_f32 v210, v136, v137\n"
"s_waitcnt lgkmcnt(5)\n"
"v_mfma_f32_16x16x32_bf16 v[132:135], a[48:51], v[64:67], v[132:135]\n"
"v_cvt_pk_bf16_f32 v211, v138, v139\n"
"v_mfma_f32_16x16x32_bf16 v[132:135], a[52:55], v[68:71], v[132:135]\n"
"v_mfma_f32_16x16x32_bf16 v[132:135], a[56:59], v[84:87], v[132:135]\n"
"ds_read_b128 v[216:219], v246 offset:12288\n"
"v_mfma_f32_16x16x32_bf16 v[132:135], a[60:63], v[88:91], v[132:135]\n"
"ds_read_b128 v[220:223], v246 offset:13312\n"
"s_waitcnt lgkmcnt(3)\n"
"v_mfma_f32_16x16x32_bf16 v[132:135], a[64:67], v[92:95], v[132:135]\n"
"ds_read_b128 v[224:227], v246 offset:14336\n"
"v_mfma_f32_16x16x32_bf16 v[132:135], a[68:71], v[108:111], v[132:135]\n"
"ds_write_b64 v244, v[210:211] offset:24712\n"
"v_mfma_f32_16x16x32_bf16 v[132:135], a[72:75], v[112:115], v[132:135]\n"
"v_mfma_f32_16x16x32_bf16 v[132:135], a[76:79], v[116:119], v[132:135]\n"
"v_mfma_f32_16x16x32_bf16 v[136:139], a[124:127], v[116:119], v[4:7]\n"
"v_mfma_f32_16x16x32_bf16 v[136:139], a[120:123], v[112:115], v[136:139]\n"
"v_mfma_f32_16x16x32_bf16 v[136:139], a[116:119], v[108:111], v[136:139]\n"
"v_mfma_f32_16x16x32_bf16 v[136:139], a[112:115], v[92:95], v[136:139]\n"
"v_cvt_pk_bf16_f32 v208, v132, v133\n"
"v_mfma_f32_16x16x32_bf16 v[136:139], a[108:111], v[88:91], v[136:139]\n"
"v_cvt_pk_bf16_f32 v209, v134, v135\n"
"v_mfma_f32_16x16x32_bf16 v[136:139], a[104:107], v[84:87], v[136:139]\n"
"v_mfma_f32_16x16x32_bf16 v[136:139], a[100:103], v[68:71], v[136:139]\n"
"v_mfma_f32_16x16x32_bf16 v[136:139], a[96:99], v[64:67], v[136:139]\n"
"s_waitcnt lgkmcnt(1)\n"
"v_mfma_f32_16x16x32_bf16 v[136:139], a[92:95], v[60:63], v[136:139]\n"
"s_mov_b64 exec, s[10:11]\n"
"s_cbranch_execz LHT15\n"
"LHR15:\n"
"s_mov_b64 exec, -1\n"
"v_mfma_f32_16x16x32_bf16 v[136:139], a[88:91], v[44:47], v[136:139]\n"
"global_store_dwordx4 v247, v[216:219], s[54:55] offset:768 sc1\n"
"ds_write_b64 v244, v[208:209] offset:0\n"
"v_mfma_f32_16x16x32_bf16 v[136:139], a[84:87], v[40:43], v[136:139]\n"
"v_mfma_f32_16x16x32_bf16 v[136:139], a[80:83], v[36:39], v[136:139]\n"
"v_mfma_f32_16x16x32_bf16 v[132:135], a[128:131], v[36:39], v[8:11]\n"
"v_mfma_f32_16x16x32_bf16 v[132:135], a[132:135], v[40:43], v[132:135]\n"
"v_mfma_f32_16x16x32_bf16 v[132:135], a[136:139], v[44:47], v[132:135]\n"
"v_mfma_f32_16x16x32_bf16 v[132:135], a[140:143], v[60:63], v[132:135]\n"
"v_cvt_pk_bf16_f32 v210, v136, v137\n"
"v_mfma_f32_16x16x32_bf16 v[132:135], a[144:147], v[64:67], v[132:135]\n"
"v_cvt_pk_bf16_f32 v211, v138, v139\n"
"v_mfma_f32_16x16x32_bf16 v[132:135], a[148:151], v[68:71], v[132:135]\n"
"v_mfma_f32_16x16x32_bf16 v[132:135], a[152:155], v[84:87], v[132:135]\n"
"v_mfma_f32_16x16x32_bf16 v[132:135], a[156:159], v[88:91], v[132:135]\n"
"v_mfma_f32_16x16x32_bf16 v[132:135], a[160:163], v[92:95], v[132:135]\n"
"v_mfma_f32_16x16x32_bf16 v[132:135], a[164:167], v[108:111], v[132:135]\n"
"global_store_dwordx4 v252, v[220:223], s[54:55] offset:768 sc1\n"
"ds_write_b64 v244, v[210:211] offset:8\n"
"v_mfma_f32_16x16x32_bf16 v[132:135], a[168:171], v[112:115], v[132:135]\n"
"v_mfma_f32_16x16x32_bf16 v[132:135], a[172:175], v[116:119], v[132:135]\n"
"v_mfma_f32_16x16x32_bf16 v[136:139], a[220:223], v[116:119], v[12:15]\n"
"v_mfma_f32_16x16x32_bf16 v[136:139], a[216:219], v[112:115], v[136:139]\n"
"v_mfma_f32_16x16x32_bf16 v[136:139], a[212:215], v[108:111], v[136:139]\n"
"v_mfma_f32_16x16x32_bf16 v[136:139], a[208:211], v[92:95], v[136:139]\n"
"v_cvt_pk_bf16_f32 v212, v132, v133\n"
"v_mfma_f32_16x16x32_bf16 v[136:139], a[204:207], v[88:91], v[136:139]\n"
"v_mov_b32 v242, 36\n"
"v_mfma_f32_16x16x32_bf16 v[136:139], a[200:203], v[84:87], v[136:139]\n"
"v_cvt_pk_bf16_f32 v213, v134, v135\n"
"v_mfma_f32_16x16x32_bf16 v[136:139], a[196:199], v[68:71], v[136:139]\n"
"s_waitcnt lgkmcnt(2)\n"
"ds_write_b32 v240, v242\n"
"v_mfma_f32_16x16x32_bf16 v[136:139], a[192:195], v[64:67], v[136:139]\n"
"v_mfma_f32_16x16x32_bf16 v[136:139], a[188:191], v[60:63], v[136:139]\n"
"v_mfma_f32_16x16x32_bf16 v[136:139], a[184:187], v[44:47], v[136:139]\n"
"v_mfma_f32_16x16x32_bf16 v[136:139], a[180:183], v[40:43], v[136:139]\n"
"global_store_dwordx4 v253, v[224:227], s[54:55] offset:768 sc1\n"
"ds_write_b64 v245, v[212:213] offset:0\n"
"v_mfma_f32_16x16x32_bf16 v[136:139], a[176:179], v[36:39], v[136:139]\n"
"v_mfma_f32_16x16x32_bf16 v[132:135], a[224:227], v[36:39], v[16:19]\n"
"v_mfma_f32_16x16x32_bf16 v[132:135], a[228:231], v[40:43], v[132:135]\n"
"v_mfma_f32_16x16x32_bf16 v[132:135], a[232:235], v[44:47], v[132:135]\n"
"ds_read_b32 v243, v241\n"
"v_mfma_f32_16x16x32_bf16 v[132:135], a[236:239], v[60:63], v[132:135]\n"
"v_cvt_pk_bf16_f32 v214, v136, v137\n"
"v_mfma_f32_16x16x32_bf16 v[132:135], a[0:3], v[64:67], v[132:135]\n"
"v_cvt_pk_bf16_f32 v215, v138, v139\n"
"v_mfma_f32_16x16x32_bf16 v[132:135], a[4:7], v[68:71], v[132:135]\n"
"v_mfma_f32_16x16x32_bf16 v[132:135], a[8:11], v[84:87], v[132:135]\n"
"s_waitcnt lgkmcnt(0)\n"
"v_cmp_gt_u32 s[56:57], 36, v243\n"
"v_mfma_f32_16x16x32_bf16 v[132:135], a[12:15], v[88:91], v[132:135]\n"
"v_mfma_f32_16x16x32_bf16 v[132:135], a[16:19], v[92:95], v[132:135]\n"
"s_cmp_lg_u64 s[56:57], 0\n"
"s_cbranch_scc1 LB36E\n"
"LB36ER:\n"
"v_mfma_f32_16x16x32_bf16 v[132:135], a[20:23], v[108:111], v[132:135]\n"
"ds_write_b64 v245, v[214:215] offset:8\n"
"v_mfma_f32_16x16x32_bf16 v[132:135], v[140:143], v[112:115], v[132:135]\n"
"v_mfma_f32_16x16x32_bf16 v[132:135], v[156:159], v[116:119], v[132:135]\n"
"v_mfma_f32_16x16x32_bf16 v[136:139], a[44:47], v[116:119], v[20:23]\n"
"v_mfma_f32_16x16x32_bf16 v[136:139], a[40:43], v[112:115], v[136:139]\n"
"v_mfma_f32_16x16x32_bf16 v[136:139], a[36:39], v[108:111], v[136:139]\n"
"v_mfma_f32_16x16x32_bf16 v[136:139], a[32:35], v[92:95], v[136:139]\n"
"v_cvt_pk_bf16_f32 v208, v132, v133\n"
"v_mfma_f32_16x16x32_bf16 v[136:139], a[28:31], v[88:91], v[136:139]\n"
"v_cvt_pk_bf16_f32 v209, v134, v135\n"
"v_mfma_f32_16x16x32_bf16 v[136:139], a[24:27], v[84:87], v[136:139]\n"
"v_mfma_f32_16x16x32_bf16 v[136:139], v[204:207], v[68:71], v[136:139]\n"
"v_mfma_f32_16x16x32_bf16 v[136:139], v[188:191], v[64:67], v[136:139]\n"
"v_mfma_f32_16x16x32_bf16 v[136:139], v[184:187], v[60:63], v[136:139]\n"
"v_mfma_f32_16x16x32_bf16 v[136:139], v[180:183], v[44:47], v[136:139]\n"
"ds_write_b64 v244, v[208:209] offset:128\n"
"v_mfma_f32_16x16x32_bf16 v[136:139], v[164:167], v[40:43], v[136:139]\n"
"LHT15:\n"
"s_cbranch_execz LHR15\n"
"v_mfma_f32_16x16x32_bf16 v[136:139], v[160:163], v[36:39], v[136:139]\n"
"s_add_u32 s54, s26, s49\n"
"s_addc_u32 s55, s27, 0\n"
"ds_read_b128 v[216:219], v246 offset:24576\n"
"ds_read_b128 v[220:223], v246 offset:25600\n"
"ds_read_b128 v[224:227], v246 offset:26624\n"
"s_nop 3\n"
"v_cvt_pk_bf16_f32 v210, v136, v137\n"
"v_cvt_pk_bf16_f32 v211, v138, v139\n"
"ds_write_b128 v244, v[208:211] offset:128\n"
"s_waitcnt lgkmcnt(3)\n"
"global_store_dwordx4 v247, v[216:219], s[54:55] offset:0 sc1\n"
"s_waitcnt lgkmcnt(2)\n"
"global_store_dwordx4 v252, v[220:223], s[54:55] offset:0 sc1\n"
"s_waitcnt lgkmcnt(1)\n"
"global_store_dwordx4 v253, v[224:227], s[54:55] offset:0 sc1\n"
"s_waitcnt lgkmcnt(0)\n"
"s_barrier\n"
"ds_read_b128 v[216:219], v246 offset:0\n"
"ds_read_b128 v[220:223], v246 offset:1024\n"
"ds_read_b128 v[224:227], v246 offset:2048\n"
"s_waitcnt lgkmcnt(2)\n"
"global_store_dwordx4 v247, v[216:219], s[54:55] offset:768 sc1\n"
"s_waitcnt lgkmcnt(1)\n"
"global_store_dwordx4 v252, v[220:223], s[54:55] offset:768 sc1\n"
"s_waitcnt lgkmcnt(0)\n"
"global_store_dwordx4 v253, v[224:227], s[54:55] offset:768 sc1\n"
"s_branch LFIN\n"
"LB2_0:\n"
"ds_read_b32 v243, v241\n"
"s_waitcnt lgkmcnt(0)\n"
"v_cmp_gt_u32 s[56:57], 2, v243\n"
"s_nop 3\n"
"s_cmp_lg_u64 s[56:57], 0\n"
"s_cbranch_scc1 LB2_0\n"
"s_branch LB2_0R\n"
"LB3:\n"
"s_cbranch_vccz LB3C\n"
"s_sleep 7\n"
"LB3C:\n"
"ds_read_b32 v243, v241\n"
"s_waitcnt lgkmcnt(0)\n"
"v_cmp_gt_u32 s[56:57], 3, v243\n"
"s_nop 3\n"
"s_cmp_lg_u64 s[56:57], 0\n"
"s_cbranch_scc1 LB3C\n"
"s_branch LB3R\n"
"LB4:\n"
"s_cbranch_vccz LB4C\n"
"s_sleep 7\n"
"LB4C:\n"
"ds_read_b32 v243, v241\n"
"s_waitcnt lgkmcnt(0)\n"
"v_cmp_gt_u32 s[56:57], 4, v243\n"
"s_nop 3\n"
"s_cmp_lg_u64 s[56:57], 0\n"
"s_cbranch_scc1 LB4C\n"
"s_branch LB4R\n"
"LB5:\n"
"s_cbranch_vccz LB5C\n"
"s_sleep 7\n"
"LB5C:\n"
"ds_read_b32 v243, v241\n"
"s_waitcnt lgkmcnt(0)\n"
"v_cmp_gt_u32 s[56:57], 5, v243\n"
"s_nop 3\n"
"s_cmp_lg_u64 s[56:57], 0\n"
"s_cbranch_scc1 LB5C\n"
"s_branch LB5R\n"
"LB6:\n"
"s_cbranch_vccz LB6C\n"
"s_sleep 7\n"
"LB6C:\n"
"ds_read_b32 v243, v241\n"
"s_waitcnt lgkmcnt(0)\n"
"v_cmp_gt_u32 s[56:57], 6, v243\n"
"s_nop 3\n"
"s_cmp_lg_u64 s[56:57], 0\n"
"s_cbranch_scc1 LB6C\n"
"s_branch LB6R\n"
"LB7:\n"
"s_cbranch_vccz LB7C\n"
"s_sleep 7\n"
"LB7C:\n"
"ds_read_b32 v243, v241\n"
"s_waitcnt lgkmcnt(0)\n"
"v_cmp_gt_u32 s[56:57], 7, v243\n"
"s_nop 3\n"
"s_cmp_lg_u64 s[56:57], 0\n"
"s_cbranch_scc1 LB7C\n"
"s_branch LB7R\n"
"LB8:\n"
"s_cbranch_vccz LB8C\n"
"s_sleep 7\n"
"LB8C:\n"
"ds_read_b32 v243, v241\n"
"s_waitcnt lgkmcnt(0)\n"
"v_cmp_gt_u32 s[56:57], 8, v243\n"
"s_nop 3\n"
"s_cmp_lg_u64 s[56:57], 0\n"
"s_cbranch_scc1 LB8C\n"
"s_branch LB8R\n"
"LB9:\n"
"s_cbranch_vccz LB9C\n"
"s_sleep 7\n"
"LB9C:\n"
"ds_read_b32 v243, v241\n"
"s_waitcnt lgkmcnt(0)\n"
"v_cmp_gt_u32 s[56:57], 9, v243\n"
"s_nop 3\n"
"s_cmp_lg_u64 s[56:57], 0\n"
"s_cbranch_scc1 LB9C\n"
"s_branch LB9R\n"
"LB10:\n"
"s_cbranch_vccz LB10C\n"
"s_sleep 7\n"
"LB10C:\n"
"ds_read_b32 v243, v241\n"
"s_waitcnt lgkmcnt(0)\n"
"v_cmp_gt_u32 s[56:57], 10, v243\n"
"s_nop 3\n"
"s_cmp_lg_u64 s[56:57], 0\n"
"s_cbranch_scc1 LB10C\n"
"s_branch LB10R\n"
"LB11:\n"
"s_cbranch_vccz LB11C\n"
"s_sleep 7\n"
"LB11C:\n"
"ds_read_b32 v243, v241\n"
"s_waitcnt lgkmcnt(0)\n"
"v_cmp_gt_u32 s[56:57], 11, v243\n"
"s_nop 3\n"
"s_cmp_lg_u64 s[56:57], 0\n"
"s_cbranch_scc1 LB11C\n"
"s_branch LB11R\n"
"LB12:\n"
"s_cbranch_vccz LB12C\n"
"s_sleep 7\n"
"LB12C:\n"
"ds_read_b32 v243, v241\n"
"s_waitcnt lgkmcnt(0)\n"
"v_cmp_gt_u32 s[56:57], 12, v243\n"
"s_nop 3\n"
"s_cmp_lg_u64 s[56:57], 0\n"
"s_cbranch_scc1 LB12C\n"
"s_branch LB12R\n"
"LB13:\n"
"s_cbranch_vccz LB13C\n"
"s_sleep 7\n"
"LB13C:\n"
"ds_read_b32 v243, v241\n"
"s_waitcnt lgkmcnt(0)\n"
"v_cmp_gt_u32 s[56:57], 13, v243\n"
"s_nop 3\n"
"s_cmp_lg_u64 s[56:57], 0\n"
"s_cbranch_scc1 LB13C\n"
"s_branch LB13R\n"
"LB14:\n"
"s_cbranch_vccz LB14C\n"
"s_sleep 7\n"
"LB14C:\n"
"ds_read_b32 v243, v241\n"
"s_waitcnt lgkmcnt(0)\n"
"v_cmp_gt_u32 s[56:57], 14, v243\n"
"s_nop 3\n"
"s_cmp_lg_u64 s[56:57], 0\n"
"s_cbranch_scc1 LB14C\n"
"s_branch LB14R\n"
"LB15:\n"
"s_cbranch_vccz LB15C\n"
"s_sleep 7\n"
"LB15C:\n"
"ds_read_b32 v243, v241\n"
"s_waitcnt lgkmcnt(0)\n"
"v_cmp_gt_u32 s[56:57], 15, v243\n"
"s_nop 3\n"
"s_cmp_lg_u64 s[56:57], 0\n"
"s_cbranch_scc1 LB15C\n"
"s_branch LB15R\n"
"LB16:\n"
"s_cbranch_vccz LB16C\n"
"s_sleep 7\n"
"LB16C:\n"
"ds_read_b32 v243, v241\n"
"s_waitcnt lgkmcnt(0)\n"
"v_cmp_gt_u32 s[56:57], 16, v243\n"
"s_nop 3\n"
"s_cmp_lg_u64 s[56:57], 0\n"
"s_cbranch_scc1 LB16C\n"
"s_branch LB16R\n"
"LB17:\n"
"s_cbranch_vccz LB17C\n"
"s_sleep 7\n"
"LB17C:\n"
"ds_read_b32 v243, v241\n"
"s_waitcnt lgkmcnt(0)\n"
"v_cmp_gt_u32 s[56:57], 17, v243\n"
"s_nop 3\n"
"s_cmp_lg_u64 s[56:57], 0\n"
"s_cbranch_scc1 LB17C\n"
"s_branch LB17R\n"
"LB18:\n"
"s_cbranch_vccz LB18C\n"
"s_sleep 7\n"
"LB18C:\n"
"ds_read_b32 v243, v241\n"
"s_waitcnt lgkmcnt(0)\n"
"v_cmp_gt_u32 s[56:57], 18, v243\n"
"s_nop 3\n"
"s_cmp_lg_u64 s[56:57], 0\n"
"s_cbranch_scc1 LB18C\n"
"s_branch LB18R\n"
"LB19:\n"
"s_cbranch_vccz LB19C\n"
"s_sleep 7\n"
"LB19C:\n"
"ds_read_b32 v243, v241\n"
"s_waitcnt lgkmcnt(0)\n"
"v_cmp_gt_u32 s[56:57], 19, v243\n"
"s_nop 3\n"
"s_cmp_lg_u64 s[56:57], 0\n"
"s_cbranch_scc1 LB19C\n"
"s_branch LB19R\n"
"LB20:\n"
"s_cbranch_vccz LB20C\n"
"s_sleep 7\n"
"LB20C:\n"
"ds_read_b32 v243, v241\n"
"s_waitcnt lgkmcnt(0)\n"
"v_cmp_gt_u32 s[56:57], 20, v243\n"
"s_nop 3\n"
"s_cmp_lg_u64 s[56:57], 0\n"
"s_cbranch_scc1 LB20C\n"
"s_branch LB20R\n"
"LB21:\n"
"s_cbranch_vccz LB21C\n"
"s_sleep 7\n"
"LB21C:\n"
"ds_read_b32 v243, v241\n"
"s_waitcnt lgkmcnt(0)\n"
"v_cmp_gt_u32 s[56:57], 21, v243\n"
"s_nop 3\n"
"s_cmp_lg_u64 s[56:57], 0\n"
"s_cbranch_scc1 LB21C\n"
"s_branch LB21R\n"
"LB22:\n"
"s_cbranch_vccz LB22C\n"
"s_sleep 7\n"
"LB22C:\n"
"ds_read_b32 v243, v241\n"
"s_waitcnt lgkmcnt(0)\n"
"v_cmp_gt_u32 s[56:57], 22, v243\n"
"s_nop 3\n"
"s_cmp_lg_u64 s[56:57], 0\n"
"s_cbranch_scc1 LB22C\n"
"s_branch LB22R\n"
"LB23:\n"
"s_cbranch_vccz LB23C\n"
"s_sleep 7\n"
"LB23C:\n"
"ds_read_b32 v243, v241\n"
"s_waitcnt lgkmcnt(0)\n"
"v_cmp_gt_u32 s[56:57], 23, v243\n"
"s_nop 3\n"
"s_cmp_lg_u64 s[56:57], 0\n"
"s_cbranch_scc1 LB23C\n"
"s_branch LB23R\n"
"LB24:\n"
"s_cbranch_vccz LB24C\n"
"s_sleep 7\n"
"LB24C:\n"
"ds_read_b32 v243, v241\n"
"s_waitcnt lgkmcnt(0)\n"
"v_cmp_gt_u32 s[56:57], 24, v243\n"
"s_nop 3\n"
"s_cmp_lg_u64 s[56:57], 0\n"
"s_cbranch_scc1 LB24C\n"
"s_branch LB24R\n"
"LB25:\n"
"s_cbranch_vccz LB25C\n"
"s_sleep 7\n"
"LB25C:\n"
"ds_read_b32 v243, v241\n"
"s_waitcnt lgkmcnt(0)\n"
"v_cmp_gt_u32 s[56:57], 25, v243\n"
"s_nop 3\n"
"s_cmp_lg_u64 s[56:57], 0\n"
"s_cbranch_scc1 LB25C\n"
"s_branch LB25R\n"
"LB26:\n"
"s_cbranch_vccz LB26C\n"
"s_sleep 7\n"
"LB26C:\n"
"ds_read_b32 v243, v241\n"
"s_waitcnt lgkmcnt(0)\n"
"v_cmp_gt_u32 s[56:57], 26, v243\n"
"s_nop 3\n"
"s_cmp_lg_u64 s[56:57], 0\n"
"s_cbranch_scc1 LB26C\n"
"s_branch LB26R\n"
"LB27:\n"
"s_cbranch_vccz LB27C\n"
"s_sleep 7\n"
"LB27C:\n"
"ds_read_b32 v243, v241\n"
"s_waitcnt lgkmcnt(0)\n"
"v_cmp_gt_u32 s[56:57], 27, v243\n"
"s_nop 3\n"
"s_cmp_lg_u64 s[56:57], 0\n"
"s_cbranch_scc1 LB27C\n"
"s_branch LB27R\n"
"LB28:\n"
"s_cbranch_vccz LB28C\n"
"s_sleep 7\n"
"LB28C:\n"
"ds_read_b32 v243, v241\n"
"s_waitcnt lgkmcnt(0)\n"
"v_cmp_gt_u32 s[56:57], 28, v243\n"
"s_nop 3\n"
"s_cmp_lg_u64 s[56:57], 0\n"
"s_cbranch_scc1 LB28C\n"
"s_branch LB28R\n"
"LB29:\n"
"s_cbranch_vccz LB29C\n"
"s_sleep 7\n"
"LB29C:\n"
"ds_read_b32 v243, v241\n"
"s_waitcnt lgkmcnt(0)\n"
"v_cmp_gt_u32 s[56:57], 29, v243\n"
"s_nop 3\n"
"s_cmp_lg_u64 s[56:57], 0\n"
"s_cbranch_scc1 LB29C\n"
"s_branch LB29R\n"
"LB30:\n"
"s_cbranch_vccz LB30C\n"
"s_sleep 7\n"
"LB30C:\n"
"ds_read_b32 v243, v241\n"
"s_waitcnt lgkmcnt(0)\n"
"v_cmp_gt_u32 s[56:57], 30, v243\n"
"s_nop 3\n"
"s_cmp_lg_u64 s[56:57], 0\n"
"s_cbranch_scc1 LB30C\n"
"s_branch LB30R\n"
"LB31:\n"
"s_cbranch_vccz LB31C\n"
"s_sleep 7\n"
"LB31C:\n"
"ds_read_b32 v243, v241\n"
"s_waitcnt lgkmcnt(0)\n"
"v_cmp_gt_u32 s[56:57], 31, v243\n"
"s_nop 3\n"
"s_cmp_lg_u64 s[56:57], 0\n"
"s_cbranch_scc1 LB31C\n"
"s_branch LB31R\n"
"LB32:\n"
"s_cbranch_vccz LB32C\n"
"s_sleep 7\n"
"LB32C:\n"
"ds_read_b32 v243, v241\n"
"s_waitcnt lgkmcnt(0)\n"
"v_cmp_gt_u32 s[56:57], 32, v243\n"
"s_nop 3\n"
"s_cmp_lg_u64 s[56:57], 0\n"
"s_cbranch_scc1 LB32C\n"
"s_branch LB32R\n"
"LB33:\n"
"s_cbranch_vccz LB33C\n"
"s_sleep 7\n"
"LB33C:\n"
"ds_read_b32 v243, v241\n"
"s_waitcnt lgkmcnt(0)\n"
"v_cmp_gt_u32 s[56:57], 33, v243\n"
"s_nop 3\n"
"s_cmp_lg_u64 s[56:57], 0\n"
"s_cbranch_scc1 LB33C\n"
"s_branch LB33R\n"
"LB34:\n"
"s_cbranch_vccz LB34C\n"
"s_sleep 7\n"
"LB34C:\n"
"ds_read_b32 v243, v241\n"
"s_waitcnt lgkmcnt(0)\n"
"v_cmp_gt_u32 s[56:57], 34, v243\n"
"s_nop 3\n"
"s_cmp_lg_u64 s[56:57], 0\n"
"s_cbranch_scc1 LB34C\n"
"s_branch LB34R\n"
"LB35:\n"
"ds_read_b32 v243, v241\n"
"s_waitcnt lgkmcnt(0)\n"
"v_cmp_gt_u32 s[56:57], 35, v243\n"
"s_nop 3\n"
"s_cmp_lg_u64 s[56:57], 0\n"
"s_cbranch_scc1 LB35\n"
"s_branch LB35R\n"
"LB36:\n"
"ds_read_b32 v243, v241\n"
"s_waitcnt lgkmcnt(0)\n"
"v_cmp_gt_u32 s[56:57], 36, v243\n"
"s_nop 3\n"
"s_cmp_lg_u64 s[56:57], 0\n"
"s_cbranch_scc1 LB36\n"
"s_branch LB36R\n"
"LB2_1:\n"
"ds_read_b32 v243, v241\n"
"s_waitcnt lgkmcnt(0)\n"
"v_cmp_gt_u32 s[56:57], 2, v243\n"
"s_nop 3\n"
"s_cmp_lg_u64 s[56:57], 0\n"
"s_cbranch_scc1 LB2_1\n"
"s_branch LB2_1R\n"
"LB29E:\n"
"s_cbranch_vccz LB29EC\n"
"s_sleep 7\n"
"LB29EC:\n"
"ds_read_b32 v243, v241\n"
"s_waitcnt lgkmcnt(0)\n"
"v_cmp_gt_u32 s[56:57], 29, v243\n"
"s_nop 3\n"
"s_cmp_lg_u64 s[56:57], 0\n"
"s_cbranch_scc1 LB29EC\n"
"s_branch LB29ER\n"
"LB30E:\n"
"s_cbranch_vccz LB30EC\n"
"s_sleep 7\n"
"LB30EC:\n"
"ds_read_b32 v243, v241\n"
"s_waitcnt lgkmcnt(0)\n"
"v_cmp_gt_u32 s[56:57], 30, v243\n"
"s_nop 3\n"
"s_cmp_lg_u64 s[56:57], 0\n"
"s_cbranch_scc1 LB30EC\n"
"s_branch LB30ER\n"
"LB31E:\n"
"s_cbranch_vccz LB31EC\n"
"s_sleep 7\n"
"LB31EC:\n"
"ds_read_b32 v243, v241\n"
"s_waitcnt lgkmcnt(0)\n"
"v_cmp_gt_u32 s[56:57], 31, v243\n"
"s_nop 3\n"
"s_cmp_lg_u64 s[56:57], 0\n"
"s_cbranch_scc1 LB31EC\n"
"s_branch LB31ER\n"
"LB32E:\n"
"s_cbranch_vccz LB32EC\n"
"s_sleep 7\n"
"LB32EC:\n"
"ds_read_b32 v243, v241\n"
"s_waitcnt lgkmcnt(0)\n"
"v_cmp_gt_u32 s[56:57], 32, v243\n"
"s_nop 3\n"
"s_cmp_lg_u64 s[56:57], 0\n"
"s_cbranch_scc1 LB32EC\n"
"s_branch LB32ER\n"
"LB33E:\n"
"s_cbranch_vccz LB33EC\n"
"s_sleep 7\n"
"LB33EC:\n"
"ds_read_b32 v243, v241\n"
"s_waitcnt lgkmcnt(0)\n"
"v_cmp_gt_u32 s[56:57], 33, v243\n"
"s_nop 3\n"
"s_cmp_lg_u64 s[56:57], 0\n"
"s_cbranch_scc1 LB33EC\n"
"s_branch LB33ER\n"
"LB34E:\n"
"s_cbranch_vccz LB34EC\n"
"s_sleep 7\n"
"LB34EC:\n"
"ds_read_b32 v243, v241\n"
"s_waitcnt lgkmcnt(0)\n"
"v_cmp_gt_u32 s[56:57], 34, v243\n"
"s_nop 3\n"
"s_cmp_lg_u64 s[56:57], 0\n"
"s_cbranch_scc1 LB34EC\n"
"s_branch LB34ER\n"
"LB35E:\n"
"ds_read_b32 v243, v241\n"
"s_waitcnt lgkmcnt(0)\n"
"v_cmp_gt_u32 s[56:57], 35, v243\n"
"s_nop 3\n"
"s_cmp_lg_u64 s[56:57], 0\n"
"s_cbranch_scc1 LB35E\n"
"s_branch LB35ER\n"
"LB36E:\n"
"ds_read_b32 v243, v241\n"
"s_waitcnt lgkmcnt(0)\n"
"v_cmp_gt_u32 s[56:57], 36, v243\n"
"s_nop 3\n"
"s_cmp_lg_u64 s[56:57], 0\n"
"s_cbranch_scc1 LB36E\n"
"s_branch LB36ER\n"
"LCEND:\n"

      : "+{s[0:1]}"(ka), "+{s2}"(b), "+{v0}"(t)
      :
      : "memory", "scc", "vcc", "v1", "v2", "v3", "v4", "v5", "v6", "v7", "v8", "v9", "v10", "v11", "v12", "v13", "v14", "v15", "v16", "v17", "v18", "v19", "v20", "v21", "v22", "v23", "v24", "v25", "v26", "v27", "v28", "v29", "v30", "v31", "v32", "v33", "v34", "v35", "v36", "v37", "v38", "v39", "v40", "v41", "v42", "v43", "v44", "v45", "v46", "v47", "v48", "v49", "v50", "v51", "v52", "v53", "v54", "v55", "v56", "v57", "v58", "v59", "v60", "v61", "v62", "v63", "v64", "v65", "v66", "v67", "v68", "v69", "v70", "v71", "v72", "v73", "v74", "v75", "v76", "v77", "v78", "v79", "v80", "v81", "v82", "v83", "v84", "v85", "v86", "v87", "v88", "v89", "v90", "v91", "v92", "v93", "v94", "v95", "v96", "v97", "v98", "v99", "v100", "v101", "v102", "v103", "v104", "v105", "v106", "v107", "v108", "v109", "v110", "v111", "v112", "v113", "v114", "v115", "v116", "v117", "v118", "v119", "v120", "v121", "v122", "v123", "v124", "v125", "v126", "v127", "v128", "v129", "v130", "v131", "v132", "v133", "v134", "v135", "v136", "v137", "v138", "v139", "v140", "v141", "v142", "v143", "v144", "v145", "v146", "v147", "v148", "v149", "v150", "v151", "v152", "v153", "v154", "v155", "v156", "v157", "v158", "v159", "v160", "v161", "v162", "v163", "v164", "v165", "v166", "v167", "v168", "v169", "v170", "v171", "v172", "v173", "v174", "v175", "v176", "v177", "v178", "v179", "v180", "v181", "v182", "v183", "v184", "v185", "v186", "v187", "v188", "v189", "v190", "v191", "v192", "v193", "v194", "v195", "v196", "v197", "v198", "v199", "v200", "v201", "v202", "v203", "v204", "v205", "v206", "v207", "v208", "v209", "v210", "v211", "v212", "v213", "v214", "v215", "v216", "v217", "v218", "v219", "v220", "v221", "v222", "v223", "v224", "v225", "v226", "v227", "v228", "v229", "v230", "v231", "v232", "v233", "v234", "v235", "v236", "v237", "v238", "v239", "v240", "v241", "v242", "v243", "v244", "v245", "v246", "v247", "v248", "v249", "v250", "v251", "v252", "v253", "v254", "v255", "a0", "a1", "a2", "a3", "a4", "a5", "a6", "a7", "a8", "a9", "a10", "a11", "a12", "a13", "a14", "a15", "a16", "a17", "a18", "a19", "a20", "a21", "a22", "a23", "a24", "a25", "a26", "a27", "a28", "a29", "a30", "a31", "a32", "a33", "a34", "a35", "a36", "a37", "a38", "a39", "a40", "a41", "a42", "a43", "a44", "a45", "a46", "a47", "a48", "a49", "a50", "a51", "a52", "a53", "a54", "a55", "a56", "a57", "a58", "a59", "a60", "a61", "a62", "a63", "a64", "a65", "a66", "a67", "a68", "a69", "a70", "a71", "a72", "a73", "a74", "a75", "a76", "a77", "a78", "a79", "a80", "a81", "a82", "a83", "a84", "a85", "a86", "a87", "a88", "a89", "a90", "a91", "a92", "a93", "a94", "a95", "a96", "a97", "a98", "a99", "a100", "a101", "a102", "a103", "a104", "a105", "a106", "a107", "a108", "a109", "a110", "a111", "a112", "a113", "a114", "a115", "a116", "a117", "a118", "a119", "a120", "a121", "a122", "a123", "a124", "a125", "a126", "a127", "a128", "a129", "a130", "a131", "a132", "a133", "a134", "a135", "a136", "a137", "a138", "a139", "a140", "a141", "a142", "a143", "a144", "a145", "a146", "a147", "a148", "a149", "a150", "a151", "a152", "a153", "a154", "a155", "a156", "a157", "a158", "a159", "a160", "a161", "a162", "a163", "a164", "a165", "a166", "a167", "a168", "a169", "a170", "a171", "a172", "a173", "a174", "a175", "a176", "a177", "a178", "a179", "a180", "a181", "a182", "a183", "a184", "a185", "a186", "a187", "a188", "a189", "a190", "a191", "a192", "a193", "a194", "a195", "a196", "a197", "a198", "a199", "a200", "a201", "a202", "a203", "a204", "a205", "a206", "a207", "a208", "a209", "a210", "a211", "a212", "a213", "a214", "a215", "a216", "a217", "a218", "a219", "a220", "a221", "a222", "a223", "a224", "a225", "a226", "a227", "a228", "a229", "a230", "a231", "a232", "a233", "a234", "a235", "a236", "a237", "a238", "a239", "a240", "a241", "a242", "a243", "a244", "a245", "a246", "a247", "a248", "a249", "a250", "a251", "a252", "a253", "a254", "a255", "s3", "s4", "s5", "s6", "s7", "s8", "s9", "s10", "s11", "s12", "s13", "s14", "s15", "s16", "s17", "s18", "s19", "s20", "s21", "s22", "s23", "s24", "s25", "s26", "s27", "s28", "s29", "s30", "s31", "s32", "s33", "s34", "s35", "s36", "s37", "s38", "s39", "s40", "s41", "s42", "s43", "s44", "s45", "s46", "s47", "s48", "s49", "s50", "s51", "s52", "s53", "s54", "s55", "s56", "s57", "s58", "s59", "s60", "s61", "s62", "s63", "s64", "s65", "s66", "s67", "s68", "s69", "s70", "s71", "s72", "s73", "s74", "s75", "s76", "s77", "s78", "s79", "s80", "s81", "s82", "s83", "s84", "s85", "s86", "s87", "s88", "s89", "s90");
}

extern "C" void kernel_launch(void* const* d_in, const int* in_sizes, int n_in,
                              void* d_out, int out_size, void* d_ws, size_t ws_size,
                              hipStream_t stream) {
  (void)hipFuncSetAttribute((const void*)fpn_kernel, hipFuncAttributeMaxDynamicSharedMemorySize, 159808);
  fpn_kernel<<<256, 256, 159808, stream>>>((const char*)d_in[0], (const char*)d_in[1], (const char*)d_in[2],
                                                (char*)d_out, (char*)d_ws, (const char*)d_in[3]);
}
```

```cpp
#include <hip/hip_runtime.h>
#include <hip/hip_bf16.h>

__global__ __launch_bounds__(256, 1) __attribute__((aligned(4096))) void fpn_kernel(const char* cf, const char* pc, const char* w1, char* out, char* ws, const char* w2) {
  const void* ka = (const void*)__builtin_amdgcn_kernarg_segment_ptr();
  unsigned b = blockIdx.x, t = threadIdx.x;
  asm volatile(
"s_load_dwordx8 s[20:27], s[0:1], 0x0\n"
"s_load_dwordx2 s[28:29], s[0:1], 0x20\n"
"s_load_dwordx2 s[66:67], s[0:1], 0x28\n"
"s_memrealtime s[84:85]\n"
"v_readfirstlane_b32 s76, v0\n"
"v_and_b32 v1, 63, v0\n"
"s_mov_b32 s51, s2\n"
"s_lshr_b32 s5, s2, 3\n"
"s_and_b32 s98, s2, 3\n"
"s_lshl2_add_u32 s98, s5, s98\n"
"v_lshlrev_b32 v239, 4, v1\n"
"s_lshr_b32 s76, s76, 6\n"
"v_lshrrev_b32 v2, 4, v1\n"
"s_movk_i32 s4, 0x300\n"
"v_and_b32 v3, 15, v1\n"
"s_lshl_b32 s3, s76, 2\n"
"s_cmpk_lt_u32 s51, 144\n"
"s_cbranch_scc1 LU0\n"
"v_and_b32 v110, 7, v1\n"
"s_sub_u32 s94, s98, 72\n"
"s_and_b32 s13, s94, 1\n"
"s_andn2_b32 s94, s94, 1\n"
"v_and_b32 v111, 1, v1\n"
"s_bfe_u32 s15, s51, 0x10002\n"
"v_mul_u32_u24 v110, 0x4100, v110\n"
"s_mul_i32 s14, s15, 2048\n"
"s_lshl1_add_u32 s15, s94, s15\n"
"s_and_b32 s12, s94, 2\n"
"s_lshl_b32 s12, s12, 12\n"
"s_add_u32 s14, s14, s12\n"
"v_add_u32 v110, s14, v110\n"
"v_lshl_add_u32 v129, v111, 12, v110\n"
"s_lshl3_add_u32 s15, s15, 0x3000\n"
"v_xor_b32 v133, 0x2000, v129\n"
"v_mov_b32 v110, s15\n"
"s_lshl_b32 s94, s94, 21\n"
"v_lshl_add_u32 v128, s13, 10, v110\n"
"s_add_u32 s94, s94, 0x100000\n"
"s_cmp_lt_u32 s76, 1\n"
"s_cselect_b32 s100, 0x3, 0\n"
"s_cselect_b32 s12, 1, 0\n"
"s_mov_b32 s101, 0\n"
"s_mov_b32 s13, 0\n"
"v_mov_b32 v0, 0\n"
"s_waitcnt lgkmcnt(0)\n"
"s_mov_b32 s83, s84\n"
"s_bfe_u32 s15, s84, 0x50000\n"
"s_lshl_b32 s15, s15, 14\n"
"s_add_u32 s94, s94, s15\n"
"s_and_b32 s14, s20, 0x1fffff\n"
"s_sub_u32 s94, s94, s14\n"
"s_max_i32 s94, s94, 0\n"
"s_min_i32 s94, s94, 0x6bd8000\n"
"s_add_u32 s94, s94, s14\n"
"s_add_u32 s94, s94, 0x3fff\n"
"s_andn2_b32 s94, s94, 0x3fff\n"
"s_sub_u32 s94, s94, s14\n"
"s_bfe_u32 s15, s84, 0x30005\n"
"s_lshl_b32 s15, s15, 7\n"
"s_add_u32 s94, s94, s15\n"
"s_bitcmp1_b32 s98, 0\n"
"s_cselect_b64 s[16:17], s[26:27], s[20:21]\n"
"s_add_u32 s16, s16, s94\n"
"s_addc_u32 s17, s17, 0\n"
"s_mov_b64 exec, s[100:101]\n"
"global_load_dword v130, v129, s[16:17] nt\n"
"global_load_dword v131, v133, s[16:17] nt\n"
"s_mov_b64 exec, -1\n"
"s_barrier\n"
"global_load_dwordx2 v[252:253], v0, s[28:29] sc1\n"
"s_waitcnt vmcnt(2)\n"
"s_memtime s[52:53]\n"
"s_waitcnt vmcnt(1)\n"
"s_memtime s[54:55]\n"
"s_waitcnt vmcnt(0)\n"
"v_readfirstlane_b32 s80, v252\n"
"v_readfirstlane_b32 s81, v253\n"
"s_nop 3\n"
"s_add_u32 s80, s80, 0x7f4a7c15\n"
"s_addc_u32 s81, s81, 0x9e3779b9\n"
"v_mov_b32 v252, s80\n"
"v_mov_b32 v253, s81\n"
"s_waitcnt lgkmcnt(0)\n"
"s_sub_u32 s14, s54, s52\n"
"v_mov_b32 v130, s80\n"
"s_min_u32 s14, s14, 250\n"
"s_pack_lh_b32_b16 s14, s14, s81\n"
"v_mov_b32 v131, s14\n"
"s_mov_b64 exec, s[12:13]\n"
"global_store_dwordx2 v128, v[130:131], s[28:29] offset:0 sc1\n"
"s_mov_b64 exec, -1\n"
"s_lshr_b32 s9, s51, 3\n"
"s_lshl_b32 s9, s9, 2\n"
"s_add_u32 s9, s9, s76\n"
"s_sub_u32 s9, s9, 72\n"
"s_mul_i32 s10, s9, 12\n"
"v_min_u32 v106, 11, v1\n"
"s_mul_i32 s9, s9, 42\n"
"v_add_lshl_u32 v106, s10, v106, 7\n"
"v_min_u32 v105, 41, v1\n"
"v_add_u32 v105, s9, v105\n"
"v_min_u32 v105, 0x8ff, v105\n"
"v_lshlrev_b32 v105, 7, v105\n"
"v_bitop3_b32 v110, s3, v1, v2 bitop3:0x96\n"
"v_lshlrev_b32 v110, 4, v110\n"
"s_mul_i32 s6, s76, 0xc00\n"
"v_and_b32 v110, 0xf0, v110\n"
"s_lshl_b32 s31, s76, 10\n"
"v_add_u32 v109, s3, v2\n"
"v_mad_u32_u24 v236, v109, s4, v110\n"
"s_add_u32 s30, s31, 0x12000\n"
"s_mul_i32 s95, s98, 191\n"
"s_mul_i32 s5, s98, 0x6000\n"
"s_lshr_b32 s95, s95, 13\n"
"s_mul_i32 s95, s95, 0x900000\n"
"s_add_u32 s96, s95, 0x900000\n"
"s_cmp_eq_u32 s96, 0x1b00000\n"
"s_cselect_b32 s96, 0, s96\n"
"s_add_u32 s97, s96, 0x900000\n"
"s_cmp_eq_u32 s97, 0x1b00000\n"
"s_cselect_b32 s97, 0, s97\n"
"s_movk_i32 s7, 0x240\n"
"v_mul_u32_u24 v110, 0xab, v239\n"
"v_add_u32 v111, 0x400, v239\n"
"v_add_u32 v112, 0x800, v239\n"
"v_lshrrev_b32 v110, 15, v110\n"
"v_mul_u32_u24 v113, 0xab, v111\n"
"v_mul_u32_u24 v114, 0xab, v112\n"
"v_mad_u32_u24 v240, v110, s7, v239\n"
"v_lshrrev_b32 v113, 15, v113\n"
"v_lshrrev_b32 v114, 15, v114\n"
"s_mul_i32 s74, s76, 0x1800\n"
"v_mad_u32_u24 v241, v113, s7, v239\n"
"v_mad_u32_u24 v242, v114, s7, v239\n"
"s_mul_i32 s8, s76, 0xc0\n"
"s_getpc_b64 s[56:57]\n"
"LC0_1:\n"
"v_min_u32 v106, LCEND-LC0_1-4, v106\n"
"global_load_dword v106, v106, s[56:57]\n"
"s_add_u32 s64, s66, s8\n"
"s_addc_u32 s65, s67, 0\n"
"s_add_u32 m0, s74, 0x0\n"
"s_add_u32 s78, s64, 0x0\n"
"s_addc_u32 s79, s65, 0\n"
"global_load_lds_dwordx4 v240, s[78:79] offset:0\n"
"global_load_lds_dwordx4 v241, s[78:79] offset:1024\n"
"global_load_lds_dwordx4 v242, s[78:79] offset:2048\n"
"s_add_u32 m0, s74, 0xc00\n"
"s_add_u32 s78, s64, 0x3000\n"
"s_addc_u32 s79, s65, 0\n"
"global_load_lds_dwordx4 v240, s[78:79] offset:0\n"
"global_load_lds_dwordx4 v241, s[78:79] offset:1024\n"
"global_load_lds_dwordx4 v242, s[78:79] offset:2048\n"
"s_add_u32 m0, s74, 0x6000\n"
"s_add_u32 s78, s64, 0x6000\n"
"s_addc_u32 s79, s65, 0\n"
"global_load_lds_dwordx4 v240, s[78:79] offset:0\n"
"global_load_lds_dwordx4 v241, s[78:79] offset:1024\n"
"global_load_lds_dwordx4 v242, s[78:79] offset:2048\n"
"s_add_u32 m0, s74, 0x6c00\n"
"s_add_u32 s78, s64, 0x9000\n"
"s_addc_u32 s79, s65, 0\n"
"global_load_lds_dwordx4 v240, s[78:79] offset:0\n"
"global_load_lds_dwordx4 v241, s[78:79] offset:1024\n"
"global_load_lds_dwordx4 v242, s[78:79] offset:2048\n"
"s_branch LV1\n"
"LU0:\n"
"s_mul_i32 s9, s51, 0xab\n"
"v_lshrrev_b32 v110, 2, v1\n"
"s_lshr_b32 s9, s9, 11\n"
"v_and_b32 v111, 48, v239\n"
"s_mul_i32 s10, s9, 12\n"
"v_mad_u32_u24 v100, v110, s4, v111\n"
"s_sub_u32 s10, s51, s10\n"
"s_mul_i32 s11, s76, 0x12000\n"
"s_lshl_b32 s12, s9, 6\n"
"s_add_u32 s12, s12, s11\n"
"s_mul_i32 s13, s10, 0x6000\n"
"s_mul_i32 s14, s76, 0xc0\n"
"s_mul_i32 s75, s76, 0x1800\n"
"s_add_u32 s13, s13, s14\n"
"s_add_u32 s75, s75, 0x1b000\n"
"s_lshr_b32 s86, s51, 3\n"
"s_mul_i32 s87, s86, 0x56\n"
"s_lshr_b32 s87, s87, 8\n"
"s_mul_i32 s87, s87, 3\n"
"s_sub_u32 s86, s86, s87\n"
"s_lshl_b32 s86, s86, 11\n"
"s_add_u32 s87, s86, 0x800\n"
"s_cmp_eq_u32 s87, 0x1800\n"
"s_cselect_b32 s87, 0, s87\n"
"s_add_u32 s90, s87, 0x800\n"
"s_cmp_eq_u32 s90, 0x1800\n"
"s_cselect_b32 s90, 0, s90\n"
"v_bitop3_b32 v110, s3, v1, v2 bitop3:0x96\n"
"v_lshlrev_b32 v110, 4, v110\n"
"s_mul_i32 s6, s76, 0xc00\n"
"v_and_b32 v110, 0xf0, v110\n"
"s_lshl_b32 s31, s76, 10\n"
"v_add_u32 v109, s3, v2\n"
"v_mad_u32_u24 v236, v109, s4, v110\n"
"s_add_u32 s30, s31, 0x12000\n"
"s_mul_i32 s95, s98, 191\n"
"s_mul_i32 s5, s98, 0x6000\n"
"s_lshr_b32 s95, s95, 13\n"
"s_mul_i32 s95, s95, 0x900000\n"
"s_add_u32 s96, s95, 0x900000\n"
"s_cmp_eq_u32 s96, 0x1b00000\n"
"s_cselect_b32 s96, 0, s96\n"
"s_add_u32 s97, s96, 0x900000\n"
"s_cmp_eq_u32 s97, 0x1b00000\n"
"s_cselect_b32 s97, 0, s97\n"
"s_movk_i32 s7, 0x240\n"
"v_mul_u32_u24 v110, 0xab, v239\n"
"v_add_u32 v111, 0x400, v239\n"
"v_add_u32 v112, 0x800, v239\n"
"v_lshrrev_b32 v110, 15, v110\n"
"v_mul_u32_u24 v113, 0xab, v111\n"
"v_mul_u32_u24 v114, 0xab, v112\n"
"v_mad_u32_u24 v240, v110, s7, v239\n"
"v_lshrrev_b32 v113, 15, v113\n"
"v_lshrrev_b32 v114, 15, v114\n"
"s_mul_i32 s74, s76, 0x1800\n"
"v_mad_u32_u24 v241, v113, s7, v239\n"
"v_mad_u32_u24 v242, v114, s7, v239\n"
"s_mul_i32 s8, s76, 0xc0\n"
"v_mov_b32 v0, 0\n"
"s_waitcnt lgkmcnt(0)\n"
"s_mov_b32 s83, s84\n"
"global_load_dwordx2 v[252:253], v0, s[28:29] sc1\n"
"s_add_u32 s68, s66, s12\n"
"s_addc_u32 s69, s67, 0\n"
"s_add_u32 m0, s75, s86\n"
"s_mul_i32 s18, s86, 12\n"
"s_add_u32 s56, s68, s18\n"
"s_addc_u32 s57, s69, 0\n"
"global_load_lds_dwordx4 v100, s[56:57]\n"
"s_add_u32 m0, m0, 0x400\n"
"s_add_u32 s56, s56, 0x3000\n"
"s_addc_u32 s57, s57, 0\n"
"global_load_lds_dwordx4 v100, s[56:57]\n"
"s_add_u32 m0, s74, 0xc000\n"
"s_add_u32 s70, s24, s13\n"
"s_addc_u32 s71, s25, 0\n"
"global_load_lds_dwordx4 v240, s[70:71] offset:0\n"
"global_load_lds_dwordx4 v241, s[70:71] offset:1024\n"
"global_load_lds_dwordx4 v242, s[70:71] offset:2048\n"
"s_add_u32 m0, s74, 0xcc00\n"
"s_add_u32 s70, s70, 0x3000\n"
"s_addc_u32 s71, s71, 0\n"
"global_load_lds_dwordx4 v240, s[70:71] offset:0\n"
"global_load_lds_dwordx4 v241, s[70:71] offset:1024\n"
"global_load_lds_dwordx4 v242, s[70:71] offset:2048\n"
"s_add_u32 s14, s20, 0x300\n"
"v_mul_u32_u24 v110, 0x600, v3\n"
"s_movk_i32 s15, 0x3000\n"
"v_add_u32 v110, s14, v110\n"
"v_xor_b32 v110, s14, v110\n"
"v_and_b32 v110, 0x2000, v110\n"
"v_cmp_lt_u32 vcc, 0, v110\n"
"s_nop 3\n"
"s_mov_b32 s99, vcc_lo\n"
"v_bfe_u32 v110, s99, v109, 1\n"
"v_mad_u32_u24 v236, v110, s15, v236\n"
"s_add_u32 s22, s22, s5\n"
"s_addc_u32 s23, s23, 0\n"
"s_add_u32 s32, s22, s95\n"
"s_addc_u32 s33, s23, 0\n"
"s_add_u32 s34, s32, 0x300000\n"
"s_addc_u32 s35, s33, 0\n"
"s_add_u32 s36, s34, 0x300000\n"
"s_addc_u32 s37, s35, 0\n"
"s_add_u32 s38, s22, s96\n"
"s_addc_u32 s39, s23, 0\n"
"s_add_u32 s40, s38, 0x300000\n"
"s_addc_u32 s41, s39, 0\n"
"s_add_u32 s42, s40, 0x300000\n"
"s_addc_u32 s43, s41, 0\n"
"s_add_u32 s44, s22, s97\n"
"s_addc_u32 s45, s23, 0\n"
"s_add_u32 s46, s44, 0x300000\n"
"s_addc_u32 s47, s45, 0\n"
"s_add_u32 s48, s46, 0x300000\n"
"s_addc_u32 s49, s47, 0\n"
"s_mul_i32 s14, s98, 191\n"
"v_min_u32 v141, 55, v1\n"
"s_lshr_b32 s14, s14, 13\n"
"v_and_b32 v141, 62, v141\n"
"v_lshlrev_b32 v141, 4, v141\n"
"s_mul_i32 s14, s14, 3\n"
"v_add_u32 v141, 0x3000, v141\n"
"v_add_u32 v110, s14, v3\n"
"s_and_b32 s14, s20, 0x1fffff\n"
"v_subrev_u32 v111, 9, v110\n"
"s_add_u32 s14, s14, 0x300\n"
"v_min_u32 v110, v110, v111\n"
"s_lshr_b32 s15, s14, 11\n"
"v_lshl_add_u32 v110, v110, 7, s98\n"
"s_xnor_b32 s15, s15, s51\n"
"v_mul_u32_u24 v111, 0xaaab, v110\n"
"s_bfe_i32 s91, s15, 0x10002\n"
"v_mul_u32_u24 v110, 0xc000, v110\n"
"v_lshrrev_b32 v111, 18, v111\n"
"v_mul_u32_u24 v111, 0x48000, v111\n"
"v_add3_u32 v110, v110, v111, s14\n"
"s_add_u32 s14, s14, 0x6dffcff\n"
"v_lshrrev_b32 v140, 21, v110\n"
"s_lshr_b32 s14, s14, 21\n"
"v_subrev_u32 v142, s14, v1\n"
"v_ashrrev_i32 v142, 31, v142\n"
"v_bfe_i32 v143, v1, 1, 1\n"
"s_bfe_u32 s14, s98, 0x40002\n"
"s_add_i32 s14, s14, -7\n"
"v_subrev_u32 v144, s14, v3\n"
"v_lshrrev_b32 v144, 31, v144\n"
"s_waitcnt vmcnt(8)\n"
"v_readfirstlane_b32 s80, v252\n"
"v_readfirstlane_b32 s81, v253\n"
"s_nop 3\n"
"s_add_u32 s80, s80, 0x7f4a7c15\n"
"s_addc_u32 s81, s81, 0x9e3779b9\n"
"v_mov_b32 v252, s80\n"
"v_mov_b32 v253, s81\n"
"v_lshlrev_b32 v110, 1, v1\n"
"v_and_b32 v111, 3, v1\n"
"s_movk_i32 s14, 0xc0\n"
"v_and_or_b32 v110, v110, 24, v111\n"
"v_and_b32 v111, 48, v1\n"
"v_mad_u32_u24 v110, v110, s14, v111\n"
"v_add_u32 v101, s74, v110\n"
"v_bfe_u32 v110, v1, 2, 2\n"
"s_mul_i32 s14, s9, 0x56\n"
"v_lshl_or_b32 v110, v2, 3, v110\n"
"s_lshr_b32 s14, s14, 8\n"
"v_and_b32 v111, 48, v239\n"
"s_mul_i32 s15, s14, 3\n"
"v_lshl_add_u32 v110, v110, 6, v111\n"
"s_sub_u32 s15, s9, s15\n"
"v_add_u32 v102, s75, v110\n"
"s_lshl_b32 s15, s15, 1\n"
"s_and_b32 s16, s76, 1\n"
"v_add_u32 v110, 0x21000, v239\n"
"s_add_u32 s15, s15, s16\n"
"v_lshl_add_u32 v103, s76, 12, v110\n"
"s_mul_i32 s15, s15, 12\n"
"v_lshl_add_u32 v104, s16, 11, v110\n"
"s_mul_i32 s14, s14, 72\n"
"s_add_u32 s15, s15, s10\n"
"s_add_u32 s15, s15, s14\n"
"s_lshl_b32 s15, s15, 10\n"
"s_add_u32 s15, s15, 0x10000\n"
"s_add_u32 s72, s28, s15\n"
"s_addc_u32 s73, s29, 0\n"
"v_bfe_u32 v110, v1, 2, 2\n"
"s_movk_i32 s14, 0xc0\n"
"v_lshl_or_b32 v110, v2, 3, v110\n"
"v_and_b32 v111, 48, v239\n"
"v_lshlrev_b32 v112, 8, v3\n"
"v_mad_u32_u24 v110, v110, s14, v111\n"
"v_xor_b32 v113, v2, v3\n"
"v_add_u32 v243, s74, v110\n"
"v_add_u32 v249, 0xfffee000, v112\n"
"v_add_u32 v112, 0x12000, v112\n"
"v_lshl_add_u32 v232, v113, 4, v112\n"
"v_xor_b32 v233, 64, v232\n"
"v_xor_b32 v234, 0x80, v232\n"
"v_xor_b32 v235, 0xc0, v232\n"
"s_lshl_b32 s77, s51, 4\n"
"s_add_u32 s77, s77, 0x1000\n"
"s_lshl_b32 s14, s76, 1\n"
"v_lshrrev_b32 v110, 5, v1\n"
"v_add_u32 v111, s3, v2\n"
"v_bitop3_b32 v110, s14, v1, v110 bitop3:0x96\n"
"s_mul_i32 s15, s76, 0x240\n"
"v_lshlrev_b32 v110, 4, v110\n"
"v_and_b32 v112, 0x70, v239\n"
"v_and_b32 v110, 0xf0, v110\n"
"v_add_u32 v113, s15, v239\n"
"v_mad_u32_u24 v237, v111, s4, v110\n"
"v_cmp_gt_u32 vcc, 36, v1\n"
"v_xor_b32 v238, 0x80, v237\n"
"s_mul_i32 s14, s76, 12\n"
"v_cndmask_b32 v112, v112, v113, vcc\n"
"v_add_u32 v238, 0x3000, v238\n"
"v_lshrrev_b32 v111, 1, v111\n"
"s_movk_i32 s15, 0x6000\n"
"v_bfe_u32 v110, s99, v111, 1\n"
"v_add_u32 v111, 8, v111\n"
"v_mad_u32_u24 v237, v110, s15, v237\n"
"v_bfe_u32 v110, s99, v111, 1\n"
"v_mad_u32_u24 v238, v110, s15, v238\n"
"v_add_u32 v248, 0x1000, v112\n"
"v_add_u32 v110, s14, v2\n"
"v_mul_u32_u24 v112, 0x300, v3\n"
"v_add_u32 v111, 4, v110\n"
"v_add_u32 v112, 0x18000, v112\n"
"v_bitop3_b32 v110, v110, v1, 7 bitop3:0x78\n"
"v_bitop3_b32 v111, v111, v1, 7 bitop3:0x78\n"
"s_add_u32 s14, s6, 0x18000\n"
"v_lshl_add_u32 v116, v110, 4, v112\n"
"v_lshl_add_u32 v117, v111, 4, v112\n"
"v_add_u32 v118, s14, v239\n"
"s_movk_i32 s14, 0x600\n"
"v_add_u32 v110, 0, v1\n"
"v_mul_u32_u24 v111, 0xab, v110\n"
"v_lshrrev_b32 v111, 13, v111\n"
"v_mul_u32_u24 v112, 48, v111\n"
"v_add_u32 v111, s3, v111\n"
"v_sub_u32 v110, v110, v112\n"
"v_bitop3_b32 v110, v110, v111, 7 bitop3:0x78\n"
"v_lshlrev_b32 v110, 4, v110\n"
"v_mad_u32_u24 v110, v111, s14, v110\n"
"v_bfe_u32 v111, s99, v111, 1\n"
"v_mad_u32_u24 v119, v111, s15, v110\n"
"v_add_u32 v110, 64, v1\n"
"v_mul_u32_u24 v111, 0xab, v110\n"
"v_lshrrev_b32 v111, 13, v111\n"
"v_mul_u32_u24 v112, 48, v111\n"
"v_add_u32 v111, s3, v111\n"
"v_sub_u32 v110, v110, v112\n"
"v_bitop3_b32 v110, v110, v111, 7 bitop3:0x78\n"
"v_lshlrev_b32 v110, 4, v110\n"
"v_mad_u32_u24 v110, v111, s14, v110\n"
"v_bfe_u32 v111, s99, v111, 1\n"
"v_mad_u32_u24 v120, v111, s15, v110\n"
"v_add_u32 v110, 128, v1\n"
"v_mul_u32_u24 v111, 0xab, v110\n"
"v_lshrrev_b32 v111, 13, v111\n"
"v_mul_u32_u24 v112, 48, v111\n"
"v_add_u32 v111, s3, v111\n"
"v_sub_u32 v110, v110, v112\n"
"v_bitop3_b32 v110, v110, v111, 7 bitop3:0x78\n"
"v_lshlrev_b32 v110, 4, v110\n"
"v_mad_u32_u24 v110, v111, s14, v110\n"
"v_bfe_u32 v111, s99, v111, 1\n"
"v_mad_u32_u24 v121, v111, s15, v110\n"
"s_add_u32 s15, s3, 0x27000\n"
"v_lshlrev_b32 v110, 2, v1\n"
"s_lshl_b32 s14, s76, 11\n"
"v_and_b32 v110, 12, v110\n"
"s_add_u32 s14, s14, 0x25000\n"
"v_mov_b32 v122, s15\n"
"v_add_u32 v123, 0x27000, v110\n"
"v_lshl_add_u32 v250, v1, 5, s14\n"
"v_mov_b32 v110, 0\n"
"ds_write_b128 v250, v[116:119]\n"
"ds_write_b128 v250, v[120:123] offset:16\n"
"ds_write_b32 v122, v110\n"
"s_add_u32 m0, s75, s87\n"
"s_mul_i32 s18, s87, 12\n"
"s_add_u32 s56, s68, s18\n"
"s_addc_u32 s57, s69, 0\n"
"global_load_lds_dwordx4 v100, s[56:57]\n"
"s_add_u32 m0, m0, 0x400\n"
"s_add_u32 s56, s56, 0x3000\n"
"s_addc_u32 s57, s57, 0\n"
"global_load_lds_dwordx4 v100, s[56:57]\n"
"s_add_u32 m0, s75, s90\n"
"s_mul_i32 s18, s90, 12\n"
"s_add_u32 s56, s68, s18\n"
"s_addc_u32 s57, s69, 0\n"
"global_load_lds_dwordx4 v100, s[56:57]\n"
"s_add_u32 m0, m0, 0x400\n"
"s_add_u32 s56, s56, 0x3000\n"
"s_addc_u32 s57, s57, 0\n"
"global_load_lds_dwordx4 v100, s[56:57]\n"
"LV0:\n"
"s_add_u32 s64, s66, s8\n"
"s_addc_u32 s65, s67, 0\n"
"s_add_u32 m0, s74, 0x0\n"
"s_add_u32 s78, s64, 0x0\n"
"s_addc_u32 s79, s65, 0\n"
"global_load_lds_dwordx4 v240, s[78:79] offset:0\n"
"global_load_lds_dwordx4 v241, s[78:79] offset:1024\n"
"global_load_lds_dwordx4 v242, s[78:79] offset:2048\n"
"s_add_u32 m0, s74, 0xc00\n"
"s_add_u32 s78, s64, 0x3000\n"
"s_addc_u32 s79, s65, 0\n"
"global_load_lds_dwordx4 v240, s[78:79] offset:0\n"
"global_load_lds_dwordx4 v241, s[78:79] offset:1024\n"
"global_load_lds_dwordx4 v242, s[78:79] offset:2048\n"
"s_add_u32 m0, s74, 0x6000\n"
"s_add_u32 s78, s64, 0x6000\n"
"s_addc_u32 s79, s65, 0\n"
"global_load_lds_dwordx4 v240, s[78:79] offset:0\n"
"global_load_lds_dwordx4 v241, s[78:79] offset:1024\n"
"global_load_lds_dwordx4 v242, s[78:79] offset:2048\n"
"s_add_u32 m0, s74, 0x6c00\n"
"s_add_u32 s78, s64, 0x9000\n"
"s_addc_u32 s79, s65, 0\n"
"global_load_lds_dwordx4 v240, s[78:79] offset:0\n"
"global_load_lds_dwordx4 v241, s[78:79] offset:1024\n"
"global_load_lds_dwordx4 v242, s[78:79] offset:2048\n"
"s_waitcnt vmcnt(12)\n"
"ds_read_b128 v[4:7], v101 offset:49152\n"
"ds_read_b128 v[8:11], v101 offset:49920\n"
"ds_read_b64_tr_b16 v[28:29], v102 offset:0\n"
"ds_read_b64_tr_b16 v[30:31], v102 offset:256\n"
"ds_read_b64_tr_b16 v[32:33], v102 offset:8\n"
"ds_read_b64_tr_b16 v[34:35], v102 offset:264\n"
"ds_read_b128 v[12:15], v101 offset:49216\n"
"ds_read_b128 v[16:19], v101 offset:49984\n"
"ds_read_b64_tr_b16 v[36:37], v102 offset:2048\n"
"ds_read_b64_tr_b16 v[38:39], v102 offset:2304\n"
"ds_read_b64_tr_b16 v[40:41], v102 offset:2056\n"
"ds_read_b64_tr_b16 v[42:43], v102 offset:2312\n"
"ds_read_b128 v[20:23], v101 offset:49280\n"
"ds_read_b128 v[24:27], v101 offset:50048\n"
"ds_read_b64_tr_b16 v[44:45], v102 offset:4096\n"
"ds_read_b64_tr_b16 v[46:47], v102 offset:4352\n"
"ds_read_b64_tr_b16 v[48:49], v102 offset:4104\n"
"ds_read_b64_tr_b16 v[50:51], v102 offset:4360\n"
"s_waitcnt lgkmcnt(0)\n"
"v_mfma_f32_16x16x32_bf16 v[52:55], v[4:7], v[28:31], 0\n"
"v_mfma_f32_16x16x32_bf16 v[56:59], v[8:11], v[28:31], 0\n"
"v_mfma_f32_16x16x32_bf16 v[60:63], v[4:7], v[32:35], 0\n"
"v_mfma_f32_16x16x32_bf16 v[64:67], v[8:11], v[32:35], 0\n"
"v_mfma_f32_16x16x32_bf16 v[52:55], v[12:15], v[36:39], v[52:55]\n"
"v_mfma_f32_16x16x32_bf16 v[56:59], v[16:19], v[36:39], v[56:59]\n"
"v_mfma_f32_16x16x32_bf16 v[60:63], v[12:15], v[40:43], v[60:63]\n"
"v_mfma_f32_16x16x32_bf16 v[64:67], v[16:19], v[40:43], v[64:67]\n"
"v_mfma_f32_16x16x32_bf16 v[52:55], v[20:23], v[44:47], v[52:55]\n"
"v_mfma_f32_16x16x32_bf16 v[56:59], v[24:27], v[44:47], v[56:59]\n"
"v_mfma_f32_16x16x32_bf16 v[60:63], v[20:23], v[48:51], v[60:63]\n"
"v_mfma_f32_16x16x32_bf16 v[64:67], v[24:27], v[48:51], v[64:67]\n"
"s_nop 7\n"
"ds_write_b128 v103, v[52:55] offset:0\n"
"ds_write_b128 v103, v[56:59] offset:1024\n"
"ds_write_b128 v103, v[60:63] offset:2048\n"
"ds_write_b128 v103, v[64:67] offset:3072\n"
"global_load_dwordx4 v[134:137], v141, s[28:29] sc1\n"
"global_load_dwordx4 v[148:151], v141, s[28:29] offset:1024 sc1\n"
"s_waitcnt lgkmcnt(0)\n"
"s_barrier\n"
"s_cmp_ge_u32 s76, 2\n"
"s_cbranch_scc1 LJ1_0\n"
"ds_read_b128 v[4:7], v104 offset:0\n"
"ds_read_b128 v[8:11], v104 offset:1024\n"
"ds_read_b128 v[12:15], v104 offset:4096\n"
"ds_read_b128 v[16:19], v104 offset:5120\n"
"ds_read_b128 v[20:23], v104 offset:8192\n"
"ds_read_b128 v[24:27], v104 offset:9216\n"
"ds_read_b128 v[28:31], v104 offset:12288\n"
"ds_read_b128 v[32:35], v104 offset:13312\n"
"s_waitcnt lgkmcnt(0)\n"
"v_add_f32 v4, v4, v12\n"
"v_add_f32 v5, v5, v13\n"
"v_add_f32 v6, v6, v14\n"
"v_add_f32 v7, v7, v15\n"
"v_add_f32 v8, v8, v16\n"
"v_add_f32 v9, v9, v17\n"
"v_add_f32 v10, v10, v18\n"
"v_add_f32 v11, v11, v19\n"
"v_add_f32 v4, v4, v20\n"
"v_add_f32 v5, v5, v21\n"
"v_add_f32 v6, v6, v22\n"
"v_add_f32 v7, v7, v23\n"
"v_add_f32 v8, v8, v24\n"
"v_add_f32 v9, v9, v25\n"
"v_add_f32 v10, v10, v26\n"
"v_add_f32 v11, v11, v27\n"
"v_add_f32 v4, v4, v28\n"
"v_add_f32 v5, v5, v29\n"
"v_add_f32 v6, v6, v30\n"
"v_add_f32 v7, v7, v31\n"
"v_add_f32 v8, v8, v32\n"
"v_add_f32 v9, v9, v33\n"
"v_add_f32 v10, v10, v34\n"
"v_add_f32 v11, v11, v35\n"
"v_cvt_pk_bf16_f32 v40, v4, v5\n"
"v_cvt_pk_bf16_f32 v41, v6, v7\n"
"v_cvt_pk_bf16_f32 v42, v8, v9\n"
"v_cvt_pk_bf16_f32 v43, v10, v11\n"
"LJ1_0:\n"
"s_cselect_b64 exec, 0, -1\n"
"global_store_dwordx4 v239, v[40:43], s[72:73] sc1\n"
"s_mov_b64 exec, -1\n"
"s_add_u32 m0, s74, 0xc000\n"
"s_add_u32 s78, s64, 0xc000\n"
"s_addc_u32 s79, s65, 0\n"
"global_load_lds_dwordx4 v240, s[78:79] offset:0\n"
"global_load_lds_dwordx4 v241, s[78:79] offset:1024\n"
"global_load_lds_dwordx4 v242, s[78:79] offset:2048\n"
"s_add_u32 m0, s74, 0xcc00\n"
"s_add_u32 s78, s64, 0xf000\n"
"s_addc_u32 s79, s65, 0\n"
"global_load_lds_dwordx4 v240, s[78:79] offset:0\n"
"global_load_lds_dwordx4 v241, s[78:79] offset:1024\n"
"global_load_lds_dwordx4 v242, s[78:79] offset:2048\n"
"s_waitcnt vmcnt(7)\n"
"v_bitop3_b32 v138, v135, v137, s81 bitop3:0x7e\n"
"v_bitop3_b32 v152, v149, v151, s81 bitop3:0x7e\n"
"v_bitop3_b32 v139, v134, v136, s80 bitop3:0x7e\n"
"v_bitop3_b32 v153, v148, v150, s80 bitop3:0x7e\n"
"v_lshrrev_b32 v138, 16, v138\n"
"v_lshrrev_b32 v152, 16, v152\n"
"v_or_b32 v138, v138, v139\n"
"v_or_b32 v152, v152, v153\n"
"v_bitop3_b32 v138, v138, v152, v142 bitop3:0xa8\n"
"v_cmp_ne_u32 vcc, 0, v138\n"
"v_sub_u32 v134, v135, v137\n"
"v_sub_u32 v148, v149, v151\n"
"v_bitop3_b32 v134, v134, v143, v142 bitop3:0x28\n"
"v_bitop3_b32 v148, v148, v143, v142 bitop3:0x28\n"
"s_cmp_lg_u64 vcc, 0\n"
"s_cbranch_scc0 LR5_0\n"
"s_mov_b32 s82, 0x40000\n"
"LR4_0:\n"
"s_sleep 1\n"
"global_load_dwordx4 v[134:137], v141, s[28:29] sc1\n"
"global_load_dwordx4 v[148:151], v141, s[28:29] offset:1024 sc1\n"
"s_sub_u32 s82, s82, 1\n"
"s_waitcnt vmcnt(0)\n"
"s_cbranch_scc1 LR5_0\n"
"v_bitop3_b32 v138, v135, v137, s81 bitop3:0x7e\n"
"v_bitop3_b32 v152, v149, v151, s81 bitop3:0x7e\n"
"v_bitop3_b32 v139, v134, v136, s80 bitop3:0x7e\n"
"v_bitop3_b32 v153, v148, v150, s80 bitop3:0x7e\n"
"v_lshrrev_b32 v138, 16, v138\n"
"v_lshrrev_b32 v152, 16, v152\n"
"v_or_b32 v138, v138, v139\n"
"v_or_b32 v152, v152, v153\n"
"v_bitop3_b32 v138, v138, v152, v142 bitop3:0xa8\n"
"v_cmp_ne_u32 vcc, 0, v138\n"
"v_sub_u32 v134, v135, v137\n"
"v_sub_u32 v148, v149, v151\n"
"v_bitop3_b32 v134, v134, v143, v142 bitop3:0x28\n"
"v_bitop3_b32 v148, v148, v143, v142 bitop3:0x28\n"
"s_cmp_lg_u64 vcc, 0\n"
"s_cbranch_scc1 LR4_0\n"
"LR5_0:\n"
"s_nop 0\n"
"v_add_u32_dpp v149, v148, v148 wave_shr:1 row_mask:0xf bank_mask:0xf bound_ctrl:0\n"
"v_add_u32_dpp v135, v134, v134 wave_shr:1 row_mask:0xf bank_mask:0xf bound_ctrl:0\n"
"v_add_u32_dpp v149, v148, v149 wave_shl:1 row_mask:0xf bank_mask:0xf bound_ctrl:0\n"
"v_add_u32_dpp v135, v134, v135 wave_shl:1 row_mask:0xf bank_mask:0xf bound_ctrl:0\n"
"v_mad_i32_i24 v150, v148, -1, v149\n"
"v_mad_i32_i24 v136, v134, -1, v135\n"
"s_nop 0\n"
"v_add_u32_dpp v150, v149, v150 wave_shr:1 row_mask:0xf bank_mask:0xf bound_ctrl:0\n"
"v_add_u32_dpp v136, v135, v136 wave_shr:1 row_mask:0xf bank_mask:0xf bound_ctrl:0\n"
"v_add_u32_dpp v150, v149, v150 wave_shl:1 row_mask:0xf bank_mask:0xf bound_ctrl:0\n"
"v_add_u32_dpp v136, v135, v136 wave_shl:1 row_mask:0xf bank_mask:0xf bound_ctrl:0\n"
"v_cmp_lt_i32 s[54:55], 0, v150\n"
"v_cmp_lt_i32 vcc, 0, v136\n"
"s_nop 1\n"
"v_lshrrev_b64 v[138:139], v140, vcc\n"
"v_lshrrev_b64 v[152:153], v140, s[54:55]\n"
"s_xor_b64 s[54:55], s[54:55], vcc\n"
"v_bfi_b32 v138, v144, v152, v138\n"
"s_lshl_b64 s[54:55], s[54:55], 10\n"
"v_bitop3_b32 v138, v138, v140, 1 bitop3:0x28\n"
"s_bcnt1_i32_b64 s71, s[54:55]\n"
"v_cmp_ne_u32 vcc, 0, v138\n"
"s_sub_i32 s70, s71, 33\n"
"s_mul_i32 s70, s70, 80\n"
"s_ashr_i32 s70, s70, 3\n"
"s_max_i32 s70, s70, 0\n"
"s_min_i32 s70, s70, 95\n"
"s_and_b32 s14, vcc_lo, 0x1ff\n"
"s_xor_b32 s91, s91, s14\n"
"s_lshl_b32 s14, s91, 1\n"
"s_xor_b32 s92, s91, s14\n"
"ds_read_b64_tr_b16 a[0:1], v243 offset:0\n"
"ds_read_b64_tr_b16 a[2:3], v243 offset:768\n"
"ds_read_b64_tr_b16 a[4:5], v243 offset:8\n"
"ds_read_b64_tr_b16 a[6:7], v243 offset:776\n"
"ds_read_b64_tr_b16 a[8:9], v243 offset:64\n"
"ds_read_b64_tr_b16 a[10:11], v243 offset:832\n"
"ds_read_b64_tr_b16 a[12:13], v243 offset:72\n"
"ds_read_b64_tr_b16 a[14:15], v243 offset:840\n"
"ds_read_b64_tr_b16 a[16:17], v243 offset:128\n"
"ds_read_b64_tr_b16 a[18:19], v243 offset:896\n"
"ds_read_b64_tr_b16 a[20:21], v243 offset:136\n"
"ds_read_b64_tr_b16 a[22:23], v243 offset:904\n"
"s_waitcnt lgkmcnt(0)\n"
"s_add_u32 m0, s74, 0x0\n"
"s_add_u32 s78, s64, 0x12000\n"
"s_addc_u32 s79, s65, 0\n"
"global_load_lds_dwordx4 v240, s[78:79] offset:0\n"
"global_load_lds_dwordx4 v241, s[78:79] offset:1024\n"
"global_load_lds_dwordx4 v242, s[78:79] offset:2048\n"
"s_add_u32 m0, s74, 0xc00\n"
"s_add_u32 s78, s64, 0x15000\n"
"s_addc_u32 s79, s65, 0\n"
"global_load_lds_dwordx4 v240, s[78:79] offset:0\n"
"global_load_lds_dwordx4 v241, s[78:79] offset:1024\n"
"global_load_lds_dwordx4 v242, s[78:79] offset:2048\n"
"ds_read_b64_tr_b16 a[24:25], v243 offset:24576\n"
"ds_read_b64_tr_b16 a[26:27], v243 offset:25344\n"
"ds_read_b64_tr_b16 a[28:29], v243 offset:24584\n"
"ds_read_b64_tr_b16 a[30:31], v243 offset:25352\n"
"ds_read_b64_tr_b16 a[32:33], v243 offset:24640\n"
"ds_read_b64_tr_b16 a[34:35], v243 offset:25408\n"
"ds_read_b64_tr_b16 a[36:37], v243 offset:24648\n"
"ds_read_b64_tr_b16 a[38:39], v243 offset:25416\n"
"ds_read_b64_tr_b16 a[40:41], v243 offset:24704\n"
"ds_read_b64_tr_b16 a[42:43], v243 offset:25472\n"
"ds_read_b64_tr_b16 a[44:45], v243 offset:24712\n"
"ds_read_b64_tr_b16 a[46:47], v243 offset:25480\n"
"s_waitcnt lgkmcnt(0)\n"
"s_add_u32 m0, s74, 0x6000\n"
"s_add_u32 s78, s64, 0x18000\n"
"s_addc_u32 s79, s65, 0\n"
"global_load_lds_dwordx4 v240, s[78:79] offset:0\n"
"global_load_lds_dwordx4 v241, s[78:79] offset:1024\n"
"global_load_lds_dwordx4 v242, s[78:79] offset:2048\n"
"s_add_u32 m0, s74, 0x6c00\n"
"s_add_u32 s78, s64, 0x1b000\n"
"s_addc_u32 s79, s65, 0\n"
"global_load_lds_dwordx4 v240, s[78:79] offset:0\n"
"global_load_lds_dwordx4 v241, s[78:79] offset:1024\n"
"global_load_lds_dwordx4 v242, s[78:79] offset:2048\n"
"v_add_u32 v245, 0x3000, v236\n"
"v_add_u32 v244, 0xffffd000, v236\n"
"v_min_u32 v245, v245, v244\n"
"s_bfe_i32 s93, s91, 0x10000\n"
"s_add_u32 m0, s30, 0x0\n"
"v_bfi_b32 v244, s93, v245, v236\n"
"global_load_lds_dwordx4 v244, s[32:33] offset:0 nt\n"
"s_bfe_i32 s93, s91, 0x10001\n"
"s_add_u32 m0, s30, 0x1000\n"
"v_bfi_b32 v244, s93, v245, v236\n"
"global_load_lds_dwordx4 v244, s[34:35] offset:0 nt\n"
"s_bfe_i32 s93, s91, 0x10002\n"
"s_add_u32 m0, s30, 0x2000\n"
"v_bfi_b32 v244, s93, v245, v236\n"
"global_load_lds_dwordx4 v244, s[36:37] offset:0 nt\n"
"s_waitcnt vmcnt(15)\n"
"ds_read_b64_tr_b16 a[48:49], v243 offset:49152\n"
"ds_read_b64_tr_b16 a[50:51], v243 offset:49920\n"
"ds_read_b64_tr_b16 a[52:53], v243 offset:49160\n"
"ds_read_b64_tr_b16 a[54:55], v243 offset:49928\n"
"ds_read_b64_tr_b16 a[56:57], v243 offset:49216\n"
"ds_read_b64_tr_b16 a[58:59], v243 offset:49984\n"
"ds_read_b64_tr_b16 a[60:61], v243 offset:49224\n"
"ds_read_b64_tr_b16 a[62:63], v243 offset:49992\n"
"ds_read_b64_tr_b16 a[64:65], v243 offset:49280\n"
"ds_read_b64_tr_b16 a[66:67], v243 offset:50048\n"
"ds_read_b64_tr_b16 a[68:69], v243 offset:49288\n"
"ds_read_b64_tr_b16 a[70:71], v243 offset:50056\n"
"s_waitcnt lgkmcnt(0)\n"
"s_add_u32 m0, s74, 0xc000\n"
"s_add_u32 s78, s64, 0x1e000\n"
"s_addc_u32 s79, s65, 0\n"
"global_load_lds_dwordx4 v240, s[78:79] offset:0\n"
"global_load_lds_dwordx4 v241, s[78:79] offset:1024\n"
"global_load_lds_dwordx4 v242, s[78:79] offset:2048\n"
"s_add_u32 m0, s74, 0xcc00\n"
"s_add_u32 s78, s64, 0x21000\n"
"s_addc_u32 s79, s65, 0\n"
"global_load_lds_dwordx4 v240, s[78:79] offset:0\n"
"global_load_lds_dwordx4 v241, s[78:79] offset:1024\n"
"global_load_lds_dwordx4 v242, s[78:79] offset:2048\n"
"s_waitcnt vmcnt(15)\n"
"ds_read_b64_tr_b16 a[72:73], v243 offset:0\n"
"ds_read_b64_tr_b16 a[74:75], v243 offset:768\n"
"ds_read_b64_tr_b16 a[76:77], v243 offset:8\n"
"ds_read_b64_tr_b16 a[78:79], v243 offset:776\n"
"ds_read_b64_tr_b16 a[80:81], v243 offset:64\n"
"ds_read_b64_tr_b16 a[82:83], v243 offset:832\n"
"ds_read_b64_tr_b16 a[84:85], v243 offset:72\n"
"ds_read_b64_tr_b16 a[86:87], v243 offset:840\n"
"ds_read_b64_tr_b16 a[88:89], v243 offset:128\n"
"ds_read_b64_tr_b16 a[90:91], v243 offset:896\n"
"ds_read_b64_tr_b16 a[92:93], v243 offset:136\n"
"ds_read_b64_tr_b16 a[94:95], v243 offset:904\n"
"s_waitcnt lgkmcnt(0)\n"
"s_add_u32 m0, s74, 0x0\n"
"s_add_u32 s78, s64, 0x24000\n"
"s_addc_u32 s79, s65, 0\n"
"global_load_lds_dwordx4 v240, s[78:79] offset:0\n"
"global_load_lds_dwordx4 v241, s[78:79] offset:1024\n"
"global_load_lds_dwordx4 v242, s[78:79] offset:2048\n"
"s_add_u32 m0, s74, 0xc00\n"
"s_add_u32 s78, s64, 0x27000\n"
"s_addc_u32 s79, s65, 0\n"
"global_load_lds_dwordx4 v240, s[78:79] offset:0\n"
"global_load_lds_dwordx4 v241, s[78:79] offset:1024\n"
"global_load_lds_dwordx4 v242, s[78:79] offset:2048\n"
"s_memrealtime s[84:85]\n"
"s_waitcnt vmcnt(15)\n"
"s_waitcnt lgkmcnt(0)\n"
"ds_read_b64_tr_b16 a[96:97], v243 offset:24576\n"
"ds_read_b64_tr_b16 a[98:99], v243 offset:25344\n"
"ds_read_b64_tr_b16 a[100:101], v243 offset:24584\n"
"ds_read_b64_tr_b16 a[102:103], v243 offset:25352\n"
"ds_read_b64_tr_b16 a[104:105], v243 offset:24640\n"
"ds_read_b64_tr_b16 a[106:107], v243 offset:25408\n"
"ds_read_b64_tr_b16 a[108:109], v243 offset:24648\n"
"ds_read_b64_tr_b16 a[110:111], v243 offset:25416\n"
"ds_read_b64_tr_b16 a[112:113], v243 offset:24704\n"
"ds_read_b64_tr_b16 a[114:115], v243 offset:25472\n"
"ds_read_b64_tr_b16 a[116:117], v243 offset:24712\n"
"ds_read_b64_tr_b16 a[118:119], v243 offset:25480\n"
"s_waitcnt vmcnt(12)\n"
"s_barrier\n"
"s_cmp_eq_u32 s76, 0\n"
"s_cselect_b32 s12, 1, 0\n"
"s_mov_b32 s13, 0\n"
"s_cmp_lg_u32 s76, 0\n"
"s_cselect_b64 s[4:5], -1, 0\n"
"s_cmp_lg_u32 s76, 1\n"
"s_cselect_b64 s[6:7], -1, 0\n"
"s_cmp_lg_u32 s76, 2\n"
"s_cselect_b64 s[8:9], -1, 0\n"
"s_cmp_lg_u32 s76, 3\n"
"s_cselect_b64 s[10:11], -1, 0\n"
"v_mov_b32 v192, s77\n"
"v_mov_b32 v196, s80\n"
"v_mov_b32 v197, s81\n"
"v_mov_b32 v198, s84\n"
"v_mov_b32 v199, 0\n"
"ds_read_b128 v[216:219], v232 offset:0\n"
"ds_read_b128 v[220:223], v232 offset:4096\n"
"ds_read_b128 v[224:227], v232 offset:8192\n"
"s_waitcnt lgkmcnt(2)\n"
"v_mfma_f32_16x16x32_bf16 v[0:3], a[0:3], v[216:219], 0\n"
"s_add_u32 s78, s64, 0x2a000\n"
"s_addc_u32 s79, s65, 0\n"
"s_add_u32 m0, s74, 0x6000\n"
"s_waitcnt vmcnt(6)\n"
"ds_read_b64_tr_b16 a[120:121], v243 offset:49152\n"
"v_mfma_f32_16x16x32_bf16 v[4:7], a[4:7], v[216:219], 0\n"
"global_load_lds_dwordx4 v240, s[78:79] offset:0\n"
"ds_read_b64_tr_b16 a[122:123], v243 offset:49920\n"
"v_mfma_f32_16x16x32_bf16 v[8:11], a[8:11], v[216:219], 0\n"
"ds_read_b128 v[228:231], v233 offset:0\n"
"ds_read_b64_tr_b16 a[124:125], v243 offset:49160\n"
"v_mfma_f32_16x16x32_bf16 v[12:15], a[12:15], v[216:219], 0\n"
"ds_read_b64_tr_b16 a[126:127], v243 offset:49928\n"
"v_mfma_f32_16x16x32_bf16 v[16:19], a[16:19], v[216:219], 0\n"
"ds_read_b64_tr_b16 a[128:129], v243 offset:49216\n"
"v_mfma_f32_16x16x32_bf16 v[20:23], a[20:23], v[216:219], 0\n"
"global_load_lds_dwordx4 v241, s[78:79] offset:1024\n"
"ds_read_b64_tr_b16 a[130:131], v243 offset:49984\n"
"s_waitcnt lgkmcnt(8)\n"
"v_mfma_f32_16x16x32_bf16 v[24:27], a[0:3], v[220:223], 0\n"
"ds_read_b64_tr_b16 a[132:133], v243 offset:49224\n"
"v_mfma_f32_16x16x32_bf16 v[28:31], a[4:7], v[220:223], 0\n"
"ds_read_b64_tr_b16 a[134:135], v243 offset:49992\n"
"v_mfma_f32_16x16x32_bf16 v[32:35], a[8:11], v[220:223], 0\n"
"s_barrier\n"
"ds_read_b128 v[216:219], v233 offset:4096\n"
"ds_read_b64_tr_b16 a[136:137], v243 offset:49280\n"
"v_mfma_f32_16x16x32_bf16 v[36:39], a[12:15], v[220:223], 0\n"
"s_mov_b64 exec, s[12:13]\n"
"global_store_dwordx4 v192, v[196:199], s[28:29] sc1\n"
"s_mov_b64 exec, -1\n"
"global_load_lds_dwordx4 v242, s[78:79] offset:2048\n"
"ds_read_b64_tr_b16 a[138:139], v243 offset:50048\n"
"v_mfma_f32_16x16x32_bf16 v[40:43], a[16:19], v[220:223], 0\n"
"ds_read_b64_tr_b16 a[140:141], v243 offset:49288\n"
"v_mfma_f32_16x16x32_bf16 v[44:47], a[20:23], v[220:223], 0\n"
"s_add_u32 s78, s64, 0x2d000\n"
"s_addc_u32 s79, s65, 0\n"
"ds_read_b64_tr_b16 a[142:143], v243 offset:50056\n"
"s_waitcnt lgkmcnt(14)\n"
"v_mfma_f32_16x16x32_bf16 v[48:51], a[0:3], v[224:227], 0\n"
"s_add_u32 m0, s74, 0x6c00\n"
"s_waitcnt vmcnt(4)\n"
"ds_read_b64_tr_b16 a[144:145], v243 offset:0\n"
"v_mfma_f32_16x16x32_bf16 v[52:55], a[4:7], v[224:227], 0\n"
"global_load_lds_dwordx4 v240, s[78:79] offset:0\n"
"ds_read_b64_tr_b16 a[146:147], v243 offset:768\n"
"v_mfma_f32_16x16x32_bf16 v[56:59], a[8:11], v[224:227], 0\n"
"ds_read_b128 v[220:223], v233 offset:8192\n"
"ds_read_b64_tr_b16 a[148:149], v243 offset:8\n"
"v_mfma_f32_16x16x32_bf16 v[60:63], a[12:15], v[224:227], 0\n"
"ds_read_b64_tr_b16 a[150:151], v243 offset:776\n"
"v_mfma_f32_16x16x32_bf16 v[64:67], a[16:19], v[224:227], 0\n"
"ds_read_b64_tr_b16 a[152:153], v243 offset:64\n"
"v_mfma_f32_16x16x32_bf16 v[68:71], a[20:23], v[224:227], 0\n"
"global_load_lds_dwordx4 v241, s[78:79] offset:1024\n"
"ds_read_b64_tr_b16 a[154:155], v243 offset:832\n"
"s_waitcnt lgkmcnt(15)\n"
"v_mfma_f32_16x16x32_bf16 v[0:3], a[24:27], v[228:231], v[0:3]\n"
"ds_read_b64_tr_b16 a[156:157], v243 offset:72\n"
"v_mfma_f32_16x16x32_bf16 v[4:7], a[28:31], v[228:231], v[4:7]\n"
"ds_read_b64_tr_b16 a[158:159], v243 offset:840\n"
"v_mfma_f32_16x16x32_bf16 v[8:11], a[32:35], v[228:231], v[8:11]\n"
"ds_read_b128 v[224:227], v234 offset:0\n"
"ds_read_b64_tr_b16 a[160:161], v243 offset:128\n"
"v_mfma_f32_16x16x32_bf16 v[12:15], a[36:39], v[228:231], v[12:15]\n"
"global_load_lds_dwordx4 v242, s[78:79] offset:2048\n"
"ds_read_b64_tr_b16 a[162:163], v243 offset:896\n"
"v_mfma_f32_16x16x32_bf16 v[16:19], a[40:43], v[228:231], v[16:19]\n"
"ds_read_b64_tr_b16 a[164:165], v243 offset:136\n"
"v_mfma_f32_16x16x32_bf16 v[20:23], a[44:47], v[228:231], v[20:23]\n"
"s_bfe_i32 s93, s91, 0x10003\n"
"ds_read_b64_tr_b16 a[166:167], v243 offset:904\n"
"s_waitcnt lgkmcnt(15)\n"
"v_mfma_f32_16x16x32_bf16 v[24:27], a[24:27], v[216:219], v[24:27]\n"
"s_add_u32 m0, s30, 0x3000\n"
"v_bfi_b32 v244, s93, v245, v236\n"
"v_mfma_f32_16x16x32_bf16 v[28:31], a[28:31], v[216:219], v[28:31]\n"
"global_load_lds_dwordx4 v244, s[38:39] offset:0 nt\n"
"v_mfma_f32_16x16x32_bf16 v[32:35], a[32:35], v[216:219], v[32:35]\n"
"ds_read_b128 v[228:231], v234 offset:4096\n"
"s_bfe_i32 s93, s91, 0x10004\n"
"v_mfma_f32_16x16x32_bf16 v[36:39], a[36:39], v[216:219], v[36:39]\n"
"s_add_u32 m0, s30, 0x4000\n"
"v_bfi_b32 v244, s93, v245, v236\n"
"v_mfma_f32_16x16x32_bf16 v[40:43], a[40:43], v[216:219], v[40:43]\n"
"global_load_lds_dwordx4 v244, s[40:41] offset:0 nt\n"
"v_mfma_f32_16x16x32_bf16 v[44:47], a[44:47], v[216:219], v[44:47]\n"
"s_bfe_i32 s93, s91, 0x10005\n"
"s_waitcnt lgkmcnt(12)\n"
"v_mfma_f32_16x16x32_bf16 v[48:51], a[24:27], v[220:223], v[48:51]\n"
"s_add_u32 m0, s30, 0x5000\n"
"v_bfi_b32 v244, s93, v245, v236\n"
"v_mfma_f32_16x16x32_bf16 v[52:55], a[28:31], v[220:223], v[52:55]\n"
"global_load_lds_dwordx4 v244, s[42:43] offset:0 nt\n"
"v_mfma_f32_16x16x32_bf16 v[56:59], a[32:35], v[220:223], v[56:59]\n"
"ds_read_b128 v[216:219], v234 offset:8192\n"
"v_mfma_f32_16x16x32_bf16 v[60:63], a[36:39], v[220:223], v[60:63]\n"
"s_mul_i32 s14, s76, 0x12000\n"
"v_mfma_f32_16x16x32_bf16 v[64:67], a[40:43], v[220:223], v[64:67]\n"
"s_add_u32 s14, s14, 0x10000\n"
"v_mfma_f32_16x16x32_bf16 v[68:71], a[44:47], v[220:223], v[68:71]\n"
"s_waitcnt lgkmcnt(6)\n"
"v_mfma_f32_16x16x32_bf16 v[0:3], a[48:51], v[224:227], v[0:3]\n"
"s_add_u32 s24, s28, s14\n"
"s_addc_u32 s25, s29, 0\n"
"v_mfma_f32_16x16x32_bf16 v[4:7], a[52:55], v[224:227], v[4:7]\n"
"s_bfe_i32 s93, s91, 0x10006\n"
"v_mfma_f32_16x16x32_bf16 v[8:11], a[56:59], v[224:227], v[8:11]\n"
"ds_read_b128 v[220:223], v235 offset:0\n"
"s_add_u32 m0, s30, 0x6000\n"
"v_bfi_b32 v244, s93, v245, v236\n"
"v_mfma_f32_16x16x32_bf16 v[12:15], a[60:63], v[224:227], v[12:15]\n"
"global_load_lds_dwordx4 v244, s[44:45] offset:0 nt\n"
"v_mfma_f32_16x16x32_bf16 v[16:19], a[64:67], v[224:227], v[16:19]\n"
"s_bfe_i32 s93, s91, 0x10007\n"
"v_mfma_f32_16x16x32_bf16 v[20:23], a[68:71], v[224:227], v[20:23]\n"
"s_add_u32 m0, s30, 0x7000\n"
"v_bfi_b32 v244, s93, v245, v236\n"
"s_waitcnt lgkmcnt(2)\n"
"v_mfma_f32_16x16x32_bf16 v[24:27], a[48:51], v[228:231], v[24:27]\n"
"global_load_lds_dwordx4 v244, s[46:47] offset:0 nt\n"
"v_mfma_f32_16x16x32_bf16 v[28:31], a[52:55], v[228:231], v[28:31]\n"
"s_bfe_i32 s93, s91, 0x10008\n"
"v_mfma_f32_16x16x32_bf16 v[32:35], a[56:59], v[228:231], v[32:35]\n"
"ds_read_b128 v[224:227], v235 offset:4096\n"
"s_add_u32 m0, s30, 0x8000\n"
"v_bfi_b32 v244, s93, v245, v236\n"
"v_mfma_f32_16x16x32_bf16 v[36:39], a[60:63], v[228:231], v[36:39]\n"
"global_load_lds_dwordx4 v244, s[48:49] offset:0 nt\n"
"v_mfma_f32_16x16x32_bf16 v[40:43], a[64:67], v[228:231], v[40:43]\n"
"s_mov_b32 s88, 0\n"
"v_mfma_f32_16x16x32_bf16 v[44:47], a[68:71], v[228:231], v[44:47]\n"
"s_waitcnt lgkmcnt(2)\n"
"v_mfma_f32_16x16x32_bf16 v[48:51], a[48:51], v[216:219], v[48:51]\n"
"s_mov_b32 s19, 25\n"
"v_mfma_f32_16x16x32_bf16 v[52:55], a[52:55], v[216:219], v[52:55]\n"
"v_mfma_f32_16x16x32_bf16 v[56:59], a[56:59], v[216:219], v[56:59]\n"
"ds_read_b128 v[228:231], v235 offset:8192\n"
"v_mfma_f32_16x16x32_bf16 v[60:63], a[60:63], v[216:219], v[60:63]\n"
"s_add_u32 s78, s64, 0x30000\n"
"s_addc_u32 s79, s65, 0\n"
"v_mfma_f32_16x16x32_bf16 v[64:67], a[64:67], v[216:219], v[64:67]\n"
"s_add_u32 m0, s74, 0xc000\n"
"v_mfma_f32_16x16x32_bf16 v[68:71], a[68:71], v[216:219], v[68:71]\n"
"global_load_lds_dwordx4 v240, s[78:79] offset:0\n"
"s_waitcnt lgkmcnt(2)\n"
"v_mfma_f32_16x16x32_bf16 v[0:3], a[72:75], v[220:223], v[0:3]\n"
"v_mfma_f32_16x16x32_bf16 v[4:7], a[76:79], v[220:223], v[4:7]\n"
"s_waitcnt vmcnt(4)\n"
"s_barrier\n"
"v_mfma_f32_16x16x32_bf16 v[8:11], a[80:83], v[220:223], v[8:11]\n"
"ds_read_b128 v[216:219], v232 offset:12288\n"
"v_mfma_f32_16x16x32_bf16 v[12:15], a[84:87], v[220:223], v[12:15]\n"
"global_load_lds_dwordx4 v241, s[78:79] offset:1024\n"
"ds_read_b64_tr_b16 a[168:169], v243 offset:24576\n"
"v_mfma_f32_16x16x32_bf16 v[16:19], a[88:91], v[220:223], v[16:19]\n"
"ds_read_b64_tr_b16 a[170:171], v243 offset:25344\n"
"v_mfma_f32_16x16x32_bf16 v[20:23], a[92:95], v[220:223], v[20:23]\n"
"ds_read_b64_tr_b16 a[172:173], v243 offset:24584\n"
"s_waitcnt lgkmcnt(5)\n"
"v_mfma_f32_16x16x32_bf16 v[24:27], a[72:75], v[224:227], v[24:27]\n"
"ds_read_b64_tr_b16 a[174:175], v243 offset:25352\n"
"v_mfma_f32_16x16x32_bf16 v[28:31], a[76:79], v[224:227], v[28:31]\n"
"global_load_lds_dwordx4 v242, s[78:79] offset:2048\n"
"ds_read_b64_tr_b16 a[176:177], v243 offset:24640\n"
"v_mfma_f32_16x16x32_bf16 v[32:35], a[80:83], v[224:227], v[32:35]\n"
"ds_read_b128 v[220:223], v232 offset:16384\n"
"ds_read_b64_tr_b16 a[178:179], v243 offset:25408\n"
"v_mfma_f32_16x16x32_bf16 v[36:39], a[84:87], v[224:227], v[36:39]\n"
"s_add_u32 s78, s64, 0x33000\n"
"s_addc_u32 s79, s65, 0\n"
"ds_read_b64_tr_b16 a[180:181], v243 offset:24648\n"
"v_mfma_f32_16x16x32_bf16 v[40:43], a[88:91], v[224:227], v[40:43]\n"
"s_add_u32 m0, s74, 0xcc00\n"
"ds_read_b64_tr_b16 a[182:183], v243 offset:25416\n"
"v_mfma_f32_16x16x32_bf16 v[44:47], a[92:95], v[224:227], v[44:47]\n"
"global_load_lds_dwordx4 v240, s[78:79] offset:0\n"
"ds_read_b64_tr_b16 a[184:185], v243 offset:24704\n"
"s_waitcnt lgkmcnt(11)\n"
"v_mfma_f32_16x16x32_bf16 v[48:51], a[72:75], v[228:231], v[48:51]\n"
"ds_read_b64_tr_b16 a[186:187], v243 offset:25472\n"
"v_mfma_f32_16x16x32_bf16 v[52:55], a[76:79], v[228:231], v[52:55]\n"
"ds_read_b64_tr_b16 a[188:189], v243 offset:24712\n"
"v_mfma_f32_16x16x32_bf16 v[56:59], a[80:83], v[228:231], v[56:59]\n"
"ds_read_b128 v[224:227], v232 offset:20480\n"
"ds_read_b64_tr_b16 a[190:191], v243 offset:25480\n"
"v_mfma_f32_16x16x32_bf16 v[60:63], a[84:87], v[228:231], v[60:63]\n"
"global_load_lds_dwordx4 v241, s[78:79] offset:1024\n"
"v_mfma_f32_16x16x32_bf16 v[64:67], a[88:91], v[228:231], v[64:67]\n"
"v_mfma_f32_16x16x32_bf16 v[68:71], a[92:95], v[228:231], v[68:71]\n"
"s_waitcnt lgkmcnt(14)\n"
"v_mfma_f32_16x16x32_bf16 v[72:75], a[0:3], v[216:219], 0\n"
"v_add_u32 v245, 0x3000, v236\n"
"v_mfma_f32_16x16x32_bf16 v[76:79], a[4:7], v[216:219], 0\n"
"global_load_lds_dwordx4 v242, s[78:79] offset:2048\n"
"v_add_u32 v244, 0xffffd000, v236\n"
"v_mfma_f32_16x16x32_bf16 v[80:83], a[8:11], v[216:219], 0\n"
"ds_read_b128 v[228:231], v233 offset:12288\n"
"v_min_u32 v245, v245, v244\n"
"v_mfma_f32_16x16x32_bf16 v[84:87], a[12:15], v[216:219], 0\n"
"s_bfe_i32 s93, s91, 0x10000\n"
"v_mfma_f32_16x16x32_bf16 v[88:91], a[16:19], v[216:219], 0\n"
"s_add_u32 m0, s30, 0x8f00\n"
"v_bfi_b32 v244, s93, v245, v236\n"
"v_mfma_f32_16x16x32_bf16 v[92:95], a[20:23], v[216:219], 0\n"
"global_load_lds_dwordx4 v244, s[32:33] offset:256 nt\n"
"s_waitcnt lgkmcnt(9)\n"
"v_mfma_f32_16x16x32_bf16 v[96:99], a[0:3], v[220:223], 0\n"
"s_bfe_i32 s93, s91, 0x10001\n"
"v_mfma_f32_16x16x32_bf16 v[100:103], a[4:7], v[220:223], 0\n"
"s_add_u32 m0, s30, 0x9f00\n"
"v_bfi_b32 v244, s93, v245, v236\n"
"v_mfma_f32_16x16x32_bf16 v[104:107], a[8:11], v[220:223], 0\n"
"ds_read_b128 v[216:219], v233 offset:16384\n"
"global_load_lds_dwordx4 v244, s[34:35] offset:256 nt\n"
"v_mfma_f32_16x16x32_bf16 v[108:111], a[12:15], v[220:223], 0\n"
"s_bfe_i32 s93, s91, 0x10002\n"
"v_mfma_f32_16x16x32_bf16 v[112:115], a[16:19], v[220:223], 0\n"
"s_add_u32 m0, s30, 0xaf00\n"
"v_bfi_b32 v244, s93, v245, v236\n"
"v_mfma_f32_16x16x32_bf16 v[116:119], a[20:23], v[220:223], 0\n"
"global_load_lds_dwordx4 v244, s[36:37] offset:256 nt\n"
"s_waitcnt lgkmcnt(3)\n"
"v_mfma_f32_16x16x32_bf16 v[120:123], a[0:3], v[224:227], 0\n"
"s_bfe_i32 s93, s91, 0x10003\n"
"v_mfma_f32_16x16x32_bf16 v[124:127], a[4:7], v[224:227], 0\n"
"s_add_u32 m0, s30, 0xbf00\n"
"v_bfi_b32 v244, s93, v245, v236\n"
"v_mfma_f32_16x16x32_bf16 v[128:131], a[8:11], v[224:227], 0\n"
"ds_read_b128 v[220:223], v233 offset:20480\n"
"global_load_lds_dwordx4 v244, s[38:39] offset:256 nt\n"
"v_mfma_f32_16x16x32_bf16 v[132:135], a[12:15], v[224:227], 0\n"
"s_bfe_i32 s93, s91, 0x10004\n"
"v_mfma_f32_16x16x32_bf16 v[136:139], a[16:19], v[224:227], 0\n"
"s_add_u32 m0, s30, 0xcf00\n"
"v_bfi_b32 v244, s93, v245, v236\n"
"v_mfma_f32_16x16x32_bf16 v[140:143], a[20:23], v[224:227], 0\n"
"global_load_lds_dwordx4 v244, s[40:41] offset:256 nt\n"
"s_waitcnt lgkmcnt(2)\n"
"v_mfma_f32_16x16x32_bf16 v[72:75], a[24:27], v[228:231], v[72:75]\n"
"s_bfe_i32 s93, s91, 0x10005\n"
"v_mfma_f32_16x16x32_bf16 v[76:79], a[28:31], v[228:231], v[76:79]\n"
"s_add_u32 m0, s30, 0xdf00\n"
"v_bfi_b32 v244, s93, v245, v236\n"
"v_mfma_f32_16x16x32_bf16 v[80:83], a[32:35], v[228:231], v[80:83]\n"
"ds_read_b128 v[224:227], v234 offset:12288\n"
"global_load_lds_dwordx4 v244, s[42:43] offset:256 nt\n"
"v_mfma_f32_16x16x32_bf16 v[84:87], a[36:39], v[228:231], v[84:87]\n"
"s_bfe_i32 s93, s91, 0x10006\n"
"v_mfma_f32_16x16x32_bf16 v[88:91], a[40:43], v[228:231], v[88:91]\n"
"s_add_u32 m0, s30, 0xef00\n"
"v_bfi_b32 v244, s93, v245, v236\n"
"v_mfma_f32_16x16x32_bf16 v[92:95], a[44:47], v[228:231], v[92:95]\n"
"global_load_lds_dwordx4 v244, s[44:45] offset:256 nt\n"
"s_waitcnt lgkmcnt(2)\n"
"v_mfma_f32_16x16x32_bf16 v[96:99], a[24:27], v[216:219], v[96:99]\n"
"s_bfe_i32 s93, s91, 0x10007\n"
"v_mfma_f32_16x16x32_bf16 v[100:103], a[28:31], v[216:219], v[100:103]\n"
"s_add_u32 m0, s30, 0xff00\n"
"v_bfi_b32 v244, s93, v245, v236\n"
"v_mfma_f32_16x16x32_bf16 v[104:107], a[32:35], v[216:219], v[104:107]\n"
"ds_read_b128 v[228:231], v234 offset:16384\n"
"global_load_lds_dwordx4 v244, s[46:47] offset:256 nt\n"
"v_mfma_f32_16x16x32_bf16 v[108:111], a[36:39], v[216:219], v[108:111]\n"
"s_bfe_i32 s93, s91, 0x10008\n"
"v_mfma_f32_16x16x32_bf16 v[112:115], a[40:43], v[216:219], v[112:115]\n"
"s_add_u32 m0, s30, 0x10f00\n"
"v_bfi_b32 v244, s93, v245, v236\n"
"v_mfma_f32_16x16x32_bf16 v[116:119], a[44:47], v[216:219], v[116:119]\n"
"global_load_lds_dwordx4 v244, s[48:49] offset:256 nt\n"
"s_waitcnt lgkmcnt(2)\n"
"v_mfma_f32_16x16x32_bf16 v[120:123], a[24:27], v[220:223], v[120:123]\n"
"v_mfma_f32_16x16x32_bf16 v[124:127], a[28:31], v[220:223], v[124:127]\n"
"v_mfma_f32_16x16x32_bf16 v[128:131], a[32:35], v[220:223], v[128:131]\n"
"ds_read_b128 v[216:219], v234 offset:20480\n"
"v_mfma_f32_16x16x32_bf16 v[132:135], a[36:39], v[220:223], v[132:135]\n"
"v_mfma_f32_16x16x32_bf16 v[136:139], a[40:43], v[220:223], v[136:139]\n"
"v_mfma_f32_16x16x32_bf16 v[140:143], a[44:47], v[220:223], v[140:143]\n"
"s_waitcnt lgkmcnt(2)\n"
"v_mfma_f32_16x16x32_bf16 v[72:75], a[48:51], v[224:227], v[72:75]\n"
"v_mfma_f32_16x16x32_bf16 v[76:79], a[52:55], v[224:227], v[76:79]\n"
"s_waitcnt vmcnt(9)\n"
"ds_read_b64_tr_b16 a[192:193], v243 offset:49152\n"
"v_mfma_f32_16x16x32_bf16 v[80:83], a[56:59], v[224:227], v[80:83]\n"
"ds_read_b128 v[220:223], v235 offset:12288\n"
"ds_read_b64_tr_b16 a[194:195], v243 offset:49920\n"
"v_mfma_f32_16x16x32_bf16 v[84:87], a[60:63], v[224:227], v[84:87]\n"
"ds_read_b64_tr_b16 a[196:197], v243 offset:49160\n"
"v_mfma_f32_16x16x32_bf16 v[88:91], a[64:67], v[224:227], v[88:91]\n"
"ds_read_b64_tr_b16 a[198:199], v243 offset:49928\n"
"v_mfma_f32_16x16x32_bf16 v[92:95], a[68:71], v[224:227], v[92:95]\n"
"ds_read_b64_tr_b16 a[200:201], v243 offset:49216\n"
"s_waitcnt lgkmcnt(7)\n"
"v_mfma_f32_16x16x32_bf16 v[96:99], a[48:51], v[228:231], v[96:99]\n"
"ds_read_b64_tr_b16 a[202:203], v243 offset:49984\n"
"v_mfma_f32_16x16x32_bf16 v[100:103], a[52:55], v[228:231], v[100:103]\n"
"ds_read_b64_tr_b16 a[204:205], v243 offset:49224\n"
"v_mfma_f32_16x16x32_bf16 v[104:107], a[56:59], v[228:231], v[104:107]\n"
"ds_read_b128 v[224:227], v235 offset:16384\n"
"ds_read_b64_tr_b16 a[206:207], v243 offset:49992\n"
"v_mfma_f32_16x16x32_bf16 v[108:111], a[60:63], v[228:231], v[108:111]\n"
"s_add_u32 s78, s64, 0x36000\n"
"s_addc_u32 s79, s65, 0\n"
"ds_read_b64_tr_b16 a[208:209], v243 offset:49280\n"
"v_mfma_f32_16x16x32_bf16 v[112:115], a[64:67], v[228:231], v[112:115]\n"
"s_add_u32 m0, s74, 0x0\n"
"ds_read_b64_tr_b16 a[210:211], v243 offset:50048\n"
"v_mfma_f32_16x16x32_bf16 v[116:119], a[68:71], v[228:231], v[116:119]\n"
"global_load_lds_dwordx4 v240, s[78:79] offset:0\n"
"ds_read_b64_tr_b16 a[212:213], v243 offset:49288\n"
"s_waitcnt lgkmcnt(13)\n"
"v_mfma_f32_16x16x32_bf16 v[120:123], a[48:51], v[216:219], v[120:123]\n"
"ds_read_b64_tr_b16 a[214:215], v243 offset:50056\n"
"v_mfma_f32_16x16x32_bf16 v[124:127], a[52:55], v[216:219], v[124:127]\n"
"v_mfma_f32_16x16x32_bf16 v[128:131], a[56:59], v[216:219], v[128:131]\n"
"ds_read_b128 v[228:231], v235 offset:20480\n"
"v_mfma_f32_16x16x32_bf16 v[132:135], a[60:63], v[216:219], v[132:135]\n"
"global_load_lds_dwordx4 v241, s[78:79] offset:1024\n"
"v_mfma_f32_16x16x32_bf16 v[136:139], a[64:67], v[216:219], v[136:139]\n"
"v_mfma_f32_16x16x32_bf16 v[140:143], a[68:71], v[216:219], v[140:143]\n"
"s_waitcnt lgkmcnt(13)\n"
"v_mfma_f32_16x16x32_bf16 v[72:75], a[72:75], v[220:223], v[72:75]\n"
"v_mfma_f32_16x16x32_bf16 v[76:79], a[76:79], v[220:223], v[76:79]\n"
"s_barrier\n"
"global_load_lds_dwordx4 v242, s[78:79] offset:2048\n"
"v_mfma_f32_16x16x32_bf16 v[80:83], a[80:83], v[220:223], v[80:83]\n"
"ds_read_b128 v[216:219], v232 offset:24576\n"
"v_mfma_f32_16x16x32_bf16 v[84:87], a[84:87], v[220:223], v[84:87]\n"
"s_add_u32 s78, s64, 0x39000\n"
"s_addc_u32 s79, s65, 0\n"
"v_mfma_f32_16x16x32_bf16 v[88:91], a[88:91], v[220:223], v[88:91]\n"
"s_add_u32 m0, s74, 0xc00\n"
"v_mfma_f32_16x16x32_bf16 v[92:95], a[92:95], v[220:223], v[92:95]\n"
"global_load_lds_dwordx4 v240, s[78:79] offset:0\n"
"s_waitcnt lgkmcnt(7)\n"
"v_mfma_f32_16x16x32_bf16 v[96:99], a[72:75], v[224:227], v[96:99]\n"
"v_mfma_f32_16x16x32_bf16 v[100:103], a[76:79], v[224:227], v[100:103]\n"
"v_mfma_f32_16x16x32_bf16 v[104:107], a[80:83], v[224:227], v[104:107]\n"
"ds_read_b128 v[220:223], v232 offset:28672\n"
"v_mfma_f32_16x16x32_bf16 v[108:111], a[84:87], v[224:227], v[108:111]\n"
"global_load_lds_dwordx4 v241, s[78:79] offset:1024\n"
"v_mfma_f32_16x16x32_bf16 v[112:115], a[88:91], v[224:227], v[112:115]\n"
"v_mfma_f32_16x16x32_bf16 v[116:119], a[92:95], v[224:227], v[116:119]\n"
"s_waitcnt lgkmcnt(2)\n"
"v_mfma_f32_16x16x32_bf16 v[120:123], a[72:75], v[228:231], v[120:123]\n"
"v_mfma_f32_16x16x32_bf16 v[124:127], a[76:79], v[228:231], v[124:127]\n"
"global_load_lds_dwordx4 v242, s[78:79] offset:2048\n"
"v_mfma_f32_16x16x32_bf16 v[128:131], a[80:83], v[228:231], v[128:131]\n"
"ds_read_b128 v[224:227], v232 offset:32768\n"
"v_add_u32 v232, 0x9000, v232\n"
"v_mfma_f32_16x16x32_bf16 v[132:135], a[84:87], v[228:231], v[132:135]\n"
"v_mfma_f32_16x16x32_bf16 v[136:139], a[88:91], v[228:231], v[136:139]\n"
"global_load_dwordx4 v[244:247], v248, s[28:29] sc1\n"
"s_add_u32 s78, s64, 0x3c000\n"
"s_addc_u32 s79, s65, 0\n"
"v_mfma_f32_16x16x32_bf16 v[140:143], a[92:95], v[228:231], v[140:143]\n"
"s_add_u32 m0, s74, 0x6000\n"
"s_waitcnt lgkmcnt(2)\n"
"v_mfma_f32_16x16x32_bf16 v[144:147], a[0:3], v[216:219], 0\n"
"global_load_lds_dwordx4 v240, s[78:79] offset:0\n"
"v_mfma_f32_16x16x32_bf16 v[148:151], a[4:7], v[216:219], 0\n"
"s_mov_b64 exec, s[4:5]\n"
"s_cbranch_execz LHT0\n"
"LHR0:\n"
"s_mov_b64 exec, -1\n"
"v_mfma_f32_16x16x32_bf16 v[152:155], a[8:11], v[216:219], 0\n"
"ds_read_b128 v[228:231], v233 offset:24576\n"
"v_mfma_f32_16x16x32_bf16 v[156:159], a[12:15], v[216:219], 0\n"
"v_mfma_f32_16x16x32_bf16 v[160:163], a[16:19], v[216:219], 0\n"
"global_load_lds_dwordx4 v241, s[78:79] offset:1024\n"
"v_mfma_f32_16x16x32_bf16 v[164:167], a[20:23], v[216:219], 0\n"
"s_waitcnt lgkmcnt(2)\n"
"v_mfma_f32_16x16x32_bf16 v[168:171], a[0:3], v[220:223], 0\n"
"v_mfma_f32_16x16x32_bf16 v[172:175], a[4:7], v[220:223], 0\n"
"v_mfma_f32_16x16x32_bf16 v[176:179], a[8:11], v[220:223], 0\n"
"ds_read_b128 v[216:219], v233 offset:28672\n"
"global_load_lds_dwordx4 v242, s[78:79] offset:2048\n"
"v_mfma_f32_16x16x32_bf16 v[180:183], a[12:15], v[220:223], 0\n"
"v_mfma_f32_16x16x32_bf16 v[184:187], a[16:19], v[220:223], 0\n"
"s_add_u32 s78, s64, 0x3f000\n"
"s_addc_u32 s79, s65, 0\n"
"v_mfma_f32_16x16x32_bf16 v[188:191], a[20:23], v[220:223], 0\n"
"s_add_u32 m0, s74, 0x6c00\n"
"s_waitcnt lgkmcnt(2)\n"
"v_mfma_f32_16x16x32_bf16 v[192:195], a[0:3], v[224:227], 0\n"
"global_load_lds_dwordx4 v240, s[78:79] offset:0\n"
"v_mfma_f32_16x16x32_bf16 v[196:199], a[4:7], v[224:227], 0\n"
"v_mfma_f32_16x16x32_bf16 v[200:203], a[8:11], v[224:227], 0\n"
"ds_read_b128 v[220:223], v233 offset:32768\n"
"v_add_u32 v233, 0x9000, v233\n"
"v_mfma_f32_16x16x32_bf16 v[204:207], a[12:15], v[224:227], 0\n"
"v_mfma_f32_16x16x32_bf16 v[208:211], a[16:19], v[224:227], 0\n"
"global_load_lds_dwordx4 v241, s[78:79] offset:1024\n"
"v_mfma_f32_16x16x32_bf16 v[212:215], a[20:23], v[224:227], 0\n"
"s_waitcnt lgkmcnt(2)\n"
"v_mfma_f32_16x16x32_bf16 v[144:147], a[24:27], v[228:231], v[144:147]\n"
"v_mfma_f32_16x16x32_bf16 v[148:151], a[28:31], v[228:231], v[148:151]\n"
"v_mfma_f32_16x16x32_bf16 v[152:155], a[32:35], v[228:231], v[152:155]\n"
"ds_read_b128 v[224:227], v234 offset:24576\n"
"global_load_lds_dwordx4 v242, s[78:79] offset:2048\n"
"v_mfma_f32_16x16x32_bf16 v[156:159], a[36:39], v[228:231], v[156:159]\n"
"v_mfma_f32_16x16x32_bf16 v[160:163], a[40:43], v[228:231], v[160:163]\n"
"s_add_u32 s78, s64, 0x42000\n"
"s_addc_u32 s79, s65, 0\n"
"v_mfma_f32_16x16x32_bf16 v[164:167], a[44:47], v[228:231], v[164:167]\n"
"s_add_u32 m0, s74, 0xc000\n"
"s_waitcnt lgkmcnt(2)\n"
"v_mfma_f32_16x16x32_bf16 v[168:171], a[24:27], v[216:219], v[168:171]\n"
"global_load_lds_dwordx4 v240, s[78:79] offset:0\n"
"v_mfma_f32_16x16x32_bf16 v[172:175], a[28:31], v[216:219], v[172:175]\n"
"v_mfma_f32_16x16x32_bf16 v[176:179], a[32:35], v[216:219], v[176:179]\n"
"ds_read_b128 v[228:231], v234 offset:28672\n"
"v_mfma_f32_16x16x32_bf16 v[180:183], a[36:39], v[216:219], v[180:183]\n"
"v_mfma_f32_16x16x32_bf16 v[184:187], a[40:43], v[216:219], v[184:187]\n"
"global_load_lds_dwordx4 v241, s[78:79] offset:1024\n"
"v_mfma_f32_16x16x32_bf16 v[188:191], a[44:47], v[216:219], v[188:191]\n"
"s_waitcnt lgkmcnt(2)\n"
"v_mfma_f32_16x16x32_bf16 v[192:195], a[24:27], v[220:223], v[192:195]\n"
"v_mfma_f32_16x16x32_bf16 v[196:199], a[28:31], v[220:223], v[196:199]\n"
"s_waitcnt vmcnt(9)\n"
"ds_read_b64_tr_b16 a[216:217], v243 offset:0\n"
"v_mfma_f32_16x16x32_bf16 v[200:203], a[32:35], v[220:223], v[200:203]\n"
"ds_read_b128 v[216:219], v234 offset:32768\n"
"v_add_u32 v234, 0x9000, v234\n"
"global_load_lds_dwordx4 v242, s[78:79] offset:2048\n"
"ds_read_b64_tr_b16 a[218:219], v243 offset:768\n"
"LHT0:\n"
"s_cbranch_execz LHR0\n"
"v_mfma_f32_16x16x32_bf16 v[204:207], a[36:39], v[220:223], v[204:207]\n"
"ds_read_b64_tr_b16 a[220:221], v243 offset:8\n"
"v_mfma_f32_16x16x32_bf16 v[208:211], a[40:43], v[220:223], v[208:211]\n"
"s_add_u32 s78, s64, 0x45000\n"
"s_addc_u32 s79, s65, 0\n"
"ds_read_b64_tr_b16 a[222:223], v243 offset:776\n"
"v_mfma_f32_16x16x32_bf16 v[212:215], a[44:47], v[220:223], v[212:215]\n"
"s_add_u32 m0, s74, 0xcc00\n"
"ds_read_b64_tr_b16 a[224:225], v243 offset:64\n"
"s_waitcnt lgkmcnt(7)\n"
"v_mfma_f32_16x16x32_bf16 v[144:147], a[48:51], v[224:227], v[144:147]\n"
"global_load_lds_dwordx4 v240, s[78:79] offset:0\n"
"ds_read_b64_tr_b16 a[226:227], v243 offset:832\n"
"v_mfma_f32_16x16x32_bf16 v[148:151], a[52:55], v[224:227], v[148:151]\n"
"ds_read_b64_tr_b16 a[228:229], v243 offset:72\n"
"v_mfma_f32_16x16x32_bf16 v[152:155], a[56:59], v[224:227], v[152:155]\n"
"ds_read_b128 v[220:223], v235 offset:24576\n"
"ds_read_b64_tr_b16 a[230:231], v243 offset:840\n"
"v_mfma_f32_16x16x32_bf16 v[156:159], a[60:63], v[224:227], v[156:159]\n"
"ds_read_b64_tr_b16 a[232:233], v243 offset:128\n"
"v_mfma_f32_16x16x32_bf16 v[160:163], a[64:67], v[224:227], v[160:163]\n"
"global_load_lds_dwordx4 v241, s[78:79] offset:1024\n"
"ds_read_b64_tr_b16 a[234:235], v243 offset:896\n"
"v_mfma_f32_16x16x32_bf16 v[164:167], a[68:71], v[224:227], v[164:167]\n"
"ds_read_b64_tr_b16 a[236:237], v243 offset:136\n"
"s_waitcnt lgkmcnt(13)\n"
"v_mfma_f32_16x16x32_bf16 v[168:171], a[48:51], v[228:231], v[168:171]\n"
"ds_read_b64_tr_b16 a[238:239], v243 offset:904\n"
"v_mfma_f32_16x16x32_bf16 v[172:175], a[52:55], v[228:231], v[172:175]\n"
"v_mfma_f32_16x16x32_bf16 v[176:179], a[56:59], v[228:231], v[176:179]\n"
"ds_read_b128 v[224:227], v235 offset:28672\n"
"global_load_lds_dwordx4 v242, s[78:79] offset:2048\n"
"v_mfma_f32_16x16x32_bf16 v[180:183], a[60:63], v[228:231], v[180:183]\n"
"v_mfma_f32_16x16x32_bf16 v[184:187], a[64:67], v[228:231], v[184:187]\n"
"v_mfma_f32_16x16x32_bf16 v[188:191], a[68:71], v[228:231], v[188:191]\n"
"s_waitcnt lgkmcnt(13)\n"
"v_mfma_f32_16x16x32_bf16 v[192:195], a[48:51], v[216:219], v[192:195]\n"
"v_mfma_f32_16x16x32_bf16 v[196:199], a[52:55], v[216:219], v[196:199]\n"
"v_mfma_f32_16x16x32_bf16 v[200:203], a[56:59], v[216:219], v[200:203]\n"
"ds_read_b128 v[228:231], v235 offset:32768\n"
"v_add_u32 v235, 0x9000, v235\n"
"v_mfma_f32_16x16x32_bf16 v[204:207], a[60:63], v[216:219], v[204:207]\n"
"v_mfma_f32_16x16x32_bf16 v[208:211], a[64:67], v[216:219], v[208:211]\n"
"s_waitcnt vmcnt(12)\n"
"v_cmp_ne_u64 vcc, s[80:81], v[244:245]\n"
"v_cmp_ne_u64 s[56:57], s[80:81], v[244:245]\n"
"s_nop 3\n"
"s_or_b64 vcc, vcc, s[56:57]\n"
"s_cbranch_scc0 LP1_0\n"
"s_mov_b32 s82, 0x40000\n"
"LP0_0:\n"
"s_sleep 2\n"
"global_load_dwordx4 v[244:247], v248, s[28:29] sc1\n"
"s_sub_u32 s82, s82, 1\n"
"s_cbranch_scc1 LP2_0\n"
"s_waitcnt vmcnt(0)\n"
"v_cmp_ne_u64 vcc, s[80:81], v[244:245]\n"
"v_cmp_ne_u64 s[56:57], s[80:81], v[244:245]\n"
"s_nop 3\n"
"s_or_b64 vcc, vcc, s[56:57]\n"
"s_cbranch_scc1 LP0_0\n"
"LP2_0:\n"
"s_waitcnt vmcnt(0)\n"
"LP1_0:\n"
"s_nop 0\n"
"v_mfma_f32_16x16x32_bf16 v[212:215], a[68:71], v[216:219], v[212:215]\n"
"s_add_u32 s58, s24, 0x1000\n"
"s_addc_u32 s59, s25, 0\n"
"global_load_dwordx4 a[240:243], v239, s[58:59] offset:-4096 sc1\n"
"s_waitcnt lgkmcnt(7)\n"
"v_mfma_f32_16x16x32_bf16 v[144:147], a[72:75], v[220:223], v[144:147]\n"
"v_subrev_u32 v246, s83, v246\n"
"v_mfma_f32_16x16x32_bf16 v[148:151], a[76:79], v[220:223], v[148:151]\n"
"s_barrier\n"
"v_mfma_f32_16x16x32_bf16 v[152:155], a[80:83], v[220:223], v[152:155]\n"
"ds_read_b128 v[216:219], v232 offset:0\n"
"v_add_u32_dpp v246, v246, v246 row_shr:1 row_mask:0xf bank_mask:0xf\n"
"s_waitcnt vmcnt(7)\n"
"ds_read_b64_tr_b16 a[0:1], v243 offset:24576\n"
"v_mfma_f32_16x16x32_bf16 v[156:159], a[84:87], v[220:223], v[156:159]\n"
"ds_read_b64_tr_b16 a[2:3], v243 offset:25344\n"
"v_mfma_f32_16x16x32_bf16 v[160:163], a[88:91], v[220:223], v[160:163]\n"
"v_add_u32_dpp v246, v246, v246 row_shr:2 row_mask:0xf bank_mask:0xf\n"
"global_load_dwordx4 a[244:247], v239, s[58:59] offset:-3072 sc1\n"
"ds_read_b64_tr_b16 a[4:5], v243 offset:24584\n"
"v_mfma_f32_16x16x32_bf16 v[164:167], a[92:95], v[220:223], v[164:167]\n"
"ds_read_b64_tr_b16 a[6:7], v243 offset:25352\n"
"s_waitcnt lgkmcnt(6)\n"
"v_mfma_f32_16x16x32_bf16 v[168:171], a[72:75], v[224:227], v[168:171]\n"
"v_add_u32_dpp v246, v246, v246 row_shr:4 row_mask:0xf bank_mask:0xf\n"
"ds_read_b64_tr_b16 a[8:9], v243 offset:24640\n"
"v_mfma_f32_16x16x32_bf16 v[172:175], a[76:79], v[224:227], v[172:175]\n"
"ds_read_b64_tr_b16 a[10:11], v243 offset:25408\n"
"v_mfma_f32_16x16x32_bf16 v[176:179], a[80:83], v[224:227], v[176:179]\n"
"ds_read_b128 v[220:223], v232 offset:4096\n"
"v_readlane_b32 s86, v246, 63\n"
"ds_read_b64_tr_b16 a[12:13], v243 offset:24648\n"
"v_mfma_f32_16x16x32_bf16 v[180:183], a[84:87], v[224:227], v[180:183]\n"
"global_load_dwordx4 a[248:251], v239, s[58:59] offset:-2048 sc1\n"
"ds_read_b64_tr_b16 a[14:15], v243 offset:25416\n"
"v_mfma_f32_16x16x32_bf16 v[184:187], a[88:91], v[224:227], v[184:187]\n"
"s_ashr_i32 s86, s86, 3\n"
"ds_read_b64_tr_b16 a[16:17], v243 offset:24704\n"
"v_mfma_f32_16x16x32_bf16 v[188:191], a[92:95], v[224:227], v[188:191]\n"
"ds_read_b64_tr_b16 a[18:19], v243 offset:25472\n"
"s_waitcnt lgkmcnt(12)\n"
"v_mfma_f32_16x16x32_bf16 v[192:195], a[72:75], v[228:231], v[192:195]\n"
"s_max_i32 s86, s86, 100\n"
"ds_read_b64_tr_b16 a[20:21], v243 offset:24712\n"
"v_mfma_f32_16x16x32_bf16 v[196:199], a[76:79], v[228:231], v[196:199]\n"
"ds_read_b64_tr_b16 a[22:23], v243 offset:25480\n"
"v_mfma_f32_16x16x32_bf16 v[200:203], a[80:83], v[228:231], v[200:203]\n"
"ds_read_b128 v[224:227], v232 offset:8192\n"
"s_min_i32 s86, s86, 600\n"
"global_load_dwordx4 a[252:255], v239, s[58:59] offset:-1024 sc1\n"
"v_mfma_f32_16x16x32_bf16 v[204:207], a[84:87], v[228:231], v[204:207]\n"
"v_mfma_f32_16x16x32_bf16 v[208:211], a[88:91], v[228:231], v[208:211]\n"
"s_add_u32 s83, s83, s86\n"
"v_mfma_f32_16x16x32_bf16 v[212:215], a[92:95], v[228:231], v[212:215]\n"
"s_waitcnt lgkmcnt(14)\n"
"v_mfma_f32_16x16x32_bf16 v[0:3], a[96:99], v[216:219], v[0:3]\n"
"s_add_u32 s83, s83, 3128\n"
"v_mfma_f32_16x16x32_bf16 v[4:7], a[100:103], v[216:219], v[4:7]\n"
"s_barrier\n"
"global_load_dwordx4 a[48:51], v239, s[58:59] offset:0 sc1\n"
"v_mfma_f32_16x16x32_bf16 v[8:11], a[104:107], v[216:219], v[8:11]\n"
"ds_read_b128 v[228:231], v232 offset:12288\n"
"v_mfma_f32_16x16x32_bf16 v[12:15], a[108:111], v[216:219], v[12:15]\n"
"v_mfma_f32_16x16x32_bf16 v[16:19], a[112:115], v[216:219], v[16:19]\n"
"v_mfma_f32_16x16x32_bf16 v[20:23], a[116:119], v[216:219], v[20:23]\n"
"s_waitcnt lgkmcnt(8)\n"
"v_mfma_f32_16x16x32_bf16 v[24:27], a[96:99], v[220:223], v[24:27]\n"
"global_load_dwordx4 a[52:55], v239, s[58:59] offset:1024 sc1\n"
"v_mfma_f32_16x16x32_bf16 v[28:31], a[100:103], v[220:223], v[28:31]\n"
"v_mfma_f32_16x16x32_bf16 v[32:35], a[104:107], v[220:223], v[32:35]\n"
"ds_read_b128 v[216:219], v232 offset:16384\n"
"s_waitcnt vmcnt(6)\n"
"ds_read_b64_tr_b16 a[24:25], v243 offset:49152\n"
"v_mfma_f32_16x16x32_bf16 v[36:39], a[108:111], v[220:223], v[36:39]\n"
"ds_read_b64_tr_b16 a[26:27], v243 offset:49920\n"
"v_mfma_f32_16x16x32_bf16 v[40:43], a[112:115], v[220:223], v[40:43]\n"
"ds_read_b64_tr_b16 a[28:29], v243 offset:49160\n"
"v_mfma_f32_16x16x32_bf16 v[44:47], a[116:119], v[220:223], v[44:47]\n"
"global_load_dwordx4 a[56:59], v239, s[58:59] offset:2048 sc1\n"
"ds_read_b64_tr_b16 a[30:31], v243 offset:49928\n"
"s_waitcnt lgkmcnt(6)\n"
"v_mfma_f32_16x16x32_bf16 v[48:51], a[96:99], v[224:227], v[48:51]\n"
"ds_read_b64_tr_b16 a[32:33], v243 offset:49216\n"
"v_mfma_f32_16x16x32_bf16 v[52:55], a[100:103], v[224:227], v[52:55]\n"
"ds_read_b64_tr_b16 a[34:35], v243 offset:49984\n"
"v_mfma_f32_16x16x32_bf16 v[56:59], a[104:107], v[224:227], v[56:59]\n"
"ds_read_b128 v[220:223], v232 offset:20480\n"
"ds_read_b64_tr_b16 a[36:37], v243 offset:49224\n"
"v_mfma_f32_16x16x32_bf16 v[60:63], a[108:111], v[224:227], v[60:63]\n"
"ds_read_b64_tr_b16 a[38:39], v243 offset:49992\n"
"v_mfma_f32_16x16x32_bf16 v[64:67], a[112:115], v[224:227], v[64:67]\n"
"global_load_dwordx4 a[60:63], v239, s[58:59] offset:3072 sc1\n"
"ds_read_b64_tr_b16 a[40:41], v243 offset:49280\n"
"v_mfma_f32_16x16x32_bf16 v[68:71], a[116:119], v[224:227], v[68:71]\n"
"ds_read_b64_tr_b16 a[42:43], v243 offset:50048\n"
"s_waitcnt lgkmcnt(12)\n"
"v_mfma_f32_16x16x32_bf16 v[72:75], a[96:99], v[228:231], v[72:75]\n"
"ds_read_b64_tr_b16 a[44:45], v243 offset:49288\n"
"v_mfma_f32_16x16x32_bf16 v[76:79], a[100:103], v[228:231], v[76:79]\n"
"ds_read_b64_tr_b16 a[46:47], v243 offset:50056\n"
"v_mfma_f32_16x16x32_bf16 v[80:83], a[104:107], v[228:231], v[80:83]\n"
"ds_read_b128 v[224:227], v232 offset:24576\n"
"s_add_u32 s58, s24, 0x3000\n"
"s_addc_u32 s59, s25, 0\n"
"v_mfma_f32_16x16x32_bf16 v[84:87], a[108:111], v[228:231], v[84:87]\n"
"global_load_dwordx4 a[64:67], v239, s[58:59] offset:-4096 sc1\n"
"v_mfma_f32_16x16x32_bf16 v[88:91], a[112:115], v[228:231], v[88:91]\n"
"ds_read_b64 v[240:241], v250 offset:24\n"
"v_mfma_f32_16x16x32_bf16 v[92:95], a[116:119], v[228:231], v[92:95]\n"
"s_waitcnt lgkmcnt(15)\n"
"v_mfma_f32_16x16x32_bf16 v[96:99], a[96:99], v[216:219], v[96:99]\n"
"v_mfma_f32_16x16x32_bf16 v[100:103], a[100:103], v[216:219], v[100:103]\n"
"v_mfma_f32_16x16x32_bf16 v[104:107], a[104:107], v[216:219], v[104:107]\n"
"ds_read_b128 v[228:231], v232 offset:28672\n"
"global_load_dwordx4 a[68:71], v239, s[58:59] offset:-3072 sc1\n"
"v_mfma_f32_16x16x32_bf16 v[108:111], a[108:111], v[216:219], v[108:111]\n"
"v_mfma_f32_16x16x32_bf16 v[112:115], a[112:115], v[216:219], v[112:115]\n"
"v_mfma_f32_16x16x32_bf16 v[116:119], a[116:119], v[216:219], v[116:119]\n"
"s_waitcnt lgkmcnt(9)\n"
"v_mfma_f32_16x16x32_bf16 v[120:123], a[96:99], v[220:223], v[120:123]\n"
"v_mfma_f32_16x16x32_bf16 v[124:127], a[100:103], v[220:223], v[124:127]\n"
"global_load_dwordx4 a[72:75], v239, s[58:59] offset:-2048 sc1\n"
"v_mfma_f32_16x16x32_bf16 v[128:131], a[104:107], v[220:223], v[128:131]\n"
"ds_read_b128 v[216:219], v232 offset:32768\n"
"v_add_u32 v232, 0xffff7000, v232\n"
"v_mfma_f32_16x16x32_bf16 v[132:135], a[108:111], v[220:223], v[132:135]\n"
"v_mfma_f32_16x16x32_bf16 v[136:139], a[112:115], v[220:223], v[136:139]\n"
"v_mfma_f32_16x16x32_bf16 v[140:143], a[116:119], v[220:223], v[140:143]\n"
"s_waitcnt lgkmcnt(3)\n"
"v_mfma_f32_16x16x32_bf16 v[144:147], a[96:99], v[224:227], v[144:147]\n"
"global_load_dwordx4 a[76:79], v239, s[58:59] offset:-1024 sc1\n"
"v_mfma_f32_16x16x32_bf16 v[148:151], a[100:103], v[224:227], v[148:151]\n"
"v_mfma_f32_16x16x32_bf16 v[152:155], a[104:107], v[224:227], v[152:155]\n"
"ds_read_b128 v[220:223], v233 offset:0\n"
"v_mfma_f32_16x16x32_bf16 v[156:159], a[108:111], v[224:227], v[156:159]\n"
"v_mfma_f32_16x16x32_bf16 v[160:163], a[112:115], v[224:227], v[160:163]\n"
"v_mfma_f32_16x16x32_bf16 v[164:167], a[116:119], v[224:227], v[164:167]\n"
"global_load_dwordx4 a[80:83], v239, s[58:59] offset:0 sc1\n"
"s_waitcnt lgkmcnt(2)\n"
"v_mfma_f32_16x16x32_bf16 v[168:171], a[96:99], v[228:231], v[168:171]\n"
"v_mfma_f32_16x16x32_bf16 v[172:175], a[100:103], v[228:231], v[172:175]\n"
"v_mfma_f32_16x16x32_bf16 v[176:179], a[104:107], v[228:231], v[176:179]\n"
"ds_read_b128 v[224:227], v233 offset:4096\n"
"v_mfma_f32_16x16x32_bf16 v[180:183], a[108:111], v[228:231], v[180:183]\n"
"v_mfma_f32_16x16x32_bf16 v[184:187], a[112:115], v[228:231], v[184:187]\n"
"global_load_dwordx4 a[84:87], v239, s[58:59] offset:1024 sc1\n"
"v_mfma_f32_16x16x32_bf16 v[188:191], a[116:119], v[228:231], v[188:191]\n"
"s_waitcnt lgkmcnt(2)\n"
"v_mfma_f32_16x16x32_bf16 v[192:195], a[96:99], v[216:219], v[192:195]\n"
"v_mfma_f32_16x16x32_bf16 v[196:199], a[100:103], v[216:219], v[196:199]\n"
"v_mfma_f32_16x16x32_bf16 v[200:203], a[104:107], v[216:219], v[200:203]\n"
"ds_read_b128 v[228:231], v233 offset:8192\n"
"v_mfma_f32_16x16x32_bf16 v[204:207], a[108:111], v[216:219], v[204:207]\n"
"global_load_dwordx4 a[88:91], v239, s[58:59] offset:2048 sc1\n"
"v_mfma_f32_16x16x32_bf16 v[208:211], a[112:115], v[216:219], v[208:211]\n"
"v_mfma_f32_16x16x32_bf16 v[212:215], a[116:119], v[216:219], v[212:215]\n"
"s_waitcnt lgkmcnt(2)\n"
"v_mfma_f32_16x16x32_bf16 v[0:3], a[120:123], v[220:223], v[0:3]\n"
"v_mfma_f32_16x16x32_bf16 v[4:7], a[124:127], v[220:223], v[4:7]\n"
"v_mfma_f32_16x16x32_bf16 v[8:11], a[128:131], v[220:223], v[8:11]\n"
"ds_read_b128 v[216:219], v233 offset:12288\n"
"global_load_dwordx4 a[92:95], v239, s[58:59] offset:3072 sc1\n"
"v_mfma_f32_16x16x32_bf16 v[12:15], a[132:135], v[220:223], v[12:15]\n"
"v_mfma_f32_16x16x32_bf16 v[16:19], a[136:139], v[220:223], v[16:19]\n"
"v_mfma_f32_16x16x32_bf16 v[20:23], a[140:143], v[220:223], v[20:23]\n"
"s_waitcnt lgkmcnt(2)\n"
"v_mfma_f32_16x16x32_bf16 v[24:27], a[120:123], v[224:227], v[24:27]\n"
"s_add_u32 s58, s24, 0x5000\n"
"s_addc_u32 s59, s25, 0\n"
"v_mfma_f32_16x16x32_bf16 v[28:31], a[124:127], v[224:227], v[28:31]\n"
"global_load_dwordx4 a[96:99], v239, s[58:59] offset:-4096 sc1\n"
"v_mfma_f32_16x16x32_bf16 v[32:35], a[128:131], v[224:227], v[32:35]\n"
"ds_read_b128 v[220:223], v233 offset:16384\n"
"v_mfma_f32_16x16x32_bf16 v[36:39], a[132:135], v[224:227], v[36:39]\n"
"v_mfma_f32_16x16x32_bf16 v[40:43], a[136:139], v[224:227], v[40:43]\n"
"v_mfma_f32_16x16x32_bf16 v[44:47], a[140:143], v[224:227], v[44:47]\n"
"s_waitcnt lgkmcnt(2)\n"
"v_mfma_f32_16x16x32_bf16 v[48:51], a[120:123], v[228:231], v[48:51]\n"
"global_load_dwordx4 a[100:103], v239, s[58:59] offset:-3072 sc1\n"
"v_mfma_f32_16x16x32_bf16 v[52:55], a[124:127], v[228:231], v[52:55]\n"
"v_mfma_f32_16x16x32_bf16 v[56:59], a[128:131], v[228:231], v[56:59]\n"
"ds_read_b128 v[224:227], v233 offset:20480\n"
"v_mfma_f32_16x16x32_bf16 v[60:63], a[132:135], v[228:231], v[60:63]\n"
"v_mfma_f32_16x16x32_bf16 v[64:67], a[136:139], v[228:231], v[64:67]\n"
"v_mfma_f32_16x16x32_bf16 v[68:71], a[140:143], v[228:231], v[68:71]\n"
"global_load_dwordx4 a[104:107], v239, s[58:59] offset:-2048 sc1\n"
"s_waitcnt lgkmcnt(2)\n"
"v_mfma_f32_16x16x32_bf16 v[72:75], a[120:123], v[216:219], v[72:75]\n"
"v_mfma_f32_16x16x32_bf16 v[76:79], a[124:127], v[216:219], v[76:79]\n"
"v_mfma_f32_16x16x32_bf16 v[80:83], a[128:131], v[216:219], v[80:83]\n"
"ds_read_b128 v[228:231], v233 offset:24576\n"
"v_mfma_f32_16x16x32_bf16 v[84:87], a[132:135], v[216:219], v[84:87]\n"
"v_mfma_f32_16x16x32_bf16 v[88:91], a[136:139], v[216:219], v[88:91]\n"
"global_load_dwordx4 a[108:111], v239, s[58:59] offset:-1024 sc1\n"
"v_mfma_f32_16x16x32_bf16 v[92:95], a[140:143], v[216:219], v[92:95]\n"
"s_waitcnt lgkmcnt(2)\n"
"v_mfma_f32_16x16x32_bf16 v[96:99], a[120:123], v[220:223], v[96:99]\n"
"v_mfma_f32_16x16x32_bf16 v[100:103], a[124:127], v[220:223], v[100:103]\n"
"v_mfma_f32_16x16x32_bf16 v[104:107], a[128:131], v[220:223], v[104:107]\n"
"ds_read_b128 v[216:219], v233 offset:28672\n"
"v_mfma_f32_16x16x32_bf16 v[108:111], a[132:135], v[220:223], v[108:111]\n"
"global_load_dwordx4 a[112:115], v239, s[58:59] offset:0 sc1\n"
"v_mfma_f32_16x16x32_bf16 v[112:115], a[136:139], v[220:223], v[112:115]\n"
"v_mfma_f32_16x16x32_bf16 v[116:119], a[140:143], v[220:223], v[116:119]\n"
"s_waitcnt lgkmcnt(2)\n"
"v_mfma_f32_16x16x32_bf16 v[120:123], a[120:123], v[224:227], v[120:123]\n"
"v_mfma_f32_16x16x32_bf16 v[124:127], a[124:127], v[224:227], v[124:127]\n"
"v_mfma_f32_16x16x32_bf16 v[128:131], a[128:131], v[224:227], v[128:131]\n"
"ds_read_b128 v[220:223], v233 offset:32768\n"
"v_add_u32 v233, 0xffff7000, v233\n"
"global_load_dwordx4 a[116:119], v239, s[58:59] offset:1024 sc1\n"
"v_mfma_f32_16x16x32_bf16 v[132:135], a[132:135], v[224:227], v[132:135]\n"
"v_add_u32 v245, 0x3000, v236\n"
"v_mfma_f32_16x16x32_bf16 v[136:139], a[136:139], v[224:227], v[136:139]\n"
"v_add_u32 v244, 0xffffd000, v236\n"
"v_mfma_f32_16x16x32_bf16 v[140:143], a[140:143], v[224:227], v[140:143]\n"
"v_min_u32 v245, v245, v244\n"
"s_waitcnt lgkmcnt(2)\n"
"v_mfma_f32_16x16x32_bf16 v[144:147], a[120:123], v[228:231], v[144:147]\n"
"s_bfe_i32 s93, s91, 0x10000\n"
"v_mfma_f32_16x16x32_bf16 v[148:151], a[124:127], v[228:231], v[148:151]\n"
"s_add_u32 m0, s30, 0xfffffe00\n"
"v_bfi_b32 v244, s93, v245, v236\n"
"v_mfma_f32_16x16x32_bf16 v[152:155], a[128:131], v[228:231], v[152:155]\n"
"ds_read_b128 v[224:227], v234 offset:0\n"
"global_load_lds_dwordx4 v244, s[32:33] offset:512 nt\n"
"v_mfma_f32_16x16x32_bf16 v[156:159], a[132:135], v[228:231], v[156:159]\n"
"s_bfe_i32 s93, s91, 0x10001\n"
"v_mfma_f32_16x16x32_bf16 v[160:163], a[136:139], v[228:231], v[160:163]\n"
"s_add_u32 m0, s30, 0xe00\n"
"v_bfi_b32 v244, s93, v245, v236\n"
"v_mfma_f32_16x16x32_bf16 v[164:167], a[140:143], v[228:231], v[164:167]\n"
"global_load_lds_dwordx4 v244, s[34:35] offset:512 nt\n"
"s_waitcnt lgkmcnt(2)\n"
"v_mfma_f32_16x16x32_bf16 v[168:171], a[120:123], v[216:219], v[168:171]\n"
"s_bfe_i32 s93, s91, 0x10002\n"
"v_mfma_f32_16x16x32_bf16 v[172:175], a[124:127], v[216:219], v[172:175]\n"
"s_add_u32 m0, s30, 0x1e00\n"
"v_bfi_b32 v244, s93, v245, v236\n"
"v_mfma_f32_16x16x32_bf16 v[176:179], a[128:131], v[216:219], v[176:179]\n"
"ds_read_b128 v[228:231], v234 offset:4096\n"
"global_load_lds_dwordx4 v244, s[36:37] offset:512 nt\n"
"v_mfma_f32_16x16x32_bf16 v[180:183], a[132:135], v[216:219], v[180:183]\n"
"s_bfe_i32 s93, s91, 0x10003\n"
"v_mfma_f32_16x16x32_bf16 v[184:187], a[136:139], v[216:219], v[184:187]\n"
"s_add_u32 m0, s30, 0x2e00\n"
"v_bfi_b32 v244, s93, v245, v236\n"
"v_mfma_f32_16x16x32_bf16 v[188:191], a[140:143], v[216:219], v[188:191]\n"
"global_load_lds_dwordx4 v244, s[38:39] offset:512 nt\n"
"s_waitcnt lgkmcnt(2)\n"
"v_mfma_f32_16x16x32_bf16 v[192:195], a[120:123], v[220:223], v[192:195]\n"
"s_bfe_i32 s93, s91, 0x10004\n"
"v_mfma_f32_16x16x32_bf16 v[196:199], a[124:127], v[220:223], v[196:199]\n"
"s_add_u32 m0, s30, 0x3e00\n"
"v_bfi_b32 v244, s93, v245, v236\n"
"v_mfma_f32_16x16x32_bf16 v[200:203], a[128:131], v[220:223], v[200:203]\n"
"ds_read_b128 v[216:219], v234 offset:8192\n"
"global_load_lds_dwordx4 v244, s[40:41] offset:512 nt\n"
"v_mfma_f32_16x16x32_bf16 v[204:207], a[132:135], v[220:223], v[204:207]\n"
"s_bfe_i32 s93, s91, 0x10005\n"
"v_mfma_f32_16x16x32_bf16 v[208:211], a[136:139], v[220:223], v[208:211]\n"
"s_add_u32 m0, s30, 0x4e00\n"
"v_bfi_b32 v244, s93, v245, v236\n"
"v_mfma_f32_16x16x32_bf16 v[212:215], a[140:143], v[220:223], v[212:215]\n"
"global_load_lds_dwordx4 v244, s[42:43] offset:512 nt\n"
"s_waitcnt lgkmcnt(2)\n"
"v_mfma_f32_16x16x32_bf16 v[0:3], a[144:147], v[224:227], v[0:3]\n"
"s_bfe_i32 s93, s91, 0x10006\n"
"v_mfma_f32_16x16x32_bf16 v[4:7], a[148:151], v[224:227], v[4:7]\n"
"s_add_u32 m0, s30, 0x5e00\n"
"v_bfi_b32 v244, s93, v245, v236\n"
"v_mfma_f32_16x16x32_bf16 v[8:11], a[152:155], v[224:227], v[8:11]\n"
"ds_read_b128 v[220:223], v234 offset:12288\n"
"global_load_lds_dwordx4 v244, s[44:45] offset:512 nt\n"
"v_mfma_f32_16x16x32_bf16 v[12:15], a[156:159], v[224:227], v[12:15]\n"
"s_bfe_i32 s93, s91, 0x10007\n"
"v_mfma_f32_16x16x32_bf16 v[16:19], a[160:163], v[224:227], v[16:19]\n"
"s_add_u32 m0, s30, 0x6e00\n"
"v_bfi_b32 v244, s93, v245, v236\n"
"v_mfma_f32_16x16x32_bf16 v[20:23], a[164:167], v[224:227], v[20:23]\n"
"global_load_lds_dwordx4 v244, s[46:47] offset:512 nt\n"
"s_waitcnt lgkmcnt(2)\n"
"v_mfma_f32_16x16x32_bf16 v[24:27], a[144:147], v[228:231], v[24:27]\n"
"s_bfe_i32 s93, s91, 0x10008\n"
"v_mfma_f32_16x16x32_bf16 v[28:31], a[148:151], v[228:231], v[28:31]\n"
"s_add_u32 m0, s30, 0x7e00\n"
"v_bfi_b32 v244, s93, v245, v236\n"
"v_mfma_f32_16x16x32_bf16 v[32:35], a[152:155], v[228:231], v[32:35]\n"
"ds_read_b128 v[224:227], v234 offset:16384\n"
"global_load_lds_dwordx4 v244, s[48:49] offset:512 nt\n"
"v_mfma_f32_16x16x32_bf16 v[36:39], a[156:159], v[228:231], v[36:39]\n"
"v_mfma_f32_16x16x32_bf16 v[40:43], a[160:163], v[228:231], v[40:43]\n"
"v_mfma_f32_16x16x32_bf16 v[44:47], a[164:167], v[228:231], v[44:47]\n"
"global_load_dwordx4 a[120:123], v239, s[58:59] offset:2048 sc1\n"
"s_waitcnt lgkmcnt(2)\n"
"v_mfma_f32_16x16x32_bf16 v[48:51], a[144:147], v[216:219], v[48:51]\n"
"s_add_u32 s14, s98, 0\n"
"v_mfma_f32_16x16x32_bf16 v[52:55], a[148:151], v[216:219], v[52:55]\n"
"s_mul_i32 s15, s14, 0xaaab\n"
"v_mfma_f32_16x16x32_bf16 v[56:59], a[152:155], v[216:219], v[56:59]\n"
"ds_read_b128 v[228:231], v234 offset:20480\n"
"v_mfma_f32_16x16x32_bf16 v[60:63], a[156:159], v[216:219], v[60:63]\n"
"s_lshr_b32 s15, s15, 18\n"
"v_mfma_f32_16x16x32_bf16 v[64:67], a[160:163], v[216:219], v[64:67]\n"
"global_load_dwordx4 a[124:127], v239, s[58:59] offset:3072 sc1\n"
"v_mfma_f32_16x16x32_bf16 v[68:71], a[164:167], v[216:219], v[68:71]\n"
"s_waitcnt lgkmcnt(2)\n"
"v_mfma_f32_16x16x32_bf16 v[72:75], a[144:147], v[220:223], v[72:75]\n"
"s_mul_i32 s15, s15, 0x48000\n"
"v_mfma_f32_16x16x32_bf16 v[76:79], a[148:151], v[220:223], v[76:79]\n"
"s_mul_i32 s14, s14, 0xc000\n"
"v_mfma_f32_16x16x32_bf16 v[80:83], a[152:155], v[220:223], v[80:83]\n"
"ds_read_b128 v[216:219], v234 offset:24576\n"
"s_add_u32 s58, s24, 0x7000\n"
"s_addc_u32 s59, s25, 0\n"
"v_mfma_f32_16x16x32_bf16 v[84:87], a[156:159], v[220:223], v[84:87]\n"
"global_load_dwordx4 a[128:131], v239, s[58:59] offset:-4096 sc1\n"
"v_mfma_f32_16x16x32_bf16 v[88:91], a[160:163], v[220:223], v[88:91]\n"
"s_add_u32 s14, s14, s15\n"
"v_mfma_f32_16x16x32_bf16 v[92:95], a[164:167], v[220:223], v[92:95]\n"
"s_waitcnt lgkmcnt(2)\n"
"v_mfma_f32_16x16x32_bf16 v[96:99], a[144:147], v[224:227], v[96:99]\n"
"s_lshl2_add_u32 s32, s95, s14\n"
"v_mfma_f32_16x16x32_bf16 v[100:103], a[148:151], v[224:227], v[100:103]\n"
"s_add_u32 s33, s32, 0x48000\n"
"v_mfma_f32_16x16x32_bf16 v[104:107], a[152:155], v[224:227], v[104:107]\n"
"ds_read_b128 v[220:223], v234 offset:28672\n"
"global_load_dwordx4 a[132:135], v239, s[58:59] offset:-3072 sc1\n"
"v_mfma_f32_16x16x32_bf16 v[108:111], a[156:159], v[224:227], v[108:111]\n"
"s_lshl2_add_u32 s38, s96, s14\n"
"v_mfma_f32_16x16x32_bf16 v[112:115], a[160:163], v[224:227], v[112:115]\n"
"s_add_u32 s39, s38, 0x48000\n"
"v_mfma_f32_16x16x32_bf16 v[116:119], a[164:167], v[224:227], v[116:119]\n"
"s_waitcnt lgkmcnt(2)\n"
"v_mfma_f32_16x16x32_bf16 v[120:123], a[144:147], v[228:231], v[120:123]\n"
"s_lshl2_add_u32 s44, s97, s14\n"
"v_mfma_f32_16x16x32_bf16 v[124:127], a[148:151], v[228:231], v[124:127]\n"
"global_load_dwordx4 a[136:139], v239, s[58:59] offset:-2048 sc1\n"
"v_mfma_f32_16x16x32_bf16 v[128:131], a[152:155], v[228:231], v[128:131]\n"
"ds_read_b128 v[224:227], v234 offset:32768\n"
"v_add_u32 v234, 0xffff7000, v234\n"
"v_mfma_f32_16x16x32_bf16 v[132:135], a[156:159], v[228:231], v[132:135]\n"
"s_add_u32 s45, s44, 0x48000\n"
"v_mfma_f32_16x16x32_bf16 v[136:139], a[160:163], v[228:231], v[136:139]\n"
"s_add_u32 s14, s98, 128\n"
"v_mfma_f32_16x16x32_bf16 v[140:143], a[164:167], v[228:231], v[140:143]\n"
"s_waitcnt lgkmcnt(2)\n"
"v_mfma_f32_16x16x32_bf16 v[144:147], a[144:147], v[216:219], v[144:147]\n"
"global_load_dwordx4 a[140:143], v239, s[58:59] offset:-1024 sc1\n"
"v_mfma_f32_16x16x32_bf16 v[148:151], a[148:151], v[216:219], v[148:151]\n"
"s_mul_i32 s15, s14, 0xaaab\n"
"v_mfma_f32_16x16x32_bf16 v[152:155], a[152:155], v[216:219], v[152:155]\n"
"ds_read_b128 v[228:231], v235 offset:0\n"
"v_mfma_f32_16x16x32_bf16 v[156:159], a[156:159], v[216:219], v[156:159]\n"
"s_lshr_b32 s15, s15, 18\n"
"v_mfma_f32_16x16x32_bf16 v[160:163], a[160:163], v[216:219], v[160:163]\n"
"s_mul_i32 s15, s15, 0x48000\n"
"v_mfma_f32_16x16x32_bf16 v[164:167], a[164:167], v[216:219], v[164:167]\n"
"s_waitcnt lgkmcnt(2)\n"
"v_mfma_f32_16x16x32_bf16 v[168:171], a[144:147], v[220:223], v[168:171]\n"
"s_mul_i32 s14, s14, 0xc000\n"
"v_mfma_f32_16x16x32_bf16 v[172:175], a[148:151], v[220:223], v[172:175]\n"
"s_add_u32 s14, s14, s15\n"
"v_mfma_f32_16x16x32_bf16 v[176:179], a[152:155], v[220:223], v[176:179]\n"
"ds_read_b128 v[216:219], v235 offset:4096\n"
"v_mfma_f32_16x16x32_bf16 v[180:183], a[156:159], v[220:223], v[180:183]\n"
"s_lshl2_add_u32 s34, s95, s14\n"
"v_mfma_f32_16x16x32_bf16 v[184:187], a[160:163], v[220:223], v[184:187]\n"
"s_add_u32 s35, s34, 0x48000\n"
"v_mfma_f32_16x16x32_bf16 v[188:191], a[164:167], v[220:223], v[188:191]\n"
"s_waitcnt lgkmcnt(2)\n"
"v_mfma_f32_16x16x32_bf16 v[192:195], a[144:147], v[224:227], v[192:195]\n"
"s_lshl2_add_u32 s40, s96, s14\n"
"v_mfma_f32_16x16x32_bf16 v[196:199], a[148:151], v[224:227], v[196:199]\n"
"s_add_u32 s41, s40, 0x48000\n"
"v_mfma_f32_16x16x32_bf16 v[200:203], a[152:155], v[224:227], v[200:203]\n"
"ds_read_b128 v[220:223], v235 offset:8192\n"
"v_mfma_f32_16x16x32_bf16 v[204:207], a[156:159], v[224:227], v[204:207]\n"
"s_lshl2_add_u32 s46, s97, s14\n"
"v_mfma_f32_16x16x32_bf16 v[208:211], a[160:163], v[224:227], v[208:211]\n"
"s_add_u32 s47, s46, 0x48000\n"
"v_mfma_f32_16x16x32_bf16 v[212:215], a[164:167], v[224:227], v[212:215]\n"
"s_waitcnt lgkmcnt(2)\n"
"v_mfma_f32_16x16x32_bf16 v[0:3], a[168:171], v[228:231], v[0:3]\n"
"global_load_dwordx4 a[144:147], v239, s[58:59] offset:0 sc1\n"
"v_mfma_f32_16x16x32_bf16 v[4:7], a[172:175], v[228:231], v[4:7]\n"
"s_add_u32 s14, s98, 256\n"
"v_mfma_f32_16x16x32_bf16 v[8:11], a[176:179], v[228:231], v[8:11]\n"
"ds_read_b128 v[224:227], v235 offset:12288\n"
"v_mfma_f32_16x16x32_bf16 v[12:15], a[180:183], v[228:231], v[12:15]\n"
"s_mul_i32 s15, s14, 0xaaab\n"
"v_mfma_f32_16x16x32_bf16 v[16:19], a[184:187], v[228:231], v[16:19]\n"
"s_lshr_b32 s15, s15, 18\n"
"v_mfma_f32_16x16x32_bf16 v[20:23], a[188:191], v[228:231], v[20:23]\n"
"global_load_dwordx4 a[148:151], v239, s[58:59] offset:1024 sc1\n"
"s_waitcnt lgkmcnt(2)\n"
"v_mfma_f32_16x16x32_bf16 v[24:27], a[168:171], v[216:219], v[24:27]\n"
"s_mul_i32 s15, s15, 0x48000\n"
"v_mfma_f32_16x16x32_bf16 v[28:31], a[172:175], v[216:219], v[28:31]\n"
"s_mul_i32 s14, s14, 0xc000\n"
"v_mfma_f32_16x16x32_bf16 v[32:35], a[176:179], v[216:219], v[32:35]\n"
"ds_read_b128 v[228:231], v235 offset:16384\n"
"v_mfma_f32_16x16x32_bf16 v[36:39], a[180:183], v[216:219], v[36:39]\n"
"s_add_u32 s14, s14, s15\n"
"v_mfma_f32_16x16x32_bf16 v[40:43], a[184:187], v[216:219], v[40:43]\n"
"global_load_dwordx4 a[152:155], v239, s[58:59] offset:2048 sc1\n"
"v_mfma_f32_16x16x32_bf16 v[44:47], a[188:191], v[216:219], v[44:47]\n"
"s_waitcnt lgkmcnt(2)\n"
"v_mfma_f32_16x16x32_bf16 v[48:51], a[168:171], v[220:223], v[48:51]\n"
"s_lshl2_add_u32 s36, s95, s14\n"
"v_mfma_f32_16x16x32_bf16 v[52:55], a[172:175], v[220:223], v[52:55]\n"
"s_add_u32 s37, s36, 0x48000\n"
"v_mfma_f32_16x16x32_bf16 v[56:59], a[176:179], v[220:223], v[56:59]\n"
"ds_read_b128 v[216:219], v235 offset:20480\n"
"v_mfma_f32_16x16x32_bf16 v[60:63], a[180:183], v[220:223], v[60:63]\n"
"global_load_dwordx4 a[156:159], v239, s[58:59] offset:3072 sc1\n"
"v_mfma_f32_16x16x32_bf16 v[64:67], a[184:187], v[220:223], v[64:67]\n"
"s_lshl2_add_u32 s42, s96, s14\n"
"v_mfma_f32_16x16x32_bf16 v[68:71], a[188:191], v[220:223], v[68:71]\n"
"s_waitcnt lgkmcnt(2)\n"
"v_mfma_f32_16x16x32_bf16 v[72:75], a[168:171], v[224:227], v[72:75]\n"
"s_add_u32 s43, s42, 0x48000\n"
"v_mfma_f32_16x16x32_bf16 v[76:79], a[172:175], v[224:227], v[76:79]\n"
"s_add_u32 s58, s24, 0x9000\n"
"s_addc_u32 s59, s25, 0\n"
"v_mfma_f32_16x16x32_bf16 v[80:83], a[176:179], v[224:227], v[80:83]\n"
"ds_read_b128 v[220:223], v235 offset:24576\n"
"global_load_dwordx4 a[160:163], v239, s[58:59] offset:-4096 sc1\n"
"v_mfma_f32_16x16x32_bf16 v[84:87], a[180:183], v[224:227], v[84:87]\n"
"s_lshl2_add_u32 s48, s97, s14\n"
"v_mfma_f32_16x16x32_bf16 v[88:91], a[184:187], v[224:227], v[88:91]\n"
"s_add_u32 s49, s48, 0x48000\n"
"v_mfma_f32_16x16x32_bf16 v[92:95], a[188:191], v[224:227], v[92:95]\n"
"s_waitcnt lgkmcnt(2)\n"
"v_mfma_f32_16x16x32_bf16 v[96:99], a[168:171], v[228:231], v[96:99]\n"
"s_bitcmp1_b32 s92, 0\n"
"s_cselect_b32 s93, 0x6000, 0\n"
"v_mfma_f32_16x16x32_bf16 v[100:103], a[172:175], v[228:231], v[100:103]\n"
"global_load_dwordx4 a[164:167], v239, s[58:59] offset:-3072 sc1\n"
"v_mfma_f32_16x16x32_bf16 v[104:107], a[176:179], v[228:231], v[104:107]\n"
"ds_read_b128 v[224:227], v235 offset:28672\n"
"v_mfma_f32_16x16x32_bf16 v[108:111], a[180:183], v[228:231], v[108:111]\n"
"s_sub_u32 s94, s93, 0xc000\n"
"v_mfma_f32_16x16x32_bf16 v[112:115], a[184:187], v[228:231], v[112:115]\n"
"v_add_u32 v247, s93, v237\n"
"v_mfma_f32_16x16x32_bf16 v[116:119], a[188:191], v[228:231], v[116:119]\n"
"s_waitcnt lgkmcnt(2)\n"
"v_mfma_f32_16x16x32_bf16 v[120:123], a[168:171], v[216:219], v[120:123]\n"
"v_add_u32 v237, s94, v237\n"
"v_mfma_f32_16x16x32_bf16 v[124:127], a[172:175], v[216:219], v[124:127]\n"
"v_min_u32 v237, v237, v247\n"
"v_mfma_f32_16x16x32_bf16 v[128:131], a[176:179], v[216:219], v[128:131]\n"
"ds_read_b128 v[228:231], v235 offset:32768\n"
"v_add_u32 v235, 0xffff7000, v235\n"
"v_mfma_f32_16x16x32_bf16 v[132:135], a[180:183], v[216:219], v[132:135]\n"
"v_add_u32 v247, s93, v238\n"
"v_mfma_f32_16x16x32_bf16 v[136:139], a[184:187], v[216:219], v[136:139]\n"
"v_add_u32 v238, s94, v238\n"
"v_mfma_f32_16x16x32_bf16 v[140:143], a[188:191], v[216:219], v[140:143]\n"
"s_waitcnt lgkmcnt(2)\n"
"v_mfma_f32_16x16x32_bf16 v[144:147], a[168:171], v[220:223], v[144:147]\n"
"v_min_u32 v238, v238, v247\n"
"v_mfma_f32_16x16x32_bf16 v[148:151], a[172:175], v[220:223], v[148:151]\n"
"s_waitcnt vmcnt(12)\n"
"s_barrier\n"
"v_mfma_f32_16x16x32_bf16 v[152:155], a[176:179], v[220:223], v[152:155]\n"
"ds_read_b128 v[216:219], v232 offset:0\n"
"v_mfma_f32_16x16x32_bf16 v[156:159], a[180:183], v[220:223], v[156:159]\n"
"s_mov_b64 exec, s[6:7]\n"
"s_cbranch_execz LHT1\n"
"LHR1:\n"
"s_mov_b64 exec, -1\n"
"v_mfma_f32_16x16x32_bf16 v[160:163], a[184:187], v[220:223], v[160:163]\n"
"v_mfma_f32_16x16x32_bf16 v[164:167], a[188:191], v[220:223], v[164:167]\n"
"s_waitcnt lgkmcnt(2)\n"
"v_mfma_f32_16x16x32_bf16 v[168:171], a[168:171], v[224:227], v[168:171]\n"
"v_mfma_f32_16x16x32_bf16 v[172:175], a[172:175], v[224:227], v[172:175]\n"
"v_mfma_f32_16x16x32_bf16 v[176:179], a[176:179], v[224:227], v[176:179]\n"
"ds_read_b128 v[220:223], v232 offset:4096\n"
"v_mfma_f32_16x16x32_bf16 v[180:183], a[180:183], v[224:227], v[180:183]\n"
"v_mfma_f32_16x16x32_bf16 v[184:187], a[184:187], v[224:227], v[184:187]\n"
"v_mfma_f32_16x16x32_bf16 v[188:191], a[188:191], v[224:227], v[188:191]\n"
"s_waitcnt lgkmcnt(2)\n"
"v_mfma_f32_16x16x32_bf16 v[192:195], a[168:171], v[228:231], v[192:195]\n"
"v_mfma_f32_16x16x32_bf16 v[196:199], a[172:175], v[228:231], v[196:199]\n"
"v_mfma_f32_16x16x32_bf16 v[200:203], a[176:179], v[228:231], v[200:203]\n"
"ds_read_b128 v[224:227], v232 offset:8192\n"
"v_mfma_f32_16x16x32_bf16 v[204:207], a[180:183], v[228:231], v[204:207]\n"
"v_mfma_f32_16x16x32_bf16 v[208:211], a[184:187], v[228:231], v[208:211]\n"
"v_mfma_f32_16x16x32_bf16 v[212:215], a[188:191], v[228:231], v[212:215]\n"
"s_waitcnt lgkmcnt(2)\n"
"v_mfma_f32_16x16x32_bf16 v[0:3], a[192:195], v[216:219], v[0:3]\n"
"global_load_dwordx4 a[168:171], v239, s[58:59] offset:-2048 sc1\n"
"v_mfma_f32_16x16x32_bf16 v[4:7], a[196:199], v[216:219], v[4:7]\n"
"v_mfma_f32_16x16x32_bf16 v[8:11], a[200:203], v[216:219], v[8:11]\n"
"ds_read_b128 v[228:231], v232 offset:12288\n"
"v_mfma_f32_16x16x32_bf16 v[12:15], a[204:207], v[216:219], v[12:15]\n"
"v_mfma_f32_16x16x32_bf16 v[16:19], a[208:211], v[216:219], v[16:19]\n"
"v_mfma_f32_16x16x32_bf16 v[20:23], a[212:215], v[216:219], v[20:23]\n"
"global_load_dwordx4 a[172:175], v239, s[58:59] offset:-1024 sc1\n"
"s_waitcnt lgkmcnt(2)\n"
"v_mfma_f32_16x16x32_bf16 v[24:27], a[192:195], v[220:223], v[24:27]\n"
"LHT1:\n"
"s_cbranch_execz LHR1\n"
"v_mfma_f32_16x16x32_bf16 v[28:31], a[196:199], v[220:223], v[28:31]\n"
"v_mfma_f32_16x16x32_bf16 v[32:35], a[200:203], v[220:223], v[32:35]\n"
"ds_read_b128 v[216:219], v232 offset:16384\n"
"v_mfma_f32_16x16x32_bf16 v[36:39], a[204:207], v[220:223], v[36:39]\n"
"v_mfma_f32_16x16x32_bf16 v[40:43], a[208:211], v[220:223], v[40:43]\n"
"global_load_dwordx4 a[176:179], v239, s[58:59] offset:0 sc1\n"
"v_mfma_f32_16x16x32_bf16 v[44:47], a[212:215], v[220:223], v[44:47]\n"
"s_waitcnt lgkmcnt(2)\n"
"v_mfma_f32_16x16x32_bf16 v[48:51], a[192:195], v[224:227], v[48:51]\n"
"v_mfma_f32_16x16x32_bf16 v[52:55], a[196:199], v[224:227], v[52:55]\n"
"v_mfma_f32_16x16x32_bf16 v[56:59], a[200:203], v[224:227], v[56:59]\n"
"ds_read_b128 v[220:223], v232 offset:20480\n"
"v_mfma_f32_16x16x32_bf16 v[60:63], a[204:207], v[224:227], v[60:63]\n"
"global_load_dwordx4 a[180:183], v239, s[58:59] offset:1024 sc1\n"
"v_mfma_f32_16x16x32_bf16 v[64:67], a[208:211], v[224:227], v[64:67]\n"
"v_mfma_f32_16x16x32_bf16 v[68:71], a[212:215], v[224:227], v[68:71]\n"
"s_waitcnt lgkmcnt(2)\n"
"v_mfma_f32_16x16x32_bf16 v[72:75], a[192:195], v[228:231], v[72:75]\n"
"v_mfma_f32_16x16x32_bf16 v[76:79], a[196:199], v[228:231], v[76:79]\n"
"v_mfma_f32_16x16x32_bf16 v[80:83], a[200:203], v[228:231], v[80:83]\n"
"ds_read_b128 v[224:227], v232 offset:24576\n"
"global_load_dwordx4 a[184:187], v239, s[58:59] offset:2048 sc1\n"
"v_mfma_f32_16x16x32_bf16 v[84:87], a[204:207], v[228:231], v[84:87]\n"
"v_mfma_f32_16x16x32_bf16 v[88:91], a[208:211], v[228:231], v[88:91]\n"
"v_mfma_f32_16x16x32_bf16 v[92:95], a[212:215], v[228:231], v[92:95]\n"
"s_waitcnt lgkmcnt(2)\n"
"v_mfma_f32_16x16x32_bf16 v[96:99], a[192:195], v[216:219], v[96:99]\n"
"v_mfma_f32_16x16x32_bf16 v[100:103], a[196:199], v[216:219], v[100:103]\n"
"s_add_u32 s52, s20, s32\n"
"s_addc_u32 s53, s21, 0\n"
"s_add_u32 m0, s31, 0x0\n"
"v_mfma_f32_16x16x32_bf16 v[104:107], a[200:203], v[216:219], v[104:107]\n"
"ds_read_b128 v[228:231], v232 offset:28672\n"
"global_load_lds_dwordx4 v237, s[52:53] offset:0\n"
"v_mfma_f32_16x16x32_bf16 v[108:111], a[204:207], v[216:219], v[108:111]\n"
"v_mfma_f32_16x16x32_bf16 v[112:115], a[208:211], v[216:219], v[112:115]\n"
"v_mfma_f32_16x16x32_bf16 v[116:119], a[212:215], v[216:219], v[116:119]\n"
"s_add_u32 m0, s31, 0x1f00\n"
"s_waitcnt lgkmcnt(2)\n"
"v_mfma_f32_16x16x32_bf16 v[120:123], a[192:195], v[220:223], v[120:123]\n"
"global_load_lds_dwordx4 v237, s[52:53] offset:256\n"
"v_mfma_f32_16x16x32_bf16 v[124:127], a[196:199], v[220:223], v[124:127]\n"
"v_mfma_f32_16x16x32_bf16 v[128:131], a[200:203], v[220:223], v[128:131]\n"
"ds_read_b128 v[216:219], v232 offset:32768\n"
"v_add_u32 v232, v249, v232\n"
"v_mfma_f32_16x16x32_bf16 v[132:135], a[204:207], v[220:223], v[132:135]\n"
"s_add_u32 m0, s31, 0x3e00\n"
"v_mfma_f32_16x16x32_bf16 v[136:139], a[208:211], v[220:223], v[136:139]\n"
"global_load_lds_dwordx4 v237, s[52:53] offset:512\n"
"v_mfma_f32_16x16x32_bf16 v[140:143], a[212:215], v[220:223], v[140:143]\n"
"s_waitcnt lgkmcnt(2)\n"
"v_mfma_f32_16x16x32_bf16 v[144:147], a[192:195], v[224:227], v[144:147]\n"
"v_mfma_f32_16x16x32_bf16 v[148:151], a[196:199], v[224:227], v[148:151]\n"
"s_add_u32 m0, s31, 0x1000\n"
"v_mfma_f32_16x16x32_bf16 v[152:155], a[200:203], v[224:227], v[152:155]\n"
"ds_read_b128 v[220:223], v233 offset:0\n"
"global_load_lds_dwordx4 v238, s[52:53] offset:0\n"
"v_mfma_f32_16x16x32_bf16 v[156:159], a[204:207], v[224:227], v[156:159]\n"
"v_mfma_f32_16x16x32_bf16 v[160:163], a[208:211], v[224:227], v[160:163]\n"
"v_mfma_f32_16x16x32_bf16 v[164:167], a[212:215], v[224:227], v[164:167]\n"
"s_add_u32 m0, s31, 0x2f00\n"
"s_waitcnt lgkmcnt(2)\n"
"v_mfma_f32_16x16x32_bf16 v[168:171], a[192:195], v[228:231], v[168:171]\n"
"global_load_lds_dwordx4 v238, s[52:53] offset:256\n"
"v_mfma_f32_16x16x32_bf16 v[172:175], a[196:199], v[228:231], v[172:175]\n"
"v_mfma_f32_16x16x32_bf16 v[176:179], a[200:203], v[228:231], v[176:179]\n"
"ds_read_b128 v[224:227], v233 offset:4096\n"
"v_mfma_f32_16x16x32_bf16 v[180:183], a[204:207], v[228:231], v[180:183]\n"
"s_add_u32 m0, s31, 0x4e00\n"
"v_mfma_f32_16x16x32_bf16 v[184:187], a[208:211], v[228:231], v[184:187]\n"
"s_memrealtime s[84:85]\n"
"global_load_lds_dwordx4 v238, s[52:53] offset:512\n"
"v_mfma_f32_16x16x32_bf16 v[188:191], a[212:215], v[228:231], v[188:191]\n"
"v_mov_b32 v244, s80\n"
"s_waitcnt lgkmcnt(2)\n"
"v_mfma_f32_16x16x32_bf16 v[192:195], a[192:195], v[216:219], v[192:195]\n"
"v_mfma_f32_16x16x32_bf16 v[196:199], a[196:199], v[216:219], v[196:199]\n"
"v_mov_b32 v245, s81\n"
"v_mfma_f32_16x16x32_bf16 v[200:203], a[200:203], v[216:219], v[200:203]\n"
"ds_read_b128 v[228:231], v233 offset:8192\n"
"v_mfma_f32_16x16x32_bf16 v[204:207], a[204:207], v[216:219], v[204:207]\n"
"v_mov_b32 v251, s77\n"
"v_mfma_f32_16x16x32_bf16 v[208:211], a[208:211], v[216:219], v[208:211]\n"
"v_mfma_f32_16x16x32_bf16 v[212:215], a[212:215], v[216:219], v[212:215]\n"
"s_waitcnt lgkmcnt(0)\n"
"v_mov_b32 v246, s84\n"
"v_mfma_f32_16x16x32_bf16 v[0:3], a[216:219], v[220:223], v[0:3]\n"
"v_mfma_f32_16x16x32_bf16 v[4:7], a[220:223], v[220:223], v[4:7]\n"
"s_lshl_b32 s90, s76, 7\n"
"s_add_u32 s90, s90, s77\n"
"s_cmp_ge_u32 s90, 4224\n"
"s_cbranch_scc1 LQ_0\n"
"s_mov_b64 exec, 1\n"
"global_store_dwordx4 v251, v[244:247], s[28:29] offset:3072 sc1\n"
"s_mov_b64 exec, -1\n"
"LQ_0:\n"
"s_nop 0\n"
"v_mfma_f32_16x16x32_bf16 v[8:11], a[224:227], v[220:223], v[8:11]\n"
"ds_read_b128 v[216:219], v233 offset:12288\n"
"v_mfma_f32_16x16x32_bf16 v[12:15], a[228:231], v[220:223], v[12:15]\n"
"v_mfma_f32_16x16x32_bf16 v[16:19], a[232:235], v[220:223], v[16:19]\n"
"v_mfma_f32_16x16x32_bf16 v[20:23], a[236:239], v[220:223], v[20:23]\n"
"v_mfma_f32_16x16x32_bf16 v[24:27], a[216:219], v[224:227], v[24:27]\n"
"v_mfma_f32_16x16x32_bf16 v[28:31], a[220:223], v[224:227], v[28:31]\n"
"v_mfma_f32_16x16x32_bf16 v[32:35], a[224:227], v[224:227], v[32:35]\n"
"ds_read_b128 v[220:223], v233 offset:16384\n"
"v_mfma_f32_16x16x32_bf16 v[36:39], a[228:231], v[224:227], v[36:39]\n"
"v_mfma_f32_16x16x32_bf16 v[40:43], a[232:235], v[224:227], v[40:43]\n"
"v_mfma_f32_16x16x32_bf16 v[44:47], a[236:239], v[224:227], v[44:47]\n"
"v_mfma_f32_16x16x32_bf16 v[48:51], a[216:219], v[228:231], v[48:51]\n"
"v_mfma_f32_16x16x32_bf16 v[52:55], a[220:223], v[228:231], v[52:55]\n"
"global_load_dwordx4 a[188:191], v239, s[58:59] offset:3072 sc1\n"
"v_mfma_f32_16x16x32_bf16 v[56:59], a[224:227], v[228:231], v[56:59]\n"
"ds_read_b128 v[224:227], v233 offset:20480\n"
"v_mfma_f32_16x16x32_bf16 v[60:63], a[228:231], v[228:231], v[60:63]\n"
"v_mfma_f32_16x16x32_bf16 v[64:67], a[232:235], v[228:231], v[64:67]\n"
"v_mfma_f32_16x16x32_bf16 v[68:71], a[236:239], v[228:231], v[68:71]\n"
"s_add_u32 s58, s24, 0xb000\n"
"s_addc_u32 s59, s25, 0\n"
"s_waitcnt lgkmcnt(2)\n"
"v_mfma_f32_16x16x32_bf16 v[72:75], a[216:219], v[216:219], v[72:75]\n"
"global_load_dwordx4 a[192:195], v239, s[58:59] offset:-4096 sc1\n"
"v_mfma_f32_16x16x32_bf16 v[76:79], a[220:223], v[216:219], v[76:79]\n"
"v_mfma_f32_16x16x32_bf16 v[80:83], a[224:227], v[216:219], v[80:83]\n"
"ds_read_b128 v[228:231], v233 offset:24576\n"
"v_mfma_f32_16x16x32_bf16 v[84:87], a[228:231], v[216:219], v[84:87]\n"
"v_mfma_f32_16x16x32_bf16 v[88:91], a[232:235], v[216:219], v[88:91]\n"
"v_mfma_f32_16x16x32_bf16 v[92:95], a[236:239], v[216:219], v[92:95]\n"
"global_load_dwordx4 a[196:199], v239, s[58:59] offset:-3072 sc1\n"
"s_waitcnt lgkmcnt(2)\n"
"v_mfma_f32_16x16x32_bf16 v[96:99], a[216:219], v[220:223], v[96:99]\n"
"v_mfma_f32_16x16x32_bf16 v[100:103], a[220:223], v[220:223], v[100:103]\n"
"v_mfma_f32_16x16x32_bf16 v[104:107], a[224:227], v[220:223], v[104:107]\n"
"ds_read_b128 v[216:219], v233 offset:28672\n"
"v_mfma_f32_16x16x32_bf16 v[108:111], a[228:231], v[220:223], v[108:111]\n"
"v_mfma_f32_16x16x32_bf16 v[112:115], a[232:235], v[220:223], v[112:115]\n"
"global_load_dwordx4 a[200:203], v239, s[58:59] offset:-2048 sc1\n"
"v_mfma_f32_16x16x32_bf16 v[116:119], a[236:239], v[220:223], v[116:119]\n"
"s_waitcnt lgkmcnt(2)\n"
"v_mfma_f32_16x16x32_bf16 v[120:123], a[216:219], v[224:227], v[120:123]\n"
"v_mfma_f32_16x16x32_bf16 v[124:127], a[220:223], v[224:227], v[124:127]\n"
"v_mfma_f32_16x16x32_bf16 v[128:131], a[224:227], v[224:227], v[128:131]\n"
"ds_read_b128 v[220:223], v233 offset:32768\n"
"v_add_u32 v233, v249, v233\n"
"v_mfma_f32_16x16x32_bf16 v[132:135], a[228:231], v[224:227], v[132:135]\n"
"global_load_dwordx4 a[204:207], v239, s[58:59] offset:-1024 sc1\n"
"v_mfma_f32_16x16x32_bf16 v[136:139], a[232:235], v[224:227], v[136:139]\n"
"v_mfma_f32_16x16x32_bf16 v[140:143], a[236:239], v[224:227], v[140:143]\n"
"s_waitcnt lgkmcnt(2)\n"
"v_mfma_f32_16x16x32_bf16 v[144:147], a[216:219], v[228:231], v[144:147]\n"
"v_mfma_f32_16x16x32_bf16 v[148:151], a[220:223], v[228:231], v[148:151]\n"
"v_mfma_f32_16x16x32_bf16 v[152:155], a[224:227], v[228:231], v[152:155]\n"
"ds_read_b128 v[224:227], v234 offset:0\n"
"global_load_dwordx4 a[208:211], v239, s[58:59] offset:0 sc1\n"
"v_mfma_f32_16x16x32_bf16 v[156:159], a[228:231], v[228:231], v[156:159]\n"
"v_mfma_f32_16x16x32_bf16 v[160:163], a[232:235], v[228:231], v[160:163]\n"
"v_mfma_f32_16x16x32_bf16 v[164:167], a[236:239], v[228:231], v[164:167]\n"
"s_waitcnt lgkmcnt(2)\n"
"v_mfma_f32_16x16x32_bf16 v[168:171], a[216:219], v[216:219], v[168:171]\n"
"v_mfma_f32_16x16x32_bf16 v[172:175], a[220:223], v[216:219], v[172:175]\n"
"global_load_dwordx4 a[212:215], v239, s[58:59] offset:1024 sc1\n"
"v_mfma_f32_16x16x32_bf16 v[176:179], a[224:227], v[216:219], v[176:179]\n"
"ds_read_b128 v[228:231], v234 offset:4096\n"
"v_mfma_f32_16x16x32_bf16 v[180:183], a[228:231], v[216:219], v[180:183]\n"
"v_mfma_f32_16x16x32_bf16 v[184:187], a[232:235], v[216:219], v[184:187]\n"
"v_mfma_f32_16x16x32_bf16 v[188:191], a[236:239], v[216:219], v[188:191]\n"
"s_waitcnt lgkmcnt(2)\n"
"v_mfma_f32_16x16x32_bf16 v[192:195], a[216:219], v[220:223], v[192:195]\n"
"s_add_u32 s52, s20, s33\n"
"s_addc_u32 s53, s21, 0\n"
"s_add_u32 m0, s31, 0x6000\n"
"v_mfma_f32_16x16x32_bf16 v[196:199], a[220:223], v[220:223], v[196:199]\n"
"global_load_lds_dwordx4 v237, s[52:53] offset:0\n"
"v_mfma_f32_16x16x32_bf16 v[200:203], a[224:227], v[220:223], v[200:203]\n"
"ds_read_b128 v[216:219], v234 offset:8192\n"
"v_mfma_f32_16x16x32_bf16 v[204:207], a[228:231], v[220:223], v[204:207]\n"
"v_mfma_f32_16x16x32_bf16 v[208:211], a[232:235], v[220:223], v[208:211]\n"
"s_add_u32 m0, s31, 0x7f00\n"
"v_mfma_f32_16x16x32_bf16 v[212:215], a[236:239], v[220:223], v[212:215]\n"
"global_load_lds_dwordx4 v237, s[52:53] offset:256\n"
"s_waitcnt lgkmcnt(2)\n"
"v_mfma_f32_16x16x32_bf16 v[0:3], a[0:3], v[224:227], v[0:3]\n"
"ds_read_b128 v[244:247], v250\n"
"v_mfma_f32_16x16x32_bf16 v[4:7], a[4:7], v[224:227], v[4:7]\n"
"ds_read_b64 v[252:253], v250 offset:16\n"
"v_mfma_f32_16x16x32_bf16 v[8:11], a[8:11], v[224:227], v[8:11]\n"
"ds_read_b128 v[220:223], v234 offset:12288\n"
"s_add_u32 m0, s31, 0x9e00\n"
"v_mfma_f32_16x16x32_bf16 v[12:15], a[12:15], v[224:227], v[12:15]\n"
"global_load_lds_dwordx4 v237, s[52:53] offset:512\n"
"v_mfma_f32_16x16x32_bf16 v[16:19], a[16:19], v[224:227], v[16:19]\n"
"v_mfma_f32_16x16x32_bf16 v[20:23], a[20:23], v[224:227], v[20:23]\n"
"s_waitcnt lgkmcnt(4)\n"
"v_mfma_f32_16x16x32_bf16 v[24:27], a[0:3], v[228:231], v[24:27]\n"
"s_add_u32 m0, s31, 0x7000\n"
"v_mfma_f32_16x16x32_bf16 v[28:31], a[4:7], v[228:231], v[28:31]\n"
"global_load_lds_dwordx4 v238, s[52:53] offset:0\n"
"v_mfma_f32_16x16x32_bf16 v[32:35], a[8:11], v[228:231], v[32:35]\n"
"ds_read_b128 v[224:227], v234 offset:16384\n"
"v_mfma_f32_16x16x32_bf16 v[36:39], a[12:15], v[228:231], v[36:39]\n"
"v_mfma_f32_16x16x32_bf16 v[40:43], a[16:19], v[228:231], v[40:43]\n"
"s_add_u32 m0, s31, 0x8f00\n"
"v_mfma_f32_16x16x32_bf16 v[44:47], a[20:23], v[228:231], v[44:47]\n"
"global_load_lds_dwordx4 v238, s[52:53] offset:256\n"
"s_waitcnt lgkmcnt(4)\n"
"v_mfma_f32_16x16x32_bf16 v[48:51], a[0:3], v[216:219], v[48:51]\n"
"v_mfma_f32_16x16x32_bf16 v[52:55], a[4:7], v[216:219], v[52:55]\n"
"v_mfma_f32_16x16x32_bf16 v[56:59], a[8:11], v[216:219], v[56:59]\n"
"ds_read_b128 v[228:231], v234 offset:20480\n"
"s_add_u32 m0, s31, 0xae00\n"
"v_mfma_f32_16x16x32_bf16 v[60:63], a[12:15], v[216:219], v[60:63]\n"
"global_load_lds_dwordx4 v238, s[52:53] offset:512\n"
"v_mfma_f32_16x16x32_bf16 v[64:67], a[16:19], v[216:219], v[64:67]\n"
"v_mfma_f32_16x16x32_bf16 v[68:71], a[20:23], v[216:219], v[68:71]\n"
"s_waitcnt lgkmcnt(2)\n"
"v_mfma_f32_16x16x32_bf16 v[72:75], a[0:3], v[220:223], v[72:75]\n"
"v_mfma_f32_16x16x32_bf16 v[76:79], a[4:7], v[220:223], v[76:79]\n"
"v_mfma_f32_16x16x32_bf16 v[80:83], a[8:11], v[220:223], v[80:83]\n"
"ds_read_b128 v[216:219], v234 offset:24576\n"
"v_mfma_f32_16x16x32_bf16 v[84:87], a[12:15], v[220:223], v[84:87]\n"
"v_mfma_f32_16x16x32_bf16 v[88:91], a[16:19], v[220:223], v[88:91]\n"
"v_mfma_f32_16x16x32_bf16 v[92:95], a[20:23], v[220:223], v[92:95]\n"
"s_waitcnt lgkmcnt(2)\n"
"v_mfma_f32_16x16x32_bf16 v[96:99], a[0:3], v[224:227], v[96:99]\n"
"v_mfma_f32_16x16x32_bf16 v[100:103], a[4:7], v[224:227], v[100:103]\n"
"v_mfma_f32_16x16x32_bf16 v[104:107], a[8:11], v[224:227], v[104:107]\n"
"ds_read_b128 v[220:223], v234 offset:28672\n"
"v_mfma_f32_16x16x32_bf16 v[108:111], a[12:15], v[224:227], v[108:111]\n"
"v_mfma_f32_16x16x32_bf16 v[112:115], a[16:19], v[224:227], v[112:115]\n"
"v_mfma_f32_16x16x32_bf16 v[116:119], a[20:23], v[224:227], v[116:119]\n"
"s_waitcnt lgkmcnt(2)\n"
"v_mfma_f32_16x16x32_bf16 v[120:123], a[0:3], v[228:231], v[120:123]\n"
"v_mfma_f32_16x16x32_bf16 v[124:127], a[4:7], v[228:231], v[124:127]\n"
"v_mfma_f32_16x16x32_bf16 v[128:131], a[8:11], v[228:231], v[128:131]\n"
"ds_read_b128 v[224:227], v234 offset:32768\n"
"v_add_u32 v234, v249, v234\n"
"v_mfma_f32_16x16x32_bf16 v[132:135], a[12:15], v[228:231], v[132:135]\n"
"v_mfma_f32_16x16x32_bf16 v[136:139], a[16:19], v[228:231], v[136:139]\n"
"v_mfma_f32_16x16x32_bf16 v[140:143], a[20:23], v[228:231], v[140:143]\n"
"s_waitcnt lgkmcnt(2)\n"
"v_mfma_f32_16x16x32_bf16 v[144:147], a[0:3], v[216:219], v[144:147]\n"
"global_load_dwordx4 a[216:219], v239, s[58:59] offset:2048 sc1\n"
"v_mfma_f32_16x16x32_bf16 v[148:151], a[4:7], v[216:219], v[148:151]\n"
"v_mfma_f32_16x16x32_bf16 v[152:155], a[8:11], v[216:219], v[152:155]\n"
"ds_read_b128 v[228:231], v235 offset:4096\n"
"v_mfma_f32_16x16x32_bf16 v[156:159], a[12:15], v[216:219], v[156:159]\n"
"v_mfma_f32_16x16x32_bf16 v[160:163], a[16:19], v[216:219], v[160:163]\n"
"v_mfma_f32_16x16x32_bf16 v[164:167], a[20:23], v[216:219], v[164:167]\n"
"global_load_dwordx4 a[220:223], v239, s[58:59] offset:3072 sc1\n"
"s_waitcnt lgkmcnt(2)\n"
"v_mfma_f32_16x16x32_bf16 v[168:171], a[0:3], v[220:223], v[168:171]\n"
"v_mfma_f32_16x16x32_bf16 v[172:175], a[4:7], v[220:223], v[172:175]\n"
"v_mfma_f32_16x16x32_bf16 v[176:179], a[8:11], v[220:223], v[176:179]\n"
"ds_read_b128 v[216:219], v235 offset:8192\n"
"v_mfma_f32_16x16x32_bf16 v[180:183], a[12:15], v[220:223], v[180:183]\n"
"s_add_u32 s58, s24, 0xd000\n"
"s_addc_u32 s59, s25, 0\n"
"v_mfma_f32_16x16x32_bf16 v[184:187], a[16:19], v[220:223], v[184:187]\n"
"global_load_dwordx4 a[224:227], v239, s[58:59] offset:-4096 sc1\n"
"v_mfma_f32_16x16x32_bf16 v[188:191], a[20:23], v[220:223], v[188:191]\n"
"s_waitcnt lgkmcnt(2)\n"
"v_mfma_f32_16x16x32_bf16 v[192:195], a[0:3], v[224:227], v[192:195]\n"
"v_mfma_f32_16x16x32_bf16 v[196:199], a[4:7], v[224:227], v[196:199]\n"
"v_mfma_f32_16x16x32_bf16 v[200:203], a[8:11], v[224:227], v[200:203]\n"
"ds_read_b128 v[220:223], v235 offset:12288\n"
"v_mfma_f32_16x16x32_bf16 v[204:207], a[12:15], v[224:227], v[204:207]\n"
"global_load_dwordx4 a[228:231], v239, s[58:59] offset:-3072 sc1\n"
"v_mfma_f32_16x16x32_bf16 v[208:211], a[16:19], v[224:227], v[208:211]\n"
"v_mfma_f32_16x16x32_bf16 v[212:215], a[20:23], v[224:227], v[212:215]\n"
"s_waitcnt lgkmcnt(2)\n"
"v_mfma_f32_16x16x32_bf16 v[24:27], a[24:27], v[228:231], v[24:27]\n"
"v_mfma_f32_16x16x32_bf16 v[28:31], a[28:31], v[228:231], v[28:31]\n"
"v_mfma_f32_16x16x32_bf16 v[32:35], a[32:35], v[228:231], v[32:35]\n"
"ds_read_b128 v[224:227], v235 offset:16384\n"
"global_load_dwordx4 a[232:235], v239, s[58:59] offset:-2048 sc1\n"
"v_mfma_f32_16x16x32_bf16 v[36:39], a[36:39], v[228:231], v[36:39]\n"
"v_mfma_f32_16x16x32_bf16 v[40:43], a[40:43], v[228:231], v[40:43]\n"
"v_mfma_f32_16x16x32_bf16 v[44:47], a[44:47], v[228:231], v[44:47]\n"
"s_waitcnt lgkmcnt(2)\n"
"v_mfma_f32_16x16x32_bf16 v[48:51], a[24:27], v[216:219], v[48:51]\n"
"v_cvt_pk_bf16_f32 v24, v24, v25\n"
"v_cvt_pk_bf16_f32 v25, v26, v27\n"
"v_mfma_f32_16x16x32_bf16 v[52:55], a[28:31], v[216:219], v[52:55]\n"
"v_cvt_pk_bf16_f32 v26, v28, v29\n"
"v_cvt_pk_bf16_f32 v27, v30, v31\n"
"global_load_dwordx4 a[236:239], v239, s[58:59] offset:-1024 sc1\n"
"v_mfma_f32_16x16x32_bf16 v[56:59], a[32:35], v[216:219], v[56:59]\n"
"ds_read_b128 v[228:231], v235 offset:20480\n"
"v_cvt_pk_bf16_f32 v28, v32, v33\n"
"v_cvt_pk_bf16_f32 v29, v34, v35\n"
"v_mfma_f32_16x16x32_bf16 v[60:63], a[36:39], v[216:219], v[60:63]\n"
"v_cvt_pk_bf16_f32 v30, v36, v37\n"
"v_cvt_pk_bf16_f32 v31, v38, v39\n"
"v_mfma_f32_16x16x32_bf16 v[64:67], a[40:43], v[216:219], v[64:67]\n"
"v_cvt_pk_bf16_f32 v32, v40, v41\n"
"v_cvt_pk_bf16_f32 v33, v42, v43\n"
"v_mfma_f32_16x16x32_bf16 v[68:71], a[44:47], v[216:219], v[68:71]\n"
"v_cvt_pk_bf16_f32 v34, v44, v45\n"
"v_cvt_pk_bf16_f32 v35, v46, v47\n"
"s_waitcnt lgkmcnt(2)\n"
"v_mfma_f32_16x16x32_bf16 v[72:75], a[24:27], v[220:223], v[72:75]\n"
"v_cvt_pk_bf16_f32 v48, v48, v49\n"
"v_cvt_pk_bf16_f32 v49, v50, v51\n"
"global_load_dwordx4 a[0:3], v239, s[58:59] offset:0 sc1\n"
"v_mfma_f32_16x16x32_bf16 v[76:79], a[28:31], v[220:223], v[76:79]\n"
"v_cvt_pk_bf16_f32 v50, v52, v53\n"
"v_cvt_pk_bf16_f32 v51, v54, v55\n"
"v_mfma_f32_16x16x32_bf16 v[80:83], a[32:35], v[220:223], v[80:83]\n"
"ds_read_b128 v[216:219], v235 offset:24576\n"
"v_cvt_pk_bf16_f32 v52, v56, v57\n"
"v_cvt_pk_bf16_f32 v53, v58, v59\n"
"v_mfma_f32_16x16x32_bf16 v[84:87], a[36:39], v[220:223], v[84:87]\n"
"v_cvt_pk_bf16_f32 v54, v60, v61\n"
"v_cvt_pk_bf16_f32 v55, v62, v63\n"
"v_mfma_f32_16x16x32_bf16 v[88:91], a[40:43], v[220:223], v[88:91]\n"
"v_cvt_pk_bf16_f32 v56, v64, v65\n"
"v_cvt_pk_bf16_f32 v57, v66, v67\n"
"v_mfma_f32_16x16x32_bf16 v[92:95], a[44:47], v[220:223], v[92:95]\n"
"v_cvt_pk_bf16_f32 v58, v68, v69\n"
"v_cvt_pk_bf16_f32 v59, v70, v71\n"
"global_load_dwordx4 a[4:7], v239, s[58:59] offset:1024 sc1\n"
"s_waitcnt lgkmcnt(2)\n"
"v_mfma_f32_16x16x32_bf16 v[96:99], a[24:27], v[224:227], v[96:99]\n"
"v_cvt_pk_bf16_f32 v72, v72, v73\n"
"v_cvt_pk_bf16_f32 v73, v74, v75\n"
"v_mfma_f32_16x16x32_bf16 v[100:103], a[28:31], v[224:227], v[100:103]\n"
"v_cvt_pk_bf16_f32 v74, v76, v77\n"
"v_cvt_pk_bf16_f32 v75, v78, v79\n"
"v_mfma_f32_16x16x32_bf16 v[104:107], a[32:35], v[224:227], v[104:107]\n"
"ds_read_b128 v[220:223], v235 offset:28672\n"
"v_cvt_pk_bf16_f32 v76, v80, v81\n"
"v_cvt_pk_bf16_f32 v77, v82, v83\n"
"v_mfma_f32_16x16x32_bf16 v[108:111], a[36:39], v[224:227], v[108:111]\n"
"v_cvt_pk_bf16_f32 v78, v84, v85\n"
"v_cvt_pk_bf16_f32 v79, v86, v87\n"
"v_mfma_f32_16x16x32_bf16 v[112:115], a[40:43], v[224:227], v[112:115]\n"
"v_cvt_pk_bf16_f32 v80, v88, v89\n"
"v_cvt_pk_bf16_f32 v81, v90, v91\n"
"global_load_dwordx4 a[8:11], v239, s[58:59] offset:2048 sc1\n"
"v_mfma_f32_16x16x32_bf16 v[116:119], a[44:47], v[224:227], v[116:119]\n"
"v_cvt_pk_bf16_f32 v82, v92, v93\n"
"v_cvt_pk_bf16_f32 v83, v94, v95\n"
"s_waitcnt lgkmcnt(2)\n"
"v_mfma_f32_16x16x32_bf16 v[120:123], a[24:27], v[228:231], v[120:123]\n"
"s_memrealtime s[84:85]\n"
"v_cvt_pk_bf16_f32 v96, v96, v97\n"
"v_cvt_pk_bf16_f32 v97, v98, v99\n"
"v_mfma_f32_16x16x32_bf16 v[124:127], a[28:31], v[228:231], v[124:127]\n"
"v_cvt_pk_bf16_f32 v98, v100, v101\n"
"v_cvt_pk_bf16_f32 v99, v102, v103\n"
"v_mfma_f32_16x16x32_bf16 v[128:131], a[32:35], v[228:231], v[128:131]\n"
"ds_read_b128 v[224:227], v235 offset:32768\n"
"v_cvt_pk_bf16_f32 v100, v104, v105\n"
"v_cvt_pk_bf16_f32 v101, v106, v107\n"
"v_mfma_f32_16x16x32_bf16 v[132:135], a[36:39], v[228:231], v[132:135]\n"
"v_cvt_pk_bf16_f32 v102, v108, v109\n"
"v_cvt_pk_bf16_f32 v103, v110, v111\n"
"global_load_dwordx4 a[12:15], v239, s[58:59] offset:3072 sc1\n"
"v_mfma_f32_16x16x32_bf16 v[136:139], a[40:43], v[228:231], v[136:139]\n"
"v_cvt_pk_bf16_f32 v104, v112, v113\n"
"v_cvt_pk_bf16_f32 v105, v114, v115\n"
"v_mfma_f32_16x16x32_bf16 v[140:143], a[44:47], v[228:231], v[140:143]\n"
"v_cvt_pk_bf16_f32 v106, v116, v117\n"
"v_cvt_pk_bf16_f32 v107, v118, v119\n"
"s_waitcnt lgkmcnt(2)\n"
"v_mfma_f32_16x16x32_bf16 v[144:147], a[24:27], v[216:219], v[144:147]\n"
"v_cvt_pk_bf16_f32 v120, v120, v121\n"
"v_cvt_pk_bf16_f32 v121, v122, v123\n"
"v_mfma_f32_16x16x32_bf16 v[148:151], a[28:31], v[216:219], v[148:151]\n"
"s_waitcnt lgkmcnt(0)\n"
"s_sub_u32 s86, s83, s84\n"
"v_cvt_pk_bf16_f32 v122, v124, v125\n"
"v_cvt_pk_bf16_f32 v123, v126, v127\n"
"s_add_u32 s58, s24, 0xf000\n"
"s_addc_u32 s59, s25, 0\n"
"v_mfma_f32_16x16x32_bf16 v[152:155], a[32:35], v[216:219], v[152:155]\n"
"ds_read_b128 v[228:231], v235 offset:0\n"
"v_add_u32 v235, v249, v235\n"
"s_waitcnt vmcnt(23)\n"
"s_barrier\n"
"v_cvt_pk_bf16_f32 v124, v128, v129\n"
"v_cvt_pk_bf16_f32 v125, v130, v131\n"
"global_load_dwordx4 a[16:19], v239, s[58:59] offset:-4096 sc1\n"
"v_mfma_f32_16x16x32_bf16 v[156:159], a[36:39], v[216:219], v[156:159]\n"
"ds_read_b128 v[36:39], v232 offset:0\n"
"v_cvt_pk_bf16_f32 v126, v132, v133\n"
"v_cvt_pk_bf16_f32 v127, v134, v135\n"
"v_mfma_f32_16x16x32_bf16 v[160:163], a[40:43], v[216:219], v[160:163]\n"
"v_cvt_pk_bf16_f32 v128, v136, v137\n"
"v_cvt_pk_bf16_f32 v129, v138, v139\n"
"v_mfma_f32_16x16x32_bf16 v[164:167], a[44:47], v[216:219], v[164:167]\n"
"ds_read_b128 v[40:43], v233 offset:0\n"
"v_cvt_pk_bf16_f32 v130, v140, v141\n"
"v_cvt_pk_bf16_f32 v131, v142, v143\n"
"v_mfma_f32_16x16x32_bf16 v[168:171], a[24:27], v[220:223], v[168:171]\n"
"v_cvt_pk_bf16_f32 v144, v144, v145\n"
"v_cvt_pk_bf16_f32 v145, v146, v147\n"
"v_mfma_f32_16x16x32_bf16 v[172:175], a[28:31], v[220:223], v[172:175]\n"
"ds_read_b128 v[44:47], v234 offset:0\n"
"v_cvt_pk_bf16_f32 v146, v148, v149\n"
"v_cvt_pk_bf16_f32 v147, v150, v151\n"
"global_load_dwordx4 a[20:23], v239, s[58:59] offset:-3072 sc1\n"
"v_mfma_f32_16x16x32_bf16 v[176:179], a[32:35], v[220:223], v[176:179]\n"
"v_cvt_pk_bf16_f32 v148, v152, v153\n"
"v_cvt_pk_bf16_f32 v149, v154, v155\n"
"v_mfma_f32_16x16x32_bf16 v[180:183], a[36:39], v[220:223], v[180:183]\n"
"ds_read_b128 v[60:63], v235 offset:0\n"
"v_cvt_pk_bf16_f32 v150, v156, v157\n"
"v_cvt_pk_bf16_f32 v151, v158, v159\n"
"v_mfma_f32_16x16x32_bf16 v[184:187], a[40:43], v[220:223], v[184:187]\n"
"v_cvt_pk_bf16_f32 v152, v160, v161\n"
"v_cvt_pk_bf16_f32 v153, v162, v163\n"
"v_mfma_f32_16x16x32_bf16 v[188:191], a[44:47], v[220:223], v[188:191]\n"
"ds_read_b128 v[64:67], v232 offset:8192\n"
"v_cvt_pk_bf16_f32 v154, v164, v165\n"
"v_cvt_pk_bf16_f32 v155, v166, v167\n"
"v_mfma_f32_16x16x32_bf16 v[192:195], a[24:27], v[224:227], v[192:195]\n"
"v_cvt_pk_bf16_f32 v168, v168, v169\n"
"v_cvt_pk_bf16_f32 v169, v170, v171\n"
"global_load_dwordx4 v[140:143], v239, s[58:59] offset:-2048 sc1\n"
"v_mfma_f32_16x16x32_bf16 v[196:199], a[28:31], v[224:227], v[196:199]\n"
"ds_read_b128 v[68:71], v233 offset:8192\n"
"v_cvt_pk_bf16_f32 v170, v172, v173\n"
"v_cvt_pk_bf16_f32 v171, v174, v175\n"
"v_mfma_f32_16x16x32_bf16 v[200:203], a[32:35], v[224:227], v[200:203]\n"
"v_cvt_pk_bf16_f32 v172, v176, v177\n"
"v_cvt_pk_bf16_f32 v173, v178, v179\n"
"v_mfma_f32_16x16x32_bf16 v[204:207], a[36:39], v[224:227], v[204:207]\n"
"ds_read_b128 v[84:87], v234 offset:8192\n"
"v_cvt_pk_bf16_f32 v174, v180, v181\n"
"v_cvt_pk_bf16_f32 v175, v182, v183\n"
"v_mfma_f32_16x16x32_bf16 v[208:211], a[40:43], v[224:227], v[208:211]\n"
"v_cvt_pk_bf16_f32 v176, v184, v185\n"
"v_cvt_pk_bf16_f32 v177, v186, v187\n"
"v_mfma_f32_16x16x32_bf16 v[212:215], a[44:47], v[224:227], v[212:215]\n"
"ds_read_b128 v[88:91], v235 offset:8192\n"
"v_cvt_pk_bf16_f32 v178, v188, v189\n"
"v_cvt_pk_bf16_f32 v179, v190, v191\n"
"global_load_dwordx4 v[156:159], v239, s[58:59] offset:-1024 sc1\n"
"s_waitcnt lgkmcnt(8)\n"
"v_mfma_f32_16x16x32_bf16 v[0:3], a[24:27], v[228:231], v[0:3]\n"
"v_cvt_pk_bf16_f32 v192, v192, v193\n"
"v_cvt_pk_bf16_f32 v193, v194, v195\n"
"v_mfma_f32_16x16x32_bf16 v[4:7], a[28:31], v[228:231], v[4:7]\n"
"ds_read_b128 v[92:95], v232 offset:16384\n"
"v_cvt_pk_bf16_f32 v194, v196, v197\n"
"v_cvt_pk_bf16_f32 v195, v198, v199\n"
"v_mfma_f32_16x16x32_bf16 v[8:11], a[32:35], v[228:231], v[8:11]\n"
"v_cvt_pk_bf16_f32 v196, v200, v201\n"
"v_cvt_pk_bf16_f32 v197, v202, v203\n"
"v_mfma_f32_16x16x32_bf16 v[12:15], a[36:39], v[228:231], v[12:15]\n"
"ds_read_b128 v[108:111], v233 offset:16384\n"
"v_cvt_pk_bf16_f32 v198, v204, v205\n"
"v_cvt_pk_bf16_f32 v199, v206, v207\n"
"v_mfma_f32_16x16x32_bf16 v[16:19], a[40:43], v[228:231], v[16:19]\n"
"ds_read_b128 v[112:115], v234 offset:16384\n"
"v_cvt_pk_bf16_f32 v200, v208, v209\n"
"v_cvt_pk_bf16_f32 v201, v210, v211\n"
"global_load_dwordx4 v[160:163], v239, s[58:59] offset:0 sc1\n"
"v_mfma_f32_16x16x32_bf16 v[20:23], a[44:47], v[228:231], v[20:23]\n"
"ds_read_b128 v[116:119], v235 offset:16384\n"
"v_cvt_pk_bf16_f32 v202, v212, v213\n"
"v_cvt_pk_bf16_f32 v203, v214, v215\n"
"s_waitcnt lgkmcnt(8)\n"
"v_mfma_f32_16x16x32_bf16 v[132:135], a[240:243], v[36:39], v[0:3]\n"
"v_mfma_f32_16x16x32_bf16 v[132:135], a[244:247], v[40:43], v[132:135]\n"
"s_bitcmp1_b32 s92, 1\n"
"s_cselect_b32 s93, 0x6000, 0\n"
"v_mfma_f32_16x16x32_bf16 v[132:135], a[248:251], v[44:47], v[132:135]\n"
"s_sub_u32 s94, s93, 0xc000\n"
"v_mfma_f32_16x16x32_bf16 v[132:135], a[252:255], v[60:63], v[132:135]\n"
"global_load_dwordx4 v[164:167], v239, s[58:59] offset:1024 sc1\n"
"s_waitcnt lgkmcnt(4)\n"
"v_mfma_f32_16x16x32_bf16 v[132:135], a[48:51], v[64:67], v[132:135]\n"
"v_add_u32 v231, s93, v237\n"
"v_mfma_f32_16x16x32_bf16 v[132:135], a[52:55], v[68:71], v[132:135]\n"
"v_add_u32 v237, s94, v237\n"
"v_mfma_f32_16x16x32_bf16 v[132:135], a[56:59], v[84:87], v[132:135]\n"
"global_load_dwordx4 v[180:183], v239, s[58:59] offset:2048 sc1\n"
"v_mfma_f32_16x16x32_bf16 v[132:135], a[60:63], v[88:91], v[132:135]\n"
"s_waitcnt lgkmcnt(0)\n"
"v_mfma_f32_16x16x32_bf16 v[132:135], a[64:67], v[92:95], v[132:135]\n"
"v_min_u32 v237, v237, v231\n"
"v_mfma_f32_16x16x32_bf16 v[132:135], a[68:71], v[108:111], v[132:135]\n"
"global_load_dwordx4 v[184:187], v239, s[58:59] offset:3072 sc1\n"
"v_mfma_f32_16x16x32_bf16 v[132:135], a[72:75], v[112:115], v[132:135]\n"
"v_add_u32 v231, s93, v238\n"
"v_mfma_f32_16x16x32_bf16 v[132:135], a[76:79], v[116:119], v[132:135]\n"
"s_mov_b64 exec, s[8:9]\n"
"s_cbranch_execz LHT2\n"
"LHR2:\n"
"s_mov_b64 exec, -1\n"
"v_mfma_f32_16x16x32_bf16 v[136:139], a[80:83], v[36:39], v[4:7]\n"
"s_add_u32 s58, s24, 0x11000\n"
"s_addc_u32 s59, s25, 0\n"
"global_load_dwordx4 v[188:191], v239, s[58:59] offset:-4096 sc1\n"
"v_mfma_f32_16x16x32_bf16 v[136:139], a[84:87], v[40:43], v[136:139]\n"
"v_add_u32 v238, s94, v238\n"
"v_mfma_f32_16x16x32_bf16 v[136:139], a[88:91], v[44:47], v[136:139]\n"
"v_min_u32 v238, v238, v231\n"
"v_mfma_f32_16x16x32_bf16 v[136:139], a[92:95], v[60:63], v[136:139]\n"
"global_load_dwordx4 v[204:207], v239, s[58:59] offset:-3072 sc1\n"
"v_mfma_f32_16x16x32_bf16 v[136:139], a[96:99], v[64:67], v[136:139]\n"
"v_cvt_pk_bf16_f32 v208, v132, v133\n"
"v_mfma_f32_16x16x32_bf16 v[136:139], a[100:103], v[68:71], v[136:139]\n"
"v_cvt_pk_bf16_f32 v209, v134, v135\n"
"v_mfma_f32_16x16x32_bf16 v[136:139], a[104:107], v[84:87], v[136:139]\n"
"global_load_dwordx4 a[24:27], v239, s[58:59] offset:-2048 sc1\n"
"v_mfma_f32_16x16x32_bf16 v[136:139], a[108:111], v[88:91], v[136:139]\n"
"v_mfma_f32_16x16x32_bf16 v[136:139], a[112:115], v[92:95], v[136:139]\n"
"v_mfma_f32_16x16x32_bf16 v[136:139], a[116:119], v[108:111], v[136:139]\n"
"global_load_dwordx4 a[28:31], v239, s[58:59] offset:-1024 sc1\n"
"v_mfma_f32_16x16x32_bf16 v[136:139], a[120:123], v[112:115], v[136:139]\n"
"ds_write_b64 v244, v[208:209] offset:12288\n"
"v_mfma_f32_16x16x32_bf16 v[136:139], a[124:127], v[116:119], v[136:139]\n"
"v_mfma_f32_16x16x32_bf16 v[132:135], a[128:131], v[36:39], v[8:11]\n"
"global_load_dwordx4 a[32:35], v239, s[58:59] offset:0 sc1\n"
"v_mfma_f32_16x16x32_bf16 v[132:135], a[132:135], v[40:43], v[132:135]\n"
"v_mfma_f32_16x16x32_bf16 v[132:135], a[136:139], v[44:47], v[132:135]\n"
"v_mfma_f32_16x16x32_bf16 v[132:135], a[140:143], v[60:63], v[132:135]\n"
"global_load_dwordx4 a[36:39], v239, s[58:59] offset:1024 sc1\n"
"v_mfma_f32_16x16x32_bf16 v[132:135], a[144:147], v[64:67], v[132:135]\n"
"v_cvt_pk_bf16_f32 v210, v136, v137\n"
"v_mfma_f32_16x16x32_bf16 v[132:135], a[148:151], v[68:71], v[132:135]\n"
"v_cvt_pk_bf16_f32 v211, v138, v139\n"
"v_mfma_f32_16x16x32_bf16 v[132:135], a[152:155], v[84:87], v[132:135]\n"
"global_load_dwordx4 a[40:43], v239, s[58:59] offset:2048 sc1\n"
"v_mfma_f32_16x16x32_bf16 v[132:135], a[156:159], v[88:91], v[132:135]\n"
"v_mfma_f32_16x16x32_bf16 v[132:135], a[160:163], v[92:95], v[132:135]\n"
"v_mfma_f32_16x16x32_bf16 v[132:135], a[164:167], v[108:111], v[132:135]\n"
"global_load_dwordx4 a[44:47], v239, s[58:59] offset:3072 sc1\n"
"v_mfma_f32_16x16x32_bf16 v[132:135], a[168:171], v[112:115], v[132:135]\n"
"ds_write_b64 v244, v[210:211] offset:12296\n"
"v_mfma_f32_16x16x32_bf16 v[132:135], a[172:175], v[116:119], v[132:135]\n"
"s_waitcnt vmcnt(38)\n"
"v_mfma_f32_16x16x32_bf16 v[136:139], a[176:179], v[36:39], v[12:15]\n"
"global_load_dwordx3 v[228:230], v248, s[28:29] offset:3072 sc1\n"
"v_mfma_f32_16x16x32_bf16 v[136:139], a[180:183], v[40:43], v[136:139]\n"
"v_mfma_f32_16x16x32_bf16 v[136:139], a[184:187], v[44:47], v[136:139]\n"
"v_mfma_f32_16x16x32_bf16 v[136:139], a[188:191], v[60:63], v[136:139]\n"
"s_add_u32 s52, s20, s34\n"
"s_addc_u32 s53, s21, 0\n"
"s_add_u32 m0, s31, 0xc000\n"
"s_waitcnt vmcnt(35)\n"
"v_mfma_f32_16x16x32_bf16 v[136:139], a[192:195], v[64:67], v[136:139]\n"
"global_load_lds_dwordx4 v237, s[52:53] offset:0\n"
"v_mfma_f32_16x16x32_bf16 v[136:139], a[196:199], v[68:71], v[136:139]\n"
"v_cvt_pk_bf16_f32 v212, v132, v133\n"
"LHT2:\n"
"s_cbranch_execz LHR2\n"
"v_mfma_f32_16x16x32_bf16 v[136:139], a[200:203], v[84:87], v[136:139]\n"
"v_cvt_pk_bf16_f32 v213, v134, v135\n"
"v_mfma_f32_16x16x32_bf16 v[136:139], a[204:207], v[88:91], v[136:139]\n"
"s_waitcnt vmcnt(26)\n"
"v_mfma_f32_16x16x32_bf16 v[136:139], a[208:211], v[92:95], v[136:139]\n"
"s_bitcmp1_b32 s92, 0\n"
"s_cselect_b32 s93, 0x6000, 0\n"
"v_mfma_f32_16x16x32_bf16 v[136:139], a[212:215], v[108:111], v[136:139]\n"
"s_add_u32 m0, s31, 0xdf00\n"
"v_mfma_f32_16x16x32_bf16 v[136:139], a[216:219], v[112:115], v[136:139]\n"
"global_load_lds_dwordx4 v237, s[52:53] offset:256\n"
"v_mfma_f32_16x16x32_bf16 v[136:139], a[220:223], v[116:119], v[136:139]\n"
"ds_write_b64 v245, v[212:213] offset:12288\n"
"s_waitcnt vmcnt(23)\n"
"v_mfma_f32_16x16x32_bf16 v[132:135], a[224:227], v[36:39], v[16:19]\n"
"s_sub_u32 s94, s93, 0xc000\n"
"v_mfma_f32_16x16x32_bf16 v[132:135], a[228:231], v[40:43], v[132:135]\n"
"v_add_u32 v231, s93, v247\n"
"v_mfma_f32_16x16x32_bf16 v[132:135], a[232:235], v[44:47], v[132:135]\n"
"v_add_u32 v247, s94, v247\n"
"v_mfma_f32_16x16x32_bf16 v[132:135], a[236:239], v[60:63], v[132:135]\n"
"s_add_u32 m0, s31, 0xfe00\n"
"s_waitcnt vmcnt(19)\n"
"v_mfma_f32_16x16x32_bf16 v[132:135], a[0:3], v[64:67], v[132:135]\n"
"global_load_lds_dwordx4 v237, s[52:53] offset:512\n"
"v_mfma_f32_16x16x32_bf16 v[132:135], a[4:7], v[68:71], v[132:135]\n"
"v_cvt_pk_bf16_f32 v214, v136, v137\n"
"v_mfma_f32_16x16x32_bf16 v[132:135], a[8:11], v[84:87], v[132:135]\n"
"v_cvt_pk_bf16_f32 v215, v138, v139\n"
"v_mfma_f32_16x16x32_bf16 v[132:135], a[12:15], v[88:91], v[132:135]\n"
"s_waitcnt vmcnt(16)\n"
"v_mfma_f32_16x16x32_bf16 v[132:135], a[16:19], v[92:95], v[132:135]\n"
"v_min_u32 v247, v247, v231\n"
"v_mfma_f32_16x16x32_bf16 v[132:135], a[20:23], v[108:111], v[132:135]\n"
"s_add_u32 m0, s31, 0xd000\n"
"v_mfma_f32_16x16x32_bf16 v[132:135], v[140:143], v[112:115], v[132:135]\n"
"global_load_lds_dwordx4 v238, s[52:53] offset:0\n"
"v_mfma_f32_16x16x32_bf16 v[132:135], v[156:159], v[116:119], v[132:135]\n"
"s_barrier\n"
"ds_write_b64 v245, v[214:215] offset:12296\n"
"s_waitcnt vmcnt(13)\n"
"v_mfma_f32_16x16x32_bf16 v[136:139], v[160:163], v[36:39], v[20:23]\n"
"ds_read_b128 v[36:39], v232 offset:256\n"
"v_mfma_f32_16x16x32_bf16 v[136:139], v[164:167], v[40:43], v[136:139]\n"
"ds_read_b128 v[40:43], v233 offset:256\n"
"v_mfma_f32_16x16x32_bf16 v[136:139], v[180:183], v[44:47], v[136:139]\n"
"ds_read_b128 v[44:47], v234 offset:256\n"
"v_mfma_f32_16x16x32_bf16 v[136:139], v[184:187], v[60:63], v[136:139]\n"
"ds_read_b128 v[60:63], v235 offset:256\n"
"s_add_u32 m0, s31, 0xef00\n"
"s_waitcnt vmcnt(9)\n"
"v_mfma_f32_16x16x32_bf16 v[136:139], v[188:191], v[64:67], v[136:139]\n"
"ds_read_b128 v[64:67], v232 offset:8448\n"
"global_load_lds_dwordx4 v238, s[52:53] offset:256\n"
"v_mfma_f32_16x16x32_bf16 v[136:139], v[204:207], v[68:71], v[136:139]\n"
"ds_read_b128 v[68:71], v233 offset:8448\n"
"v_cvt_pk_bf16_f32 v208, v132, v133\n"
"v_mfma_f32_16x16x32_bf16 v[136:139], a[24:27], v[84:87], v[136:139]\n"
"ds_read_b128 v[84:87], v234 offset:8448\n"
"v_cvt_pk_bf16_f32 v209, v134, v135\n"
"v_mfma_f32_16x16x32_bf16 v[136:139], a[28:31], v[88:91], v[136:139]\n"
"ds_read_b128 v[88:91], v235 offset:8448\n"
"s_waitcnt vmcnt(6)\n"
"v_mfma_f32_16x16x32_bf16 v[136:139], a[32:35], v[92:95], v[136:139]\n"
"ds_read_b128 v[92:95], v232 offset:16640\n"
"v_mfma_f32_16x16x32_bf16 v[136:139], a[36:39], v[108:111], v[136:139]\n"
"ds_read_b128 v[108:111], v233 offset:16640\n"
"s_add_u32 m0, s31, 0x10e00\n"
"v_mfma_f32_16x16x32_bf16 v[136:139], a[40:43], v[112:115], v[136:139]\n"
"ds_read_b128 v[112:115], v234 offset:16640\n"
"global_load_lds_dwordx4 v238, s[52:53] offset:512\n"
"v_mfma_f32_16x16x32_bf16 v[136:139], a[44:47], v[116:119], v[136:139]\n"
"ds_read_b128 v[116:119], v235 offset:16640\n"
"s_waitcnt lgkmcnt(8)\n"
"v_mfma_f32_16x16x32_bf16 v[132:135], a[240:243], v[36:39], v[0:3]\n"
"ds_write_b64 v244, v[208:209] offset:12416\n"
"v_mfma_f32_16x16x32_bf16 v[132:135], a[244:247], v[40:43], v[132:135]\n"
"v_add_u32 v231, s93, v252\n"
"v_mfma_f32_16x16x32_bf16 v[132:135], a[248:251], v[44:47], v[132:135]\n"
"v_add_u32 v252, s94, v252\n"
"v_mfma_f32_16x16x32_bf16 v[132:135], a[252:255], v[60:63], v[132:135]\n"
"v_cvt_pk_bf16_f32 v210, v136, v137\n"
"s_waitcnt lgkmcnt(5)\n"
"v_mfma_f32_16x16x32_bf16 v[132:135], a[48:51], v[64:67], v[132:135]\n"
"v_cvt_pk_bf16_f32 v211, v138, v139\n"
"v_mfma_f32_16x16x32_bf16 v[132:135], a[52:55], v[68:71], v[132:135]\n"
"v_min_u32 v252, v252, v231\n"
"v_mfma_f32_16x16x32_bf16 v[132:135], a[56:59], v[84:87], v[132:135]\n"
"v_add_u32 v231, s93, v253\n"
"v_mfma_f32_16x16x32_bf16 v[132:135], a[60:63], v[88:91], v[132:135]\n"
"s_waitcnt lgkmcnt(1)\n"
"v_mfma_f32_16x16x32_bf16 v[132:135], a[64:67], v[92:95], v[132:135]\n"
"v_add_u32 v253, s94, v253\n"
"v_mfma_f32_16x16x32_bf16 v[132:135], a[68:71], v[108:111], v[132:135]\n"
"ds_write_b64 v244, v[210:211] offset:12424\n"
"v_mfma_f32_16x16x32_bf16 v[132:135], a[72:75], v[112:115], v[132:135]\n"
"v_min_u32 v253, v253, v231\n"
"v_mfma_f32_16x16x32_bf16 v[132:135], a[76:79], v[116:119], v[132:135]\n"
"s_add_u32 s52, s20, s35\n"
"s_addc_u32 s53, s21, 0\n"
"s_add_u32 m0, s31, 0x12000\n"
"v_mfma_f32_16x16x32_bf16 v[136:139], a[124:127], v[116:119], v[4:7]\n"
"global_load_lds_dwordx4 v237, s[52:53] offset:0\n"
"v_mfma_f32_16x16x32_bf16 v[136:139], a[120:123], v[112:115], v[136:139]\n"
"v_mfma_f32_16x16x32_bf16 v[136:139], a[116:119], v[108:111], v[136:139]\n"
"v_mfma_f32_16x16x32_bf16 v[136:139], a[112:115], v[92:95], v[136:139]\n"
"v_cvt_pk_bf16_f32 v208, v132, v133\n"
"v_mfma_f32_16x16x32_bf16 v[136:139], a[108:111], v[88:91], v[136:139]\n"
"v_cvt_pk_bf16_f32 v209, v134, v135\n"
"v_mfma_f32_16x16x32_bf16 v[136:139], a[104:107], v[84:87], v[136:139]\n"
"s_add_u32 m0, s31, 0x13f00\n"
"v_mfma_f32_16x16x32_bf16 v[136:139], a[100:103], v[68:71], v[136:139]\n"
"s_add_i32 s86, s86, s70\n"
"global_load_lds_dwordx4 v237, s[52:53] offset:256\n"
"v_mfma_f32_16x16x32_bf16 v[136:139], a[96:99], v[64:67], v[136:139]\n"
"v_mfma_f32_16x16x32_bf16 v[136:139], a[92:95], v[60:63], v[136:139]\n"
"s_waitcnt vmcnt(8)\n"
"v_mfma_f32_16x16x32_bf16 v[136:139], a[88:91], v[44:47], v[136:139]\n"
"v_cmp_ne_u64 s[58:59], s[80:81], v[228:229]\n"
"ds_write_b64 v244, v[208:209] offset:24576\n"
"v_mfma_f32_16x16x32_bf16 v[136:139], a[84:87], v[40:43], v[136:139]\n"
"v_subrev_u32 v230, s83, v230\n"
"v_mfma_f32_16x16x32_bf16 v[136:139], a[80:83], v[36:39], v[136:139]\n"
"s_add_u32 m0, s31, 0x15e00\n"
"v_mfma_f32_16x16x32_bf16 v[132:135], a[128:131], v[36:39], v[8:11]\n"
"v_add_u32_dpp v230, v230, v230 row_shr:1 row_mask:0xf bank_mask:0xf\n"
"global_load_lds_dwordx4 v237, s[52:53] offset:512\n"
"v_mfma_f32_16x16x32_bf16 v[132:135], a[132:135], v[40:43], v[132:135]\n"
"v_mfma_f32_16x16x32_bf16 v[132:135], a[136:139], v[44:47], v[132:135]\n"
"v_add_u32_dpp v230, v230, v230 row_shr:2 row_mask:0xf bank_mask:0xf\n"
"v_mfma_f32_16x16x32_bf16 v[132:135], a[140:143], v[60:63], v[132:135]\n"
"v_cvt_pk_bf16_f32 v210, v136, v137\n"
"v_mfma_f32_16x16x32_bf16 v[132:135], a[144:147], v[64:67], v[132:135]\n"
"v_add_u32_dpp v230, v230, v230 row_shr:4 row_mask:0xf bank_mask:0xf\n"
"v_mfma_f32_16x16x32_bf16 v[132:135], a[148:151], v[68:71], v[132:135]\n"
"s_add_u32 m0, s31, 0x13000\n"
"v_mfma_f32_16x16x32_bf16 v[132:135], a[152:155], v[84:87], v[132:135]\n"
"v_readlane_b32 s90, v230, 63\n"
"global_load_lds_dwordx4 v238, s[52:53] offset:0\n"
"v_mfma_f32_16x16x32_bf16 v[132:135], a[156:159], v[88:91], v[132:135]\n"
"v_cvt_pk_bf16_f32 v211, v138, v139\n"
"v_mfma_f32_16x16x32_bf16 v[132:135], a[160:163], v[92:95], v[132:135]\n"
"s_ashr_i32 s90, s90, 3\n"
"v_mfma_f32_16x16x32_bf16 v[132:135], a[164:167], v[108:111], v[132:135]\n"
"s_add_i32 s90, s90, 2494\n"
"v_mfma_f32_16x16x32_bf16 v[132:135], a[168:171], v[112:115], v[132:135]\n"
"s_max_i32 s90, s90, 0\n"
"v_mfma_f32_16x16x32_bf16 v[132:135], a[172:175], v[116:119], v[132:135]\n"
"s_min_i32 s90, s90, 120\n"
"s_add_u32 m0, s31, 0x14f00\n"
"v_mfma_f32_16x16x32_bf16 v[136:139], a[220:223], v[116:119], v[12:15]\n"
"s_mul_i32 s90, s90, 13\n"
"global_load_lds_dwordx4 v238, s[52:53] offset:256\n"
"v_mfma_f32_16x16x32_bf16 v[136:139], a[216:219], v[112:115], v[136:139]\n"
"s_ashr_i32 s90, s90, 3\n"
"ds_write_b64 v244, v[210:211] offset:24584\n"
"v_mfma_f32_16x16x32_bf16 v[136:139], a[212:215], v[108:111], v[136:139]\n"
"s_lshr_b32 s59, s59, 24\n"
"s_cselect_b32 s90, 0, s90\n"
"v_mfma_f32_16x16x32_bf16 v[136:139], a[208:211], v[92:95], v[136:139]\n"
"s_add_i32 s86, s86, s90\n"
"v_mfma_f32_16x16x32_bf16 v[136:139], a[204:207], v[88:91], v[136:139]\n"
"s_ashr_i32 s86, s86, 4\n"
"v_mov_b32 v242, 2\n"
"v_mfma_f32_16x16x32_bf16 v[136:139], a[200:203], v[84:87], v[136:139]\n"
"s_max_i32 s86, s86, 100\n"
"s_add_u32 m0, s31, 0x16e00\n"
"v_mfma_f32_16x16x32_bf16 v[136:139], a[196:199], v[68:71], v[136:139]\n"
"s_min_i32 s86, s86, 185\n"
"s_waitcnt lgkmcnt(2)\n"
"ds_write_b32 v240, v242\n"
"global_load_lds_dwordx4 v238, s[52:53] offset:512\n"
"v_mfma_f32_16x16x32_bf16 v[136:139], a[192:195], v[64:67], v[136:139]\n"
"s_mov_b32 s83, s84\n"
"v_mfma_f32_16x16x32_bf16 v[136:139], a[188:191], v[60:63], v[136:139]\n"
"v_cvt_pk_bf16_f32 v212, v132, v133\n"
"v_mfma_f32_16x16x32_bf16 v[136:139], a[184:187], v[44:47], v[136:139]\n"
"v_cvt_pk_bf16_f32 v213, v134, v135\n"
"v_mfma_f32_16x16x32_bf16 v[136:139], a[180:183], v[40:43], v[136:139]\n"
"s_bitcmp1_b32 s92, 2\n"
"s_cselect_b32 s93, 0x6000, 0\n"
"v_mfma_f32_16x16x32_bf16 v[136:139], a[176:179], v[36:39], v[136:139]\n"
"s_sub_u32 s94, s93, 0xc000\n"
"v_mfma_f32_16x16x32_bf16 v[132:135], a[224:227], v[36:39], v[16:19]\n"
"v_add_u32 v231, s93, v237\n"
"v_mfma_f32_16x16x32_bf16 v[132:135], a[228:231], v[40:43], v[132:135]\n"
"v_add_u32 v237, s94, v237\n"
"v_mfma_f32_16x16x32_bf16 v[132:135], a[232:235], v[44:47], v[132:135]\n"
"ds_read_b32 v243, v241\n"
"s_add_u32 s83, s83, s86\n"
"v_mfma_f32_16x16x32_bf16 v[132:135], a[236:239], v[60:63], v[132:135]\n"
"s_memrealtime s[84:85]\n"
"v_cvt_pk_bf16_f32 v215, v138, v139\n"
"v_mfma_f32_16x16x32_bf16 v[132:135], a[0:3], v[64:67], v[132:135]\n"
"ds_write_b64 v245, v[212:213] offset:24576\n"
"s_sub_i32 s88, s88, 6\n"
"v_mfma_f32_16x16x32_bf16 v[132:135], a[4:7], v[68:71], v[132:135]\n"
"v_cvt_pk_bf16_f32 v214, v136, v137\n"
"s_max_i32 s88, s88, 0\n"
"v_mfma_f32_16x16x32_bf16 v[132:135], a[8:11], v[84:87], v[132:135]\n"
"s_waitcnt lgkmcnt(0)\n"
"v_cmp_gt_u32 s[56:57], 2, v243\n"
"v_mfma_f32_16x16x32_bf16 v[132:135], a[12:15], v[88:91], v[132:135]\n"
"s_sub_u32 s84, s84, s83\n"
"v_mfma_f32_16x16x32_bf16 v[132:135], a[16:19], v[92:95], v[132:135]\n"
"s_cmp_lg_u64 s[56:57], 0\n"
"s_cbranch_scc1 LB2_0\n"
"LB2_0R:\n"
"v_mfma_f32_16x16x32_bf16 v[132:135], a[20:23], v[108:111], v[132:135]\n"
"s_cmp_lt_i32 s84, s88\n"
"s_cselect_b64 vcc, -1, 0\n"
"v_mfma_f32_16x16x32_bf16 v[132:135], v[140:143], v[112:115], v[132:135]\n"
"ds_write_b64 v245, v[214:215] offset:24584\n"
"v_mfma_f32_16x16x32_bf16 v[132:135], v[156:159], v[116:119], v[132:135]\n"
"v_min_u32 v237, v237, v231\n"
"v_mfma_f32_16x16x32_bf16 v[136:139], a[44:47], v[116:119], v[20:23]\n"
"ds_read_b128 v[116:119], v235 offset:40960\n"
"v_mfma_f32_16x16x32_bf16 v[136:139], a[40:43], v[112:115], v[136:139]\n"
"ds_read_b128 v[112:115], v234 offset:40960\n"
"v_mfma_f32_16x16x32_bf16 v[136:139], a[36:39], v[108:111], v[136:139]\n"
"ds_read_b128 v[108:111], v233 offset:40960\n"
"v_mfma_f32_16x16x32_bf16 v[136:139], a[32:35], v[92:95], v[136:139]\n"
"ds_read_b128 v[92:95], v232 offset:40960\n"
"v_cvt_pk_bf16_f32 v208, v132, v133\n"
"v_mfma_f32_16x16x32_bf16 v[136:139], a[28:31], v[88:91], v[136:139]\n"
"ds_read_b128 v[88:91], v235 offset:32768\n"
"v_cvt_pk_bf16_f32 v209, v134, v135\n"
"v_mfma_f32_16x16x32_bf16 v[136:139], a[24:27], v[84:87], v[136:139]\n"
"ds_read_b128 v[84:87], v234 offset:32768\n"
"v_mfma_f32_16x16x32_bf16 v[136:139], v[204:207], v[68:71], v[136:139]\n"
"ds_read_b128 v[68:71], v233 offset:32768\n"
"v_mfma_f32_16x16x32_bf16 v[136:139], v[188:191], v[64:67], v[136:139]\n"
"ds_read_b128 v[64:67], v232 offset:32768\n"
"v_mfma_f32_16x16x32_bf16 v[136:139], v[184:187], v[60:63], v[136:139]\n"
"ds_read_b128 v[60:63], v235 offset:24576\n"
"v_mfma_f32_16x16x32_bf16 v[136:139], v[180:183], v[44:47], v[136:139]\n"
"ds_read_b128 v[44:47], v234 offset:24576\n"
"v_mfma_f32_16x16x32_bf16 v[136:139], v[164:167], v[40:43], v[136:139]\n"
"ds_read_b128 v[40:43], v233 offset:24576\n"
"v_mfma_f32_16x16x32_bf16 v[136:139], v[160:163], v[36:39], v[136:139]\n"
"ds_read_b128 v[36:39], v232 offset:24576\n"
"s_branch LJOIN\n"
"LV1:\n"
"s_add_u32 s14, s20, 0x300\n"
"v_mul_u32_u24 v110, 0x600, v3\n"
"s_movk_i32 s15, 0x3000\n"
"v_add_u32 v110, s14, v110\n"
"v_xor_b32 v110, s14, v110\n"
"v_and_b32 v110, 0x2000, v110\n"
"v_cmp_lt_u32 vcc, 0, v110\n"
"s_nop 3\n"
"s_mov_b32 s99, vcc_lo\n"
"v_bfe_u32 v110, s99, v109, 1\n"
"v_mad_u32_u24 v236, v110, s15, v236\n"
"s_add_u32 s22, s22, s5\n"
"s_addc_u32 s23, s23, 0\n"
"s_add_u32 s32, s22, s95\n"
"s_addc_u32 s33, s23, 0\n"
"s_add_u32 s34, s32, 0x300000\n"
"s_addc_u32 s35, s33, 0\n"
"s_add_u32 s36, s34, 0x300000\n"
"s_addc_u32 s37, s35, 0\n"
"s_add_u32 s38, s22, s96\n"
"s_addc_u32 s39, s23, 0\n"
"s_add_u32 s40, s38, 0x300000\n"
"s_addc_u32 s41, s39, 0\n"
"s_add_u32 s42, s40, 0x300000\n"
"s_addc_u32 s43, s41, 0\n"
"s_add_u32 s44, s22, s97\n"
"s_addc_u32 s45, s23, 0\n"
"s_add_u32 s46, s44, 0x300000\n"
"s_addc_u32 s47, s45, 0\n"
"s_add_u32 s48, s46, 0x300000\n"
"s_addc_u32 s49, s47, 0\n"
"s_mul_i32 s14, s98, 191\n"
"v_min_u32 v141, 55, v1\n"
"s_lshr_b32 s14, s14, 13\n"
"v_and_b32 v141, 62, v141\n"
"v_lshlrev_b32 v141, 4, v141\n"
"s_mul_i32 s14, s14, 3\n"
"v_add_u32 v141, 0x3000, v141\n"
"v_add_u32 v110, s14, v3\n"
"s_and_b32 s14, s20, 0x1fffff\n"
"v_subrev_u32 v111, 9, v110\n"
"s_add_u32 s14, s14, 0x300\n"
"v_min_u32 v110, v110, v111\n"
"s_lshr_b32 s15, s14, 11\n"
"v_lshl_add_u32 v110, v110, 7, s98\n"
"s_xnor_b32 s15, s15, s51\n"
"v_mul_u32_u24 v111, 0xaaab, v110\n"
"s_bfe_i32 s91, s15, 0x10002\n"
"v_mul_u32_u24 v110, 0xc000, v110\n"
"v_lshrrev_b32 v111, 18, v111\n"
"v_mul_u32_u24 v111, 0x48000, v111\n"
"v_add3_u32 v110, v110, v111, s14\n"
"s_add_u32 s14, s14, 0x6dffcff\n"
"v_lshrrev_b32 v140, 21, v110\n"
"s_lshr_b32 s14, s14, 21\n"
"v_subrev_u32 v142, s14, v1\n"
"v_ashrrev_i32 v142, 31, v142\n"
"v_bfe_i32 v143, v1, 1, 1\n"
"s_bfe_u32 s14, s98, 0x40002\n"
"s_add_i32 s14, s14, -7\n"
"v_subrev_u32 v144, s14, v3\n"
"v_lshrrev_b32 v144, 31, v144\n"
"v_bfe_u32 v110, v1, 2, 2\n"
"s_movk_i32 s14, 0xc0\n"
"v_lshl_or_b32 v110, v2, 3, v110\n"
"v_and_b32 v111, 48, v239\n"
"v_lshlrev_b32 v112, 8, v3\n"
"v_mad_u32_u24 v110, v110, s14, v111\n"
"v_xor_b32 v113, v2, v3\n"
"v_add_u32 v243, s74, v110\n"
"v_add_u32 v249, 0xfffee000, v112\n"
"v_add_u32 v112, 0x12000, v112\n"
"v_lshl_add_u32 v232, v113, 4, v112\n"
"v_xor_b32 v233, 64, v232\n"
"v_xor_b32 v234, 0x80, v232\n"
"v_xor_b32 v235, 0xc0, v232\n"
"s_lshl_b32 s77, s51, 3\n"
"s_add_u32 s77, s77, 0x2000\n"
"s_lshl_b32 s14, s76, 1\n"
"v_lshrrev_b32 v110, 5, v1\n"
"v_add_u32 v111, s3, v2\n"
"v_bitop3_b32 v110, s14, v1, v110 bitop3:0x96\n"
"s_mul_i32 s15, s76, 0x240\n"
"v_lshlrev_b32 v110, 4, v110\n"
"v_and_b32 v112, 0x70, v239\n"
"v_and_b32 v110, 0xf0, v110\n"
"v_add_u32 v113, s15, v239\n"
"v_mad_u32_u24 v237, v111, s4, v110\n"
"v_cmp_gt_u32 vcc, 36, v1\n"
"v_xor_b32 v238, 0x80, v237\n"
"s_mul_i32 s14, s76, 12\n"
"v_cndmask_b32 v112, v112, v113, vcc\n"
"v_add_u32 v238, 0x3000, v238\n"
"v_lshrrev_b32 v111, 1, v111\n"
"s_movk_i32 s15, 0x6000\n"
"v_bfe_u32 v110, s99, v111, 1\n"
"v_add_u32 v111, 8, v111\n"
"v_mad_u32_u24 v237, v110, s15, v237\n"
"v_bfe_u32 v110, s99, v111, 1\n"
"v_mad_u32_u24 v238, v110, s15, v238\n"
"v_add_u32 v248, 0x1000, v112\n"
"v_add_u32 v110, s14, v2\n"
"v_mul_u32_u24 v112, 0x300, v3\n"
"v_add_u32 v111, 4, v110\n"
"v_add_u32 v112, 0x18000, v112\n"
"v_bitop3_b32 v110, v110, v1, 7 bitop3:0x78\n"
"v_bitop3_b32 v111, v111, v1, 7 bitop3:0x78\n"
"s_add_u32 s14, s6, 0x18000\n"
"v_lshl_add_u32 v116, v110, 4, v112\n"
"v_lshl_add_u32 v117, v111, 4, v112\n"
"v_add_u32 v118, s14, v239\n"
"s_movk_i32 s14, 0x600\n"
"v_add_u32 v110, 0, v1\n"
"v_mul_u32_u24 v111, 0xab, v110\n"
"v_lshrrev_b32 v111, 13, v111\n"
"v_mul_u32_u24 v112, 48, v111\n"
"v_add_u32 v111, s3, v111\n"
"v_sub_u32 v110, v110, v112\n"
"v_bitop3_b32 v110, v110, v111, 7 bitop3:0x78\n"
"v_lshlrev_b32 v110, 4, v110\n"
"v_mad_u32_u24 v110, v111, s14, v110\n"
"v_bfe_u32 v111, s99, v111, 1\n"
"v_mad_u32_u24 v119, v111, s15, v110\n"
"v_add_u32 v110, 64, v1\n"
"v_mul_u32_u24 v111, 0xab, v110\n"
"v_lshrrev_b32 v111, 13, v111\n"
"v_mul_u32_u24 v112, 48, v111\n"
"v_add_u32 v111, s3, v111\n"
"v_sub_u32 v110, v110, v112\n"
"v_bitop3_b32 v110, v110, v111, 7 bitop3:0x78\n"
"v_lshlrev_b32 v110, 4, v110\n"
"v_mad_u32_u24 v110, v111, s14, v110\n"
"v_bfe_u32 v111, s99, v111, 1\n"
"v_mad_u32_u24 v120, v111, s15, v110\n"
"v_add_u32 v110, 128, v1\n"
"v_mul_u32_u24 v111, 0xab, v110\n"
"v_lshrrev_b32 v111, 13, v111\n"
"v_mul_u32_u24 v112, 48, v111\n"
"v_add_u32 v111, s3, v111\n"
"v_sub_u32 v110, v110, v112\n"
"v_bitop3_b32 v110, v110, v111, 7 bitop3:0x78\n"
"v_lshlrev_b32 v110, 4, v110\n"
"v_mad_u32_u24 v110, v111, s14, v110\n"
"v_bfe_u32 v111, s99, v111, 1\n"
"v_mad_u32_u24 v121, v111, s15, v110\n"
"s_add_u32 s15, s3, 0x27000\n"
"v_lshlrev_b32 v110, 2, v1\n"
"s_lshl_b32 s14, s76, 11\n"
"v_and_b32 v110, 12, v110\n"
"s_add_u32 s14, s14, 0x25000\n"
"v_mov_b32 v122, s15\n"
"v_add_u32 v123, 0x27000, v110\n"
"v_lshl_add_u32 v250, v1, 5, s14\n"
"v_mov_b32 v110, 0\n"
"ds_write_b128 v250, v[116:119]\n"
"ds_write_b128 v250, v[120:123] offset:16\n"
"ds_write_b32 v122, v110\n"
"s_add_u32 m0, s74, 0xc000\n"
"s_add_u32 s78, s64, 0xc000\n"
"s_addc_u32 s79, s65, 0\n"
"global_load_lds_dwordx4 v240, s[78:79] offset:0\n"
"global_load_lds_dwordx4 v241, s[78:79] offset:1024\n"
"global_load_lds_dwordx4 v242, s[78:79] offset:2048\n"
"s_add_u32 m0, s74, 0xcc00\n"
"s_add_u32 s78, s64, 0xf000\n"
"s_addc_u32 s79, s65, 0\n"
"global_load_lds_dwordx4 v240, s[78:79] offset:0\n"
"global_load_lds_dwordx4 v241, s[78:79] offset:1024\n"
"global_load_lds_dwordx4 v242, s[78:79] offset:2048\n"
"global_load_dwordx4 v[134:137], v141, s[28:29] sc1\n"
"global_load_dwordx4 v[148:151], v141, s[28:29] offset:1024 sc1\n"
"s_waitcnt vmcnt(14)\n"
"ds_read_b64_tr_b16 a[0:1], v243 offset:0\n"
"ds_read_b64_tr_b16 a[2:3], v243 offset:768\n"
"ds_read_b64_tr_b16 a[4:5], v243 offset:8\n"
"ds_read_b64_tr_b16 a[6:7], v243 offset:776\n"
"ds_read_b64_tr_b16 a[8:9], v243 offset:64\n"
"ds_read_b64_tr_b16 a[10:11], v243 offset:832\n"
"ds_read_b64_tr_b16 a[12:13], v243 offset:72\n"
"ds_read_b64_tr_b16 a[14:15], v243 offset:840\n"
"ds_read_b64_tr_b16 a[16:17], v243 offset:128\n"
"ds_read_b64_tr_b16 a[18:19], v243 offset:896\n"
"ds_read_b64_tr_b16 a[20:21], v243 offset:136\n"
"ds_read_b64_tr_b16 a[22:23], v243 offset:904\n"
"s_waitcnt lgkmcnt(0)\n"
"s_add_u32 m0, s74, 0x0\n"
"s_add_u32 s78, s64, 0x12000\n"
"s_addc_u32 s79, s65, 0\n"
"global_load_lds_dwordx4 v240, s[78:79] offset:0\n"
"global_load_lds_dwordx4 v241, s[78:79] offset:1024\n"
"global_load_lds_dwordx4 v242, s[78:79] offset:2048\n"
"s_add_u32 m0, s74, 0xc00\n"
"s_add_u32 s78, s64, 0x15000\n"
"s_addc_u32 s79, s65, 0\n"
"global_load_lds_dwordx4 v240, s[78:79] offset:0\n"
"global_load_lds_dwordx4 v241, s[78:79] offset:1024\n"
"global_load_lds_dwordx4 v242, s[78:79] offset:2048\n"
"s_waitcnt vmcnt(6)\n"
"v_bitop3_b32 v138, v135, v137, s81 bitop3:0x7e\n"
"v_bitop3_b32 v152, v149, v151, s81 bitop3:0x7e\n"
"v_bitop3_b32 v139, v134, v136, s80 bitop3:0x7e\n"
"v_bitop3_b32 v153, v148, v150, s80 bitop3:0x7e\n"
"v_lshrrev_b32 v138, 16, v138\n"
"v_lshrrev_b32 v152, 16, v152\n"
"v_or_b32 v138, v138, v139\n"
"v_or_b32 v152, v152, v153\n"
"v_bitop3_b32 v138, v138, v152, v142 bitop3:0xa8\n"
"v_cmp_ne_u32 vcc, 0, v138\n"
"v_sub_u32 v134, v135, v137\n"
"v_sub_u32 v148, v149, v151\n"
"v_bitop3_b32 v134, v134, v143, v142 bitop3:0x28\n"
"v_bitop3_b32 v148, v148, v143, v142 bitop3:0x28\n"
"s_cmp_lg_u64 vcc, 0\n"
"s_cbranch_scc0 LR5_1\n"
"s_mov_b32 s82, 0x40000\n"
"LR4_1:\n"
"s_sleep 1\n"
"global_load_dwordx4 v[134:137], v141, s[28:29] sc1\n"
"global_load_dwordx4 v[148:151], v141, s[28:29] offset:1024 sc1\n"
"s_sub_u32 s82, s82, 1\n"
"s_waitcnt vmcnt(0)\n"
"s_cbranch_scc1 LR5_1\n"
"v_bitop3_b32 v138, v135, v137, s81 bitop3:0x7e\n"
"v_bitop3_b32 v152, v149, v151, s81 bitop3:0x7e\n"
"v_bitop3_b32 v139, v134, v136, s80 bitop3:0x7e\n"
"v_bitop3_b32 v153, v148, v150, s80 bitop3:0x7e\n"
"v_lshrrev_b32 v138, 16, v138\n"
"v_lshrrev_b32 v152, 16, v152\n"
"v_or_b32 v138, v138, v139\n"
"v_or_b32 v152, v152, v153\n"
"v_bitop3_b32 v138, v138, v152, v142 bitop3:0xa8\n"
"v_cmp_ne_u32 vcc, 0, v138\n"
"v_sub_u32 v134, v135, v137\n"
"v_sub_u32 v148, v149, v151\n"
"v_bitop3_b32 v134, v134, v143, v142 bitop3:0x28\n"
"v_bitop3_b32 v148, v148, v143, v142 bitop3:0x28\n"
"s_cmp_lg_u64 vcc, 0\n"
"s_cbranch_scc1 LR4_1\n"
"LR5_1:\n"
"s_nop 0\n"
"v_add_u32_dpp v149, v148, v148 wave_shr:1 row_mask:0xf bank_mask:0xf bound_ctrl:0\n"
"v_add_u32_dpp v135, v134, v134 wave_shr:1 row_mask:0xf bank_mask:0xf bound_ctrl:0\n"
"v_add_u32_dpp v149, v148, v149 wave_shl:1 row_mask:0xf bank_mask:0xf bound_ctrl:0\n"
"v_add_u32_dpp v135, v134, v135 wave_shl:1 row_mask:0xf bank_mask:0xf bound_ctrl:0\n"
"v_mad_i32_i24 v150, v148, -1, v149\n"
"v_mad_i32_i24 v136, v134, -1, v135\n"
"s_nop 0\n"
"v_add_u32_dpp v150, v149, v150 wave_shr:1 row_mask:0xf bank_mask:0xf bound_ctrl:0\n"
"v_add_u32_dpp v136, v135, v136 wave_shr:1 row_mask:0xf bank_mask:0xf bound_ctrl:0\n"
"v_add_u32_dpp v150, v149, v150 wave_shl:1 row_mask:0xf bank_mask:0xf bound_ctrl:0\n"
"v_add_u32_dpp v136, v135, v136 wave_shl:1 row_mask:0xf bank_mask:0xf bound_ctrl:0\n"
"v_cmp_lt_i32 s[54:55], 0, v150\n"
"v_cmp_lt_i32 vcc, 0, v136\n"
"s_nop 1\n"
"v_lshrrev_b64 v[138:139], v140, vcc\n"
"v_lshrrev_b64 v[152:153], v140, s[54:55]\n"
"s_xor_b64 s[54:55], s[54:55], vcc\n"
"v_bfi_b32 v138, v144, v152, v138\n"
"s_lshl_b64 s[54:55], s[54:55], 10\n"
"v_bitop3_b32 v138, v138, v140, 1 bitop3:0x28\n"
"s_bcnt1_i32_b64 s71, s[54:55]\n"
"v_cmp_ne_u32 vcc, 0, v138\n"
"s_sub_i32 s70, s71, 33\n"
"s_mul_i32 s70, s70, 80\n"
"s_ashr_i32 s70, s70, 3\n"
"s_max_i32 s70, s70, 0\n"
"s_min_i32 s70, s70, 95\n"
"s_and_b32 s14, vcc_lo, 0x1ff\n"
"s_xor_b32 s91, s91, s14\n"
"s_lshl_b32 s14, s91, 1\n"
"s_xor_b32 s92, s91, s14\n"
"v_add_u32 v245, 0x3000, v236\n"
"v_add_u32 v244, 0xffffd000, v236\n"
"v_min_u32 v245, v245, v244\n"
"s_bfe_i32 s93, s91, 0x10000\n"
"s_add_u32 m0, s30, 0x0\n"
"v_bfi_b32 v244, s93, v245, v236\n"
"global_load_lds_dwordx4 v244, s[32:33] offset:0 nt\n"
"s_bfe_i32 s93, s91, 0x10001\n"
"s_add_u32 m0, s30, 0x1000\n"
"v_bfi_b32 v244, s93, v245, v236\n"
"global_load_lds_dwordx4 v244, s[34:35] offset:0 nt\n"
"s_bfe_i32 s93, s91, 0x10002\n"
"s_add_u32 m0, s30, 0x2000\n"
"v_bfi_b32 v244, s93, v245, v236\n"
"global_load_lds_dwordx4 v244, s[36:37] offset:0 nt\n"
"ds_read_b64_tr_b16 a[24:25], v243 offset:24576\n"
"ds_read_b64_tr_b16 a[26:27], v243 offset:25344\n"
"ds_read_b64_tr_b16 a[28:29], v243 offset:24584\n"
"ds_read_b64_tr_b16 a[30:31], v243 offset:25352\n"
"ds_read_b64_tr_b16 a[32:33], v243 offset:24640\n"
"ds_read_b64_tr_b16 a[34:35], v243 offset:25408\n"
"ds_read_b64_tr_b16 a[36:37], v243 offset:24648\n"
"ds_read_b64_tr_b16 a[38:39], v243 offset:25416\n"
"ds_read_b64_tr_b16 a[40:41], v243 offset:24704\n"
"ds_read_b64_tr_b16 a[42:43], v243 offset:25472\n"
"ds_read_b64_tr_b16 a[44:45], v243 offset:24712\n"
"ds_read_b64_tr_b16 a[46:47], v243 offset:25480\n"
"s_waitcnt lgkmcnt(0)\n"
"s_add_u32 m0, s74, 0x6000\n"
"s_add_u32 s78, s64, 0x18000\n"
"s_addc_u32 s79, s65, 0\n"
"global_load_lds_dwordx4 v240, s[78:79] offset:0\n"
"global_load_lds_dwordx4 v241, s[78:79] offset:1024\n"
"global_load_lds_dwordx4 v242, s[78:79] offset:2048\n"
"s_add_u32 m0, s74, 0x6c00\n"
"s_add_u32 s78, s64, 0x1b000\n"
"s_addc_u32 s79, s65, 0\n"
"global_load_lds_dwordx4 v240, s[78:79] offset:0\n"
"global_load_lds_dwordx4 v241, s[78:79] offset:1024\n"
"global_load_lds_dwordx4 v242, s[78:79] offset:2048\n"
"ds_read_b64_tr_b16 a[48:49], v243 offset:49152\n"
"ds_read_b64_tr_b16 a[50:51], v243 offset:49920\n"
"ds_read_b64_tr_b16 a[52:53], v243 offset:49160\n"
"ds_read_b64_tr_b16 a[54:55], v243 offset:49928\n"
"ds_read_b64_tr_b16 a[56:57], v243 offset:49216\n"
"ds_read_b64_tr_b16 a[58:59], v243 offset:49984\n"
"ds_read_b64_tr_b16 a[60:61], v243 offset:49224\n"
"ds_read_b64_tr_b16 a[62:63], v243 offset:49992\n"
"ds_read_b64_tr_b16 a[64:65], v243 offset:49280\n"
"ds_read_b64_tr_b16 a[66:67], v243 offset:50048\n"
"ds_read_b64_tr_b16 a[68:69], v243 offset:49288\n"
"ds_read_b64_tr_b16 a[70:71], v243 offset:50056\n"
"s_waitcnt lgkmcnt(0)\n"
"s_add_u32 m0, s74, 0xc000\n"
"s_add_u32 s78, s64, 0x1e000\n"
"s_addc_u32 s79, s65, 0\n"
"global_load_lds_dwordx4 v240, s[78:79] offset:0\n"
"global_load_lds_dwordx4 v241, s[78:79] offset:1024\n"
"global_load_lds_dwordx4 v242, s[78:79] offset:2048\n"
"s_add_u32 m0, s74, 0xcc00\n"
"s_add_u32 s78, s64, 0x21000\n"
"s_addc_u32 s79, s65, 0\n"
"global_load_lds_dwordx4 v240, s[78:79] offset:0\n"
"global_load_lds_dwordx4 v241, s[78:79] offset:1024\n"
"global_load_lds_dwordx4 v242, s[78:79] offset:2048\n"
"s_waitcnt vmcnt(15)\n"
"ds_read_b64_tr_b16 a[72:73], v243 offset:0\n"
"ds_read_b64_tr_b16 a[74:75], v243 offset:768\n"
"ds_read_b64_tr_b16 a[76:77], v243 offset:8\n"
"ds_read_b64_tr_b16 a[78:79], v243 offset:776\n"
"ds_read_b64_tr_b16 a[80:81], v243 offset:64\n"
"ds_read_b64_tr_b16 a[82:83], v243 offset:832\n"
"ds_read_b64_tr_b16 a[84:85], v243 offset:72\n"
"ds_read_b64_tr_b16 a[86:87], v243 offset:840\n"
"ds_read_b64_tr_b16 a[88:89], v243 offset:128\n"
"ds_read_b64_tr_b16 a[90:91], v243 offset:896\n"
"ds_read_b64_tr_b16 a[92:93], v243 offset:136\n"
"ds_read_b64_tr_b16 a[94:95], v243 offset:904\n"
"s_waitcnt vmcnt(12)\n"
"s_barrier\n"
"s_cmp_eq_u32 s76, 0\n"
"s_cselect_b32 s12, 1, 0\n"
"s_mov_b32 s13, 0\n"
"s_cmp_lg_u32 s76, 0\n"
"s_cselect_b64 s[4:5], -1, 0\n"
"s_cmp_lg_u32 s76, 1\n"
"s_cselect_b64 s[6:7], -1, 0\n"
"s_cmp_lg_u32 s76, 2\n"
"s_cselect_b64 s[8:9], -1, 0\n"
"s_cmp_lg_u32 s76, 3\n"
"s_cselect_b64 s[10:11], -1, 0\n"
"v_mov_b32 v251, s77\n"
"ds_read_b128 v[216:219], v232 offset:0\n"
"ds_read_b128 v[220:223], v232 offset:4096\n"
"ds_read_b128 v[224:227], v232 offset:8192\n"
"s_waitcnt lgkmcnt(2)\n"
"v_mfma_f32_16x16x32_bf16 v[0:3], a[0:3], v[216:219], 0\n"
"s_bfe_i32 s93, s91, 0x10003\n"
"s_add_u32 m0, s30, 0x3000\n"
"v_bfi_b32 v244, s93, v245, v236\n"
"v_mfma_f32_16x16x32_bf16 v[4:7], a[4:7], v[216:219], 0\n"
"global_load_lds_dwordx4 v244, s[38:39] offset:0 nt\n"
"s_bfe_i32 s93, s91, 0x10004\n"
"v_mfma_f32_16x16x32_bf16 v[8:11], a[8:11], v[216:219], 0\n"
"ds_read_b128 v[228:231], v233 offset:0\n"
"s_add_u32 m0, s30, 0x4000\n"
"v_bfi_b32 v244, s93, v245, v236\n"
"v_mfma_f32_16x16x32_bf16 v[12:15], a[12:15], v[216:219], 0\n"
"global_load_lds_dwordx4 v244, s[40:41] offset:0 nt\n"
"s_bfe_i32 s93, s91, 0x10005\n"
"v_mfma_f32_16x16x32_bf16 v[16:19], a[16:19], v[216:219], 0\n"
"s_add_u32 m0, s30, 0x5000\n"
"v_bfi_b32 v244, s93, v245, v236\n"
"v_mfma_f32_16x16x32_bf16 v[20:23], a[20:23], v[216:219], 0\n"
"global_load_lds_dwordx4 v244, s[42:43] offset:0 nt\n"
"s_waitcnt lgkmcnt(2)\n"
"v_mfma_f32_16x16x32_bf16 v[24:27], a[0:3], v[220:223], 0\n"
"s_waitcnt vmcnt(9)\n"
"ds_read_b64_tr_b16 a[96:97], v243 offset:24576\n"
"v_mfma_f32_16x16x32_bf16 v[28:31], a[4:7], v[220:223], 0\n"
"ds_read_b64_tr_b16 a[98:99], v243 offset:25344\n"
"v_mfma_f32_16x16x32_bf16 v[32:35], a[8:11], v[220:223], 0\n"
"ds_read_b128 v[216:219], v233 offset:4096\n"
"ds_read_b64_tr_b16 a[100:101], v243 offset:24584\n"
"v_mfma_f32_16x16x32_bf16 v[36:39], a[12:15], v[220:223], 0\n"
"ds_read_b64_tr_b16 a[102:103], v243 offset:25352\n"
"v_mfma_f32_16x16x32_bf16 v[40:43], a[16:19], v[220:223], 0\n"
"ds_read_b64_tr_b16 a[104:105], v243 offset:24640\n"
"v_mfma_f32_16x16x32_bf16 v[44:47], a[20:23], v[220:223], 0\n"
"ds_read_b64_tr_b16 a[106:107], v243 offset:25408\n"
"s_waitcnt lgkmcnt(8)\n"
"v_mfma_f32_16x16x32_bf16 v[48:51], a[0:3], v[224:227], 0\n"
"ds_read_b64_tr_b16 a[108:109], v243 offset:24648\n"
"v_mfma_f32_16x16x32_bf16 v[52:55], a[4:7], v[224:227], 0\n"
"ds_read_b64_tr_b16 a[110:111], v243 offset:25416\n"
"v_mfma_f32_16x16x32_bf16 v[56:59], a[8:11], v[224:227], 0\n"
"ds_read_b128 v[220:223], v233 offset:8192\n"
"ds_read_b64_tr_b16 a[112:113], v243 offset:24704\n"
"v_mfma_f32_16x16x32_bf16 v[60:63], a[12:15], v[224:227], 0\n"
"ds_read_b64_tr_b16 a[114:115], v243 offset:25472\n"
"v_mfma_f32_16x16x32_bf16 v[64:67], a[16:19], v[224:227], 0\n"
"ds_read_b64_tr_b16 a[116:117], v243 offset:24712\n"
"v_mfma_f32_16x16x32_bf16 v[68:71], a[20:23], v[224:227], 0\n"
"ds_read_b64_tr_b16 a[118:119], v243 offset:25480\n"
"s_waitcnt lgkmcnt(14)\n"
"v_mfma_f32_16x16x32_bf16 v[0:3], a[24:27], v[228:231], v[0:3]\n"
"s_waitcnt vmcnt(3)\n"
"ds_read_b64_tr_b16 a[120:121], v243 offset:49152\n"
"v_mfma_f32_16x16x32_bf16 v[4:7], a[28:31], v[228:231], v[4:7]\n"
"ds_read_b64_tr_b16 a[122:123], v243 offset:49920\n"
"v_mfma_f32_16x16x32_bf16 v[8:11], a[32:35], v[228:231], v[8:11]\n"
"ds_read_b128 v[224:227], v234 offset:0\n"
"ds_read_b64_tr_b16 a[124:125], v243 offset:49160\n"
"v_mfma_f32_16x16x32_bf16 v[12:15], a[36:39], v[228:231], v[12:15]\n"
"ds_read_b64_tr_b16 a[126:127], v243 offset:49928\n"
"v_mfma_f32_16x16x32_bf16 v[16:19], a[40:43], v[228:231], v[16:19]\n"
"ds_read_b64_tr_b16 a[128:129], v243 offset:49216\n"
"v_mfma_f32_16x16x32_bf16 v[20:23], a[44:47], v[228:231], v[20:23]\n"
"ds_read_b64_tr_b16 a[130:131], v243 offset:49984\n"
"s_waitcnt lgkmcnt(15)\n"
"v_mfma_f32_16x16x32_bf16 v[24:27], a[24:27], v[216:219], v[24:27]\n"
"ds_read_b64_tr_b16 a[132:133], v243 offset:49224\n"
"v_mfma_f32_16x16x32_bf16 v[28:31], a[28:31], v[216:219], v[28:31]\n"
"ds_read_b64_tr_b16 a[134:135], v243 offset:49992\n"
"v_mfma_f32_16x16x32_bf16 v[32:35], a[32:35], v[216:219], v[32:35]\n"
"ds_read_b128 v[228:231], v234 offset:4096\n"
"ds_read_b64_tr_b16 a[136:137], v243 offset:49280\n"
"v_mfma_f32_16x16x32_bf16 v[36:39], a[36:39], v[216:219], v[36:39]\n"
"ds_read_b64_tr_b16 a[138:139], v243 offset:50048\n"
"v_mfma_f32_16x16x32_bf16 v[40:43], a[40:43], v[216:219], v[40:43]\n"
"ds_read_b64_tr_b16 a[140:141], v243 offset:49288\n"
"v_mfma_f32_16x16x32_bf16 v[44:47], a[44:47], v[216:219], v[44:47]\n"
"s_bfe_i32 s93, s91, 0x10006\n"
"ds_read_b64_tr_b16 a[142:143], v243 offset:50056\n"
"s_waitcnt lgkmcnt(15)\n"
"v_mfma_f32_16x16x32_bf16 v[48:51], a[24:27], v[220:223], v[48:51]\n"
"s_add_u32 m0, s30, 0x6000\n"
"v_bfi_b32 v244, s93, v245, v236\n"
"v_mfma_f32_16x16x32_bf16 v[52:55], a[28:31], v[220:223], v[52:55]\n"
"global_load_lds_dwordx4 v244, s[44:45] offset:0 nt\n"
"s_bfe_i32 s93, s91, 0x10007\n"
"v_mfma_f32_16x16x32_bf16 v[56:59], a[32:35], v[220:223], v[56:59]\n"
"ds_read_b128 v[216:219], v234 offset:8192\n"
"s_add_u32 m0, s30, 0x7000\n"
"v_bfi_b32 v244, s93, v245, v236\n"
"v_mfma_f32_16x16x32_bf16 v[60:63], a[36:39], v[220:223], v[60:63]\n"
"global_load_lds_dwordx4 v244, s[46:47] offset:0 nt\n"
"s_bfe_i32 s93, s91, 0x10008\n"
"v_mfma_f32_16x16x32_bf16 v[64:67], a[40:43], v[220:223], v[64:67]\n"
"s_add_u32 m0, s30, 0x8000\n"
"v_bfi_b32 v244, s93, v245, v236\n"
"v_mfma_f32_16x16x32_bf16 v[68:71], a[44:47], v[220:223], v[68:71]\n"
"global_load_lds_dwordx4 v244, s[48:49] offset:0 nt\n"
"s_waitcnt lgkmcnt(12)\n"
"v_mfma_f32_16x16x32_bf16 v[0:3], a[48:51], v[224:227], v[0:3]\n"
"s_mul_i32 s14, s76, 0x12000\n"
"v_mfma_f32_16x16x32_bf16 v[4:7], a[52:55], v[224:227], v[4:7]\n"
"v_add_u32 v245, 0x3000, v236\n"
"v_mfma_f32_16x16x32_bf16 v[8:11], a[56:59], v[224:227], v[8:11]\n"
"ds_read_b128 v[220:223], v235 offset:0\n"
"v_add_u32 v244, 0xffffd000, v236\n"
"v_mfma_f32_16x16x32_bf16 v[12:15], a[60:63], v[224:227], v[12:15]\n"
"v_min_u32 v245, v245, v244\n"
"v_mfma_f32_16x16x32_bf16 v[16:19], a[64:67], v[224:227], v[16:19]\n"
"s_bfe_i32 s93, s91, 0x10000\n"
"v_mfma_f32_16x16x32_bf16 v[20:23], a[68:71], v[224:227], v[20:23]\n"
"s_add_u32 m0, s30, 0x8f00\n"
"v_bfi_b32 v244, s93, v245, v236\n"
"s_waitcnt lgkmcnt(6)\n"
"v_mfma_f32_16x16x32_bf16 v[24:27], a[48:51], v[228:231], v[24:27]\n"
"global_load_lds_dwordx4 v244, s[32:33] offset:256 nt\n"
"s_bfe_i32 s93, s91, 0x10001\n"
"v_mfma_f32_16x16x32_bf16 v[28:31], a[52:55], v[228:231], v[28:31]\n"
"s_add_u32 m0, s30, 0x9f00\n"
"v_bfi_b32 v244, s93, v245, v236\n"
"v_mfma_f32_16x16x32_bf16 v[32:35], a[56:59], v[228:231], v[32:35]\n"
"ds_read_b128 v[224:227], v235 offset:4096\n"
"global_load_lds_dwordx4 v244, s[34:35] offset:256 nt\n"
"s_bfe_i32 s93, s91, 0x10002\n"
"v_mfma_f32_16x16x32_bf16 v[36:39], a[60:63], v[228:231], v[36:39]\n"
"s_add_u32 m0, s30, 0xaf00\n"
"v_bfi_b32 v244, s93, v245, v236\n"
"v_mfma_f32_16x16x32_bf16 v[40:43], a[64:67], v[228:231], v[40:43]\n"
"global_load_lds_dwordx4 v244, s[36:37] offset:256 nt\n"
"s_bfe_i32 s93, s91, 0x10003\n"
"v_mfma_f32_16x16x32_bf16 v[44:47], a[68:71], v[228:231], v[44:47]\n"
"s_add_u32 m0, s30, 0xbf00\n"
"v_bfi_b32 v244, s93, v245, v236\n"
"s_waitcnt lgkmcnt(2)\n"
"v_mfma_f32_16x16x32_bf16 v[48:51], a[48:51], v[216:219], v[48:51]\n"
"global_load_lds_dwordx4 v244, s[38:39] offset:256 nt\n"
"s_bfe_i32 s93, s91, 0x10004\n"
"v_mfma_f32_16x16x32_bf16 v[52:55], a[52:55], v[216:219], v[52:55]\n"
"s_add_u32 m0, s30, 0xcf00\n"
"v_bfi_b32 v244, s93, v245, v236\n"
"v_mfma_f32_16x16x32_bf16 v[56:59], a[56:59], v[216:219], v[56:59]\n"
"ds_read_b128 v[228:231], v235 offset:8192\n"
"global_load_lds_dwordx4 v244, s[40:41] offset:256 nt\n"
"s_bfe_i32 s93, s91, 0x10005\n"
"v_mfma_f32_16x16x32_bf16 v[60:63], a[60:63], v[216:219], v[60:63]\n"
"s_add_u32 m0, s30, 0xdf00\n"
"v_bfi_b32 v244, s93, v245, v236\n"
"v_mfma_f32_16x16x32_bf16 v[64:67], a[64:67], v[216:219], v[64:67]\n"
"global_load_lds_dwordx4 v244, s[42:43] offset:256 nt\n"
"s_bfe_i32 s93, s91, 0x10006\n"
"v_mfma_f32_16x16x32_bf16 v[68:71], a[68:71], v[216:219], v[68:71]\n"
"s_add_u32 m0, s30, 0xef00\n"
"v_bfi_b32 v244, s93, v245, v236\n"
"s_waitcnt lgkmcnt(2)\n"
"v_mfma_f32_16x16x32_bf16 v[0:3], a[72:75], v[220:223], v[0:3]\n"
"global_load_lds_dwordx4 v244, s[44:45] offset:256 nt\n"
"s_bfe_i32 s93, s91, 0x10007\n"
"v_mfma_f32_16x16x32_bf16 v[4:7], a[76:79], v[220:223], v[4:7]\n"
"s_waitcnt vmcnt(10)\n"
"s_barrier\n"
"s_add_u32 m0, s30, 0xff00\n"
"v_bfi_b32 v244, s93, v245, v236\n"
"v_mfma_f32_16x16x32_bf16 v[8:11], a[80:83], v[220:223], v[8:11]\n"
"ds_read_b128 v[216:219], v232 offset:12288\n"
"global_load_lds_dwordx4 v244, s[46:47] offset:256 nt\n"
"s_bfe_i32 s93, s91, 0x10008\n"
"v_mfma_f32_16x16x32_bf16 v[12:15], a[84:87], v[220:223], v[12:15]\n"
"s_add_u32 m0, s30, 0x10f00\n"
"v_bfi_b32 v244, s93, v245, v236\n"
"v_mfma_f32_16x16x32_bf16 v[16:19], a[88:91], v[220:223], v[16:19]\n"
"global_load_lds_dwordx4 v244, s[48:49] offset:256 nt\n"
"v_mfma_f32_16x16x32_bf16 v[20:23], a[92:95], v[220:223], v[20:23]\n"
"s_waitcnt lgkmcnt(2)\n"
"v_mfma_f32_16x16x32_bf16 v[24:27], a[72:75], v[224:227], v[24:27]\n"
"s_add_u32 s14, s14, 0x10000\n"
"v_mfma_f32_16x16x32_bf16 v[28:31], a[76:79], v[224:227], v[28:31]\n"
"s_add_u32 s24, s28, s14\n"
"s_addc_u32 s25, s29, 0\n"
"v_mfma_f32_16x16x32_bf16 v[32:35], a[80:83], v[224:227], v[32:35]\n"
"ds_read_b128 v[220:223], v232 offset:16384\n"
"v_mfma_f32_16x16x32_bf16 v[36:39], a[84:87], v[224:227], v[36:39]\n"
"s_mov_b32 s88, 0\n"
"v_mfma_f32_16x16x32_bf16 v[40:43], a[88:91], v[224:227], v[40:43]\n"
"s_mov_b32 s19, 25\n"
"v_mfma_f32_16x16x32_bf16 v[44:47], a[92:95], v[224:227], v[44:47]\n"
"s_waitcnt lgkmcnt(2)\n"
"v_mfma_f32_16x16x32_bf16 v[48:51], a[72:75], v[228:231], v[48:51]\n"
"v_mfma_f32_16x16x32_bf16 v[52:55], a[76:79], v[228:231], v[52:55]\n"
"v_mfma_f32_16x16x32_bf16 v[56:59], a[80:83], v[228:231], v[56:59]\n"
"ds_read_b128 v[224:227], v232 offset:20480\n"
"v_mfma_f32_16x16x32_bf16 v[60:63], a[84:87], v[228:231], v[60:63]\n"
"v_mfma_f32_16x16x32_bf16 v[64:67], a[88:91], v[228:231], v[64:67]\n"
"v_mfma_f32_16x16x32_bf16 v[68:71], a[92:95], v[228:231], v[68:71]\n"
"s_waitcnt lgkmcnt(2)\n"
"v_mfma_f32_16x16x32_bf16 v[72:75], a[0:3], v[216:219], 0\n"
"v_mfma_f32_16x16x32_bf16 v[76:79], a[4:7], v[216:219], 0\n"
"v_mfma_f32_16x16x32_bf16 v[80:83], a[8:11], v[216:219], 0\n"
"ds_read_b128 v[228:231], v233 offset:12288\n"
"v_mfma_f32_16x16x32_bf16 v[84:87], a[12:15], v[216:219], 0\n"
"v_mfma_f32_16x16x32_bf16 v[88:91], a[16:19], v[216:219], 0\n"
"v_mfma_f32_16x16x32_bf16 v[92:95], a[20:23], v[216:219], 0\n"
"s_waitcnt lgkmcnt(2)\n"
"v_mfma_f32_16x16x32_bf16 v[96:99], a[0:3], v[220:223], 0\n"
"v_mfma_f32_16x16x32_bf16 v[100:103], a[4:7], v[220:223], 0\n"
"v_mfma_f32_16x16x32_bf16 v[104:107], a[8:11], v[220:223], 0\n"
"ds_read_b128 v[216:219], v233 offset:16384\n"
"v_mfma_f32_16x16x32_bf16 v[108:111], a[12:15], v[220:223], 0\n"
"s_mov_b64 exec, s[12:13]\n"
"global_store_dwordx2 v251, v[252:253], s[28:29] sc1\n"
"s_mov_b64 exec, -1\n"
"v_mfma_f32_16x16x32_bf16 v[112:115], a[16:19], v[220:223], 0\n"
"v_mfma_f32_16x16x32_bf16 v[116:119], a[20:23], v[220:223], 0\n"
"s_waitcnt lgkmcnt(2)\n"
"v_mfma_f32_16x16x32_bf16 v[120:123], a[0:3], v[224:227], 0\n"
"v_mfma_f32_16x16x32_bf16 v[124:127], a[4:7], v[224:227], 0\n"
"v_mfma_f32_16x16x32_bf16 v[128:131], a[8:11], v[224:227], 0\n"
"ds_read_b128 v[220:223], v233 offset:20480\n"
"v_mfma_f32_16x16x32_bf16 v[132:135], a[12:15], v[224:227], 0\n"
"v_mfma_f32_16x16x32_bf16 v[136:139], a[16:19], v[224:227], 0\n"
"v_mfma_f32_16x16x32_bf16 v[140:143], a[20:23], v[224:227], 0\n"
"s_waitcnt lgkmcnt(2)\n"
"v_mfma_f32_16x16x32_bf16 v[72:75], a[24:27], v[228:231], v[72:75]\n"
"v_mfma_f32_16x16x32_bf16 v[76:79], a[28:31], v[228:231], v[76:79]\n"
"v_mfma_f32_16x16x32_bf16 v[80:83], a[32:35], v[228:231], v[80:83]\n"
"ds_read_b128 v[224:227], v234 offset:12288\n"
"v_mfma_f32_16x16x32_bf16 v[84:87], a[36:39], v[228:231], v[84:87]\n"
"v_mfma_f32_16x16x32_bf16 v[88:91], a[40:43], v[228:231], v[88:91]\n"
"v_mfma_f32_16x16x32_bf16 v[92:95], a[44:47], v[228:231], v[92:95]\n"
"s_waitcnt lgkmcnt(2)\n"
"v_mfma_f32_16x16x32_bf16 v[96:99], a[24:27], v[216:219], v[96:99]\n"
"v_mfma_f32_16x16x32_bf16 v[100:103], a[28:31], v[216:219], v[100:103]\n"
"v_mfma_f32_16x16x32_bf16 v[104:107], a[32:35], v[216:219], v[104:107]\n"
"ds_read_b128 v[228:231], v234 offset:16384\n"
"v_mfma_f32_16x16x32_bf16 v[108:111], a[36:39], v[216:219], v[108:111]\n"
"v_mfma_f32_16x16x32_bf16 v[112:115], a[40:43], v[216:219], v[112:115]\n"
"v_mfma_f32_16x16x32_bf16 v[116:119], a[44:47], v[216:219], v[116:119]\n"
"s_waitcnt lgkmcnt(2)\n"
"v_mfma_f32_16x16x32_bf16 v[120:123], a[24:27], v[220:223], v[120:123]\n"
"v_mfma_f32_16x16x32_bf16 v[124:127], a[28:31], v[220:223], v[124:127]\n"
"v_mfma_f32_16x16x32_bf16 v[128:131], a[32:35], v[220:223], v[128:131]\n"
"ds_read_b128 v[216:219], v234 offset:20480\n"
"v_mfma_f32_16x16x32_bf16 v[132:135], a[36:39], v[220:223], v[132:135]\n"
"v_mfma_f32_16x16x32_bf16 v[136:139], a[40:43], v[220:223], v[136:139]\n"
"v_mfma_f32_16x16x32_bf16 v[140:143], a[44:47], v[220:223], v[140:143]\n"
"s_waitcnt lgkmcnt(2)\n"
"v_mfma_f32_16x16x32_bf16 v[72:75], a[48:51], v[224:227], v[72:75]\n"
"v_mfma_f32_16x16x32_bf16 v[76:79], a[52:55], v[224:227], v[76:79]\n"
"v_mfma_f32_16x16x32_bf16 v[80:83], a[56:59], v[224:227], v[80:83]\n"
"ds_read_b128 v[220:223], v235 offset:12288\n"
"v_mfma_f32_16x16x32_bf16 v[84:87], a[60:63], v[224:227], v[84:87]\n"
"v_mfma_f32_16x16x32_bf16 v[88:91], a[64:67], v[224:227], v[88:91]\n"
"v_mfma_f32_16x16x32_bf16 v[92:95], a[68:71], v[224:227], v[92:95]\n"
"s_add_u32 s78, s64, 0x24000\n"
"s_addc_u32 s79, s65, 0\n"
"s_waitcnt lgkmcnt(2)\n"
"v_mfma_f32_16x16x32_bf16 v[96:99], a[48:51], v[228:231], v[96:99]\n"
"s_add_u32 m0, s74, 0x0\n"
"v_mfma_f32_16x16x32_bf16 v[100:103], a[52:55], v[228:231], v[100:103]\n"
"global_load_lds_dwordx4 v240, s[78:79] offset:0\n"
"v_mfma_f32_16x16x32_bf16 v[104:107], a[56:59], v[228:231], v[104:107]\n"
"ds_read_b128 v[224:227], v235 offset:16384\n"
"v_mfma_f32_16x16x32_bf16 v[108:111], a[60:63], v[228:231], v[108:111]\n"
"v_mfma_f32_16x16x32_bf16 v[112:115], a[64:67], v[228:231], v[112:115]\n"
"global_load_lds_dwordx4 v241, s[78:79] offset:1024\n"
"v_mfma_f32_16x16x32_bf16 v[116:119], a[68:71], v[228:231], v[116:119]\n"
"s_waitcnt lgkmcnt(2)\n"
"v_mfma_f32_16x16x32_bf16 v[120:123], a[48:51], v[216:219], v[120:123]\n"
"v_mfma_f32_16x16x32_bf16 v[124:127], a[52:55], v[216:219], v[124:127]\n"
"global_load_lds_dwordx4 v242, s[78:79] offset:2048\n"
"v_mfma_f32_16x16x32_bf16 v[128:131], a[56:59], v[216:219], v[128:131]\n"
"ds_read_b128 v[228:231], v235 offset:20480\n"
"s_add_u32 s78, s64, 0x27000\n"
"s_addc_u32 s79, s65, 0\n"
"v_mfma_f32_16x16x32_bf16 v[132:135], a[60:63], v[216:219], v[132:135]\n"
"s_add_u32 m0, s74, 0xc00\n"
"v_mfma_f32_16x16x32_bf16 v[136:139], a[64:67], v[216:219], v[136:139]\n"
"global_load_lds_dwordx4 v240, s[78:79] offset:0\n"
"v_mfma_f32_16x16x32_bf16 v[140:143], a[68:71], v[216:219], v[140:143]\n"
"s_waitcnt lgkmcnt(2)\n"
"v_mfma_f32_16x16x32_bf16 v[72:75], a[72:75], v[220:223], v[72:75]\n"
"v_mfma_f32_16x16x32_bf16 v[76:79], a[76:79], v[220:223], v[76:79]\n"
"s_waitcnt vmcnt(14)\n"
"s_barrier\n"
"global_load_lds_dwordx4 v241, s[78:79] offset:1024\n"
"v_mfma_f32_16x16x32_bf16 v[80:83], a[80:83], v[220:223], v[80:83]\n"
"ds_read_b128 v[216:219], v232 offset:24576\n"
"v_mfma_f32_16x16x32_bf16 v[84:87], a[84:87], v[220:223], v[84:87]\n"
"v_mfma_f32_16x16x32_bf16 v[88:91], a[88:91], v[220:223], v[88:91]\n"
"global_load_lds_dwordx4 v242, s[78:79] offset:2048\n"
"v_mfma_f32_16x16x32_bf16 v[92:95], a[92:95], v[220:223], v[92:95]\n"
"s_add_u32 s78, s64, 0x2a000\n"
"s_addc_u32 s79, s65, 0\n"
"s_waitcnt lgkmcnt(2)\n"
"v_mfma_f32_16x16x32_bf16 v[96:99], a[72:75], v[224:227], v[96:99]\n"
"s_add_u32 m0, s74, 0x6000\n"
"v_mfma_f32_16x16x32_bf16 v[100:103], a[76:79], v[224:227], v[100:103]\n"
"global_load_lds_dwordx4 v240, s[78:79] offset:0\n"
"v_mfma_f32_16x16x32_bf16 v[104:107], a[80:83], v[224:227], v[104:107]\n"
"ds_read_b128 v[220:223], v232 offset:28672\n"
"v_mfma_f32_16x16x32_bf16 v[108:111], a[84:87], v[224:227], v[108:111]\n"
"v_mfma_f32_16x16x32_bf16 v[112:115], a[88:91], v[224:227], v[112:115]\n"
"global_load_lds_dwordx4 v241, s[78:79] offset:1024\n"
"v_mfma_f32_16x16x32_bf16 v[116:119], a[92:95], v[224:227], v[116:119]\n"
"s_waitcnt lgkmcnt(2)\n"
"v_mfma_f32_16x16x32_bf16 v[120:123], a[72:75], v[228:231], v[120:123]\n"
"v_mfma_f32_16x16x32_bf16 v[124:127], a[76:79], v[228:231], v[124:127]\n"
"global_load_lds_dwordx4 v242, s[78:79] offset:2048\n"
"v_mfma_f32_16x16x32_bf16 v[128:131], a[80:83], v[228:231], v[128:131]\n"
"ds_read_b128 v[224:227], v232 offset:32768\n"
"v_add_u32 v232, 0x9000, v232\n"
"s_add_u32 s78, s64, 0x2d000\n"
"s_addc_u32 s79, s65, 0\n"
"v_mfma_f32_16x16x32_bf16 v[132:135], a[84:87], v[228:231], v[132:135]\n"
"s_add_u32 m0, s74, 0x6c00\n"
"v_mfma_f32_16x16x32_bf16 v[136:139], a[88:91], v[228:231], v[136:139]\n"
"global_load_lds_dwordx4 v240, s[78:79] offset:0\n"
"v_mfma_f32_16x16x32_bf16 v[140:143], a[92:95], v[228:231], v[140:143]\n"
"s_waitcnt lgkmcnt(2)\n"
"v_mfma_f32_16x16x32_bf16 v[144:147], a[0:3], v[216:219], 0\n"
"v_mfma_f32_16x16x32_bf16 v[148:151], a[4:7], v[216:219], 0\n"
"global_load_lds_dwordx4 v241, s[78:79] offset:1024\n"
"v_mfma_f32_16x16x32_bf16 v[152:155], a[8:11], v[216:219], 0\n"
"ds_read_b128 v[228:231], v233 offset:24576\n"
"v_mfma_f32_16x16x32_bf16 v[156:159], a[12:15], v[216:219], 0\n"
"v_mfma_f32_16x16x32_bf16 v[160:163], a[16:19], v[216:219], 0\n"
"global_load_lds_dwordx4 v242, s[78:79] offset:2048\n"
"s_waitcnt vmcnt(6)\n"
"ds_read_b64_tr_b16 a[144:145], v243 offset:0\n"
"v_mfma_f32_16x16x32_bf16 v[164:167], a[20:23], v[216:219], 0\n"
"s_add_u32 s78, s64, 0x30000\n"
"s_addc_u32 s79, s65, 0\n"
"s_waitcnt lgkmcnt(3)\n"
"v_mfma_f32_16x16x32_bf16 v[168:171], a[0:3], v[220:223], 0\n"
"s_add_u32 m0, s74, 0xc000\n"
"ds_read_b64_tr_b16 a[146:147], v243 offset:768\n"
"v_mfma_f32_16x16x32_bf16 v[172:175], a[4:7], v[220:223], 0\n"
"global_load_lds_dwordx4 v240, s[78:79] offset:0\n"
"v_mfma_f32_16x16x32_bf16 v[176:179], a[8:11], v[220:223], 0\n"
"ds_read_b128 v[216:219], v233 offset:28672\n"
"ds_read_b64_tr_b16 a[148:149], v243 offset:8\n"
"v_mfma_f32_16x16x32_bf16 v[180:183], a[12:15], v[220:223], 0\n"
"ds_read_b64_tr_b16 a[150:151], v243 offset:776\n"
"v_mfma_f32_16x16x32_bf16 v[184:187], a[16:19], v[220:223], 0\n"
"global_load_lds_dwordx4 v241, s[78:79] offset:1024\n"
"ds_read_b64_tr_b16 a[152:153], v243 offset:64\n"
"v_mfma_f32_16x16x32_bf16 v[188:191], a[20:23], v[220:223], 0\n"
"ds_read_b64_tr_b16 a[154:155], v243 offset:832\n"
"s_waitcnt lgkmcnt(8)\n"
"v_mfma_f32_16x16x32_bf16 v[192:195], a[0:3], v[224:227], 0\n"
"ds_read_b64_tr_b16 a[156:157], v243 offset:72\n"
"v_mfma_f32_16x16x32_bf16 v[196:199], a[4:7], v[224:227], 0\n"
"global_load_lds_dwordx4 v242, s[78:79] offset:2048\n"
"ds_read_b64_tr_b16 a[158:159], v243 offset:840\n"
"v_mfma_f32_16x16x32_bf16 v[200:203], a[8:11], v[224:227], 0\n"
"ds_read_b128 v[220:223], v233 offset:32768\n"
"v_add_u32 v233, 0x9000, v233\n"
"s_add_u32 s78, s64, 0x33000\n"
"s_addc_u32 s79, s65, 0\n"
"v_mfma_f32_16x16x32_bf16 v[204:207], a[12:15], v[224:227], 0\n"
"s_add_u32 m0, s74, 0xcc00\n"
"ds_read_b64_tr_b16 a[160:161], v243 offset:128\n"
"v_mfma_f32_16x16x32_bf16 v[208:211], a[16:19], v[224:227], 0\n"
"global_load_lds_dwordx4 v240, s[78:79] offset:0\n"
"ds_read_b64_tr_b16 a[162:163], v243 offset:896\n"
"v_mfma_f32_16x16x32_bf16 v[212:215], a[20:23], v[224:227], 0\n"
"ds_read_b64_tr_b16 a[164:165], v243 offset:136\n"
"s_waitcnt lgkmcnt(13)\n"
"v_mfma_f32_16x16x32_bf16 v[144:147], a[24:27], v[228:231], v[144:147]\n"
"ds_read_b64_tr_b16 a[166:167], v243 offset:904\n"
"v_mfma_f32_16x16x32_bf16 v[148:151], a[28:31], v[228:231], v[148:151]\n"
"global_load_lds_dwordx4 v241, s[78:79] offset:1024\n"
"v_mfma_f32_16x16x32_bf16 v[152:155], a[32:35], v[228:231], v[152:155]\n"
"ds_read_b128 v[224:227], v234 offset:24576\n"
"v_mfma_f32_16x16x32_bf16 v[156:159], a[36:39], v[228:231], v[156:159]\n"
"v_mfma_f32_16x16x32_bf16 v[160:163], a[40:43], v[228:231], v[160:163]\n"
"global_load_lds_dwordx4 v242, s[78:79] offset:2048\n"
"s_waitcnt vmcnt(6)\n"
"ds_read_b64_tr_b16 a[168:169], v243 offset:24576\n"
"v_mfma_f32_16x16x32_bf16 v[164:167], a[44:47], v[228:231], v[164:167]\n"
"ds_read_b64_tr_b16 a[170:171], v243 offset:25344\n"
"s_waitcnt lgkmcnt(14)\n"
"v_mfma_f32_16x16x32_bf16 v[168:171], a[24:27], v[216:219], v[168:171]\n"
"ds_read_b64_tr_b16 a[172:173], v243 offset:24584\n"
"v_mfma_f32_16x16x32_bf16 v[172:175], a[28:31], v[216:219], v[172:175]\n"
"s_add_u32 s78, s64, 0x36000\n"
"s_addc_u32 s79, s65, 0\n"
"ds_read_b64_tr_b16 a[174:175], v243 offset:25352\n"
"v_mfma_f32_16x16x32_bf16 v[176:179], a[32:35], v[216:219], v[176:179]\n"
"ds_read_b128 v[228:231], v234 offset:28672\n"
"s_add_u32 m0, s74, 0x0\n"
"ds_read_b64_tr_b16 a[176:177], v243 offset:24640\n"
"v_mfma_f32_16x16x32_bf16 v[180:183], a[36:39], v[216:219], v[180:183]\n"
"s_waitcnt lgkmcnt(7)\n"
"global_load_lds_dwordx4 v240, s[78:79] offset:0\n"
"ds_read_b64_tr_b16 a[178:179], v243 offset:25408\n"
"v_mfma_f32_16x16x32_bf16 v[184:187], a[40:43], v[216:219], v[184:187]\n"
"ds_read_b64_tr_b16 a[180:181], v243 offset:24648\n"
"v_mfma_f32_16x16x32_bf16 v[188:191], a[44:47], v[216:219], v[188:191]\n"
"ds_read_b64_tr_b16 a[182:183], v243 offset:25416\n"
"v_mfma_f32_16x16x32_bf16 v[192:195], a[24:27], v[220:223], v[192:195]\n"
"global_load_lds_dwordx4 v241, s[78:79] offset:1024\n"
"ds_read_b64_tr_b16 a[184:185], v243 offset:24704\n"
"v_mfma_f32_16x16x32_bf16 v[196:199], a[28:31], v[220:223], v[196:199]\n"
"ds_read_b64_tr_b16 a[186:187], v243 offset:25472\n"
"v_mfma_f32_16x16x32_bf16 v[200:203], a[32:35], v[220:223], v[200:203]\n"
"ds_read_b128 v[216:219], v234 offset:32768\n"
"v_add_u32 v234, 0x9000, v234\n"
"v_mfma_f32_16x16x32_bf16 v[204:207], a[36:39], v[220:223], v[204:207]\n"
"global_load_lds_dwordx4 v242, s[78:79] offset:2048\n"
"ds_read_b64_tr_b16 a[188:189], v243 offset:24712\n"
"v_mfma_f32_16x16x32_bf16 v[208:211], a[40:43], v[220:223], v[208:211]\n"
"s_add_u32 s78, s64, 0x39000\n"
"s_addc_u32 s79, s65, 0\n"
"v_mfma_f32_16x16x32_bf16 v[212:215], a[44:47], v[220:223], v[212:215]\n"
"s_add_u32 m0, s74, 0xc00\n"
"s_waitcnt lgkmcnt(13)\n"
"v_mfma_f32_16x16x32_bf16 v[144:147], a[48:51], v[224:227], v[144:147]\n"
"global_load_lds_dwordx4 v240, s[78:79] offset:0\n"
"ds_read_b64_tr_b16 a[190:191], v243 offset:25480\n"
"v_mfma_f32_16x16x32_bf16 v[148:151], a[52:55], v[224:227], v[148:151]\n"
"v_mfma_f32_16x16x32_bf16 v[152:155], a[56:59], v[224:227], v[152:155]\n"
"ds_read_b128 v[220:223], v235 offset:24576\n"
"v_mfma_f32_16x16x32_bf16 v[156:159], a[60:63], v[224:227], v[156:159]\n"
"global_load_lds_dwordx4 v241, s[78:79] offset:1024\n"
"v_mfma_f32_16x16x32_bf16 v[160:163], a[64:67], v[224:227], v[160:163]\n"
"s_waitcnt vmcnt(5)\n"
"ds_read_b64_tr_b16 a[192:193], v243 offset:49152\n"
"v_mfma_f32_16x16x32_bf16 v[164:167], a[68:71], v[224:227], v[164:167]\n"
"ds_read_b64_tr_b16 a[194:195], v243 offset:49920\n"
"s_waitcnt lgkmcnt(12)\n"
"v_mfma_f32_16x16x32_bf16 v[168:171], a[48:51], v[228:231], v[168:171]\n"
"global_load_lds_dwordx4 v242, s[78:79] offset:2048\n"
"ds_read_b64_tr_b16 a[196:197], v243 offset:49160\n"
"v_mfma_f32_16x16x32_bf16 v[172:175], a[52:55], v[228:231], v[172:175]\n"
"ds_read_b64_tr_b16 a[198:199], v243 offset:49928\n"
"v_mfma_f32_16x16x32_bf16 v[176:179], a[56:59], v[228:231], v[176:179]\n"
"ds_read_b128 v[224:227], v235 offset:28672\n"
"global_load_dwordx4 v[244:247], v248, s[28:29] sc1\n"
"s_add_u32 s78, s64, 0x3c000\n"
"s_addc_u32 s79, s65, 0\n"
"v_mfma_f32_16x16x32_bf16 v[180:183], a[60:63], v[228:231], v[180:183]\n"
"s_add_u32 m0, s74, 0x6000\n"
"ds_read_b64_tr_b16 a[200:201], v243 offset:49216\n"
"v_mfma_f32_16x16x32_bf16 v[184:187], a[64:67], v[228:231], v[184:187]\n"
"s_waitcnt lgkmcnt(7)\n"
"global_load_lds_dwordx4 v240, s[78:79] offset:0\n"
"ds_read_b64_tr_b16 a[202:203], v243 offset:49984\n"
"v_mfma_f32_16x16x32_bf16 v[188:191], a[68:71], v[228:231], v[188:191]\n"
"ds_read_b64_tr_b16 a[204:205], v243 offset:49224\n"
"v_mfma_f32_16x16x32_bf16 v[192:195], a[48:51], v[216:219], v[192:195]\n"
"ds_read_b64_tr_b16 a[206:207], v243 offset:49992\n"
"v_mfma_f32_16x16x32_bf16 v[196:199], a[52:55], v[216:219], v[196:199]\n"
"global_load_lds_dwordx4 v241, s[78:79] offset:1024\n"
"ds_read_b64_tr_b16 a[208:209], v243 offset:49280\n"
"v_mfma_f32_16x16x32_bf16 v[200:203], a[56:59], v[216:219], v[200:203]\n"
"ds_read_b128 v[228:231], v235 offset:32768\n"
"v_add_u32 v235, 0x9000, v235\n"
"v_mfma_f32_16x16x32_bf16 v[204:207], a[60:63], v[216:219], v[204:207]\n"
"ds_read_b64_tr_b16 a[210:211], v243 offset:50048\n"
"v_mfma_f32_16x16x32_bf16 v[208:211], a[64:67], v[216:219], v[208:211]\n"
"global_load_lds_dwordx4 v242, s[78:79] offset:2048\n"
"ds_read_b64_tr_b16 a[212:213], v243 offset:49288\n"
"v_mfma_f32_16x16x32_bf16 v[212:215], a[68:71], v[216:219], v[212:215]\n"
"s_add_u32 s78, s64, 0x3f000\n"
"s_addc_u32 s79, s65, 0\n"
"s_waitcnt lgkmcnt(13)\n"
"v_mfma_f32_16x16x32_bf16 v[144:147], a[72:75], v[220:223], v[144:147]\n"
"s_add_u32 m0, s74, 0x6c00\n"
"ds_read_b64_tr_b16 a[214:215], v243 offset:50056\n"
"v_mfma_f32_16x16x32_bf16 v[148:151], a[76:79], v[220:223], v[148:151]\n"
"s_barrier\n"
"global_load_lds_dwordx4 v240, s[78:79] offset:0\n"
"v_mfma_f32_16x16x32_bf16 v[152:155], a[80:83], v[220:223], v[152:155]\n"
"ds_read_b128 v[216:219], v232 offset:0\n"
"v_mfma_f32_16x16x32_bf16 v[156:159], a[84:87], v[220:223], v[156:159]\n"
"s_mov_b64 exec, s[10:11]\n"
"s_cbranch_execz LHT3\n"
"LHR3:\n"
"s_mov_b64 exec, -1\n"
"v_mfma_f32_16x16x32_bf16 v[160:163], a[88:91], v[220:223], v[160:163]\n"
"global_load_lds_dwordx4 v241, s[78:79] offset:1024\n"
"v_mfma_f32_16x16x32_bf16 v[164:167], a[92:95], v[220:223], v[164:167]\n"
"s_waitcnt lgkmcnt(10)\n"
"v_mfma_f32_16x16x32_bf16 v[168:171], a[72:75], v[224:227], v[168:171]\n"
"s_waitcnt vmcnt(5)\n"
"v_cmp_ne_u64 vcc, s[80:81], v[244:245]\n"
"v_cmp_ne_u64 s[56:57], s[80:81], v[244:245]\n"
"s_nop 3\n"
"s_or_b64 vcc, vcc, s[56:57]\n"
"s_cbranch_scc0 LP1_1\n"
"s_mov_b32 s82, 0x40000\n"
"LP0_1:\n"
"s_sleep 2\n"
"global_load_dwordx4 v[244:247], v248, s[28:29] sc1\n"
"s_sub_u32 s82, s82, 1\n"
"s_cbranch_scc1 LP2_1\n"
"s_waitcnt vmcnt(0)\n"
"v_cmp_ne_u64 vcc, s[80:81], v[244:245]\n"
"v_cmp_ne_u64 s[56:57], s[80:81], v[244:245]\n"
"s_nop 3\n"
"s_or_b64 vcc, vcc, s[56:57]\n"
"s_cbranch_scc1 LP0_1\n"
"LP2_1:\n"
"s_waitcnt vmcnt(0)\n"
"LP1_1:\n"
"s_nop 0\n"
"v_mfma_f32_16x16x32_bf16 v[172:175], a[76:79], v[224:227], v[172:175]\n"
"ds_read_b64_tr_b16 a[216:217], v243 offset:0\n"
"v_mfma_f32_16x16x32_bf16 v[176:179], a[80:83], v[224:227], v[176:179]\n"
"ds_read_b128 v[220:223], v232 offset:4096\n"
"v_subrev_u32 v246, s83, v246\n"
"global_load_lds_dwordx4 v242, s[78:79] offset:2048\n"
"ds_read_b64_tr_b16 a[218:219], v243 offset:768\n"
"v_mfma_f32_16x16x32_bf16 v[180:183], a[84:87], v[224:227], v[180:183]\n"
"s_add_u32 s78, s64, 0x42000\n"
"s_addc_u32 s79, s65, 0\n"
"ds_read_b64_tr_b16 a[220:221], v243 offset:8\n"
"v_mfma_f32_16x16x32_bf16 v[184:187], a[88:91], v[224:227], v[184:187]\n"
"v_add_u32_dpp v246, v246, v246 row_shr:1 row_mask:0xf bank_mask:0xf\n"
"s_add_u32 m0, s74, 0xc000\n"
"ds_read_b64_tr_b16 a[222:223], v243 offset:776\n"
"v_mfma_f32_16x16x32_bf16 v[188:191], a[92:95], v[224:227], v[188:191]\n"
"s_waitcnt lgkmcnt(6)\n"
"global_load_lds_dwordx4 v240, s[78:79] offset:0\n"
"ds_read_b64_tr_b16 a[224:225], v243 offset:64\n"
"v_mfma_f32_16x16x32_bf16 v[192:195], a[72:75], v[228:231], v[192:195]\n"
"v_add_u32_dpp v246, v246, v246 row_shr:2 row_mask:0xf bank_mask:0xf\n"
"ds_read_b64_tr_b16 a[226:227], v243 offset:832\n"
"v_mfma_f32_16x16x32_bf16 v[196:199], a[76:79], v[228:231], v[196:199]\n"
"ds_read_b64_tr_b16 a[228:229], v243 offset:72\n"
"v_mfma_f32_16x16x32_bf16 v[200:203], a[80:83], v[228:231], v[200:203]\n"
"ds_read_b128 v[224:227], v232 offset:8192\n"
"v_add_u32_dpp v246, v246, v246 row_shr:4 row_mask:0xf bank_mask:0xf\n"
"global_load_lds_dwordx4 v241, s[78:79] offset:1024\n"
"ds_read_b64_tr_b16 a[230:231], v243 offset:840\n"
"v_mfma_f32_16x16x32_bf16 v[204:207], a[84:87], v[228:231], v[204:207]\n"
"ds_read_b64_tr_b16 a[232:233], v243 offset:128\n"
"v_mfma_f32_16x16x32_bf16 v[208:211], a[88:91], v[228:231], v[208:211]\n"
"v_readlane_b32 s86, v246, 63\n"
"ds_read_b64_tr_b16 a[234:235], v243 offset:896\n"
"v_mfma_f32_16x16x32_bf16 v[212:215], a[92:95], v[228:231], v[212:215]\n"
"global_load_lds_dwordx4 v242, s[78:79] offset:2048\n"
"ds_read_b64_tr_b16 a[236:237], v243 offset:136\n"
"s_waitcnt lgkmcnt(13)\n"
"v_mfma_f32_16x16x32_bf16 v[0:3], a[96:99], v[216:219], v[0:3]\n"
"s_ashr_i32 s86, s86, 3\n"
"s_add_u32 s78, s64, 0x45000\n"
"s_addc_u32 s79, s65, 0\n"
"v_mfma_f32_16x16x32_bf16 v[4:7], a[100:103], v[216:219], v[4:7]\n"
"s_barrier\n"
"s_add_u32 m0, s74, 0xcc00\n"
"ds_read_b64_tr_b16 a[238:239], v243 offset:904\n"
"v_mfma_f32_16x16x32_bf16 v[8:11], a[104:107], v[216:219], v[8:11]\n"
"ds_read_b128 v[228:231], v232 offset:12288\n"
"s_max_i32 s86, s86, 100\n"
"global_load_lds_dwordx4 v240, s[78:79] offset:0\n"
"v_mfma_f32_16x16x32_bf16 v[12:15], a[108:111], v[216:219], v[12:15]\n"
"v_mfma_f32_16x16x32_bf16 v[16:19], a[112:115], v[216:219], v[16:19]\n"
"s_min_i32 s86, s86, 600\n"
"v_mfma_f32_16x16x32_bf16 v[20:23], a[116:119], v[216:219], v[20:23]\n"
"global_load_lds_dwordx4 v241, s[78:79] offset:1024\n"
"s_waitcnt lgkmcnt(13)\n"
"v_mfma_f32_16x16x32_bf16 v[24:27], a[96:99], v[220:223], v[24:27]\n"
"s_add_u32 s83, s83, s86\n"
"v_mfma_f32_16x16x32_bf16 v[28:31], a[100:103], v[220:223], v[28:31]\n"
"v_mfma_f32_16x16x32_bf16 v[32:35], a[104:107], v[220:223], v[32:35]\n"
"ds_read_b128 v[216:219], v232 offset:16384\n"
"s_add_u32 s83, s83, 3128\n"
"global_load_lds_dwordx4 v242, s[78:79] offset:2048\n"
"LHT3:\n"
"s_cbranch_execz LHR3\n"
"v_mfma_f32_16x16x32_bf16 v[36:39], a[108:111], v[220:223], v[36:39]\n"
"s_add_u32 s58, s24, 0x1000\n"
"s_addc_u32 s59, s25, 0\n"
"v_mfma_f32_16x16x32_bf16 v[40:43], a[112:115], v[220:223], v[40:43]\n"
"global_load_dwordx4 a[240:243], v239, s[58:59] offset:-4096 sc1\n"
"s_waitcnt vmcnt(7)\n"
"ds_read_b64_tr_b16 a[0:1], v243 offset:24576\n"
"v_mfma_f32_16x16x32_bf16 v[44:47], a[116:119], v[220:223], v[44:47]\n"
"ds_read_b64_tr_b16 a[2:3], v243 offset:25344\n"
"s_waitcnt lgkmcnt(9)\n"
"v_mfma_f32_16x16x32_bf16 v[48:51], a[96:99], v[224:227], v[48:51]\n"
"ds_read_b64_tr_b16 a[4:5], v243 offset:24584\n"
"v_mfma_f32_16x16x32_bf16 v[52:55], a[100:103], v[224:227], v[52:55]\n"
"ds_read_b64_tr_b16 a[6:7], v243 offset:25352\n"
"v_mfma_f32_16x16x32_bf16 v[56:59], a[104:107], v[224:227], v[56:59]\n"
"ds_read_b128 v[220:223], v232 offset:20480\n"
"ds_read_b64_tr_b16 a[8:9], v243 offset:24640\n"
"v_mfma_f32_16x16x32_bf16 v[60:63], a[108:111], v[224:227], v[60:63]\n"
"global_load_dwordx4 a[244:247], v239, s[58:59] offset:-3072 sc1\n"
"ds_read_b64_tr_b16 a[10:11], v243 offset:25408\n"
"v_mfma_f32_16x16x32_bf16 v[64:67], a[112:115], v[224:227], v[64:67]\n"
"ds_read_b64_tr_b16 a[12:13], v243 offset:24648\n"
"v_mfma_f32_16x16x32_bf16 v[68:71], a[116:119], v[224:227], v[68:71]\n"
"ds_read_b64_tr_b16 a[14:15], v243 offset:25416\n"
"s_waitcnt lgkmcnt(10)\n"
"v_mfma_f32_16x16x32_bf16 v[72:75], a[96:99], v[228:231], v[72:75]\n"
"ds_read_b64_tr_b16 a[16:17], v243 offset:24704\n"
"v_mfma_f32_16x16x32_bf16 v[76:79], a[100:103], v[228:231], v[76:79]\n"
"ds_read_b64_tr_b16 a[18:19], v243 offset:25472\n"
"v_mfma_f32_16x16x32_bf16 v[80:83], a[104:107], v[228:231], v[80:83]\n"
"ds_read_b128 v[224:227], v232 offset:24576\n"
"global_load_dwordx4 a[248:251], v239, s[58:59] offset:-2048 sc1\n"
"v_mfma_f32_16x16x32_bf16 v[84:87], a[108:111], v[228:231], v[84:87]\n"
"ds_read_b64_tr_b16 a[20:21], v243 offset:24712\n"
"v_mfma_f32_16x16x32_bf16 v[88:91], a[112:115], v[228:231], v[88:91]\n"
"ds_read_b64_tr_b16 a[22:23], v243 offset:25480\n"
"v_mfma_f32_16x16x32_bf16 v[92:95], a[116:119], v[228:231], v[92:95]\n"
"s_waitcnt lgkmcnt(14)\n"
"v_mfma_f32_16x16x32_bf16 v[96:99], a[96:99], v[216:219], v[96:99]\n"
"v_mfma_f32_16x16x32_bf16 v[100:103], a[100:103], v[216:219], v[100:103]\n"
"global_load_dwordx4 a[252:255], v239, s[58:59] offset:-1024 sc1\n"
"v_mfma_f32_16x16x32_bf16 v[104:107], a[104:107], v[216:219], v[104:107]\n"
"ds_read_b128 v[228:231], v232 offset:28672\n"
"s_waitcnt vmcnt(4)\n"
"ds_read_b64_tr_b16 a[24:25], v243 offset:49152\n"
"v_mfma_f32_16x16x32_bf16 v[108:111], a[108:111], v[216:219], v[108:111]\n"
"ds_read_b64_tr_b16 a[26:27], v243 offset:49920\n"
"v_mfma_f32_16x16x32_bf16 v[112:115], a[112:115], v[216:219], v[112:115]\n"
"ds_read_b64_tr_b16 a[28:29], v243 offset:49160\n"
"v_mfma_f32_16x16x32_bf16 v[116:119], a[116:119], v[216:219], v[116:119]\n"
"ds_read_b64_tr_b16 a[30:31], v243 offset:49928\n"
"s_waitcnt lgkmcnt(14)\n"
"v_mfma_f32_16x16x32_bf16 v[120:123], a[96:99], v[220:223], v[120:123]\n"
"global_load_dwordx4 a[48:51], v239, s[58:59] offset:0 sc1\n"
"ds_read_b64_tr_b16 a[32:33], v243 offset:49216\n"
"v_mfma_f32_16x16x32_bf16 v[124:127], a[100:103], v[220:223], v[124:127]\n"
"ds_read_b64_tr_b16 a[34:35], v243 offset:49984\n"
"v_mfma_f32_16x16x32_bf16 v[128:131], a[104:107], v[220:223], v[128:131]\n"
"ds_read_b128 v[216:219], v232 offset:32768\n"
"v_add_u32 v232, 0xffff7000, v232\n"
"v_mfma_f32_16x16x32_bf16 v[132:135], a[108:111], v[220:223], v[132:135]\n"
"ds_read_b64_tr_b16 a[36:37], v243 offset:49224\n"
"v_mfma_f32_16x16x32_bf16 v[136:139], a[112:115], v[220:223], v[136:139]\n"
"ds_read_b64_tr_b16 a[38:39], v243 offset:49992\n"
"v_mfma_f32_16x16x32_bf16 v[140:143], a[116:119], v[220:223], v[140:143]\n"
"global_load_dwordx4 a[52:55], v239, s[58:59] offset:1024 sc1\n"
"s_waitcnt lgkmcnt(12)\n"
"v_mfma_f32_16x16x32_bf16 v[144:147], a[96:99], v[224:227], v[144:147]\n"
"ds_read_b64_tr_b16 a[40:41], v243 offset:49280\n"
"v_mfma_f32_16x16x32_bf16 v[148:151], a[100:103], v[224:227], v[148:151]\n"
"ds_read_b64_tr_b16 a[42:43], v243 offset:50048\n"
"v_mfma_f32_16x16x32_bf16 v[152:155], a[104:107], v[224:227], v[152:155]\n"
"ds_read_b128 v[220:223], v233 offset:0\n"
"ds_read_b64_tr_b16 a[44:45], v243 offset:49288\n"
"v_mfma_f32_16x16x32_bf16 v[156:159], a[108:111], v[224:227], v[156:159]\n"
"ds_read_b64_tr_b16 a[46:47], v243 offset:50056\n"
"v_mfma_f32_16x16x32_bf16 v[160:163], a[112:115], v[224:227], v[160:163]\n"
"global_load_dwordx4 a[56:59], v239, s[58:59] offset:2048 sc1\n"
"v_mfma_f32_16x16x32_bf16 v[164:167], a[116:119], v[224:227], v[164:167]\n"
"s_waitcnt lgkmcnt(14)\n"
"v_mfma_f32_16x16x32_bf16 v[168:171], a[96:99], v[228:231], v[168:171]\n"
"ds_read_b64 v[240:241], v250 offset:24\n"
"v_mfma_f32_16x16x32_bf16 v[172:175], a[100:103], v[228:231], v[172:175]\n"
"v_mfma_f32_16x16x32_bf16 v[176:179], a[104:107], v[228:231], v[176:179]\n"
"ds_read_b128 v[224:227], v233 offset:4096\n"
"v_mfma_f32_16x16x32_bf16 v[180:183], a[108:111], v[228:231], v[180:183]\n"
"global_load_dwordx4 a[60:63], v239, s[58:59] offset:3072 sc1\n"
"v_mfma_f32_16x16x32_bf16 v[184:187], a[112:115], v[228:231], v[184:187]\n"
"v_mfma_f32_16x16x32_bf16 v[188:191], a[116:119], v[228:231], v[188:191]\n"
"s_waitcnt lgkmcnt(9)\n"
"v_mfma_f32_16x16x32_bf16 v[192:195], a[96:99], v[216:219], v[192:195]\n"
"v_mfma_f32_16x16x32_bf16 v[196:199], a[100:103], v[216:219], v[196:199]\n"
"s_add_u32 s58, s24, 0x3000\n"
"s_addc_u32 s59, s25, 0\n"
"v_mfma_f32_16x16x32_bf16 v[200:203], a[104:107], v[216:219], v[200:203]\n"
"ds_read_b128 v[228:231], v233 offset:8192\n"
"global_load_dwordx4 a[64:67], v239, s[58:59] offset:-4096 sc1\n"
"v_mfma_f32_16x16x32_bf16 v[204:207], a[108:111], v[216:219], v[204:207]\n"
"v_mfma_f32_16x16x32_bf16 v[208:211], a[112:115], v[216:219], v[208:211]\n"
"v_mfma_f32_16x16x32_bf16 v[212:215], a[116:119], v[216:219], v[212:215]\n"
"s_waitcnt lgkmcnt(5)\n"
"v_mfma_f32_16x16x32_bf16 v[0:3], a[120:123], v[220:223], v[0:3]\n"
"v_mfma_f32_16x16x32_bf16 v[4:7], a[124:127], v[220:223], v[4:7]\n"
"global_load_dwordx4 a[68:71], v239, s[58:59] offset:-3072 sc1\n"
"v_mfma_f32_16x16x32_bf16 v[8:11], a[128:131], v[220:223], v[8:11]\n"
"ds_read_b128 v[216:219], v233 offset:12288\n"
"v_mfma_f32_16x16x32_bf16 v[12:15], a[132:135], v[220:223], v[12:15]\n"
"v_mfma_f32_16x16x32_bf16 v[16:19], a[136:139], v[220:223], v[16:19]\n"
"v_mfma_f32_16x16x32_bf16 v[20:23], a[140:143], v[220:223], v[20:23]\n"
"s_waitcnt lgkmcnt(2)\n"
"v_mfma_f32_16x16x32_bf16 v[24:27], a[120:123], v[224:227], v[24:27]\n"
"global_load_dwordx4 a[72:75], v239, s[58:59] offset:-2048 sc1\n"
"v_mfma_f32_16x16x32_bf16 v[28:31], a[124:127], v[224:227], v[28:31]\n"
"v_add_u32 v245, 0x3000, v236\n"
"v_mfma_f32_16x16x32_bf16 v[32:35], a[128:131], v[224:227], v[32:35]\n"
"ds_read_b128 v[220:223], v233 offset:16384\n"
"v_add_u32 v244, 0xffffd000, v236\n"
"v_mfma_f32_16x16x32_bf16 v[36:39], a[132:135], v[224:227], v[36:39]\n"
"v_min_u32 v245, v245, v244\n"
"v_mfma_f32_16x16x32_bf16 v[40:43], a[136:139], v[224:227], v[40:43]\n"
"s_bfe_i32 s93, s91, 0x10000\n"
"v_mfma_f32_16x16x32_bf16 v[44:47], a[140:143], v[224:227], v[44:47]\n"
"s_add_u32 m0, s30, 0xfffffe00\n"
"v_bfi_b32 v244, s93, v245, v236\n"
"s_waitcnt lgkmcnt(2)\n"
"v_mfma_f32_16x16x32_bf16 v[48:51], a[120:123], v[228:231], v[48:51]\n"
"global_load_lds_dwordx4 v244, s[32:33] offset:512 nt\n"
"s_bfe_i32 s93, s91, 0x10001\n"
"v_mfma_f32_16x16x32_bf16 v[52:55], a[124:127], v[228:231], v[52:55]\n"
"s_add_u32 m0, s30, 0xe00\n"
"v_bfi_b32 v244, s93, v245, v236\n"
"v_mfma_f32_16x16x32_bf16 v[56:59], a[128:131], v[228:231], v[56:59]\n"
"ds_read_b128 v[224:227], v233 offset:20480\n"
"global_load_lds_dwordx4 v244, s[34:35] offset:512 nt\n"
"s_bfe_i32 s93, s91, 0x10002\n"
"v_mfma_f32_16x16x32_bf16 v[60:63], a[132:135], v[228:231], v[60:63]\n"
"s_add_u32 m0, s30, 0x1e00\n"
"v_bfi_b32 v244, s93, v245, v236\n"
"v_mfma_f32_16x16x32_bf16 v[64:67], a[136:139], v[228:231], v[64:67]\n"
"global_load_lds_dwordx4 v244, s[36:37] offset:512 nt\n"
"s_bfe_i32 s93, s91, 0x10003\n"
"v_mfma_f32_16x16x32_bf16 v[68:71], a[140:143], v[228:231], v[68:71]\n"
"s_add_u32 m0, s30, 0x2e00\n"
"v_bfi_b32 v244, s93, v245, v236\n"
"s_waitcnt lgkmcnt(2)\n"
"v_mfma_f32_16x16x32_bf16 v[72:75], a[120:123], v[216:219], v[72:75]\n"
"global_load_lds_dwordx4 v244, s[38:39] offset:512 nt\n"
"s_bfe_i32 s93, s91, 0x10004\n"
"v_mfma_f32_16x16x32_bf16 v[76:79], a[124:127], v[216:219], v[76:79]\n"
"s_add_u32 m0, s30, 0x3e00\n"
"v_bfi_b32 v244, s93, v245, v236\n"
"v_mfma_f32_16x16x32_bf16 v[80:83], a[128:131], v[216:219], v[80:83]\n"
"ds_read_b128 v[228:231], v233 offset:24576\n"
"global_load_lds_dwordx4 v244, s[40:41] offset:512 nt\n"
"s_bfe_i32 s93, s91, 0x10005\n"
"v_mfma_f32_16x16x32_bf16 v[84:87], a[132:135], v[216:219], v[84:87]\n"
"s_add_u32 m0, s30, 0x4e00\n"
"v_bfi_b32 v244, s93, v245, v236\n"
"v_mfma_f32_16x16x32_bf16 v[88:91], a[136:139], v[216:219], v[88:91]\n"
"global_load_lds_dwordx4 v244, s[42:43] offset:512 nt\n"
"s_bfe_i32 s93, s91, 0x10006\n"
"v_mfma_f32_16x16x32_bf16 v[92:95], a[140:143], v[216:219], v[92:95]\n"
"s_add_u32 m0, s30, 0x5e00\n"
"v_bfi_b32 v244, s93, v245, v236\n"
"s_waitcnt lgkmcnt(2)\n"
"v_mfma_f32_16x16x32_bf16 v[96:99], a[120:123], v[220:223], v[96:99]\n"
"global_load_lds_dwordx4 v244, s[44:45] offset:512 nt\n"
"s_bfe_i32 s93, s91, 0x10007\n"
"v_mfma_f32_16x16x32_bf16 v[100:103], a[124:127], v[220:223], v[100:103]\n"
"s_add_u32 m0, s30, 0x6e00\n"
"v_bfi_b32 v244, s93, v245, v236\n"
"v_mfma_f32_16x16x32_bf16 v[104:107], a[128:131], v[220:223], v[104:107]\n"
"ds_read_b128 v[216:219], v233 offset:28672\n"
"global_load_lds_dwordx4 v244, s[46:47] offset:512 nt\n"
"s_bfe_i32 s93, s91, 0x10008\n"
"v_mfma_f32_16x16x32_bf16 v[108:111], a[132:135], v[220:223], v[108:111]\n"
"s_add_u32 m0, s30, 0x7e00\n"
"v_bfi_b32 v244, s93, v245, v236\n"
"v_mfma_f32_16x16x32_bf16 v[112:115], a[136:139], v[220:223], v[112:115]\n"
"global_load_lds_dwordx4 v244, s[48:49] offset:512 nt\n"
"v_mfma_f32_16x16x32_bf16 v[116:119], a[140:143], v[220:223], v[116:119]\n"
"s_waitcnt lgkmcnt(2)\n"
"v_mfma_f32_16x16x32_bf16 v[120:123], a[120:123], v[224:227], v[120:123]\n"
"s_add_u32 s14, s98, 0\n"
"v_mfma_f32_16x16x32_bf16 v[124:127], a[124:127], v[224:227], v[124:127]\n"
"s_mul_i32 s15, s14, 0xaaab\n"
"v_mfma_f32_16x16x32_bf16 v[128:131], a[128:131], v[224:227], v[128:131]\n"
"ds_read_b128 v[220:223], v233 offset:32768\n"
"v_add_u32 v233, 0xffff7000, v233\n"
"v_mfma_f32_16x16x32_bf16 v[132:135], a[132:135], v[224:227], v[132:135]\n"
"s_lshr_b32 s15, s15, 18\n"
"v_mfma_f32_16x16x32_bf16 v[136:139], a[136:139], v[224:227], v[136:139]\n"
"s_mul_i32 s15, s15, 0x48000\n"
"v_mfma_f32_16x16x32_bf16 v[140:143], a[140:143], v[224:227], v[140:143]\n"
"s_waitcnt lgkmcnt(2)\n"
"v_mfma_f32_16x16x32_bf16 v[144:147], a[120:123], v[228:231], v[144:147]\n"
"s_mul_i32 s14, s14, 0xc000\n"
"v_mfma_f32_16x16x32_bf16 v[148:151], a[124:127], v[228:231], v[148:151]\n"
"s_add_u32 s14, s14, s15\n"
"v_mfma_f32_16x16x32_bf16 v[152:155], a[128:131], v[228:231], v[152:155]\n"
"ds_read_b128 v[224:227], v234 offset:0\n"
"v_mfma_f32_16x16x32_bf16 v[156:159], a[132:135], v[228:231], v[156:159]\n"
"s_lshl2_add_u32 s32, s95, s14\n"
"v_mfma_f32_16x16x32_bf16 v[160:163], a[136:139], v[228:231], v[160:163]\n"
"s_add_u32 s33, s32, 0x48000\n"
"v_mfma_f32_16x16x32_bf16 v[164:167], a[140:143], v[228:231], v[164:167]\n"
"global_load_dwordx4 a[76:79], v239, s[58:59] offset:-1024 sc1\n"
"s_waitcnt lgkmcnt(2)\n"
"v_mfma_f32_16x16x32_bf16 v[168:171], a[120:123], v[216:219], v[168:171]\n"
"s_lshl2_add_u32 s38, s96, s14\n"
"v_mfma_f32_16x16x32_bf16 v[172:175], a[124:127], v[216:219], v[172:175]\n"
"s_add_u32 s39, s38, 0x48000\n"
"v_mfma_f32_16x16x32_bf16 v[176:179], a[128:131], v[216:219], v[176:179]\n"
"ds_read_b128 v[228:231], v234 offset:4096\n"
"v_mfma_f32_16x16x32_bf16 v[180:183], a[132:135], v[216:219], v[180:183]\n"
"s_lshl2_add_u32 s44, s97, s14\n"
"v_mfma_f32_16x16x32_bf16 v[184:187], a[136:139], v[216:219], v[184:187]\n"
"global_load_dwordx4 a[80:83], v239, s[58:59] offset:0 sc1\n"
"v_mfma_f32_16x16x32_bf16 v[188:191], a[140:143], v[216:219], v[188:191]\n"
"s_waitcnt lgkmcnt(2)\n"
"v_mfma_f32_16x16x32_bf16 v[192:195], a[120:123], v[220:223], v[192:195]\n"
"s_add_u32 s45, s44, 0x48000\n"
"v_mfma_f32_16x16x32_bf16 v[196:199], a[124:127], v[220:223], v[196:199]\n"
"s_add_u32 s14, s98, 128\n"
"v_mfma_f32_16x16x32_bf16 v[200:203], a[128:131], v[220:223], v[200:203]\n"
"ds_read_b128 v[216:219], v234 offset:8192\n"
"v_mfma_f32_16x16x32_bf16 v[204:207], a[132:135], v[220:223], v[204:207]\n"
"global_load_dwordx4 a[84:87], v239, s[58:59] offset:1024 sc1\n"
"v_mfma_f32_16x16x32_bf16 v[208:211], a[136:139], v[220:223], v[208:211]\n"
"s_mul_i32 s15, s14, 0xaaab\n"
"v_mfma_f32_16x16x32_bf16 v[212:215], a[140:143], v[220:223], v[212:215]\n"
"s_waitcnt lgkmcnt(2)\n"
"v_mfma_f32_16x16x32_bf16 v[0:3], a[144:147], v[224:227], v[0:3]\n"
"s_lshr_b32 s15, s15, 18\n"
"v_mfma_f32_16x16x32_bf16 v[4:7], a[148:151], v[224:227], v[4:7]\n"
"s_mul_i32 s15, s15, 0x48000\n"
"v_mfma_f32_16x16x32_bf16 v[8:11], a[152:155], v[224:227], v[8:11]\n"
"ds_read_b128 v[220:223], v234 offset:12288\n"
"global_load_dwordx4 a[88:91], v239, s[58:59] offset:2048 sc1\n"
"v_mfma_f32_16x16x32_bf16 v[12:15], a[156:159], v[224:227], v[12:15]\n"
"s_mul_i32 s14, s14, 0xc000\n"
"v_mfma_f32_16x16x32_bf16 v[16:19], a[160:163], v[224:227], v[16:19]\n"
"s_add_u32 s14, s14, s15\n"
"v_mfma_f32_16x16x32_bf16 v[20:23], a[164:167], v[224:227], v[20:23]\n"
"s_waitcnt lgkmcnt(2)\n"
"v_mfma_f32_16x16x32_bf16 v[24:27], a[144:147], v[228:231], v[24:27]\n"
"s_lshl2_add_u32 s34, s95, s14\n"
"v_mfma_f32_16x16x32_bf16 v[28:31], a[148:151], v[228:231], v[28:31]\n"
"global_load_dwordx4 a[92:95], v239, s[58:59] offset:3072 sc1\n"
"v_mfma_f32_16x16x32_bf16 v[32:35], a[152:155], v[228:231], v[32:35]\n"
"ds_read_b128 v[224:227], v234 offset:16384\n"
"v_mfma_f32_16x16x32_bf16 v[36:39], a[156:159], v[228:231], v[36:39]\n"
"s_add_u32 s35, s34, 0x48000\n"
"v_mfma_f32_16x16x32_bf16 v[40:43], a[160:163], v[228:231], v[40:43]\n"
"s_lshl2_add_u32 s40, s96, s14\n"
"v_mfma_f32_16x16x32_bf16 v[44:47], a[164:167], v[228:231], v[44:47]\n"
"s_add_u32 s58, s24, 0x5000\n"
"s_addc_u32 s59, s25, 0\n"
"s_waitcnt lgkmcnt(2)\n"
"v_mfma_f32_16x16x32_bf16 v[48:51], a[144:147], v[216:219], v[48:51]\n"
"global_load_dwordx4 a[96:99], v239, s[58:59] offset:-4096 sc1\n"
"v_mfma_f32_16x16x32_bf16 v[52:55], a[148:151], v[216:219], v[52:55]\n"
"s_add_u32 s41, s40, 0x48000\n"
"v_mfma_f32_16x16x32_bf16 v[56:59], a[152:155], v[216:219], v[56:59]\n"
"ds_read_b128 v[228:231], v234 offset:20480\n"
"v_mfma_f32_16x16x32_bf16 v[60:63], a[156:159], v[216:219], v[60:63]\n"
"s_lshl2_add_u32 s46, s97, s14\n"
"v_mfma_f32_16x16x32_bf16 v[64:67], a[160:163], v[216:219], v[64:67]\n"
"s_add_u32 s47, s46, 0x48000\n"
"v_mfma_f32_16x16x32_bf16 v[68:71], a[164:167], v[216:219], v[68:71]\n"
"global_load_dwordx4 a[100:103], v239, s[58:59] offset:-3072 sc1\n"
"s_waitcnt lgkmcnt(2)\n"
"v_mfma_f32_16x16x32_bf16 v[72:75], a[144:147], v[220:223], v[72:75]\n"
"s_add_u32 s14, s98, 256\n"
"v_mfma_f32_16x16x32_bf16 v[76:79], a[148:151], v[220:223], v[76:79]\n"
"s_mul_i32 s15, s14, 0xaaab\n"
"v_mfma_f32_16x16x32_bf16 v[80:83], a[152:155], v[220:223], v[80:83]\n"
"ds_read_b128 v[216:219], v234 offset:24576\n"
"v_mfma_f32_16x16x32_bf16 v[84:87], a[156:159], v[220:223], v[84:87]\n"
"s_lshr_b32 s15, s15, 18\n"
"v_mfma_f32_16x16x32_bf16 v[88:91], a[160:163], v[220:223], v[88:91]\n"
"global_load_dwordx4 a[104:107], v239, s[58:59] offset:-2048 sc1\n"
"v_mfma_f32_16x16x32_bf16 v[92:95], a[164:167], v[220:223], v[92:95]\n"
"s_waitcnt lgkmcnt(2)\n"
"v_mfma_f32_16x16x32_bf16 v[96:99], a[144:147], v[224:227], v[96:99]\n"
"s_mul_i32 s15, s15, 0x48000\n"
"v_mfma_f32_16x16x32_bf16 v[100:103], a[148:151], v[224:227], v[100:103]\n"
"s_mul_i32 s14, s14, 0xc000\n"
"v_mfma_f32_16x16x32_bf16 v[104:107], a[152:155], v[224:227], v[104:107]\n"
"ds_read_b128 v[220:223], v234 offset:28672\n"
"v_mfma_f32_16x16x32_bf16 v[108:111], a[156:159], v[224:227], v[108:111]\n"
"global_load_dwordx4 a[108:111], v239, s[58:59] offset:-1024 sc1\n"
"v_mfma_f32_16x16x32_bf16 v[112:115], a[160:163], v[224:227], v[112:115]\n"
"s_add_u32 s14, s14, s15\n"
"v_mfma_f32_16x16x32_bf16 v[116:119], a[164:167], v[224:227], v[116:119]\n"
"s_waitcnt lgkmcnt(2)\n"
"v_mfma_f32_16x16x32_bf16 v[120:123], a[144:147], v[228:231], v[120:123]\n"
"s_lshl2_add_u32 s36, s95, s14\n"
"v_mfma_f32_16x16x32_bf16 v[124:127], a[148:151], v[228:231], v[124:127]\n"
"s_add_u32 s37, s36, 0x48000\n"
"v_mfma_f32_16x16x32_bf16 v[128:131], a[152:155], v[228:231], v[128:131]\n"
"ds_read_b128 v[224:227], v234 offset:32768\n"
"v_add_u32 v234, 0xffff7000, v234\n"
"global_load_dwordx4 a[112:115], v239, s[58:59] offset:0 sc1\n"
"v_mfma_f32_16x16x32_bf16 v[132:135], a[156:159], v[228:231], v[132:135]\n"
"s_lshl2_add_u32 s42, s96, s14\n"
"v_mfma_f32_16x16x32_bf16 v[136:139], a[160:163], v[228:231], v[136:139]\n"
"s_add_u32 s43, s42, 0x48000\n"
"v_mfma_f32_16x16x32_bf16 v[140:143], a[164:167], v[228:231], v[140:143]\n"
"s_waitcnt lgkmcnt(2)\n"
"v_mfma_f32_16x16x32_bf16 v[144:147], a[144:147], v[216:219], v[144:147]\n"
"s_lshl2_add_u32 s48, s97, s14\n"
"v_mfma_f32_16x16x32_bf16 v[148:151], a[148:151], v[216:219], v[148:151]\n"
"global_load_dwordx4 a[116:119], v239, s[58:59] offset:1024 sc1\n"
"v_mfma_f32_16x16x32_bf16 v[152:155], a[152:155], v[216:219], v[152:155]\n"
"ds_read_b128 v[228:231], v235 offset:0\n"
"v_mfma_f32_16x16x32_bf16 v[156:159], a[156:159], v[216:219], v[156:159]\n"
"s_add_u32 s49, s48, 0x48000\n"
"v_mfma_f32_16x16x32_bf16 v[160:163], a[160:163], v[216:219], v[160:163]\n"
"s_bitcmp1_b32 s92, 0\n"
"s_cselect_b32 s93, 0x6000, 0\n"
"v_mfma_f32_16x16x32_bf16 v[164:167], a[164:167], v[216:219], v[164:167]\n"
"s_waitcnt lgkmcnt(2)\n"
"v_mfma_f32_16x16x32_bf16 v[168:171], a[144:147], v[220:223], v[168:171]\n"
"global_load_dwordx4 a[120:123], v239, s[58:59] offset:2048 sc1\n"
"v_mfma_f32_16x16x32_bf16 v[172:175], a[148:151], v[220:223], v[172:175]\n"
"s_sub_u32 s94, s93, 0xc000\n"
"v_mfma_f32_16x16x32_bf16 v[176:179], a[152:155], v[220:223], v[176:179]\n"
"ds_read_b128 v[216:219], v235 offset:4096\n"
"v_mfma_f32_16x16x32_bf16 v[180:183], a[156:159], v[220:223], v[180:183]\n"
"v_add_u32 v247, s93, v237\n"
"v_mfma_f32_16x16x32_bf16 v[184:187], a[160:163], v[220:223], v[184:187]\n"
"v_add_u32 v237, s94, v237\n"
"v_mfma_f32_16x16x32_bf16 v[188:191], a[164:167], v[220:223], v[188:191]\n"
"global_load_dwordx4 a[124:127], v239, s[58:59] offset:3072 sc1\n"
"s_waitcnt lgkmcnt(2)\n"
"v_mfma_f32_16x16x32_bf16 v[192:195], a[144:147], v[224:227], v[192:195]\n"
"v_min_u32 v237, v237, v247\n"
"v_mfma_f32_16x16x32_bf16 v[196:199], a[148:151], v[224:227], v[196:199]\n"
"v_add_u32 v247, s93, v238\n"
"v_mfma_f32_16x16x32_bf16 v[200:203], a[152:155], v[224:227], v[200:203]\n"
"ds_read_b128 v[220:223], v235 offset:8192\n"
"v_mfma_f32_16x16x32_bf16 v[204:207], a[156:159], v[224:227], v[204:207]\n"
"s_add_u32 s58, s24, 0x7000\n"
"s_addc_u32 s59, s25, 0\n"
"v_mfma_f32_16x16x32_bf16 v[208:211], a[160:163], v[224:227], v[208:211]\n"
"global_load_dwordx4 a[128:131], v239, s[58:59] offset:-4096 sc1\n"
"v_mfma_f32_16x16x32_bf16 v[212:215], a[164:167], v[224:227], v[212:215]\n"
"s_waitcnt lgkmcnt(2)\n"
"v_mfma_f32_16x16x32_bf16 v[0:3], a[168:171], v[228:231], v[0:3]\n"
"v_add_u32 v238, s94, v238\n"
"v_mfma_f32_16x16x32_bf16 v[4:7], a[172:175], v[228:231], v[4:7]\n"
"v_min_u32 v238, v238, v247\n"
"v_mfma_f32_16x16x32_bf16 v[8:11], a[176:179], v[228:231], v[8:11]\n"
"ds_read_b128 v[224:227], v235 offset:12288\n"
"v_mfma_f32_16x16x32_bf16 v[12:15], a[180:183], v[228:231], v[12:15]\n"
"global_load_dwordx4 a[132:135], v239, s[58:59] offset:-3072 sc1\n"
"v_mfma_f32_16x16x32_bf16 v[16:19], a[184:187], v[228:231], v[16:19]\n"
"v_mfma_f32_16x16x32_bf16 v[20:23], a[188:191], v[228:231], v[20:23]\n"
"s_waitcnt lgkmcnt(2)\n"
"v_mfma_f32_16x16x32_bf16 v[24:27], a[168:171], v[216:219], v[24:27]\n"
"v_mfma_f32_16x16x32_bf16 v[28:31], a[172:175], v[216:219], v[28:31]\n"
"v_mfma_f32_16x16x32_bf16 v[32:35], a[176:179], v[216:219], v[32:35]\n"
"ds_read_b128 v[228:231], v235 offset:16384\n"
"global_load_dwordx4 a[136:139], v239, s[58:59] offset:-2048 sc1\n"
"v_mfma_f32_16x16x32_bf16 v[36:39], a[180:183], v[216:219], v[36:39]\n"
"v_mfma_f32_16x16x32_bf16 v[40:43], a[184:187], v[216:219], v[40:43]\n"
"v_mfma_f32_16x16x32_bf16 v[44:47], a[188:191], v[216:219], v[44:47]\n"
"s_waitcnt lgkmcnt(2)\n"
"v_mfma_f32_16x16x32_bf16 v[48:51], a[168:171], v[220:223], v[48:51]\n"
"v_mfma_f32_16x16x32_bf16 v[52:55], a[172:175], v[220:223], v[52:55]\n"
"global_load_dwordx4 a[140:143], v239, s[58:59] offset:-1024 sc1\n"
"v_mfma_f32_16x16x32_bf16 v[56:59], a[176:179], v[220:223], v[56:59]\n"
"ds_read_b128 v[216:219], v235 offset:20480\n"
"v_mfma_f32_16x16x32_bf16 v[60:63], a[180:183], v[220:223], v[60:63]\n"
"v_mfma_f32_16x16x32_bf16 v[64:67], a[184:187], v[220:223], v[64:67]\n"
"v_mfma_f32_16x16x32_bf16 v[68:71], a[188:191], v[220:223], v[68:71]\n"
"s_waitcnt lgkmcnt(2)\n"
"v_mfma_f32_16x16x32_bf16 v[72:75], a[168:171], v[224:227], v[72:75]\n"
"global_load_dwordx4 a[144:147], v239, s[58:59] offset:0 sc1\n"
"v_mfma_f32_16x16x32_bf16 v[76:79], a[172:175], v[224:227], v[76:79]\n"
"v_mfma_f32_16x16x32_bf16 v[80:83], a[176:179], v[224:227], v[80:83]\n"
"ds_read_b128 v[220:223], v235 offset:24576\n"
"v_mfma_f32_16x16x32_bf16 v[84:87], a[180:183], v[224:227], v[84:87]\n"
"v_mfma_f32_16x16x32_bf16 v[88:91], a[184:187], v[224:227], v[88:91]\n"
"v_mfma_f32_16x16x32_bf16 v[92:95], a[188:191], v[224:227], v[92:95]\n"
"global_load_dwordx4 a[148:151], v239, s[58:59] offset:1024 sc1\n"
"s_waitcnt lgkmcnt(2)\n"
"v_mfma_f32_16x16x32_bf16 v[96:99], a[168:171], v[228:231], v[96:99]\n"
"v_mfma_f32_16x16x32_bf16 v[100:103], a[172:175], v[228:231], v[100:103]\n"
"v_mfma_f32_16x16x32_bf16 v[104:107], a[176:179], v[228:231], v[104:107]\n"
"ds_read_b128 v[224:227], v235 offset:28672\n"
"v_mfma_f32_16x16x32_bf16 v[108:111], a[180:183], v[228:231], v[108:111]\n"
"v_mfma_f32_16x16x32_bf16 v[112:115], a[184:187], v[228:231], v[112:115]\n"
"global_load_dwordx4 a[152:155], v239, s[58:59] offset:2048 sc1\n"
"v_mfma_f32_16x16x32_bf16 v[116:119], a[188:191], v[228:231], v[116:119]\n"
"s_waitcnt lgkmcnt(2)\n"
"v_mfma_f32_16x16x32_bf16 v[120:123], a[168:171], v[216:219], v[120:123]\n"
"v_mfma_f32_16x16x32_bf16 v[124:127], a[172:175], v[216:219], v[124:127]\n"
"v_mfma_f32_16x16x32_bf16 v[128:131], a[176:179], v[216:219], v[128:131]\n"
"ds_read_b128 v[228:231], v235 offset:32768\n"
"v_add_u32 v235, 0xffff7000, v235\n"
"v_mfma_f32_16x16x32_bf16 v[132:135], a[180:183], v[216:219], v[132:135]\n"
"global_load_dwordx4 a[156:159], v239, s[58:59] offset:3072 sc1\n"
"v_mfma_f32_16x16x32_bf16 v[136:139], a[184:187], v[216:219], v[136:139]\n"
"v_mfma_f32_16x16x32_bf16 v[140:143], a[188:191], v[216:219], v[140:143]\n"
"s_waitcnt lgkmcnt(2)\n"
"v_mfma_f32_16x16x32_bf16 v[144:147], a[168:171], v[220:223], v[144:147]\n"
"v_mfma_f32_16x16x32_bf16 v[148:151], a[172:175], v[220:223], v[148:151]\n"
"s_waitcnt vmcnt(21)\n"
"s_barrier\n"
"s_add_u32 s58, s24, 0x9000\n"
"s_addc_u32 s59, s25, 0\n"
"v_mfma_f32_16x16x32_bf16 v[152:155], a[176:179], v[220:223], v[152:155]\n"
"ds_read_b128 v[216:219], v232 offset:0\n"
"global_load_dwordx4 a[160:163], v239, s[58:59] offset:-4096 sc1\n"
"v_mfma_f32_16x16x32_bf16 v[156:159], a[180:183], v[220:223], v[156:159]\n"
"v_mfma_f32_16x16x32_bf16 v[160:163], a[184:187], v[220:223], v[160:163]\n"
"v_mfma_f32_16x16x32_bf16 v[164:167], a[188:191], v[220:223], v[164:167]\n"
"s_waitcnt lgkmcnt(2)\n"
"v_mfma_f32_16x16x32_bf16 v[168:171], a[168:171], v[224:227], v[168:171]\n"
"v_mfma_f32_16x16x32_bf16 v[172:175], a[172:175], v[224:227], v[172:175]\n"
"global_load_dwordx4 a[164:167], v239, s[58:59] offset:-3072 sc1\n"
"v_mfma_f32_16x16x32_bf16 v[176:179], a[176:179], v[224:227], v[176:179]\n"
"ds_read_b128 v[220:223], v232 offset:4096\n"
"v_mfma_f32_16x16x32_bf16 v[180:183], a[180:183], v[224:227], v[180:183]\n"
"v_mfma_f32_16x16x32_bf16 v[184:187], a[184:187], v[224:227], v[184:187]\n"
"v_mfma_f32_16x16x32_bf16 v[188:191], a[188:191], v[224:227], v[188:191]\n"
"s_waitcnt lgkmcnt(2)\n"
"v_mfma_f32_16x16x32_bf16 v[192:195], a[168:171], v[228:231], v[192:195]\n"
"v_mfma_f32_16x16x32_bf16 v[196:199], a[172:175], v[228:231], v[196:199]\n"
"v_mfma_f32_16x16x32_bf16 v[200:203], a[176:179], v[228:231], v[200:203]\n"
"ds_read_b128 v[224:227], v232 offset:8192\n"
"v_mfma_f32_16x16x32_bf16 v[204:207], a[180:183], v[228:231], v[204:207]\n"
"v_mfma_f32_16x16x32_bf16 v[208:211], a[184:187], v[228:231], v[208:211]\n"
"v_mfma_f32_16x16x32_bf16 v[212:215], a[188:191], v[228:231], v[212:215]\n"
"s_waitcnt lgkmcnt(2)\n"
"v_mfma_f32_16x16x32_bf16 v[0:3], a[192:195], v[216:219], v[0:3]\n"
"global_load_dwordx4 a[168:171], v239, s[58:59] offset:-2048 sc1\n"
"v_mfma_f32_16x16x32_bf16 v[4:7], a[196:199], v[216:219], v[4:7]\n"
"v_mfma_f32_16x16x32_bf16 v[8:11], a[200:203], v[216:219], v[8:11]\n"
"ds_read_b128 v[228:231], v232 offset:12288\n"
"v_mfma_f32_16x16x32_bf16 v[12:15], a[204:207], v[216:219], v[12:15]\n"
"v_mfma_f32_16x16x32_bf16 v[16:19], a[208:211], v[216:219], v[16:19]\n"
"v_mfma_f32_16x16x32_bf16 v[20:23], a[212:215], v[216:219], v[20:23]\n"
"global_load_dwordx4 a[172:175], v239, s[58:59] offset:-1024 sc1\n"
"s_waitcnt lgkmcnt(2)\n"
"v_mfma_f32_16x16x32_bf16 v[24:27], a[192:195], v[220:223], v[24:27]\n"
"v_mfma_f32_16x16x32_bf16 v[28:31], a[196:199], v[220:223], v[28:31]\n"
"v_mfma_f32_16x16x32_bf16 v[32:35], a[200:203], v[220:223], v[32:35]\n"
"ds_read_b128 v[216:219], v232 offset:16384\n"
"v_mfma_f32_16x16x32_bf16 v[36:39], a[204:207], v[220:223], v[36:39]\n"
"v_mfma_f32_16x16x32_bf16 v[40:43], a[208:211], v[220:223], v[40:43]\n"
"global_load_dwordx4 a[176:179], v239, s[58:59] offset:0 sc1\n"
"v_mfma_f32_16x16x32_bf16 v[44:47], a[212:215], v[220:223], v[44:47]\n"
"s_waitcnt lgkmcnt(2)\n"
"v_mfma_f32_16x16x32_bf16 v[48:51], a[192:195], v[224:227], v[48:51]\n"
"v_mfma_f32_16x16x32_bf16 v[52:55], a[196:199], v[224:227], v[52:55]\n"
"v_mfma_f32_16x16x32_bf16 v[56:59], a[200:203], v[224:227], v[56:59]\n"
"ds_read_b128 v[220:223], v232 offset:20480\n"
"v_mfma_f32_16x16x32_bf16 v[60:63], a[204:207], v[224:227], v[60:63]\n"
"global_load_dwordx4 a[180:183], v239, s[58:59] offset:1024 sc1\n"
"v_mfma_f32_16x16x32_bf16 v[64:67], a[208:211], v[224:227], v[64:67]\n"
"v_mfma_f32_16x16x32_bf16 v[68:71], a[212:215], v[224:227], v[68:71]\n"
"s_waitcnt lgkmcnt(2)\n"
"v_mfma_f32_16x16x32_bf16 v[72:75], a[192:195], v[228:231], v[72:75]\n"
"v_mfma_f32_16x16x32_bf16 v[76:79], a[196:199], v[228:231], v[76:79]\n"
"v_mfma_f32_16x16x32_bf16 v[80:83], a[200:203], v[228:231], v[80:83]\n"
"ds_read_b128 v[224:227], v232 offset:24576\n"
"s_add_u32 s52, s20, s32\n"
"s_addc_u32 s53, s21, 0\n"
"s_add_u32 m0, s31, 0x0\n"
"v_mfma_f32_16x16x32_bf16 v[84:87], a[204:207], v[228:231], v[84:87]\n"
"global_load_lds_dwordx4 v237, s[52:53] offset:0\n"
"v_mfma_f32_16x16x32_bf16 v[88:91], a[208:211], v[228:231], v[88:91]\n"
"v_mfma_f32_16x16x32_bf16 v[92:95], a[212:215], v[228:231], v[92:95]\n"
"s_waitcnt lgkmcnt(2)\n"
"v_mfma_f32_16x16x32_bf16 v[96:99], a[192:195], v[216:219], v[96:99]\n"
"s_add_u32 m0, s31, 0x1f00\n"
"v_mfma_f32_16x16x32_bf16 v[100:103], a[196:199], v[216:219], v[100:103]\n"
"global_load_lds_dwordx4 v237, s[52:53] offset:256\n"
"v_mfma_f32_16x16x32_bf16 v[104:107], a[200:203], v[216:219], v[104:107]\n"
"ds_read_b128 v[228:231], v232 offset:28672\n"
"v_mfma_f32_16x16x32_bf16 v[108:111], a[204:207], v[216:219], v[108:111]\n"
"v_mfma_f32_16x16x32_bf16 v[112:115], a[208:211], v[216:219], v[112:115]\n"
"s_add_u32 m0, s31, 0x3e00\n"
"v_mfma_f32_16x16x32_bf16 v[116:119], a[212:215], v[216:219], v[116:119]\n"
"global_load_lds_dwordx4 v237, s[52:53] offset:512\n"
"s_waitcnt lgkmcnt(2)\n"
"v_mfma_f32_16x16x32_bf16 v[120:123], a[192:195], v[220:223], v[120:123]\n"
"v_mfma_f32_16x16x32_bf16 v[124:127], a[196:199], v[220:223], v[124:127]\n"
"v_mfma_f32_16x16x32_bf16 v[128:131], a[200:203], v[220:223], v[128:131]\n"
"ds_read_b128 v[216:219], v232 offset:32768\n"
"v_add_u32 v232, v249, v232\n"
"s_add_u32 m0, s31, 0x1000\n"
"v_mfma_f32_16x16x32_bf16 v[132:135], a[204:207], v[220:223], v[132:135]\n"
"global_load_lds_dwordx4 v238, s[52:53] offset:0\n"
"v_mfma_f32_16x16x32_bf16 v[136:139], a[208:211], v[220:223], v[136:139]\n"
"v_mfma_f32_16x16x32_bf16 v[140:143], a[212:215], v[220:223], v[140:143]\n"
"s_waitcnt lgkmcnt(2)\n"
"v_mfma_f32_16x16x32_bf16 v[144:147], a[192:195], v[224:227], v[144:147]\n"
"s_add_u32 m0, s31, 0x2f00\n"
"v_mfma_f32_16x16x32_bf16 v[148:151], a[196:199], v[224:227], v[148:151]\n"
"global_load_lds_dwordx4 v238, s[52:53] offset:256\n"
"v_mfma_f32_16x16x32_bf16 v[152:155], a[200:203], v[224:227], v[152:155]\n"
"ds_read_b128 v[220:223], v233 offset:0\n"
"v_mfma_f32_16x16x32_bf16 v[156:159], a[204:207], v[224:227], v[156:159]\n"
"s_mov_b64 exec, s[4:5]\n"
"s_cbranch_execz LHT4\n"
"LHR4:\n"
"s_mov_b64 exec, -1\n"
"v_mfma_f32_16x16x32_bf16 v[160:163], a[208:211], v[224:227], v[160:163]\n"
"s_add_u32 m0, s31, 0x4e00\n"
"v_mfma_f32_16x16x32_bf16 v[164:167], a[212:215], v[224:227], v[164:167]\n"
"global_load_lds_dwordx4 v238, s[52:53] offset:512\n"
"s_waitcnt lgkmcnt(2)\n"
"v_mfma_f32_16x16x32_bf16 v[168:171], a[192:195], v[228:231], v[168:171]\n"
"v_mfma_f32_16x16x32_bf16 v[172:175], a[196:199], v[228:231], v[172:175]\n"
"v_mfma_f32_16x16x32_bf16 v[176:179], a[200:203], v[228:231], v[176:179]\n"
"ds_read_b128 v[224:227], v233 offset:4096\n"
"v_mfma_f32_16x16x32_bf16 v[180:183], a[204:207], v[228:231], v[180:183]\n"
"v_mfma_f32_16x16x32_bf16 v[184:187], a[208:211], v[228:231], v[184:187]\n"
"v_mfma_f32_16x16x32_bf16 v[188:191], a[212:215], v[228:231], v[188:191]\n"
"s_waitcnt lgkmcnt(2)\n"
"v_mfma_f32_16x16x32_bf16 v[192:195], a[192:195], v[216:219], v[192:195]\n"
"v_mfma_f32_16x16x32_bf16 v[196:199], a[196:199], v[216:219], v[196:199]\n"
"v_mfma_f32_16x16x32_bf16 v[200:203], a[200:203], v[216:219], v[200:203]\n"
"ds_read_b128 v[228:231], v233 offset:8192\n"
"v_mfma_f32_16x16x32_bf16 v[204:207], a[204:207], v[216:219], v[204:207]\n"
"v_mfma_f32_16x16x32_bf16 v[208:211], a[208:211], v[216:219], v[208:211]\n"
"v_mfma_f32_16x16x32_bf16 v[212:215], a[212:215], v[216:219], v[212:215]\n"
"s_waitcnt lgkmcnt(2)\n"
"v_mfma_f32_16x16x32_bf16 v[0:3], a[216:219], v[220:223], v[0:3]\n"
"v_mfma_f32_16x16x32_bf16 v[4:7], a[220:223], v[220:223], v[4:7]\n"
"v_mfma_f32_16x16x32_bf16 v[8:11], a[224:227], v[220:223], v[8:11]\n"
"ds_read_b128 v[216:219], v233 offset:12288\n"
"v_mfma_f32_16x16x32_bf16 v[12:15], a[228:231], v[220:223], v[12:15]\n"
"v_mfma_f32_16x16x32_bf16 v[16:19], a[232:235], v[220:223], v[16:19]\n"
"v_mfma_f32_16x16x32_bf16 v[20:23], a[236:239], v[220:223], v[20:23]\n"
"s_waitcnt lgkmcnt(2)\n"
"v_mfma_f32_16x16x32_bf16 v[24:27], a[216:219], v[224:227], v[24:27]\n"
"v_mfma_f32_16x16x32_bf16 v[28:31], a[220:223], v[224:227], v[28:31]\n"
"v_mfma_f32_16x16x32_bf16 v[32:35], a[224:227], v[224:227], v[32:35]\n"
"ds_read_b128 v[220:223], v233 offset:16384\n"
"v_mfma_f32_16x16x32_bf16 v[36:39], a[228:231], v[224:227], v[36:39]\n"
"v_mfma_f32_16x16x32_bf16 v[40:43], a[232:235], v[224:227], v[40:43]\n"
"v_mfma_f32_16x16x32_bf16 v[44:47], a[236:239], v[224:227], v[44:47]\n"
"s_waitcnt lgkmcnt(2)\n"
"v_mfma_f32_16x16x32_bf16 v[48:51], a[216:219], v[228:231], v[48:51]\n"
"v_mfma_f32_16x16x32_bf16 v[52:55], a[220:223], v[228:231], v[52:55]\n"
"global_load_dwordx4 a[184:187], v239, s[58:59] offset:2048 sc1\n"
"v_mfma_f32_16x16x32_bf16 v[56:59], a[224:227], v[228:231], v[56:59]\n"
"ds_read_b128 v[224:227], v233 offset:20480\n"
"v_mfma_f32_16x16x32_bf16 v[60:63], a[228:231], v[228:231], v[60:63]\n"
"LHT4:\n"
"s_cbranch_execz LHR4\n"
"v_mfma_f32_16x16x32_bf16 v[64:67], a[232:235], v[228:231], v[64:67]\n"
"v_mfma_f32_16x16x32_bf16 v[68:71], a[236:239], v[228:231], v[68:71]\n"
"s_waitcnt lgkmcnt(2)\n"
"v_mfma_f32_16x16x32_bf16 v[72:75], a[216:219], v[216:219], v[72:75]\n"
"global_load_dwordx4 a[188:191], v239, s[58:59] offset:3072 sc1\n"
"v_mfma_f32_16x16x32_bf16 v[76:79], a[220:223], v[216:219], v[76:79]\n"
"v_mfma_f32_16x16x32_bf16 v[80:83], a[224:227], v[216:219], v[80:83]\n"
"ds_read_b128 v[228:231], v233 offset:24576\n"
"v_mfma_f32_16x16x32_bf16 v[84:87], a[228:231], v[216:219], v[84:87]\n"
"v_mfma_f32_16x16x32_bf16 v[88:91], a[232:235], v[216:219], v[88:91]\n"
"s_add_u32 s58, s24, 0xb000\n"
"s_addc_u32 s59, s25, 0\n"
"v_mfma_f32_16x16x32_bf16 v[92:95], a[236:239], v[216:219], v[92:95]\n"
"global_load_dwordx4 a[192:195], v239, s[58:59] offset:-4096 sc1\n"
"s_waitcnt lgkmcnt(2)\n"
"v_mfma_f32_16x16x32_bf16 v[96:99], a[216:219], v[220:223], v[96:99]\n"
"v_mfma_f32_16x16x32_bf16 v[100:103], a[220:223], v[220:223], v[100:103]\n"
"v_mfma_f32_16x16x32_bf16 v[104:107], a[224:227], v[220:223], v[104:107]\n"
"ds_read_b128 v[216:219], v233 offset:28672\n"
"v_mfma_f32_16x16x32_bf16 v[108:111], a[228:231], v[220:223], v[108:111]\n"
"v_mfma_f32_16x16x32_bf16 v[112:115], a[232:235], v[220:223], v[112:115]\n"
"global_load_dwordx4 a[196:199], v239, s[58:59] offset:-3072 sc1\n"
"v_mfma_f32_16x16x32_bf16 v[116:119], a[236:239], v[220:223], v[116:119]\n"
"s_waitcnt lgkmcnt(2)\n"
"v_mfma_f32_16x16x32_bf16 v[120:123], a[216:219], v[224:227], v[120:123]\n"
"v_mfma_f32_16x16x32_bf16 v[124:127], a[220:223], v[224:227], v[124:127]\n"
"v_mfma_f32_16x16x32_bf16 v[128:131], a[224:227], v[224:227], v[128:131]\n"
"ds_read_b128 v[220:223], v233 offset:32768\n"
"v_add_u32 v233, v249, v233\n"
"v_mfma_f32_16x16x32_bf16 v[132:135], a[228:231], v[224:227], v[132:135]\n"
"s_add_u32 s52, s20, s33\n"
"s_addc_u32 s53, s21, 0\n"
"s_add_u32 m0, s31, 0x6000\n"
"v_mfma_f32_16x16x32_bf16 v[136:139], a[232:235], v[224:227], v[136:139]\n"
"global_load_lds_dwordx4 v237, s[52:53] offset:0\n"
"v_mfma_f32_16x16x32_bf16 v[140:143], a[236:239], v[224:227], v[140:143]\n"
"s_waitcnt lgkmcnt(2)\n"
"v_mfma_f32_16x16x32_bf16 v[144:147], a[216:219], v[228:231], v[144:147]\n"
"v_mfma_f32_16x16x32_bf16 v[148:151], a[220:223], v[228:231], v[148:151]\n"
"s_add_u32 m0, s31, 0x7f00\n"
"v_mfma_f32_16x16x32_bf16 v[152:155], a[224:227], v[228:231], v[152:155]\n"
"ds_read_b128 v[224:227], v234 offset:0\n"
"global_load_lds_dwordx4 v237, s[52:53] offset:256\n"
"v_mfma_f32_16x16x32_bf16 v[156:159], a[228:231], v[228:231], v[156:159]\n"
"v_mfma_f32_16x16x32_bf16 v[160:163], a[232:235], v[228:231], v[160:163]\n"
"v_mfma_f32_16x16x32_bf16 v[164:167], a[236:239], v[228:231], v[164:167]\n"
"s_add_u32 m0, s31, 0x9e00\n"
"s_waitcnt lgkmcnt(2)\n"
"v_mfma_f32_16x16x32_bf16 v[168:171], a[216:219], v[216:219], v[168:171]\n"
"global_load_lds_dwordx4 v237, s[52:53] offset:512\n"
"v_mfma_f32_16x16x32_bf16 v[172:175], a[220:223], v[216:219], v[172:175]\n"
"v_mfma_f32_16x16x32_bf16 v[176:179], a[224:227], v[216:219], v[176:179]\n"
"ds_read_b128 v[228:231], v234 offset:4096\n"
"v_mfma_f32_16x16x32_bf16 v[180:183], a[228:231], v[216:219], v[180:183]\n"
"s_add_u32 m0, s31, 0x7000\n"
"v_mfma_f32_16x16x32_bf16 v[184:187], a[232:235], v[216:219], v[184:187]\n"
"global_load_lds_dwordx4 v238, s[52:53] offset:0\n"
"v_mfma_f32_16x16x32_bf16 v[188:191], a[236:239], v[216:219], v[188:191]\n"
"s_waitcnt lgkmcnt(2)\n"
"v_mfma_f32_16x16x32_bf16 v[192:195], a[216:219], v[220:223], v[192:195]\n"
"v_mfma_f32_16x16x32_bf16 v[196:199], a[220:223], v[220:223], v[196:199]\n"
"s_add_u32 m0, s31, 0x8f00\n"
"v_mfma_f32_16x16x32_bf16 v[200:203], a[224:227], v[220:223], v[200:203]\n"
"ds_read_b128 v[216:219], v234 offset:8192\n"
"global_load_lds_dwordx4 v238, s[52:53] offset:256\n"
"v_mfma_f32_16x16x32_bf16 v[204:207], a[228:231], v[220:223], v[204:207]\n"
"v_mfma_f32_16x16x32_bf16 v[208:211], a[232:235], v[220:223], v[208:211]\n"
"v_mfma_f32_16x16x32_bf16 v[212:215], a[236:239], v[220:223], v[212:215]\n"
"s_add_u32 m0, s31, 0xae00\n"
"s_waitcnt lgkmcnt(2)\n"
"v_mfma_f32_16x16x32_bf16 v[0:3], a[0:3], v[224:227], v[0:3]\n"
"global_load_lds_dwordx4 v238, s[52:53] offset:512\n"
"ds_read_b128 v[244:247], v250\n"
"v_mfma_f32_16x16x32_bf16 v[4:7], a[4:7], v[224:227], v[4:7]\n"
"ds_read_b64 v[252:253], v250 offset:16\n"
"v_mfma_f32_16x16x32_bf16 v[8:11], a[8:11], v[224:227], v[8:11]\n"
"ds_read_b128 v[220:223], v234 offset:12288\n"
"v_mfma_f32_16x16x32_bf16 v[12:15], a[12:15], v[224:227], v[12:15]\n"
"v_mfma_f32_16x16x32_bf16 v[16:19], a[16:19], v[224:227], v[16:19]\n"
"v_mfma_f32_16x16x32_bf16 v[20:23], a[20:23], v[224:227], v[20:23]\n"
"s_waitcnt lgkmcnt(4)\n"
"v_mfma_f32_16x16x32_bf16 v[24:27], a[0:3], v[228:231], v[24:27]\n"
"v_mfma_f32_16x16x32_bf16 v[28:31], a[4:7], v[228:231], v[28:31]\n"
"v_mfma_f32_16x16x32_bf16 v[32:35], a[8:11], v[228:231], v[32:35]\n"
"ds_read_b128 v[224:227], v234 offset:16384\n"
"v_mfma_f32_16x16x32_bf16 v[36:39], a[12:15], v[228:231], v[36:39]\n"
"v_mfma_f32_16x16x32_bf16 v[40:43], a[16:19], v[228:231], v[40:43]\n"
"v_mfma_f32_16x16x32_bf16 v[44:47], a[20:23], v[228:231], v[44:47]\n"
"s_waitcnt lgkmcnt(4)\n"
"v_mfma_f32_16x16x32_bf16 v[48:51], a[0:3], v[216:219], v[48:51]\n"
"v_mfma_f32_16x16x32_bf16 v[52:55], a[4:7], v[216:219], v[52:55]\n"
"v_mfma_f32_16x16x32_bf16 v[56:59], a[8:11], v[216:219], v[56:59]\n"
"ds_read_b128 v[228:231], v234 offset:20480\n"
"v_mfma_f32_16x16x32_bf16 v[60:63], a[12:15], v[216:219], v[60:63]\n"
"v_mfma_f32_16x16x32_bf16 v[64:67], a[16:19], v[216:219], v[64:67]\n"
"v_mfma_f32_16x16x32_bf16 v[68:71], a[20:23], v[216:219], v[68:71]\n"
"s_waitcnt lgkmcnt(2)\n"
"v_mfma_f32_16x16x32_bf16 v[72:75], a[0:3], v[220:223], v[72:75]\n"
"v_mfma_f32_16x16x32_bf16 v[76:79], a[4:7], v[220:223], v[76:79]\n"
"v_mfma_f32_16x16x32_bf16 v[80:83], a[8:11], v[220:223], v[80:83]\n"
"ds_read_b128 v[216:219], v234 offset:24576\n"
"v_mfma_f32_16x16x32_bf16 v[84:87], a[12:15], v[220:223], v[84:87]\n"
"v_mfma_f32_16x16x32_bf16 v[88:91], a[16:19], v[220:223], v[88:91]\n"
"v_mfma_f32_16x16x32_bf16 v[92:95], a[20:23], v[220:223], v[92:95]\n"
"s_waitcnt lgkmcnt(2)\n"
"v_mfma_f32_16x16x32_bf16 v[96:99], a[0:3], v[224:227], v[96:99]\n"
"v_mfma_f32_16x16x32_bf16 v[100:103], a[4:7], v[224:227], v[100:103]\n"
"v_mfma_f32_16x16x32_bf16 v[104:107], a[8:11], v[224:227], v[104:107]\n"
"ds_read_b128 v[220:223], v234 offset:28672\n"
"global_load_dwordx4 a[200:203], v239, s[58:59] offset:-2048 sc1\n"
"v_mfma_f32_16x16x32_bf16 v[108:111], a[12:15], v[224:227], v[108:111]\n"
"v_mfma_f32_16x16x32_bf16 v[112:115], a[16:19], v[224:227], v[112:115]\n"
"v_mfma_f32_16x16x32_bf16 v[116:119], a[20:23], v[224:227], v[116:119]\n"
"s_waitcnt lgkmcnt(2)\n"
"v_mfma_f32_16x16x32_bf16 v[120:123], a[0:3], v[228:231], v[120:123]\n"
"v_mfma_f32_16x16x32_bf16 v[124:127], a[4:7], v[228:231], v[124:127]\n"
"global_load_dwordx4 a[204:207], v239, s[58:59] offset:-1024 sc1\n"
"v_mfma_f32_16x16x32_bf16 v[128:131], a[8:11], v[228:231], v[128:131]\n"
"ds_read_b128 v[224:227], v234 offset:32768\n"
"v_add_u32 v234, v249, v234\n"
"v_mfma_f32_16x16x32_bf16 v[132:135], a[12:15], v[228:231], v[132:135]\n"
"v_mfma_f32_16x16x32_bf16 v[136:139], a[16:19], v[228:231], v[136:139]\n"
"v_mfma_f32_16x16x32_bf16 v[140:143], a[20:23], v[228:231], v[140:143]\n"
"s_waitcnt lgkmcnt(2)\n"
"v_mfma_f32_16x16x32_bf16 v[144:147], a[0:3], v[216:219], v[144:147]\n"
"global_load_dwordx4 a[208:211], v239, s[58:59] offset:0 sc1\n"
"v_mfma_f32_16x16x32_bf16 v[148:151], a[4:7], v[216:219], v[148:151]\n"
"v_mfma_f32_16x16x32_bf16 v[152:155], a[8:11], v[216:219], v[152:155]\n"
"ds_read_b128 v[228:231], v235 offset:4096\n"
"v_mfma_f32_16x16x32_bf16 v[156:159], a[12:15], v[216:219], v[156:159]\n"
"v_mfma_f32_16x16x32_bf16 v[160:163], a[16:19], v[216:219], v[160:163]\n"
"v_mfma_f32_16x16x32_bf16 v[164:167], a[20:23], v[216:219], v[164:167]\n"
"global_load_dwordx4 a[212:215], v239, s[58:59] offset:1024 sc1\n"
"s_waitcnt lgkmcnt(2)\n"
"v_mfma_f32_16x16x32_bf16 v[168:171], a[0:3], v[220:223], v[168:171]\n"
"v_mfma_f32_16x16x32_bf16 v[172:175], a[4:7], v[220:223], v[172:175]\n"
"v_mfma_f32_16x16x32_bf16 v[176:179], a[8:11], v[220:223], v[176:179]\n"
"ds_read_b128 v[216:219], v235 offset:8192\n"
"v_mfma_f32_16x16x32_bf16 v[180:183], a[12:15], v[220:223], v[180:183]\n"
"v_mfma_f32_16x16x32_bf16 v[184:187], a[16:19], v[220:223], v[184:187]\n"
"global_load_dwordx4 a[216:219], v239, s[58:59] offset:2048 sc1\n"
"v_mfma_f32_16x16x32_bf16 v[188:191], a[20:23], v[220:223], v[188:191]\n"
"s_waitcnt lgkmcnt(2)\n"
"v_mfma_f32_16x16x32_bf16 v[192:195], a[0:3], v[224:227], v[192:195]\n"
"v_mfma_f32_16x16x32_bf16 v[196:199], a[4:7], v[224:227], v[196:199]\n"
"v_mfma_f32_16x16x32_bf16 v[200:203], a[8:11], v[224:227], v[200:203]\n"
"ds_read_b128 v[220:223], v235 offset:12288\n"
"v_mfma_f32_16x16x32_bf16 v[204:207], a[12:15], v[224:227], v[204:207]\n"
"global_load_dwordx4 a[220:223], v239, s[58:59] offset:3072 sc1\n"
"v_mfma_f32_16x16x32_bf16 v[208:211], a[16:19], v[224:227], v[208:211]\n"
"v_mfma_f32_16x16x32_bf16 v[212:215], a[20:23], v[224:227], v[212:215]\n"
"s_waitcnt lgkmcnt(2)\n"
"v_mfma_f32_16x16x32_bf16 v[24:27], a[24:27], v[228:231], v[24:27]\n"
"v_mfma_f32_16x16x32_bf16 v[28:31], a[28:31], v[228:231], v[28:31]\n"
"s_add_u32 s58, s24, 0xd000\n"
"s_addc_u32 s59, s25, 0\n"
"v_mfma_f32_16x16x32_bf16 v[32:35], a[32:35], v[228:231], v[32:35]\n"
"ds_read_b128 v[224:227], v235 offset:16384\n"
"global_load_dwordx4 a[224:227], v239, s[58:59] offset:-4096 sc1\n"
"v_mfma_f32_16x16x32_bf16 v[36:39], a[36:39], v[228:231], v[36:39]\n"
"v_mfma_f32_16x16x32_bf16 v[40:43], a[40:43], v[228:231], v[40:43]\n"
"v_mfma_f32_16x16x32_bf16 v[44:47], a[44:47], v[228:231], v[44:47]\n"
"s_waitcnt lgkmcnt(2)\n"
"v_mfma_f32_16x16x32_bf16 v[48:51], a[24:27], v[216:219], v[48:51]\n"
"v_cvt_pk_bf16_f32 v24, v24, v25\n"
"v_cvt_pk_bf16_f32 v25, v26, v27\n"
"v_mfma_f32_16x16x32_bf16 v[52:55], a[28:31], v[216:219], v[52:55]\n"
"v_cvt_pk_bf16_f32 v26, v28, v29\n"
"v_cvt_pk_bf16_f32 v27, v30, v31\n"
"global_load_dwordx4 a[228:231], v239, s[58:59] offset:-3072 sc1\n"
"v_mfma_f32_16x16x32_bf16 v[56:59], a[32:35], v[216:219], v[56:59]\n"
"ds_read_b128 v[228:231], v235 offset:20480\n"
"v_cvt_pk_bf16_f32 v28, v32, v33\n"
"v_cvt_pk_bf16_f32 v29, v34, v35\n"
"v_mfma_f32_16x16x32_bf16 v[60:63], a[36:39], v[216:219], v[60:63]\n"
"v_cvt_pk_bf16_f32 v30, v36, v37\n"
"v_cvt_pk_bf16_f32 v31, v38, v39\n"
"v_mfma_f32_16x16x32_bf16 v[64:67], a[40:43], v[216:219], v[64:67]\n"
"v_cvt_pk_bf16_f32 v32, v40, v41\n"
"v_cvt_pk_bf16_f32 v33, v42, v43\n"
"v_mfma_f32_16x16x32_bf16 v[68:71], a[44:47], v[216:219], v[68:71]\n"
"v_cvt_pk_bf16_f32 v34, v44, v45\n"
"v_cvt_pk_bf16_f32 v35, v46, v47\n"
"s_waitcnt lgkmcnt(2)\n"
"v_mfma_f32_16x16x32_bf16 v[72:75], a[24:27], v[220:223], v[72:75]\n"
"v_cvt_pk_bf16_f32 v48, v48, v49\n"
"v_cvt_pk_bf16_f32 v49, v50, v51\n"
"global_load_dwordx4 a[232:235], v239, s[58:59] offset:-2048 sc1\n"
"v_mfma_f32_16x16x32_bf16 v[76:79], a[28:31], v[220:223], v[76:79]\n"
"v_cvt_pk_bf16_f32 v50, v52, v53\n"
"v_cvt_pk_bf16_f32 v51, v54, v55\n"
"v_mfma_f32_16x16x32_bf16 v[80:83], a[32:35], v[220:223], v[80:83]\n"
"ds_read_b128 v[216:219], v235 offset:24576\n"
"v_cvt_pk_bf16_f32 v52, v56, v57\n"
"v_cvt_pk_bf16_f32 v53, v58, v59\n"
"v_mfma_f32_16x16x32_bf16 v[84:87], a[36:39], v[220:223], v[84:87]\n"
"v_cvt_pk_bf16_f32 v54, v60, v61\n"
"v_cvt_pk_bf16_f32 v55, v62, v63\n"
"v_mfma_f32_16x16x32_bf16 v[88:91], a[40:43], v[220:223], v[88:91]\n"
"v_cvt_pk_bf16_f32 v56, v64, v65\n"
"v_cvt_pk_bf16_f32 v57, v66, v67\n"
"v_mfma_f32_16x16x32_bf16 v[92:95], a[44:47], v[220:223], v[92:95]\n"
"v_cvt_pk_bf16_f32 v58, v68, v69\n"
"v_cvt_pk_bf16_f32 v59, v70, v71\n"
"global_load_dwordx4 a[236:239], v239, s[58:59] offset:-1024 sc1\n"
"s_waitcnt lgkmcnt(2)\n"
"v_mfma_f32_16x16x32_bf16 v[96:99], a[24:27], v[224:227], v[96:99]\n"
"v_cvt_pk_bf16_f32 v72, v72, v73\n"
"v_cvt_pk_bf16_f32 v73, v74, v75\n"
"v_mfma_f32_16x16x32_bf16 v[100:103], a[28:31], v[224:227], v[100:103]\n"
"v_cvt_pk_bf16_f32 v74, v76, v77\n"
"v_cvt_pk_bf16_f32 v75, v78, v79\n"
"v_mfma_f32_16x16x32_bf16 v[104:107], a[32:35], v[224:227], v[104:107]\n"
"ds_read_b128 v[220:223], v235 offset:28672\n"
"v_cvt_pk_bf16_f32 v76, v80, v81\n"
"v_cvt_pk_bf16_f32 v77, v82, v83\n"
"v_mfma_f32_16x16x32_bf16 v[108:111], a[36:39], v[224:227], v[108:111]\n"
"v_cvt_pk_bf16_f32 v78, v84, v85\n"
"v_cvt_pk_bf16_f32 v79, v86, v87\n"
"v_mfma_f32_16x16x32_bf16 v[112:115], a[40:43], v[224:227], v[112:115]\n"
"v_cvt_pk_bf16_f32 v80, v88, v89\n"
"v_cvt_pk_bf16_f32 v81, v90, v91\n"
"global_load_dwordx4 a[0:3], v239, s[58:59] offset:0 sc1\n"
"v_mfma_f32_16x16x32_bf16 v[116:119], a[44:47], v[224:227], v[116:119]\n"
"v_cvt_pk_bf16_f32 v82, v92, v93\n"
"v_cvt_pk_bf16_f32 v83, v94, v95\n"
"s_waitcnt lgkmcnt(2)\n"
"v_mfma_f32_16x16x32_bf16 v[120:123], a[24:27], v[228:231], v[120:123]\n"
"s_memrealtime s[84:85]\n"
"v_cvt_pk_bf16_f32 v96, v96, v97\n"
"v_cvt_pk_bf16_f32 v97, v98, v99\n"
"v_mfma_f32_16x16x32_bf16 v[124:127], a[28:31], v[228:231], v[124:127]\n"
"v_cvt_pk_bf16_f32 v98, v100, v101\n"
"v_cvt_pk_bf16_f32 v99, v102, v103\n"
"v_mfma_f32_16x16x32_bf16 v[128:131], a[32:35], v[228:231], v[128:131]\n"
"ds_read_b128 v[224:227], v235 offset:32768\n"
"v_cvt_pk_bf16_f32 v100, v104, v105\n"
"v_cvt_pk_bf16_f32 v101, v106, v107\n"
"v_mfma_f32_16x16x32_bf16 v[132:135], a[36:39], v[228:231], v[132:135]\n"
"v_cvt_pk_bf16_f32 v102, v108, v109\n"
"v_cvt_pk_bf16_f32 v103, v110, v111\n"
"global_load_dwordx4 a[4:7], v239, s[58:59] offset:1024 sc1\n"
"v_mfma_f32_16x16x32_bf16 v[136:139], a[40:43], v[228:231], v[136:139]\n"
"v_cvt_pk_bf16_f32 v104, v112, v113\n"
"v_cvt_pk_bf16_f32 v105, v114, v115\n"
"v_mfma_f32_16x16x32_bf16 v[140:143], a[44:47], v[228:231], v[140:143]\n"
"v_cvt_pk_bf16_f32 v106, v116, v117\n"
"v_cvt_pk_bf16_f32 v107, v118, v119\n"
"s_waitcnt lgkmcnt(2)\n"
"v_mfma_f32_16x16x32_bf16 v[144:147], a[24:27], v[216:219], v[144:147]\n"
"v_cvt_pk_bf16_f32 v120, v120, v121\n"
"v_cvt_pk_bf16_f32 v121, v122, v123\n"
"v_mfma_f32_16x16x32_bf16 v[148:151], a[28:31], v[216:219], v[148:151]\n"
"s_waitcnt lgkmcnt(0)\n"
"s_sub_u32 s86, s83, s84\n"
"v_cvt_pk_bf16_f32 v122, v124, v125\n"
"v_cvt_pk_bf16_f32 v123, v126, v127\n"
"v_mfma_f32_16x16x32_bf16 v[152:155], a[32:35], v[216:219], v[152:155]\n"
"ds_read_b128 v[228:231], v235 offset:0\n"
"v_add_u32 v235, v249, v235\n"
"s_waitcnt vmcnt(22)\n"
"s_barrier\n"
"v_cvt_pk_bf16_f32 v124, v128, v129\n"
"v_cvt_pk_bf16_f32 v125, v130, v131\n"
"global_load_dwordx4 a[8:11], v239, s[58:59] offset:2048 sc1\n"
"v_mfma_f32_16x16x32_bf16 v[156:159], a[36:39], v[216:219], v[156:159]\n"
"ds_read_b128 v[36:39], v232 offset:0\n"
"v_cvt_pk_bf16_f32 v126, v132, v133\n"
"v_cvt_pk_bf16_f32 v127, v134, v135\n"
"v_mfma_f32_16x16x32_bf16 v[160:163], a[40:43], v[216:219], v[160:163]\n"
"v_cvt_pk_bf16_f32 v128, v136, v137\n"
"v_cvt_pk_bf16_f32 v129, v138, v139\n"
"v_mfma_f32_16x16x32_bf16 v[164:167], a[44:47], v[216:219], v[164:167]\n"
"ds_read_b128 v[40:43], v233 offset:0\n"
"v_cvt_pk_bf16_f32 v130, v140, v141\n"
"v_cvt_pk_bf16_f32 v131, v142, v143\n"
"v_mfma_f32_16x16x32_bf16 v[168:171], a[24:27], v[220:223], v[168:171]\n"
"v_cvt_pk_bf16_f32 v144, v144, v145\n"
"v_cvt_pk_bf16_f32 v145, v146, v147\n"
"v_mfma_f32_16x16x32_bf16 v[172:175], a[28:31], v[220:223], v[172:175]\n"
"ds_read_b128 v[44:47], v234 offset:0\n"
"v_cvt_pk_bf16_f32 v146, v148, v149\n"
"v_cvt_pk_bf16_f32 v147, v150, v151\n"
"global_load_dwordx4 a[12:15], v239, s[58:59] offset:3072 sc1\n"
"v_mfma_f32_16x16x32_bf16 v[176:179], a[32:35], v[220:223], v[176:179]\n"
"v_cvt_pk_bf16_f32 v148, v152, v153\n"
"v_cvt_pk_bf16_f32 v149, v154, v155\n"
"v_mfma_f32_16x16x32_bf16 v[180:183], a[36:39], v[220:223], v[180:183]\n"
"ds_read_b128 v[60:63], v235 offset:0\n"
"v_cvt_pk_bf16_f32 v150, v156, v157\n"
"v_cvt_pk_bf16_f32 v151, v158, v159\n"
"v_mfma_f32_16x16x32_bf16 v[184:187], a[40:43], v[220:223], v[184:187]\n"
"v_cvt_pk_bf16_f32 v152, v160, v161\n"
"v_cvt_pk_bf16_f32 v153, v162, v163\n"
"v_mfma_f32_16x16x32_bf16 v[188:191], a[44:47], v[220:223], v[188:191]\n"
"ds_read_b128 v[64:67], v232 offset:8192\n"
"v_cvt_pk_bf16_f32 v154, v164, v165\n"
"v_cvt_pk_bf16_f32 v155, v166, v167\n"
"s_add_u32 s58, s24, 0xf000\n"
"s_addc_u32 s59, s25, 0\n"
"v_mfma_f32_16x16x32_bf16 v[192:195], a[24:27], v[224:227], v[192:195]\n"
"v_cvt_pk_bf16_f32 v168, v168, v169\n"
"v_cvt_pk_bf16_f32 v169, v170, v171\n"
"global_load_dwordx4 a[16:19], v239, s[58:59] offset:-4096 sc1\n"
"v_mfma_f32_16x16x32_bf16 v[196:199], a[28:31], v[224:227], v[196:199]\n"
"ds_read_b128 v[68:71], v233 offset:8192\n"
"v_cvt_pk_bf16_f32 v170, v172, v173\n"
"v_cvt_pk_bf16_f32 v171, v174, v175\n"
"v_mfma_f32_16x16x32_bf16 v[200:203], a[32:35], v[224:227], v[200:203]\n"
"v_cvt_pk_bf16_f32 v172, v176, v177\n"
"v_cvt_pk_bf16_f32 v173, v178, v179\n"
"v_mfma_f32_16x16x32_bf16 v[204:207], a[36:39], v[224:227], v[204:207]\n"
"ds_read_b128 v[84:87], v234 offset:8192\n"
"v_cvt_pk_bf16_f32 v174, v180, v181\n"
"v_cvt_pk_bf16_f32 v175, v182, v183\n"
"v_mfma_f32_16x16x32_bf16 v[208:211], a[40:43], v[224:227], v[208:211]\n"
"v_cvt_pk_bf16_f32 v176, v184, v185\n"
"v_cvt_pk_bf16_f32 v177, v186, v187\n"
"v_mfma_f32_16x16x32_bf16 v[212:215], a[44:47], v[224:227], v[212:215]\n"
"ds_read_b128 v[88:91], v235 offset:8192\n"
"v_cvt_pk_bf16_f32 v178, v188, v189\n"
"v_cvt_pk_bf16_f32 v179, v190, v191\n"
"global_load_dwordx4 a[20:23], v239, s[58:59] offset:-3072 sc1\n"
"s_waitcnt lgkmcnt(8)\n"
"v_mfma_f32_16x16x32_bf16 v[0:3], a[24:27], v[228:231], v[0:3]\n"
"v_cvt_pk_bf16_f32 v192, v192, v193\n"
"v_cvt_pk_bf16_f32 v193, v194, v195\n"
"v_mfma_f32_16x16x32_bf16 v[4:7], a[28:31], v[228:231], v[4:7]\n"
"ds_read_b128 v[92:95], v232 offset:16384\n"
"v_cvt_pk_bf16_f32 v194, v196, v197\n"
"v_cvt_pk_bf16_f32 v195, v198, v199\n"
"v_mfma_f32_16x16x32_bf16 v[8:11], a[32:35], v[228:231], v[8:11]\n"
"v_cvt_pk_bf16_f32 v196, v200, v201\n"
"v_cvt_pk_bf16_f32 v197, v202, v203\n"
"v_mfma_f32_16x16x32_bf16 v[12:15], a[36:39], v[228:231], v[12:15]\n"
"ds_read_b128 v[108:111], v233 offset:16384\n"
"v_cvt_pk_bf16_f32 v198, v204, v205\n"
"v_cvt_pk_bf16_f32 v199, v206, v207\n"
"v_mfma_f32_16x16x32_bf16 v[16:19], a[40:43], v[228:231], v[16:19]\n"
"ds_read_b128 v[112:115], v234 offset:16384\n"
"v_cvt_pk_bf16_f32 v200, v208, v209\n"
"v_cvt_pk_bf16_f32 v201, v210, v211\n"
"global_load_dwordx4 v[140:143], v239, s[58:59] offset:-2048 sc1\n"
"v_mfma_f32_16x16x32_bf16 v[20:23], a[44:47], v[228:231], v[20:23]\n"
"ds_read_b128 v[116:119], v235 offset:16384\n"
"v_cvt_pk_bf16_f32 v202, v212, v213\n"
"v_cvt_pk_bf16_f32 v203, v214, v215\n"
"s_waitcnt lgkmcnt(8)\n"
"v_mfma_f32_16x16x32_bf16 v[132:135], a[240:243], v[36:39], v[0:3]\n"
"v_mfma_f32_16x16x32_bf16 v[132:135], a[244:247], v[40:43], v[132:135]\n"
"s_bitcmp1_b32 s92, 1\n"
"s_cselect_b32 s93, 0x6000, 0\n"
"v_mfma_f32_16x16x32_bf16 v[132:135], a[248:251], v[44:47], v[132:135]\n"
"s_sub_u32 s94, s93, 0xc000\n"
"v_mfma_f32_16x16x32_bf16 v[132:135], a[252:255], v[60:63], v[132:135]\n"
"global_load_dwordx4 v[156:159], v239, s[58:59] offset:-1024 sc1\n"
"s_waitcnt lgkmcnt(4)\n"
"v_mfma_f32_16x16x32_bf16 v[132:135], a[48:51], v[64:67], v[132:135]\n"
"v_add_u32 v231, s93, v237\n"
"v_mfma_f32_16x16x32_bf16 v[132:135], a[52:55], v[68:71], v[132:135]\n"
"v_add_u32 v237, s94, v237\n"
"v_mfma_f32_16x16x32_bf16 v[132:135], a[56:59], v[84:87], v[132:135]\n"
"global_load_dwordx4 v[160:163], v239, s[58:59] offset:0 sc1\n"
"v_mfma_f32_16x16x32_bf16 v[132:135], a[60:63], v[88:91], v[132:135]\n"
"s_waitcnt lgkmcnt(0)\n"
"v_mfma_f32_16x16x32_bf16 v[132:135], a[64:67], v[92:95], v[132:135]\n"
"v_min_u32 v237, v237, v231\n"
"v_mfma_f32_16x16x32_bf16 v[132:135], a[68:71], v[108:111], v[132:135]\n"
"global_load_dwordx4 v[164:167], v239, s[58:59] offset:1024 sc1\n"
"v_mfma_f32_16x16x32_bf16 v[132:135], a[72:75], v[112:115], v[132:135]\n"
"v_add_u32 v231, s93, v238\n"
"v_mfma_f32_16x16x32_bf16 v[132:135], a[76:79], v[116:119], v[132:135]\n"
"v_mfma_f32_16x16x32_bf16 v[136:139], a[80:83], v[36:39], v[4:7]\n"
"global_load_dwordx4 v[180:183], v239, s[58:59] offset:2048 sc1\n"
"v_mfma_f32_16x16x32_bf16 v[136:139], a[84:87], v[40:43], v[136:139]\n"
"v_add_u32 v238, s94, v238\n"
"v_mfma_f32_16x16x32_bf16 v[136:139], a[88:91], v[44:47], v[136:139]\n"
"v_min_u32 v238, v238, v231\n"
"v_mfma_f32_16x16x32_bf16 v[136:139], a[92:95], v[60:63], v[136:139]\n"
"global_load_dwordx4 v[184:187], v239, s[58:59] offset:3072 sc1\n"
"v_mfma_f32_16x16x32_bf16 v[136:139], a[96:99], v[64:67], v[136:139]\n"
"v_cvt_pk_bf16_f32 v208, v132, v133\n"
"v_mfma_f32_16x16x32_bf16 v[136:139], a[100:103], v[68:71], v[136:139]\n"
"v_cvt_pk_bf16_f32 v209, v134, v135\n"
"v_mfma_f32_16x16x32_bf16 v[136:139], a[104:107], v[84:87], v[136:139]\n"
"s_add_u32 s58, s24, 0x11000\n"
"s_addc_u32 s59, s25, 0\n"
"global_load_dwordx4 v[188:191], v239, s[58:59] offset:-4096 sc1\n"
"v_mfma_f32_16x16x32_bf16 v[136:139], a[108:111], v[88:91], v[136:139]\n"
"v_mfma_f32_16x16x32_bf16 v[136:139], a[112:115], v[92:95], v[136:139]\n"
"v_mfma_f32_16x16x32_bf16 v[136:139], a[116:119], v[108:111], v[136:139]\n"
"global_load_dwordx4 v[204:207], v239, s[58:59] offset:-3072 sc1\n"
"v_mfma_f32_16x16x32_bf16 v[136:139], a[120:123], v[112:115], v[136:139]\n"
"ds_write_b64 v244, v[208:209] offset:12288\n"
"v_mfma_f32_16x16x32_bf16 v[136:139], a[124:127], v[116:119], v[136:139]\n"
"v_mfma_f32_16x16x32_bf16 v[132:135], a[128:131], v[36:39], v[8:11]\n"
"global_load_dwordx4 a[24:27], v239, s[58:59] offset:-2048 sc1\n"
"v_mfma_f32_16x16x32_bf16 v[132:135], a[132:135], v[40:43], v[132:135]\n"
"v_mfma_f32_16x16x32_bf16 v[132:135], a[136:139], v[44:47], v[132:135]\n"
"v_mfma_f32_16x16x32_bf16 v[132:135], a[140:143], v[60:63], v[132:135]\n"
"global_load_dwordx4 a[28:31], v239, s[58:59] offset:-1024 sc1\n"
"v_mfma_f32_16x16x32_bf16 v[132:135], a[144:147], v[64:67], v[132:135]\n"
"v_cvt_pk_bf16_f32 v210, v136, v137\n"
"v_mfma_f32_16x16x32_bf16 v[132:135], a[148:151], v[68:71], v[132:135]\n"
"v_cvt_pk_bf16_f32 v211, v138, v139\n"
"v_mfma_f32_16x16x32_bf16 v[132:135], a[152:155], v[84:87], v[132:135]\n"
"global_load_dwordx4 a[32:35], v239, s[58:59] offset:0 sc1\n"
"v_mfma_f32_16x16x32_bf16 v[132:135], a[156:159], v[88:91], v[132:135]\n"
"v_mfma_f32_16x16x32_bf16 v[132:135], a[160:163], v[92:95], v[132:135]\n"
"v_mfma_f32_16x16x32_bf16 v[132:135], a[164:167], v[108:111], v[132:135]\n"
"global_load_dwordx4 a[36:39], v239, s[58:59] offset:1024 sc1\n"
"v_mfma_f32_16x16x32_bf16 v[132:135], a[168:171], v[112:115], v[132:135]\n"
"ds_write_b64 v244, v[210:211] offset:12296\n"
"v_mfma_f32_16x16x32_bf16 v[132:135], a[172:175], v[116:119], v[132:135]\n"
"s_waitcnt vmcnt(36)\n"
"v_mfma_f32_16x16x32_bf16 v[136:139], a[176:179], v[36:39], v[12:15]\n"
"global_load_dwordx4 a[40:43], v239, s[58:59] offset:2048 sc1\n"
"v_mfma_f32_16x16x32_bf16 v[136:139], a[180:183], v[40:43], v[136:139]\n"
"v_mfma_f32_16x16x32_bf16 v[136:139], a[184:187], v[44:47], v[136:139]\n"
"v_mfma_f32_16x16x32_bf16 v[136:139], a[188:191], v[60:63], v[136:139]\n"
"global_load_dwordx4 a[44:47], v239, s[58:59] offset:3072 sc1\n"
"s_waitcnt vmcnt(28)\n"
"v_mfma_f32_16x16x32_bf16 v[136:139], a[192:195], v[64:67], v[136:139]\n"
"v_cvt_pk_bf16_f32 v212, v132, v133\n"
"v_mfma_f32_16x16x32_bf16 v[136:139], a[196:199], v[68:71], v[136:139]\n"
"v_cvt_pk_bf16_f32 v213, v134, v135\n"
"v_mfma_f32_16x16x32_bf16 v[136:139], a[200:203], v[84:87], v[136:139]\n"
"global_load_dwordx3 v[228:230], v248, s[28:29] offset:3072 sc1\n"
"v_mfma_f32_16x16x32_bf16 v[136:139], a[204:207], v[88:91], v[136:139]\n"
"s_waitcnt vmcnt(25)\n"
"v_mfma_f32_16x16x32_bf16 v[136:139], a[208:211], v[92:95], v[136:139]\n"
"s_mov_b64 exec, s[6:7]\n"
"s_cbranch_execz LHT5\n"
"LHR5:\n"
"s_mov_b64 exec, -1\n"
"v_mfma_f32_16x16x32_bf16 v[136:139], a[212:215], v[108:111], v[136:139]\n"
"s_add_u32 s52, s20, s34\n"
"s_addc_u32 s53, s21, 0\n"
"s_add_u32 m0, s31, 0xc000\n"
"v_mfma_f32_16x16x32_bf16 v[136:139], a[216:219], v[112:115], v[136:139]\n"
"global_load_lds_dwordx4 v237, s[52:53] offset:0\n"
"v_mfma_f32_16x16x32_bf16 v[136:139], a[220:223], v[116:119], v[136:139]\n"
"ds_write_b64 v245, v[212:213] offset:12288\n"
"s_waitcnt vmcnt(22)\n"
"v_mfma_f32_16x16x32_bf16 v[132:135], a[224:227], v[36:39], v[16:19]\n"
"s_bitcmp1_b32 s92, 0\n"
"s_cselect_b32 s93, 0x6000, 0\n"
"v_mfma_f32_16x16x32_bf16 v[132:135], a[228:231], v[40:43], v[132:135]\n"
"s_sub_u32 s94, s93, 0xc000\n"
"v_mfma_f32_16x16x32_bf16 v[132:135], a[232:235], v[44:47], v[132:135]\n"
"s_add_u32 m0, s31, 0xdf00\n"
"v_mfma_f32_16x16x32_bf16 v[132:135], a[236:239], v[60:63], v[132:135]\n"
"global_load_lds_dwordx4 v237, s[52:53] offset:256\n"
"s_waitcnt vmcnt(19)\n"
"v_mfma_f32_16x16x32_bf16 v[132:135], a[0:3], v[64:67], v[132:135]\n"
"v_cvt_pk_bf16_f32 v214, v136, v137\n"
"v_mfma_f32_16x16x32_bf16 v[132:135], a[4:7], v[68:71], v[132:135]\n"
"v_cvt_pk_bf16_f32 v215, v138, v139\n"
"v_mfma_f32_16x16x32_bf16 v[132:135], a[8:11], v[84:87], v[132:135]\n"
"v_add_u32 v231, s93, v247\n"
"v_mfma_f32_16x16x32_bf16 v[132:135], a[12:15], v[88:91], v[132:135]\n"
"s_add_u32 m0, s31, 0xfe00\n"
"s_waitcnt vmcnt(15)\n"
"v_mfma_f32_16x16x32_bf16 v[132:135], a[16:19], v[92:95], v[132:135]\n"
"global_load_lds_dwordx4 v237, s[52:53] offset:512\n"
"v_mfma_f32_16x16x32_bf16 v[132:135], a[20:23], v[108:111], v[132:135]\n"
"v_add_u32 v247, s94, v247\n"
"v_mfma_f32_16x16x32_bf16 v[132:135], v[140:143], v[112:115], v[132:135]\n"
"ds_write_b64 v245, v[214:215] offset:12296\n"
"v_mfma_f32_16x16x32_bf16 v[132:135], v[156:159], v[116:119], v[132:135]\n"
"s_barrier\n"
"s_waitcnt vmcnt(12)\n"
"v_mfma_f32_16x16x32_bf16 v[136:139], v[160:163], v[36:39], v[20:23]\n"
"ds_read_b128 v[36:39], v232 offset:256\n"
"s_add_u32 m0, s31, 0xd000\n"
"v_mfma_f32_16x16x32_bf16 v[136:139], v[164:167], v[40:43], v[136:139]\n"
"ds_read_b128 v[40:43], v233 offset:256\n"
"global_load_lds_dwordx4 v238, s[52:53] offset:0\n"
"v_mfma_f32_16x16x32_bf16 v[136:139], v[180:183], v[44:47], v[136:139]\n"
"ds_read_b128 v[44:47], v234 offset:256\n"
"v_mfma_f32_16x16x32_bf16 v[136:139], v[184:187], v[60:63], v[136:139]\n"
"ds_read_b128 v[60:63], v235 offset:256\n"
"v_cvt_pk_bf16_f32 v208, v132, v133\n"
"s_waitcnt vmcnt(9)\n"
"v_mfma_f32_16x16x32_bf16 v[136:139], v[188:191], v[64:67], v[136:139]\n"
"ds_read_b128 v[64:67], v232 offset:8448\n"
"v_cvt_pk_bf16_f32 v209, v134, v135\n"
"v_mfma_f32_16x16x32_bf16 v[136:139], v[204:207], v[68:71], v[136:139]\n"
"ds_read_b128 v[68:71], v233 offset:8448\n"
"s_add_u32 m0, s31, 0xef00\n"
"v_mfma_f32_16x16x32_bf16 v[136:139], a[24:27], v[84:87], v[136:139]\n"
"ds_read_b128 v[84:87], v234 offset:8448\n"
"global_load_lds_dwordx4 v238, s[52:53] offset:256\n"
"v_mfma_f32_16x16x32_bf16 v[136:139], a[28:31], v[88:91], v[136:139]\n"
"ds_read_b128 v[88:91], v235 offset:8448\n"
"s_waitcnt vmcnt(6)\n"
"v_mfma_f32_16x16x32_bf16 v[136:139], a[32:35], v[92:95], v[136:139]\n"
"ds_read_b128 v[92:95], v232 offset:16640\n"
"v_mfma_f32_16x16x32_bf16 v[136:139], a[36:39], v[108:111], v[136:139]\n"
"ds_read_b128 v[108:111], v233 offset:16640\n"
"v_mfma_f32_16x16x32_bf16 v[136:139], a[40:43], v[112:115], v[136:139]\n"
"ds_read_b128 v[112:115], v234 offset:16640\n"
"s_add_u32 m0, s31, 0x10e00\n"
"v_mfma_f32_16x16x32_bf16 v[136:139], a[44:47], v[116:119], v[136:139]\n"
"ds_read_b128 v[116:119], v235 offset:16640\n"
"global_load_lds_dwordx4 v238, s[52:53] offset:512\n"
"s_waitcnt lgkmcnt(8)\n"
"v_mfma_f32_16x16x32_bf16 v[132:135], a[240:243], v[36:39], v[0:3]\n"
"ds_write_b64 v244, v[208:209] offset:12416\n"
"v_mfma_f32_16x16x32_bf16 v[132:135], a[244:247], v[40:43], v[132:135]\n"
"v_min_u32 v247, v247, v231\n"
"v_mfma_f32_16x16x32_bf16 v[132:135], a[248:251], v[44:47], v[132:135]\n"
"v_add_u32 v231, s93, v252\n"
"v_mfma_f32_16x16x32_bf16 v[132:135], a[252:255], v[60:63], v[132:135]\n"
"v_cvt_pk_bf16_f32 v210, v136, v137\n"
"s_waitcnt lgkmcnt(5)\n"
"v_mfma_f32_16x16x32_bf16 v[132:135], a[48:51], v[64:67], v[132:135]\n"
"s_add_u32 s52, s20, s35\n"
"s_addc_u32 s53, s21, 0\n"
"s_add_u32 m0, s31, 0x12000\n"
"v_mfma_f32_16x16x32_bf16 v[132:135], a[52:55], v[68:71], v[132:135]\n"
"global_load_lds_dwordx4 v237, s[52:53] offset:0\n"
"v_mfma_f32_16x16x32_bf16 v[132:135], a[56:59], v[84:87], v[132:135]\n"
"v_cvt_pk_bf16_f32 v211, v138, v139\n"
"v_mfma_f32_16x16x32_bf16 v[132:135], a[60:63], v[88:91], v[132:135]\n"
"s_waitcnt lgkmcnt(1)\n"
"v_mfma_f32_16x16x32_bf16 v[132:135], a[64:67], v[92:95], v[132:135]\n"
"v_add_u32 v252, s94, v252\n"
"v_mfma_f32_16x16x32_bf16 v[132:135], a[68:71], v[108:111], v[132:135]\n"
"s_add_u32 m0, s31, 0x13f00\n"
"v_mfma_f32_16x16x32_bf16 v[132:135], a[72:75], v[112:115], v[132:135]\n"
"global_load_lds_dwordx4 v237, s[52:53] offset:256\n"
"v_mfma_f32_16x16x32_bf16 v[132:135], a[76:79], v[116:119], v[132:135]\n"
"ds_write_b64 v244, v[210:211] offset:12424\n"
"v_mfma_f32_16x16x32_bf16 v[136:139], a[124:127], v[116:119], v[4:7]\n"
"v_min_u32 v252, v252, v231\n"
"v_mfma_f32_16x16x32_bf16 v[136:139], a[120:123], v[112:115], v[136:139]\n"
"v_add_u32 v231, s93, v253\n"
"v_mfma_f32_16x16x32_bf16 v[136:139], a[116:119], v[108:111], v[136:139]\n"
"s_add_u32 m0, s31, 0x15e00\n"
"v_add_u32 v253, s94, v253\n"
"v_mfma_f32_16x16x32_bf16 v[136:139], a[112:115], v[92:95], v[136:139]\n"
"global_load_lds_dwordx4 v237, s[52:53] offset:512\n"
"v_min_u32 v253, v253, v231\n"
"v_mfma_f32_16x16x32_bf16 v[136:139], a[108:111], v[88:91], v[136:139]\n"
"v_cvt_pk_bf16_f32 v208, v132, v133\n"
"v_mfma_f32_16x16x32_bf16 v[136:139], a[104:107], v[84:87], v[136:139]\n"
"v_cvt_pk_bf16_f32 v209, v134, v135\n"
"v_mfma_f32_16x16x32_bf16 v[136:139], a[100:103], v[68:71], v[136:139]\n"
"s_add_i32 s86, s86, s70\n"
"v_mfma_f32_16x16x32_bf16 v[136:139], a[96:99], v[64:67], v[136:139]\n"
"s_add_u32 m0, s31, 0x13000\n"
"LHT5:\n"
"s_cbranch_execz LHR5\n"
"v_mfma_f32_16x16x32_bf16 v[136:139], a[92:95], v[60:63], v[136:139]\n"
"s_waitcnt vmcnt(9)\n"
"global_load_lds_dwordx4 v238, s[52:53] offset:0\n"
"v_mfma_f32_16x16x32_bf16 v[136:139], a[88:91], v[44:47], v[136:139]\n"
"v_cmp_ne_u64 s[58:59], s[80:81], v[228:229]\n"
"v_mfma_f32_16x16x32_bf16 v[136:139], a[84:87], v[40:43], v[136:139]\n"
"v_subrev_u32 v230, s83, v230\n"
"v_mfma_f32_16x16x32_bf16 v[136:139], a[80:83], v[36:39], v[136:139]\n"
"ds_write_b64 v244, v[208:209] offset:24576\n"
"v_mfma_f32_16x16x32_bf16 v[132:135], a[128:131], v[36:39], v[8:11]\n"
"v_add_u32_dpp v230, v230, v230 row_shr:1 row_mask:0xf bank_mask:0xf\n"
"s_add_u32 m0, s31, 0x14f00\n"
"v_mfma_f32_16x16x32_bf16 v[132:135], a[132:135], v[40:43], v[132:135]\n"
"global_load_lds_dwordx4 v238, s[52:53] offset:256\n"
"v_mfma_f32_16x16x32_bf16 v[132:135], a[136:139], v[44:47], v[132:135]\n"
"v_add_u32_dpp v230, v230, v230 row_shr:2 row_mask:0xf bank_mask:0xf\n"
"v_mfma_f32_16x16x32_bf16 v[132:135], a[140:143], v[60:63], v[132:135]\n"
"v_cvt_pk_bf16_f32 v210, v136, v137\n"
"v_mfma_f32_16x16x32_bf16 v[132:135], a[144:147], v[64:67], v[132:135]\n"
"v_add_u32_dpp v230, v230, v230 row_shr:4 row_mask:0xf bank_mask:0xf\n"
"v_mfma_f32_16x16x32_bf16 v[132:135], a[148:151], v[68:71], v[132:135]\n"
"s_add_u32 m0, s31, 0x16e00\n"
"v_mfma_f32_16x16x32_bf16 v[132:135], a[152:155], v[84:87], v[132:135]\n"
"v_readlane_b32 s90, v230, 63\n"
"global_load_lds_dwordx4 v238, s[52:53] offset:512\n"
"v_mfma_f32_16x16x32_bf16 v[132:135], a[156:159], v[88:91], v[132:135]\n"
"v_cvt_pk_bf16_f32 v211, v138, v139\n"
"v_mfma_f32_16x16x32_bf16 v[132:135], a[160:163], v[92:95], v[132:135]\n"
"s_ashr_i32 s90, s90, 3\n"
"v_mfma_f32_16x16x32_bf16 v[132:135], a[164:167], v[108:111], v[132:135]\n"
"s_add_i32 s90, s90, 2494\n"
"v_mfma_f32_16x16x32_bf16 v[132:135], a[168:171], v[112:115], v[132:135]\n"
"s_max_i32 s90, s90, 0\n"
"v_mfma_f32_16x16x32_bf16 v[132:135], a[172:175], v[116:119], v[132:135]\n"
"s_min_i32 s90, s90, 120\n"
"v_mfma_f32_16x16x32_bf16 v[136:139], a[220:223], v[116:119], v[12:15]\n"
"s_mul_i32 s90, s90, 13\n"
"ds_write_b64 v244, v[210:211] offset:24584\n"
"v_mfma_f32_16x16x32_bf16 v[136:139], a[216:219], v[112:115], v[136:139]\n"
"s_ashr_i32 s90, s90, 3\n"
"v_mfma_f32_16x16x32_bf16 v[136:139], a[212:215], v[108:111], v[136:139]\n"
"s_lshr_b32 s59, s59, 24\n"
"s_cselect_b32 s90, 0, s90\n"
"v_mfma_f32_16x16x32_bf16 v[136:139], a[208:211], v[92:95], v[136:139]\n"
"s_add_i32 s86, s86, s90\n"
"v_mfma_f32_16x16x32_bf16 v[136:139], a[204:207], v[88:91], v[136:139]\n"
"s_ashr_i32 s86, s86, 4\n"
"v_mov_b32 v242, 2\n"
"v_mfma_f32_16x16x32_bf16 v[136:139], a[200:203], v[84:87], v[136:139]\n"
"s_max_i32 s86, s86, 100\n"
"v_mfma_f32_16x16x32_bf16 v[136:139], a[196:199], v[68:71], v[136:139]\n"
"s_min_i32 s86, s86, 185\n"
"s_waitcnt lgkmcnt(2)\n"
"ds_write_b32 v240, v242\n"
"v_mfma_f32_16x16x32_bf16 v[136:139], a[192:195], v[64:67], v[136:139]\n"
"s_mov_b32 s83, s84\n"
"v_mfma_f32_16x16x32_bf16 v[136:139], a[188:191], v[60:63], v[136:139]\n"
"v_cvt_pk_bf16_f32 v212, v132, v133\n"
"v_mfma_f32_16x16x32_bf16 v[136:139], a[184:187], v[44:47], v[136:139]\n"
"v_cvt_pk_bf16_f32 v213, v134, v135\n"
"v_mfma_f32_16x16x32_bf16 v[136:139], a[180:183], v[40:43], v[136:139]\n"
"s_bitcmp1_b32 s92, 2\n"
"s_cselect_b32 s93, 0x6000, 0\n"
"v_mfma_f32_16x16x32_bf16 v[136:139], a[176:179], v[36:39], v[136:139]\n"
"s_sub_u32 s94, s93, 0xc000\n"
"v_mfma_f32_16x16x32_bf16 v[132:135], a[224:227], v[36:39], v[16:19]\n"
"v_add_u32 v231, s93, v237\n"
"v_mfma_f32_16x16x32_bf16 v[132:135], a[228:231], v[40:43], v[132:135]\n"
"v_add_u32 v237, s94, v237\n"
"v_mfma_f32_16x16x32_bf16 v[132:135], a[232:235], v[44:47], v[132:135]\n"
"ds_read_b32 v243, v241\n"
"s_add_u32 s83, s83, s86\n"
"v_mfma_f32_16x16x32_bf16 v[132:135], a[236:239], v[60:63], v[132:135]\n"
"s_memrealtime s[84:85]\n"
"v_cvt_pk_bf16_f32 v215, v138, v139\n"
"v_mfma_f32_16x16x32_bf16 v[132:135], a[0:3], v[64:67], v[132:135]\n"
"ds_write_b64 v245, v[212:213] offset:24576\n"
"s_sub_i32 s88, s88, 6\n"
"v_mfma_f32_16x16x32_bf16 v[132:135], a[4:7], v[68:71], v[132:135]\n"
"v_cvt_pk_bf16_f32 v214, v136, v137\n"
"s_max_i32 s88, s88, 0\n"
"v_mfma_f32_16x16x32_bf16 v[132:135], a[8:11], v[84:87], v[132:135]\n"
"s_waitcnt lgkmcnt(0)\n"
"v_cmp_gt_u32 s[56:57], 2, v243\n"
"v_mfma_f32_16x16x32_bf16 v[132:135], a[12:15], v[88:91], v[132:135]\n"
"s_sub_u32 s84, s84, s83\n"
"v_mfma_f32_16x16x32_bf16 v[132:135], a[16:19], v[92:95], v[132:135]\n"
"s_cmp_lg_u64 s[56:57], 0\n"
"s_cbranch_scc1 LB2_1\n"
"LB2_1R:\n"
"v_mfma_f32_16x16x32_bf16 v[132:135], a[20:23], v[108:111], v[132:135]\n"
"s_cmp_lt_i32 s84, s88\n"
"s_cselect_b64 vcc, -1, 0\n"
"v_mfma_f32_16x16x32_bf16 v[132:135], v[140:143], v[112:115], v[132:135]\n"
"ds_write_b64 v245, v[214:215] offset:24584\n"
"v_mfma_f32_16x16x32_bf16 v[132:135], v[156:159], v[116:119], v[132:135]\n"
"v_min_u32 v237, v237, v231\n"
"v_mfma_f32_16x16x32_bf16 v[136:139], a[44:47], v[116:119], v[20:23]\n"
"ds_read_b128 v[116:119], v235 offset:40960\n"
"v_mfma_f32_16x16x32_bf16 v[136:139], a[40:43], v[112:115], v[136:139]\n"
"ds_read_b128 v[112:115], v234 offset:40960\n"
"v_mfma_f32_16x16x32_bf16 v[136:139], a[36:39], v[108:111], v[136:139]\n"
"ds_read_b128 v[108:111], v233 offset:40960\n"
"v_mfma_f32_16x16x32_bf16 v[136:139], a[32:35], v[92:95], v[136:139]\n"
"ds_read_b128 v[92:95], v232 offset:40960\n"
"v_cvt_pk_bf16_f32 v208, v132, v133\n"
"v_mfma_f32_16x16x32_bf16 v[136:139], a[28:31], v[88:91], v[136:139]\n"
"ds_read_b128 v[88:91], v235 offset:32768\n"
"v_cvt_pk_bf16_f32 v209, v134, v135\n"
"v_mfma_f32_16x16x32_bf16 v[136:139], a[24:27], v[84:87], v[136:139]\n"
"ds_read_b128 v[84:87], v234 offset:32768\n"
"v_mfma_f32_16x16x32_bf16 v[136:139], v[204:207], v[68:71], v[136:139]\n"
"ds_read_b128 v[68:71], v233 offset:32768\n"
"v_mfma_f32_16x16x32_bf16 v[136:139], v[188:191], v[64:67], v[136:139]\n"
"ds_read_b128 v[64:67], v232 offset:32768\n"
"v_mfma_f32_16x16x32_bf16 v[136:139], v[184:187], v[60:63], v[136:139]\n"
"ds_read_b128 v[60:63], v235 offset:24576\n"
"v_mfma_f32_16x16x32_bf16 v[136:139], v[180:183], v[44:47], v[136:139]\n"
"ds_read_b128 v[44:47], v234 offset:24576\n"
"v_mfma_f32_16x16x32_bf16 v[136:139], v[164:167], v[40:43], v[136:139]\n"
"ds_read_b128 v[40:43], v233 offset:24576\n"
"v_mfma_f32_16x16x32_bf16 v[136:139], v[160:163], v[36:39], v[136:139]\n"
"ds_read_b128 v[36:39], v232 offset:24576\n"
"LJOIN:\n"
"s_waitcnt lgkmcnt(8)\n"
"v_mfma_f32_16x16x32_bf16 v[132:135], a[76:79], v[116:119], v[0:3]\n"
"ds_write_b64 v244, v[208:209] offset:24704\n"
"v_mfma_f32_16x16x32_bf16 v[132:135], a[72:75], v[112:115], v[132:135]\n"
"s_add_u32 s54, s26, s32\n"
"s_addc_u32 s55, s27, 0\n"
"v_mfma_f32_16x16x32_bf16 v[132:135], a[68:71], v[108:111], v[132:135]\n"
"s_sub_i32 s84, s84, s19\n"
"v_mfma_f32_16x16x32_bf16 v[132:135], a[64:67], v[92:95], v[132:135]\n"
"v_cvt_pk_bf16_f32 v210, v136, v137\n"
"s_waitcnt lgkmcnt(5)\n"
"v_mfma_f32_16x16x32_bf16 v[132:135], a[60:63], v[88:91], v[132:135]\n"
"v_cvt_pk_bf16_f32 v211, v138, v139\n"
"v_mfma_f32_16x16x32_bf16 v[132:135], a[56:59], v[84:87], v[132:135]\n"
"s_max_i32 s88, s88, s84\n"
"v_mfma_f32_16x16x32_bf16 v[132:135], a[52:55], v[68:71], v[132:135]\n"
"ds_read_b128 v[216:219], v246 offset:12288\n"
"v_mfma_f32_16x16x32_bf16 v[132:135], a[48:51], v[64:67], v[132:135]\n"
"ds_read_b128 v[220:223], v246 offset:13312\n"
"s_waitcnt lgkmcnt(3)\n"
"v_mfma_f32_16x16x32_bf16 v[132:135], a[252:255], v[60:63], v[132:135]\n"
"ds_read_b128 v[224:227], v246 offset:14336\n"
"v_mfma_f32_16x16x32_bf16 v[132:135], a[248:251], v[44:47], v[132:135]\n"
"ds_write_b64 v244, v[210:211] offset:24712\n"
"v_mfma_f32_16x16x32_bf16 v[132:135], a[244:247], v[40:43], v[132:135]\n"
"v_add_u32 v231, s93, v238\n"
"v_mfma_f32_16x16x32_bf16 v[132:135], a[240:243], v[36:39], v[132:135]\n"
"v_add_u32 v238, s94, v238\n"
"v_mfma_f32_16x16x32_bf16 v[136:139], a[80:83], v[36:39], v[4:7]\n"
"v_min_u32 v238, v238, v231\n"
"v_mfma_f32_16x16x32_bf16 v[136:139], a[84:87], v[40:43], v[136:139]\n"
"v_mfma_f32_16x16x32_bf16 v[136:139], a[88:91], v[44:47], v[136:139]\n"
"v_mfma_f32_16x16x32_bf16 v[136:139], a[92:95], v[60:63], v[136:139]\n"
"v_cvt_pk_bf16_f32 v208, v132, v133\n"
"v_mfma_f32_16x16x32_bf16 v[136:139], a[96:99], v[64:67], v[136:139]\n"
"v_cvt_pk_bf16_f32 v209, v134, v135\n"
"v_mfma_f32_16x16x32_bf16 v[136:139], a[100:103], v[68:71], v[136:139]\n"
"v_mfma_f32_16x16x32_bf16 v[136:139], a[104:107], v[84:87], v[136:139]\n"
"v_mfma_f32_16x16x32_bf16 v[136:139], a[108:111], v[88:91], v[136:139]\n"
"s_waitcnt lgkmcnt(1)\n"
"v_mfma_f32_16x16x32_bf16 v[136:139], a[112:115], v[92:95], v[136:139]\n"
"v_mfma_f32_16x16x32_bf16 v[136:139], a[116:119], v[108:111], v[136:139]\n"
"global_store_dwordx4 v247, v[216:219], s[54:55] offset:0 sc1\n"
"ds_write_b64 v244, v[208:209] offset:0\n"
"v_mfma_f32_16x16x32_bf16 v[136:139], a[120:123], v[112:115], v[136:139]\n"
"v_mfma_f32_16x16x32_bf16 v[136:139], a[124:127], v[116:119], v[136:139]\n"
"v_mfma_f32_16x16x32_bf16 v[132:135], a[172:175], v[116:119], v[8:11]\n"
"v_mfma_f32_16x16x32_bf16 v[132:135], a[168:171], v[112:115], v[132:135]\n"
"v_mfma_f32_16x16x32_bf16 v[132:135], a[164:167], v[108:111], v[132:135]\n"
"v_mfma_f32_16x16x32_bf16 v[132:135], a[160:163], v[92:95], v[132:135]\n"
"v_cvt_pk_bf16_f32 v210, v136, v137\n"
"v_mfma_f32_16x16x32_bf16 v[132:135], a[156:159], v[88:91], v[132:135]\n"
"v_cvt_pk_bf16_f32 v211, v138, v139\n"
"v_mfma_f32_16x16x32_bf16 v[132:135], a[152:155], v[84:87], v[132:135]\n"
"v_mfma_f32_16x16x32_bf16 v[132:135], a[148:151], v[68:71], v[132:135]\n"
"v_mfma_f32_16x16x32_bf16 v[132:135], a[144:147], v[64:67], v[132:135]\n"
"v_mfma_f32_16x16x32_bf16 v[132:135], a[140:143], v[60:63], v[132:135]\n"
"v_mfma_f32_16x16x32_bf16 v[132:135], a[136:139], v[44:47], v[132:135]\n"
"global_store_dwordx4 v252, v[220:223], s[54:55] offset:0 sc1\n"
"ds_write_b64 v244, v[210:211] offset:8\n"
"v_mfma_f32_16x16x32_bf16 v[132:135], a[132:135], v[40:43], v[132:135]\n"
"s_add_u32 s52, s20, s36\n"
"s_addc_u32 s53, s21, 0\n"
"v_mfma_f32_16x16x32_bf16 v[132:135], a[128:131], v[36:39], v[132:135]\n"
"s_add_u32 m0, s31, 0x0\n"
"v_mfma_f32_16x16x32_bf16 v[136:139], a[176:179], v[36:39], v[12:15]\n"
"global_load_lds_dwordx4 v237, s[52:53] offset:0 nt\n"
"v_mfma_f32_16x16x32_bf16 v[136:139], a[180:183], v[40:43], v[136:139]\n"
"v_mfma_f32_16x16x32_bf16 v[136:139], a[184:187], v[44:47], v[136:139]\n"
"v_mfma_f32_16x16x32_bf16 v[136:139], a[188:191], v[60:63], v[136:139]\n"
"v_cvt_pk_bf16_f32 v212, v132, v133\n"
"v_mfma_f32_16x16x32_bf16 v[136:139], a[192:195], v[64:67], v[136:139]\n"
"v_mov_b32 v242, 3\n"
"v_mfma_f32_16x16x32_bf16 v[136:139], a[196:199], v[68:71], v[136:139]\n"
"v_cvt_pk_bf16_f32 v213, v134, v135\n"
"v_mfma_f32_16x16x32_bf16 v[136:139], a[200:203], v[84:87], v[136:139]\n"
"s_waitcnt lgkmcnt(2)\n"
"ds_write_b32 v240, v242\n"
"v_mfma_f32_16x16x32_bf16 v[136:139], a[204:207], v[88:91], v[136:139]\n"
"v_mfma_f32_16x16x32_bf16 v[136:139], a[208:211], v[92:95], v[136:139]\n"
"v_mfma_f32_16x16x32_bf16 v[136:139], a[212:215], v[108:111], v[136:139]\n"
"v_mfma_f32_16x16x32_bf16 v[136:139], a[216:219], v[112:115], v[136:139]\n"
"global_store_dwordx4 v253, v[224:227], s[54:55] offset:0 sc1\n"
"ds_write_b64 v245, v[212:213] offset:0\n"
"v_mfma_f32_16x16x32_bf16 v[136:139], a[220:223], v[116:119], v[136:139]\n"
"v_mfma_f32_16x16x32_bf16 v[132:135], v[156:159], v[116:119], v[16:19]\n"
"v_mfma_f32_16x16x32_bf16 v[132:135], v[140:143], v[112:115], v[132:135]\n"
"v_mfma_f32_16x16x32_bf16 v[132:135], a[20:23], v[108:111], v[132:135]\n"
"ds_read_b32 v243, v241\n"
"v_mfma_f32_16x16x32_bf16 v[132:135], a[16:19], v[92:95], v[132:135]\n"
"v_cvt_pk_bf16_f32 v214, v136, v137\n"
"v_mfma_f32_16x16x32_bf16 v[132:135], a[12:15], v[88:91], v[132:135]\n"
"v_cvt_pk_bf16_f32 v215, v138, v139\n"
"v_mfma_f32_16x16x32_bf16 v[132:135], a[8:11], v[84:87], v[132:135]\n"
"v_mfma_f32_16x16x32_bf16 v[132:135], a[4:7], v[68:71], v[132:135]\n"
"s_waitcnt lgkmcnt(0)\n"
"v_cmp_gt_u32 s[56:57], 3, v243\n"
"v_mfma_f32_16x16x32_bf16 v[132:135], a[0:3], v[64:67], v[132:135]\n"
"s_add_u32 m0, s31, 0x1f00\n"
"v_mfma_f32_16x16x32_bf16 v[132:135], a[236:239], v[60:63], v[132:135]\n"
"s_or_b64 s[56:57], s[56:57], vcc\n"
"s_cbranch_scc1 LB3\n"
"LB3R:\n"
"v_mfma_f32_16x16x32_bf16 v[132:135], a[232:235], v[44:47], v[132:135]\n"
"ds_write_b64 v245, v[214:215] offset:8\n"
"v_mfma_f32_16x16x32_bf16 v[132:135], a[228:231], v[40:43], v[132:135]\n"
"global_load_lds_dwordx4 v237, s[52:53] offset:256 nt\n"
"v_mfma_f32_16x16x32_bf16 v[132:135], a[224:227], v[36:39], v[132:135]\n"
"v_mfma_f32_16x16x32_bf16 v[136:139], v[160:163], v[36:39], v[20:23]\n"
"ds_read_b128 v[36:39], v232 offset:24832\n"
"v_mfma_f32_16x16x32_bf16 v[136:139], v[164:167], v[40:43], v[136:139]\n"
"ds_read_b128 v[40:43], v233 offset:24832\n"
"v_mfma_f32_16x16x32_bf16 v[136:139], v[180:183], v[44:47], v[136:139]\n"
"ds_read_b128 v[44:47], v234 offset:24832\n"
"v_mfma_f32_16x16x32_bf16 v[136:139], v[184:187], v[60:63], v[136:139]\n"
"ds_read_b128 v[60:63], v235 offset:24832\n"
"v_cvt_pk_bf16_f32 v208, v132, v133\n"
"v_mfma_f32_16x16x32_bf16 v[136:139], v[188:191], v[64:67], v[136:139]\n"
"ds_read_b128 v[64:67], v232 offset:33024\n"
"v_cvt_pk_bf16_f32 v209, v134, v135\n"
"v_mfma_f32_16x16x32_bf16 v[136:139], v[204:207], v[68:71], v[136:139]\n"
"ds_read_b128 v[68:71], v233 offset:33024\n"
"v_mfma_f32_16x16x32_bf16 v[136:139], a[24:27], v[84:87], v[136:139]\n"
"ds_read_b128 v[84:87], v234 offset:33024\n"
"v_mfma_f32_16x16x32_bf16 v[136:139], a[28:31], v[88:91], v[136:139]\n"
"ds_read_b128 v[88:91], v235 offset:33024\n"
"v_mfma_f32_16x16x32_bf16 v[136:139], a[32:35], v[92:95], v[136:139]\n"
"ds_read_b128 v[92:95], v232 offset:41216\n"
"v_mfma_f32_16x16x32_bf16 v[136:139], a[36:39], v[108:111], v[136:139]\n"
"ds_read_b128 v[108:111], v233 offset:41216\n"
"ds_write_b64 v244, v[208:209] offset:128\n"
"v_mfma_f32_16x16x32_bf16 v[136:139], a[40:43], v[112:115], v[136:139]\n"
"ds_read_b128 v[112:115], v234 offset:41216\n"
"v_mfma_f32_16x16x32_bf16 v[136:139], a[44:47], v[116:119], v[136:139]\n"
"ds_read_b128 v[116:119], v235 offset:41216\n"
"s_waitcnt lgkmcnt(9)\n"
"v_mfma_f32_16x16x32_bf16 v[132:135], a[240:243], v[36:39], v[0:3]\n"
"v_add_u32 v232, 0xc000, v232\n"
"v_mfma_f32_16x16x32_bf16 v[132:135], a[244:247], v[40:43], v[132:135]\n"
"v_add_u32 v233, 0xc000, v233\n"
"v_mfma_f32_16x16x32_bf16 v[132:135], a[248:251], v[44:47], v[132:135]\n"
"v_add_u32 v234, 0xc000, v234\n"
"v_mfma_f32_16x16x32_bf16 v[132:135], a[252:255], v[60:63], v[132:135]\n"
"v_add_u32 v235, 0xc000, v235\n"
"s_waitcnt lgkmcnt(5)\n"
"v_mfma_f32_16x16x32_bf16 v[132:135], a[48:51], v[64:67], v[132:135]\n"
"v_cvt_pk_bf16_f32 v210, v136, v137\n"
"v_mfma_f32_16x16x32_bf16 v[132:135], a[52:55], v[68:71], v[132:135]\n"
"v_cvt_pk_bf16_f32 v211, v138, v139\n"
"v_mfma_f32_16x16x32_bf16 v[132:135], a[56:59], v[84:87], v[132:135]\n"
"ds_read_b128 v[216:219], v246 offset:24576\n"
"v_mfma_f32_16x16x32_bf16 v[132:135], a[60:63], v[88:91], v[132:135]\n"
"ds_read_b128 v[220:223], v246 offset:25600\n"
"s_waitcnt lgkmcnt(2)\n"
"v_mfma_f32_16x16x32_bf16 v[132:135], a[64:67], v[92:95], v[132:135]\n"
"ds_read_b128 v[224:227], v246 offset:26624\n"
"v_mfma_f32_16x16x32_bf16 v[132:135], a[68:71], v[108:111], v[132:135]\n"
"s_add_u32 m0, s31, 0x3e00\n"
"v_mfma_f32_16x16x32_bf16 v[132:135], a[72:75], v[112:115], v[132:135]\n"
"ds_write_b64 v244, v[210:211] offset:136\n"
"v_mfma_f32_16x16x32_bf16 v[132:135], a[76:79], v[116:119], v[132:135]\n"
"global_load_lds_dwordx4 v237, s[52:53] offset:512 nt\n"
"v_mfma_f32_16x16x32_bf16 v[136:139], a[124:127], v[116:119], v[4:7]\n"
"v_lshlrev_b32 v0, 16, v24\n"
"v_mfma_f32_16x16x32_bf16 v[136:139], a[120:123], v[112:115], v[136:139]\n"
"v_and_b32 v1, 0xffff0000, v24\n"
"v_mfma_f32_16x16x32_bf16 v[136:139], a[116:119], v[108:111], v[136:139]\n"
"v_lshlrev_b32 v2, 16, v25\n"
"v_mfma_f32_16x16x32_bf16 v[136:139], a[112:115], v[92:95], v[136:139]\n"
"v_cvt_pk_bf16_f32 v208, v132, v133\n"
"v_mfma_f32_16x16x32_bf16 v[136:139], a[108:111], v[88:91], v[136:139]\n"
"v_cvt_pk_bf16_f32 v209, v134, v135\n"
"v_mfma_f32_16x16x32_bf16 v[136:139], a[104:107], v[84:87], v[136:139]\n"
"v_and_b32 v3, 0xffff0000, v25\n"
"v_mfma_f32_16x16x32_bf16 v[136:139], a[100:103], v[68:71], v[136:139]\n"
"v_lshlrev_b32 v4, 16, v26\n"
"v_mfma_f32_16x16x32_bf16 v[136:139], a[96:99], v[64:67], v[136:139]\n"
"s_waitcnt lgkmcnt(1)\n"
"v_mfma_f32_16x16x32_bf16 v[136:139], a[92:95], v[60:63], v[136:139]\n"
"v_and_b32 v5, 0xffff0000, v26\n"
"v_mfma_f32_16x16x32_bf16 v[136:139], a[88:91], v[44:47], v[136:139]\n"
"global_store_dwordx4 v247, v[216:219], s[54:55] offset:768 sc1\n"
"ds_write_b64 v244, v[208:209] offset:12288\n"
"v_mfma_f32_16x16x32_bf16 v[136:139], a[84:87], v[40:43], v[136:139]\n"
"v_lshlrev_b32 v6, 16, v27\n"
"v_mfma_f32_16x16x32_bf16 v[136:139], a[80:83], v[36:39], v[136:139]\n"
"v_and_b32 v7, 0xffff0000, v27\n"
"v_mfma_f32_16x16x32_bf16 v[132:135], a[128:131], v[36:39], v[8:11]\n"
"s_add_u32 s83, s83, s86\n"
"v_mfma_f32_16x16x32_bf16 v[132:135], a[132:135], v[40:43], v[132:135]\n"
"s_sub_i32 s88, s88, 6\n"
"v_mfma_f32_16x16x32_bf16 v[132:135], a[136:139], v[44:47], v[132:135]\n"
"s_max_i32 s88, s88, 0\n"
"v_mfma_f32_16x16x32_bf16 v[132:135], a[140:143], v[60:63], v[132:135]\n"
"v_cvt_pk_bf16_f32 v210, v136, v137\n"
"v_mfma_f32_16x16x32_bf16 v[132:135], a[144:147], v[64:67], v[132:135]\n"
"v_cvt_pk_bf16_f32 v211, v138, v139\n"
"v_mfma_f32_16x16x32_bf16 v[132:135], a[148:151], v[68:71], v[132:135]\n"
"v_lshlrev_b32 v8, 16, v28\n"
"v_mfma_f32_16x16x32_bf16 v[132:135], a[152:155], v[84:87], v[132:135]\n"
"v_and_b32 v9, 0xffff0000, v28\n"
"v_mfma_f32_16x16x32_bf16 v[132:135], a[156:159], v[88:91], v[132:135]\n"
"v_lshlrev_b32 v10, 16, v29\n"
"v_mfma_f32_16x16x32_bf16 v[132:135], a[160:163], v[92:95], v[132:135]\n"
"v_and_b32 v11, 0xffff0000, v29\n"
"v_mfma_f32_16x16x32_bf16 v[132:135], a[164:167], v[108:111], v[132:135]\n"
"s_memrealtime s[84:85]\n"
"v_mfma_f32_16x16x32_bf16 v[132:135], a[168:171], v[112:115], v[132:135]\n"
"global_store_dwordx4 v252, v[220:223], s[54:55] offset:768 sc1\n"
"ds_write_b64 v244, v[210:211] offset:12296\n"
"v_mfma_f32_16x16x32_bf16 v[132:135], a[172:175], v[116:119], v[132:135]\n"
"s_add_u32 m0, s31, 0x1000\n"
"v_mfma_f32_16x16x32_bf16 v[136:139], a[220:223], v[116:119], v[12:15]\n"
"global_load_lds_dwordx4 v238, s[52:53] offset:0 nt\n"
"v_mfma_f32_16x16x32_bf16 v[136:139], a[216:219], v[112:115], v[136:139]\n"
"v_mfma_f32_16x16x32_bf16 v[136:139], a[212:215], v[108:111], v[136:139]\n"
"v_mfma_f32_16x16x32_bf16 v[136:139], a[208:211], v[92:95], v[136:139]\n"
"v_cvt_pk_bf16_f32 v212, v132, v133\n"
"v_mfma_f32_16x16x32_bf16 v[136:139], a[204:207], v[88:91], v[136:139]\n"
"v_mov_b32 v242, 4\n"
"v_mfma_f32_16x16x32_bf16 v[136:139], a[200:203], v[84:87], v[136:139]\n"
"v_cvt_pk_bf16_f32 v213, v134, v135\n"
"v_mfma_f32_16x16x32_bf16 v[136:139], a[196:199], v[68:71], v[136:139]\n"
"s_waitcnt vmcnt(15) lgkmcnt(2)\n"
"ds_write_b32 v240, v242\n"
"v_mfma_f32_16x16x32_bf16 v[136:139], a[192:195], v[64:67], v[136:139]\n"
"v_lshlrev_b32 v12, 16, v30\n"
"v_mfma_f32_16x16x32_bf16 v[136:139], a[188:191], v[60:63], v[136:139]\n"
"v_and_b32 v13, 0xffff0000, v30\n"
"v_mfma_f32_16x16x32_bf16 v[136:139], a[184:187], v[44:47], v[136:139]\n"
"v_lshlrev_b32 v14, 16, v31\n"
"v_mfma_f32_16x16x32_bf16 v[136:139], a[180:183], v[40:43], v[136:139]\n"
"global_store_dwordx4 v253, v[224:227], s[54:55] offset:768 sc1\n"
"ds_write_b64 v245, v[212:213] offset:12288\n"
"v_mfma_f32_16x16x32_bf16 v[136:139], a[176:179], v[36:39], v[136:139]\n"
"v_and_b32 v15, 0xffff0000, v31\n"
"v_mfma_f32_16x16x32_bf16 v[132:135], a[224:227], v[36:39], v[16:19]\n"
"v_mfma_f32_16x16x32_bf16 v[132:135], a[228:231], v[40:43], v[132:135]\n"
"v_mfma_f32_16x16x32_bf16 v[132:135], a[232:235], v[44:47], v[132:135]\n"
"ds_read_b32 v243, v241\n"
"v_mfma_f32_16x16x32_bf16 v[132:135], a[236:239], v[60:63], v[132:135]\n"
"v_cvt_pk_bf16_f32 v214, v136, v137\n"
"v_mfma_f32_16x16x32_bf16 v[132:135], a[0:3], v[64:67], v[132:135]\n"
"v_cvt_pk_bf16_f32 v215, v138, v139\n"
"v_mfma_f32_16x16x32_bf16 v[132:135], a[4:7], v[68:71], v[132:135]\n"
"v_lshlrev_b32 v16, 16, v32\n"
"v_mfma_f32_16x16x32_bf16 v[132:135], a[8:11], v[84:87], v[132:135]\n"
"s_waitcnt lgkmcnt(0)\n"
"v_cmp_gt_u32 s[56:57], 4, v243\n"
"v_mfma_f32_16x16x32_bf16 v[132:135], a[12:15], v[88:91], v[132:135]\n"
"s_sub_u32 s84, s84, s83\n"
"v_mfma_f32_16x16x32_bf16 v[132:135], a[16:19], v[92:95], v[132:135]\n"
"s_or_b64 s[56:57], s[56:57], vcc\n"
"s_cbranch_scc1 LB4\n"
"LB4R:\n"
"v_mfma_f32_16x16x32_bf16 v[132:135], a[20:23], v[108:111], v[132:135]\n"
"s_cmp_lt_i32 s84, s88\n"
"s_cselect_b64 vcc, -1, 0\n"
"v_mfma_f32_16x16x32_bf16 v[132:135], v[140:143], v[112:115], v[132:135]\n"
"ds_write_b64 v245, v[214:215] offset:12296\n"
"v_mfma_f32_16x16x32_bf16 v[132:135], v[156:159], v[116:119], v[132:135]\n"
"v_and_b32 v17, 0xffff0000, v32\n"
"v_mfma_f32_16x16x32_bf16 v[136:139], a[44:47], v[116:119], v[20:23]\n"
"ds_read_b128 v[116:119], v235 offset:16384\n"
"v_mfma_f32_16x16x32_bf16 v[136:139], a[40:43], v[112:115], v[136:139]\n"
"ds_read_b128 v[112:115], v234 offset:16384\n"
"v_mfma_f32_16x16x32_bf16 v[136:139], a[36:39], v[108:111], v[136:139]\n"
"ds_read_b128 v[108:111], v233 offset:16384\n"
"v_mfma_f32_16x16x32_bf16 v[136:139], a[32:35], v[92:95], v[136:139]\n"
"ds_read_b128 v[92:95], v232 offset:16384\n"
"v_cvt_pk_bf16_f32 v208, v132, v133\n"
"v_mfma_f32_16x16x32_bf16 v[136:139], a[28:31], v[88:91], v[136:139]\n"
"ds_read_b128 v[88:91], v235 offset:8192\n"
"v_cvt_pk_bf16_f32 v209, v134, v135\n"
"v_mfma_f32_16x16x32_bf16 v[136:139], a[24:27], v[84:87], v[136:139]\n"
"ds_read_b128 v[84:87], v234 offset:8192\n"
"v_mfma_f32_16x16x32_bf16 v[136:139], v[204:207], v[68:71], v[136:139]\n"
"ds_read_b128 v[68:71], v233 offset:8192\n"
"v_mfma_f32_16x16x32_bf16 v[136:139], v[188:191], v[64:67], v[136:139]\n"
"ds_read_b128 v[64:67], v232 offset:8192\n"
"v_mfma_f32_16x16x32_bf16 v[136:139], v[184:187], v[60:63], v[136:139]\n"
"ds_read_b128 v[60:63], v235 offset:0\n"
"v_mfma_f32_16x16x32_bf16 v[136:139], v[180:183], v[44:47], v[136:139]\n"
"ds_read_b128 v[44:47], v234 offset:0\n"
"v_mfma_f32_16x16x32_bf16 v[136:139], v[164:167], v[40:43], v[136:139]\n"
"ds_read_b128 v[40:43], v233 offset:0\n"
"v_mfma_f32_16x16x32_bf16 v[136:139], v[160:163], v[36:39], v[136:139]\n"
"ds_read_b128 v[36:39], v232 offset:0\n"
"s_waitcnt lgkmcnt(8)\n"
"v_mfma_f32_16x16x32_bf16 v[132:135], a[76:79], v[116:119], v[0:3]\n"
"ds_write_b64 v244, v[208:209] offset:12416\n"
"v_mfma_f32_16x16x32_bf16 v[132:135], a[72:75], v[112:115], v[132:135]\n"
"s_add_u32 m0, s31, 0x2f00\n"
"v_mfma_f32_16x16x32_bf16 v[132:135], a[68:71], v[108:111], v[132:135]\n"
"global_load_lds_dwordx4 v238, s[52:53] offset:256 nt\n"
"v_mfma_f32_16x16x32_bf16 v[132:135], a[64:67], v[92:95], v[132:135]\n"
"v_lshlrev_b32 v18, 16, v33\n"
"v_cvt_pk_bf16_f32 v211, v138, v139\n"
"s_waitcnt lgkmcnt(5)\n"
"v_mfma_f32_16x16x32_bf16 v[132:135], a[60:63], v[88:91], v[132:135]\n"
"v_and_b32 v19, 0xffff0000, v33\n"
"v_mfma_f32_16x16x32_bf16 v[132:135], a[56:59], v[84:87], v[132:135]\n"
"v_lshlrev_b32 v20, 16, v34\n"
"v_mfma_f32_16x16x32_bf16 v[132:135], a[52:55], v[68:71], v[132:135]\n"
"v_and_b32 v21, 0xffff0000, v34\n"
"v_mfma_f32_16x16x32_bf16 v[132:135], a[48:51], v[64:67], v[132:135]\n"
"v_lshlrev_b32 v22, 16, v35\n"
"s_waitcnt lgkmcnt(1)\n"
"v_mfma_f32_16x16x32_bf16 v[132:135], a[252:255], v[60:63], v[132:135]\n"
"v_and_b32 v23, 0xffff0000, v35\n"
"v_mfma_f32_16x16x32_bf16 v[132:135], a[248:251], v[44:47], v[132:135]\n"
"v_cvt_pk_bf16_f32 v210, v136, v137\n"
"v_mfma_f32_16x16x32_bf16 v[132:135], a[244:247], v[40:43], v[132:135]\n"
"s_add_u32 s54, s26, s33\n"
"s_addc_u32 s55, s27, 0\n"
"v_mfma_f32_16x16x32_bf16 v[132:135], a[240:243], v[36:39], v[132:135]\n"
"ds_read_b128 v[216:219], v246 offset:0\n"
"v_mfma_f32_16x16x32_bf16 v[136:139], a[80:83], v[36:39], v[4:7]\n"
"ds_read_b128 v[220:223], v246 offset:1024\n"
"v_mfma_f32_16x16x32_bf16 v[136:139], a[84:87], v[40:43], v[136:139]\n"
"ds_read_b128 v[224:227], v246 offset:2048\n"
"v_mfma_f32_16x16x32_bf16 v[136:139], a[88:91], v[44:47], v[136:139]\n"
"ds_write_b64 v244, v[210:211] offset:12424\n"
"v_mfma_f32_16x16x32_bf16 v[136:139], a[92:95], v[60:63], v[136:139]\n"
"v_cvt_pk_bf16_f32 v208, v132, v133\n"
"v_mfma_f32_16x16x32_bf16 v[136:139], a[96:99], v[64:67], v[136:139]\n"
"v_cvt_pk_bf16_f32 v209, v134, v135\n"
"v_mfma_f32_16x16x32_bf16 v[136:139], a[100:103], v[68:71], v[136:139]\n"
"s_add_u32 m0, s31, 0x4e00\n"
"v_mfma_f32_16x16x32_bf16 v[136:139], a[104:107], v[84:87], v[136:139]\n"
"global_load_lds_dwordx4 v238, s[52:53] offset:512 nt\n"
"v_mfma_f32_16x16x32_bf16 v[136:139], a[108:111], v[88:91], v[136:139]\n"
"s_waitcnt lgkmcnt(1)\n"
"v_mfma_f32_16x16x32_bf16 v[136:139], a[112:115], v[92:95], v[136:139]\n"
"s_sub_i32 s84, s84, s19\n"
"v_mfma_f32_16x16x32_bf16 v[136:139], a[116:119], v[108:111], v[136:139]\n"
"global_store_dwordx4 v247, v[216:219], s[54:55] offset:0 sc1\n"
"ds_write_b64 v244, v[208:209] offset:24576\n"
"v_mfma_f32_16x16x32_bf16 v[136:139], a[120:123], v[112:115], v[136:139]\n"
"s_max_i32 s88, s88, s84\n"
"v_mfma_f32_16x16x32_bf16 v[136:139], a[124:127], v[116:119], v[136:139]\n"
"v_mfma_f32_16x16x32_bf16 v[132:135], a[172:175], v[116:119], v[8:11]\n"
"v_mfma_f32_16x16x32_bf16 v[132:135], a[168:171], v[112:115], v[132:135]\n"
"v_mfma_f32_16x16x32_bf16 v[132:135], a[164:167], v[108:111], v[132:135]\n"
"v_mfma_f32_16x16x32_bf16 v[132:135], a[160:163], v[92:95], v[132:135]\n"
"v_cvt_pk_bf16_f32 v210, v136, v137\n"
"v_mfma_f32_16x16x32_bf16 v[132:135], a[156:159], v[88:91], v[132:135]\n"
"v_cvt_pk_bf16_f32 v211, v138, v139\n"
"v_mfma_f32_16x16x32_bf16 v[132:135], a[152:155], v[84:87], v[132:135]\n"
"v_mfma_f32_16x16x32_bf16 v[132:135], a[148:151], v[68:71], v[132:135]\n"
"v_mfma_f32_16x16x32_bf16 v[132:135], a[144:147], v[64:67], v[132:135]\n"
"v_mfma_f32_16x16x32_bf16 v[132:135], a[140:143], v[60:63], v[132:135]\n"
"v_mfma_f32_16x16x32_bf16 v[132:135], a[136:139], v[44:47], v[132:135]\n"
"global_store_dwordx4 v252, v[220:223], s[54:55] offset:0 sc1\n"
"ds_write_b64 v244, v[210:211] offset:24584\n"
"v_mfma_f32_16x16x32_bf16 v[132:135], a[132:135], v[40:43], v[132:135]\n"
"s_add_u32 s52, s20, s37\n"
"s_addc_u32 s53, s21, 0\n"
"v_mfma_f32_16x16x32_bf16 v[132:135], a[128:131], v[36:39], v[132:135]\n"
"s_add_u32 m0, s31, 0x6000\n"
"v_mfma_f32_16x16x32_bf16 v[136:139], a[176:179], v[36:39], v[12:15]\n"
"global_load_lds_dwordx4 v237, s[52:53] offset:0 nt\n"
"v_mfma_f32_16x16x32_bf16 v[136:139], a[180:183], v[40:43], v[136:139]\n"
"v_mfma_f32_16x16x32_bf16 v[136:139], a[184:187], v[44:47], v[136:139]\n"
"v_mfma_f32_16x16x32_bf16 v[136:139], a[188:191], v[60:63], v[136:139]\n"
"v_cvt_pk_bf16_f32 v212, v132, v133\n"
"v_mfma_f32_16x16x32_bf16 v[136:139], a[192:195], v[64:67], v[136:139]\n"
"v_mov_b32 v242, 5\n"
"v_mfma_f32_16x16x32_bf16 v[136:139], a[196:199], v[68:71], v[136:139]\n"
"v_cvt_pk_bf16_f32 v213, v134, v135\n"
"v_mfma_f32_16x16x32_bf16 v[136:139], a[200:203], v[84:87], v[136:139]\n"
"s_waitcnt lgkmcnt(2)\n"
"ds_write_b32 v240, v242\n"
"v_mfma_f32_16x16x32_bf16 v[136:139], a[204:207], v[88:91], v[136:139]\n"
"s_mov_b64 exec, s[8:9]\n"
"s_cbranch_execz LHT6\n"
"LHR6:\n"
"s_mov_b64 exec, -1\n"
"v_mfma_f32_16x16x32_bf16 v[136:139], a[208:211], v[92:95], v[136:139]\n"
"v_mfma_f32_16x16x32_bf16 v[136:139], a[212:215], v[108:111], v[136:139]\n"
"v_mfma_f32_16x16x32_bf16 v[136:139], a[216:219], v[112:115], v[136:139]\n"
"global_store_dwordx4 v253, v[224:227], s[54:55] offset:0 sc1\n"
"ds_write_b64 v245, v[212:213] offset:24576\n"
"v_mfma_f32_16x16x32_bf16 v[136:139], a[220:223], v[116:119], v[136:139]\n"
"v_mfma_f32_16x16x32_bf16 v[132:135], v[156:159], v[116:119], v[16:19]\n"
"v_mfma_f32_16x16x32_bf16 v[132:135], v[140:143], v[112:115], v[132:135]\n"
"v_mfma_f32_16x16x32_bf16 v[132:135], a[20:23], v[108:111], v[132:135]\n"
"ds_read_b32 v243, v241\n"
"v_mfma_f32_16x16x32_bf16 v[132:135], a[16:19], v[92:95], v[132:135]\n"
"v_cvt_pk_bf16_f32 v214, v136, v137\n"
"v_mfma_f32_16x16x32_bf16 v[132:135], a[12:15], v[88:91], v[132:135]\n"
"v_cvt_pk_bf16_f32 v215, v138, v139\n"
"v_mfma_f32_16x16x32_bf16 v[132:135], a[8:11], v[84:87], v[132:135]\n"
"v_mfma_f32_16x16x32_bf16 v[132:135], a[4:7], v[68:71], v[132:135]\n"
"s_waitcnt lgkmcnt(0)\n"
"v_cmp_gt_u32 s[56:57], 5, v243\n"
"v_mfma_f32_16x16x32_bf16 v[132:135], a[0:3], v[64:67], v[132:135]\n"
"v_mfma_f32_16x16x32_bf16 v[132:135], a[236:239], v[60:63], v[132:135]\n"
"s_or_b64 s[56:57], s[56:57], vcc\n"
"s_cbranch_scc1 LB5\n"
"LB5R:\n"
"v_mfma_f32_16x16x32_bf16 v[132:135], a[232:235], v[44:47], v[132:135]\n"
"ds_write_b64 v245, v[214:215] offset:24584\n"
"v_mfma_f32_16x16x32_bf16 v[132:135], a[228:231], v[40:43], v[132:135]\n"
"v_mfma_f32_16x16x32_bf16 v[132:135], a[224:227], v[36:39], v[132:135]\n"
"v_mfma_f32_16x16x32_bf16 v[136:139], v[160:163], v[36:39], v[20:23]\n"
"ds_read_b128 v[36:39], v232 offset:256\n"
"v_mfma_f32_16x16x32_bf16 v[136:139], v[164:167], v[40:43], v[136:139]\n"
"ds_read_b128 v[40:43], v233 offset:256\n"
"v_mfma_f32_16x16x32_bf16 v[136:139], v[180:183], v[44:47], v[136:139]\n"
"ds_read_b128 v[44:47], v234 offset:256\n"
"v_mfma_f32_16x16x32_bf16 v[136:139], v[184:187], v[60:63], v[136:139]\n"
"ds_read_b128 v[60:63], v235 offset:256\n"
"v_cvt_pk_bf16_f32 v208, v132, v133\n"
"v_mfma_f32_16x16x32_bf16 v[136:139], v[188:191], v[64:67], v[136:139]\n"
"ds_read_b128 v[64:67], v232 offset:8448\n"
"v_cvt_pk_bf16_f32 v209, v134, v135\n"
"v_mfma_f32_16x16x32_bf16 v[136:139], v[204:207], v[68:71], v[136:139]\n"
"ds_read_b128 v[68:71], v233 offset:8448\n"
"v_mfma_f32_16x16x32_bf16 v[136:139], a[24:27], v[84:87], v[136:139]\n"
"ds_read_b128 v[84:87], v234 offset:8448\n"
"v_mfma_f32_16x16x32_bf16 v[136:139], a[28:31], v[88:91], v[136:139]\n"
"ds_read_b128 v[88:91], v235 offset:8448\n"
"v_mfma_f32_16x16x32_bf16 v[136:139], a[32:35], v[92:95], v[136:139]\n"
"ds_read_b128 v[92:95], v232 offset:16640\n"
"v_mfma_f32_16x16x32_bf16 v[136:139], a[36:39], v[108:111], v[136:139]\n"
"ds_read_b128 v[108:111], v233 offset:16640\n"
"v_mfma_f32_16x16x32_bf16 v[136:139], a[40:43], v[112:115], v[136:139]\n"
"ds_read_b128 v[112:115], v234 offset:16640\n"
"v_mfma_f32_16x16x32_bf16 v[136:139], a[44:47], v[116:119], v[136:139]\n"
"ds_read_b128 v[116:119], v235 offset:16640\n"
"s_waitcnt lgkmcnt(8)\n"
"v_mfma_f32_16x16x32_bf16 v[132:135], a[240:243], v[36:39], v[0:3]\n"
"ds_write_b64 v244, v[208:209] offset:24704\n"
"v_mfma_f32_16x16x32_bf16 v[132:135], a[244:247], v[40:43], v[132:135]\n"
"s_add_u32 m0, s31, 0x7f00\n"
"v_mfma_f32_16x16x32_bf16 v[132:135], a[248:251], v[44:47], v[132:135]\n"
"global_load_lds_dwordx4 v237, s[52:53] offset:256 nt\n"
"v_mfma_f32_16x16x32_bf16 v[132:135], a[252:255], v[60:63], v[132:135]\n"
"v_cvt_pk_bf16_f32 v210, v136, v137\n"
"s_waitcnt lgkmcnt(5)\n"
"v_mfma_f32_16x16x32_bf16 v[132:135], a[48:51], v[64:67], v[132:135]\n"
"v_cvt_pk_bf16_f32 v211, v138, v139\n"
"v_mfma_f32_16x16x32_bf16 v[132:135], a[52:55], v[68:71], v[132:135]\n"
"LHT6:\n"
"s_cbranch_execz LHR6\n"
"v_mfma_f32_16x16x32_bf16 v[132:135], a[56:59], v[84:87], v[132:135]\n"
"ds_read_b128 v[216:219], v246 offset:12288\n"
"v_mfma_f32_16x16x32_bf16 v[132:135], a[60:63], v[88:91], v[132:135]\n"
"ds_read_b128 v[220:223], v246 offset:13312\n"
"s_waitcnt lgkmcnt(3)\n"
"v_mfma_f32_16x16x32_bf16 v[132:135], a[64:67], v[92:95], v[132:135]\n"
"ds_read_b128 v[224:227], v246 offset:14336\n"
"v_mfma_f32_16x16x32_bf16 v[132:135], a[68:71], v[108:111], v[132:135]\n"
"ds_write_b64 v244, v[210:211] offset:24712\n"
"v_mfma_f32_16x16x32_bf16 v[132:135], a[72:75], v[112:115], v[132:135]\n"
"v_mfma_f32_16x16x32_bf16 v[132:135], a[76:79], v[116:119], v[132:135]\n"
"v_mfma_f32_16x16x32_bf16 v[136:139], a[124:127], v[116:119], v[4:7]\n"
"s_add_u32 s83, s83, s86\n"
"v_mfma_f32_16x16x32_bf16 v[136:139], a[120:123], v[112:115], v[136:139]\n"
"s_add_u32 m0, s31, 0x9e00\n"
"v_mfma_f32_16x16x32_bf16 v[136:139], a[116:119], v[108:111], v[136:139]\n"
"global_load_lds_dwordx4 v237, s[52:53] offset:512 nt\n"
"v_mfma_f32_16x16x32_bf16 v[136:139], a[112:115], v[92:95], v[136:139]\n"
"v_cvt_pk_bf16_f32 v208, v132, v133\n"
"v_mfma_f32_16x16x32_bf16 v[136:139], a[108:111], v[88:91], v[136:139]\n"
"v_cvt_pk_bf16_f32 v209, v134, v135\n"
"v_mfma_f32_16x16x32_bf16 v[136:139], a[104:107], v[84:87], v[136:139]\n"
"s_sub_i32 s88, s88, 6\n"
"v_mfma_f32_16x16x32_bf16 v[136:139], a[100:103], v[68:71], v[136:139]\n"
"s_max_i32 s88, s88, 0\n"
"v_mfma_f32_16x16x32_bf16 v[136:139], a[96:99], v[64:67], v[136:139]\n"
"s_waitcnt lgkmcnt(1)\n"
"v_mfma_f32_16x16x32_bf16 v[136:139], a[92:95], v[60:63], v[136:139]\n"
"v_mfma_f32_16x16x32_bf16 v[136:139], a[88:91], v[44:47], v[136:139]\n"
"global_store_dwordx4 v247, v[216:219], s[54:55] offset:768 sc1\n"
"ds_write_b64 v244, v[208:209] offset:0\n"
"v_mfma_f32_16x16x32_bf16 v[136:139], a[84:87], v[40:43], v[136:139]\n"
"v_mfma_f32_16x16x32_bf16 v[136:139], a[80:83], v[36:39], v[136:139]\n"
"v_mfma_f32_16x16x32_bf16 v[132:135], a[128:131], v[36:39], v[8:11]\n"
"v_mfma_f32_16x16x32_bf16 v[132:135], a[132:135], v[40:43], v[132:135]\n"
"v_mfma_f32_16x16x32_bf16 v[132:135], a[136:139], v[44:47], v[132:135]\n"
"v_mfma_f32_16x16x32_bf16 v[132:135], a[140:143], v[60:63], v[132:135]\n"
"v_cvt_pk_bf16_f32 v210, v136, v137\n"
"v_mfma_f32_16x16x32_bf16 v[132:135], a[144:147], v[64:67], v[132:135]\n"
"v_cvt_pk_bf16_f32 v211, v138, v139\n"
"v_mfma_f32_16x16x32_bf16 v[132:135], a[148:151], v[68:71], v[132:135]\n"
"v_mfma_f32_16x16x32_bf16 v[132:135], a[152:155], v[84:87], v[132:135]\n"
"v_mfma_f32_16x16x32_bf16 v[132:135], a[156:159], v[88:91], v[132:135]\n"
"v_mfma_f32_16x16x32_bf16 v[132:135], a[160:163], v[92:95], v[132:135]\n"
"v_mfma_f32_16x16x32_bf16 v[132:135], a[164:167], v[108:111], v[132:135]\n"
"s_memrealtime s[84:85]\n"
"v_mfma_f32_16x16x32_bf16 v[132:135], a[168:171], v[112:115], v[132:135]\n"
"global_store_dwordx4 v252, v[220:223], s[54:55] offset:768 sc1\n"
"ds_write_b64 v244, v[210:211] offset:8\n"
"v_mfma_f32_16x16x32_bf16 v[132:135], a[172:175], v[116:119], v[132:135]\n"
"v_mfma_f32_16x16x32_bf16 v[136:139], a[220:223], v[116:119], v[12:15]\n"
"v_mfma_f32_16x16x32_bf16 v[136:139], a[216:219], v[112:115], v[136:139]\n"
"v_mfma_f32_16x16x32_bf16 v[136:139], a[212:215], v[108:111], v[136:139]\n"
"s_add_u32 m0, s31, 0x7000\n"
"v_mfma_f32_16x16x32_bf16 v[136:139], a[208:211], v[92:95], v[136:139]\n"
"v_cvt_pk_bf16_f32 v212, v132, v133\n"
"v_mfma_f32_16x16x32_bf16 v[136:139], a[204:207], v[88:91], v[136:139]\n"
"v_mov_b32 v242, 6\n"
"v_mfma_f32_16x16x32_bf16 v[136:139], a[200:203], v[84:87], v[136:139]\n"
"v_cvt_pk_bf16_f32 v213, v134, v135\n"
"v_mfma_f32_16x16x32_bf16 v[136:139], a[196:199], v[68:71], v[136:139]\n"
"s_waitcnt vmcnt(20) lgkmcnt(2)\n"
"ds_write_b32 v240, v242\n"
"v_mfma_f32_16x16x32_bf16 v[136:139], a[192:195], v[64:67], v[136:139]\n"
"global_load_lds_dwordx4 v238, s[52:53] offset:0 nt\n"
"v_mfma_f32_16x16x32_bf16 v[136:139], a[188:191], v[60:63], v[136:139]\n"
"v_mfma_f32_16x16x32_bf16 v[136:139], a[184:187], v[44:47], v[136:139]\n"
"v_mfma_f32_16x16x32_bf16 v[136:139], a[180:183], v[40:43], v[136:139]\n"
"global_store_dwordx4 v253, v[224:227], s[54:55] offset:768 sc1\n"
"ds_write_b64 v245, v[212:213] offset:0\n"
"v_mfma_f32_16x16x32_bf16 v[136:139], a[176:179], v[36:39], v[136:139]\n"
"s_bitcmp1_b32 s92, 1\n"
"s_cselect_b32 s93, 0x6000, 0\n"
"v_mfma_f32_16x16x32_bf16 v[132:135], a[224:227], v[36:39], v[16:19]\n"
"s_sub_u32 s94, s93, 0xc000\n"
"v_mfma_f32_16x16x32_bf16 v[132:135], a[228:231], v[40:43], v[132:135]\n"
"v_add_u32 v231, s93, v247\n"
"v_mfma_f32_16x16x32_bf16 v[132:135], a[232:235], v[44:47], v[132:135]\n"
"ds_read_b32 v243, v241\n"
"v_mfma_f32_16x16x32_bf16 v[132:135], a[236:239], v[60:63], v[132:135]\n"
"v_cvt_pk_bf16_f32 v214, v136, v137\n"
"v_mfma_f32_16x16x32_bf16 v[132:135], a[0:3], v[64:67], v[132:135]\n"
"v_cvt_pk_bf16_f32 v215, v138, v139\n"
"v_mfma_f32_16x16x32_bf16 v[132:135], a[4:7], v[68:71], v[132:135]\n"
"v_add_u32 v247, s94, v247\n"
"v_mfma_f32_16x16x32_bf16 v[132:135], a[8:11], v[84:87], v[132:135]\n"
"s_waitcnt lgkmcnt(0)\n"
"v_cmp_gt_u32 s[56:57], 6, v243\n"
"v_mfma_f32_16x16x32_bf16 v[132:135], a[12:15], v[88:91], v[132:135]\n"
"s_sub_u32 s84, s84, s83\n"
"v_mfma_f32_16x16x32_bf16 v[132:135], a[16:19], v[92:95], v[132:135]\n"
"s_or_b64 s[56:57], s[56:57], vcc\n"
"s_cbranch_scc1 LB6\n"
"LB6R:\n"
"v_mfma_f32_16x16x32_bf16 v[132:135], a[20:23], v[108:111], v[132:135]\n"
"s_cmp_lt_i32 s84, s88\n"
"s_cselect_b64 vcc, -1, 0\n"
"v_mfma_f32_16x16x32_bf16 v[132:135], v[140:143], v[112:115], v[132:135]\n"
"ds_write_b64 v245, v[214:215] offset:8\n"
"v_mfma_f32_16x16x32_bf16 v[132:135], v[156:159], v[116:119], v[132:135]\n"
"v_min_u32 v247, v247, v231\n"
"v_mfma_f32_16x16x32_bf16 v[136:139], a[44:47], v[116:119], v[20:23]\n"
"ds_read_b128 v[116:119], v235 offset:40960\n"
"v_mfma_f32_16x16x32_bf16 v[136:139], a[40:43], v[112:115], v[136:139]\n"
"ds_read_b128 v[112:115], v234 offset:40960\n"
"v_mfma_f32_16x16x32_bf16 v[136:139], a[36:39], v[108:111], v[136:139]\n"
"ds_read_b128 v[108:111], v233 offset:40960\n"
"v_mfma_f32_16x16x32_bf16 v[136:139], a[32:35], v[92:95], v[136:139]\n"
"ds_read_b128 v[92:95], v232 offset:40960\n"
"v_cvt_pk_bf16_f32 v208, v132, v133\n"
"v_mfma_f32_16x16x32_bf16 v[136:139], a[28:31], v[88:91], v[136:139]\n"
"ds_read_b128 v[88:91], v235 offset:32768\n"
"v_cvt_pk_bf16_f32 v209, v134, v135\n"
"v_mfma_f32_16x16x32_bf16 v[136:139], a[24:27], v[84:87], v[136:139]\n"
"ds_read_b128 v[84:87], v234 offset:32768\n"
"v_mfma_f32_16x16x32_bf16 v[136:139], v[204:207], v[68:71], v[136:139]\n"
"ds_read_b128 v[68:71], v233 offset:32768\n"
"v_mfma_f32_16x16x32_bf16 v[136:139], v[188:191], v[64:67], v[136:139]\n"
"ds_read_b128 v[64:67], v232 offset:32768\n"
"v_mfma_f32_16x16x32_bf16 v[136:139], v[184:187], v[60:63], v[136:139]\n"
"ds_read_b128 v[60:63], v235 offset:24576\n"
"v_mfma_f32_16x16x32_bf16 v[136:139], v[180:183], v[44:47], v[136:139]\n"
"ds_read_b128 v[44:47], v234 offset:24576\n"
"v_mfma_f32_16x16x32_bf16 v[136:139], v[164:167], v[40:43], v[136:139]\n"
"ds_read_b128 v[40:43], v233 offset:24576\n"
"v_mfma_f32_16x16x32_bf16 v[136:139], v[160:163], v[36:39], v[136:139]\n"
"ds_read_b128 v[36:39], v232 offset:24576\n"
"s_waitcnt lgkmcnt(8)\n"
"v_mfma_f32_16x16x32_bf16 v[132:135], a[76:79], v[116:119], v[0:3]\n"
"ds_write_b64 v244, v[208:209] offset:128\n"
"v_mfma_f32_16x16x32_bf16 v[132:135], a[72:75], v[112:115], v[132:135]\n"
"s_add_u32 m0, s31, 0x8f00\n"
"v_mfma_f32_16x16x32_bf16 v[132:135], a[68:71], v[108:111], v[132:135]\n"
"global_load_lds_dwordx4 v238, s[52:53] offset:256 nt\n"
"v_mfma_f32_16x16x32_bf16 v[132:135], a[64:67], v[92:95], v[132:135]\n"
"v_cvt_pk_bf16_f32 v210, v136, v137\n"
"s_waitcnt lgkmcnt(5)\n"
"v_mfma_f32_16x16x32_bf16 v[132:135], a[60:63], v[88:91], v[132:135]\n"
"v_cvt_pk_bf16_f32 v211, v138, v139\n"
"v_mfma_f32_16x16x32_bf16 v[132:135], a[56:59], v[84:87], v[132:135]\n"
"s_add_u32 s54, s26, s34\n"
"s_addc_u32 s55, s27, 0\n"
"v_mfma_f32_16x16x32_bf16 v[132:135], a[52:55], v[68:71], v[132:135]\n"
"ds_read_b128 v[216:219], v246 offset:24576\n"
"v_mfma_f32_16x16x32_bf16 v[132:135], a[48:51], v[64:67], v[132:135]\n"
"ds_read_b128 v[220:223], v246 offset:25600\n"
"s_waitcnt lgkmcnt(3)\n"
"v_mfma_f32_16x16x32_bf16 v[132:135], a[252:255], v[60:63], v[132:135]\n"
"ds_read_b128 v[224:227], v246 offset:26624\n"
"v_mfma_f32_16x16x32_bf16 v[132:135], a[248:251], v[44:47], v[132:135]\n"
"ds_write_b64 v244, v[210:211] offset:136\n"
"v_mfma_f32_16x16x32_bf16 v[132:135], a[244:247], v[40:43], v[132:135]\n"
"s_sub_i32 s84, s84, s19\n"
"v_mfma_f32_16x16x32_bf16 v[132:135], a[240:243], v[36:39], v[132:135]\n"
"s_max_i32 s88, s88, s84\n"
"v_mfma_f32_16x16x32_bf16 v[136:139], a[80:83], v[36:39], v[4:7]\n"
"v_add_u32 v231, s93, v252\n"
"v_mfma_f32_16x16x32_bf16 v[136:139], a[84:87], v[40:43], v[136:139]\n"
"v_add_u32 v252, s94, v252\n"
"v_mfma_f32_16x16x32_bf16 v[136:139], a[88:91], v[44:47], v[136:139]\n"
"v_min_u32 v252, v252, v231\n"
"v_mfma_f32_16x16x32_bf16 v[136:139], a[92:95], v[60:63], v[136:139]\n"
"v_cvt_pk_bf16_f32 v208, v132, v133\n"
"v_mfma_f32_16x16x32_bf16 v[136:139], a[96:99], v[64:67], v[136:139]\n"
"v_cvt_pk_bf16_f32 v209, v134, v135\n"
"v_mfma_f32_16x16x32_bf16 v[136:139], a[100:103], v[68:71], v[136:139]\n"
"v_add_u32 v231, s93, v253\n"
"v_mfma_f32_16x16x32_bf16 v[136:139], a[104:107], v[84:87], v[136:139]\n"
"v_add_u32 v253, s94, v253\n"
"v_mfma_f32_16x16x32_bf16 v[136:139], a[108:111], v[88:91], v[136:139]\n"
"s_waitcnt lgkmcnt(1)\n"
"v_mfma_f32_16x16x32_bf16 v[136:139], a[112:115], v[92:95], v[136:139]\n"
"v_min_u32 v253, v253, v231\n"
"v_mfma_f32_16x16x32_bf16 v[136:139], a[116:119], v[108:111], v[136:139]\n"
"global_store_dwordx4 v247, v[216:219], s[54:55] offset:0 sc1\n"
"ds_write_b64 v244, v[208:209] offset:12288\n"
"v_mfma_f32_16x16x32_bf16 v[136:139], a[120:123], v[112:115], v[136:139]\n"
"s_add_u32 m0, s31, 0xae00\n"
"v_mfma_f32_16x16x32_bf16 v[136:139], a[124:127], v[116:119], v[136:139]\n"
"global_load_lds_dwordx4 v238, s[52:53] offset:512 nt\n"
"v_mfma_f32_16x16x32_bf16 v[132:135], a[172:175], v[116:119], v[8:11]\n"
"v_min_u32 v28, 0x370, v239\n"
"v_mfma_f32_16x16x32_bf16 v[132:135], a[168:171], v[112:115], v[132:135]\n"
"s_bitcmp1_b32 s92, 3\n"
"s_cselect_b32 s93, 0x6000, 0\n"
"v_mfma_f32_16x16x32_bf16 v[132:135], a[164:167], v[108:111], v[132:135]\n"
"s_sub_u32 s94, s93, 0xc000\n"
"v_mfma_f32_16x16x32_bf16 v[132:135], a[160:163], v[92:95], v[132:135]\n"
"v_cvt_pk_bf16_f32 v210, v136, v137\n"
"v_mfma_f32_16x16x32_bf16 v[132:135], a[156:159], v[88:91], v[132:135]\n"
"v_cvt_pk_bf16_f32 v211, v138, v139\n"
"v_mfma_f32_16x16x32_bf16 v[132:135], a[152:155], v[84:87], v[132:135]\n"
"v_add_u32 v28, 0x2480, v28\n"
"v_mfma_f32_16x16x32_bf16 v[132:135], a[148:151], v[68:71], v[132:135]\n"
"global_load_dwordx4 v[24:27], v28, s[28:29] sc1\n"
"v_mfma_f32_16x16x32_bf16 v[132:135], a[144:147], v[64:67], v[132:135]\n"
"v_add_u32 v231, s93, v237\n"
"v_mfma_f32_16x16x32_bf16 v[132:135], a[140:143], v[60:63], v[132:135]\n"
"v_add_u32 v237, s94, v237\n"
"v_mfma_f32_16x16x32_bf16 v[132:135], a[136:139], v[44:47], v[132:135]\n"
"global_store_dwordx4 v252, v[220:223], s[54:55] offset:0 sc1\n"
"ds_write_b64 v244, v[210:211] offset:12296\n"
"v_mfma_f32_16x16x32_bf16 v[132:135], a[132:135], v[40:43], v[132:135]\n"
"v_min_u32 v237, v237, v231\n"
"v_mfma_f32_16x16x32_bf16 v[132:135], a[128:131], v[36:39], v[132:135]\n"
"v_add_u32 v231, s93, v238\n"
"v_mfma_f32_16x16x32_bf16 v[136:139], a[176:179], v[36:39], v[12:15]\n"
"v_add_u32 v238, s94, v238\n"
"v_mfma_f32_16x16x32_bf16 v[136:139], a[180:183], v[40:43], v[136:139]\n"
"v_min_u32 v238, v238, v231\n"
"v_mfma_f32_16x16x32_bf16 v[136:139], a[184:187], v[44:47], v[136:139]\n"
"v_mfma_f32_16x16x32_bf16 v[136:139], a[188:191], v[60:63], v[136:139]\n"
"v_cvt_pk_bf16_f32 v212, v132, v133\n"
"v_mfma_f32_16x16x32_bf16 v[136:139], a[192:195], v[64:67], v[136:139]\n"
"v_mov_b32 v242, 7\n"
"v_mfma_f32_16x16x32_bf16 v[136:139], a[196:199], v[68:71], v[136:139]\n"
"v_cvt_pk_bf16_f32 v213, v134, v135\n"
"v_mfma_f32_16x16x32_bf16 v[136:139], a[200:203], v[84:87], v[136:139]\n"
"s_waitcnt lgkmcnt(2)\n"
"ds_write_b32 v240, v242\n"
"v_mfma_f32_16x16x32_bf16 v[136:139], a[204:207], v[88:91], v[136:139]\n"
"v_mfma_f32_16x16x32_bf16 v[136:139], a[208:211], v[92:95], v[136:139]\n"
"v_mfma_f32_16x16x32_bf16 v[136:139], a[212:215], v[108:111], v[136:139]\n"
"v_mfma_f32_16x16x32_bf16 v[136:139], a[216:219], v[112:115], v[136:139]\n"
"global_store_dwordx4 v253, v[224:227], s[54:55] offset:0 sc1\n"
"ds_write_b64 v245, v[212:213] offset:12288\n"
"v_mfma_f32_16x16x32_bf16 v[136:139], a[220:223], v[116:119], v[136:139]\n"
"s_add_u32 s52, s20, s38\n"
"s_addc_u32 s53, s21, 0\n"
"v_mfma_f32_16x16x32_bf16 v[132:135], v[156:159], v[116:119], v[16:19]\n"
"s_add_u32 m0, s31, 0xc000\n"
"v_mfma_f32_16x16x32_bf16 v[132:135], v[140:143], v[112:115], v[132:135]\n"
"global_load_lds_dwordx4 v237, s[52:53] offset:0 nt\n"
"v_mfma_f32_16x16x32_bf16 v[132:135], a[20:23], v[108:111], v[132:135]\n"
"ds_read_b32 v243, v241\n"
"v_mfma_f32_16x16x32_bf16 v[132:135], a[16:19], v[92:95], v[132:135]\n"
"v_cvt_pk_bf16_f32 v214, v136, v137\n"
"v_mfma_f32_16x16x32_bf16 v[132:135], a[12:15], v[88:91], v[132:135]\n"
"v_cvt_pk_bf16_f32 v215, v138, v139\n"
"v_mfma_f32_16x16x32_bf16 v[132:135], a[8:11], v[84:87], v[132:135]\n"
"v_mfma_f32_16x16x32_bf16 v[132:135], a[4:7], v[68:71], v[132:135]\n"
"s_waitcnt lgkmcnt(0)\n"
"v_cmp_gt_u32 s[56:57], 7, v243\n"
"v_mfma_f32_16x16x32_bf16 v[132:135], a[0:3], v[64:67], v[132:135]\n"
"v_mfma_f32_16x16x32_bf16 v[132:135], a[236:239], v[60:63], v[132:135]\n"
"s_or_b64 s[56:57], s[56:57], vcc\n"
"s_cbranch_scc1 LB7\n"
"LB7R:\n"
"v_mfma_f32_16x16x32_bf16 v[132:135], a[232:235], v[44:47], v[132:135]\n"
"ds_write_b64 v245, v[214:215] offset:12296\n"
"v_mfma_f32_16x16x32_bf16 v[132:135], a[228:231], v[40:43], v[132:135]\n"
"v_mfma_f32_16x16x32_bf16 v[132:135], a[224:227], v[36:39], v[132:135]\n"
"v_mfma_f32_16x16x32_bf16 v[136:139], v[160:163], v[36:39], v[20:23]\n"
"ds_read_b128 v[36:39], v232 offset:24832\n"
"v_mfma_f32_16x16x32_bf16 v[136:139], v[164:167], v[40:43], v[136:139]\n"
"ds_read_b128 v[40:43], v233 offset:24832\n"
"v_mfma_f32_16x16x32_bf16 v[136:139], v[180:183], v[44:47], v[136:139]\n"
"ds_read_b128 v[44:47], v234 offset:24832\n"
"v_mfma_f32_16x16x32_bf16 v[136:139], v[184:187], v[60:63], v[136:139]\n"
"ds_read_b128 v[60:63], v235 offset:24832\n"
"v_cvt_pk_bf16_f32 v208, v132, v133\n"
"v_mfma_f32_16x16x32_bf16 v[136:139], v[188:191], v[64:67], v[136:139]\n"
"ds_read_b128 v[64:67], v232 offset:33024\n"
"v_cvt_pk_bf16_f32 v209, v134, v135\n"
"v_mfma_f32_16x16x32_bf16 v[136:139], v[204:207], v[68:71], v[136:139]\n"
"ds_read_b128 v[68:71], v233 offset:33024\n"
"v_mfma_f32_16x16x32_bf16 v[136:139], a[24:27], v[84:87], v[136:139]\n"
"ds_read_b128 v[84:87], v234 offset:33024\n"
"v_mfma_f32_16x16x32_bf16 v[136:139], a[28:31], v[88:91], v[136:139]\n"
"ds_read_b128 v[88:91], v235 offset:33024\n"
"v_mfma_f32_16x16x32_bf16 v[136:139], a[32:35], v[92:95], v[136:139]\n"
"ds_read_b128 v[92:95], v232 offset:41216\n"
"v_mfma_f32_16x16x32_bf16 v[136:139], a[36:39], v[108:111], v[136:139]\n"
"ds_read_b128 v[108:111], v233 offset:41216\n"
"ds_write_b64 v244, v[208:209] offset:12416\n"
"v_mfma_f32_16x16x32_bf16 v[136:139], a[40:43], v[112:115], v[136:139]\n"
"ds_read_b128 v[112:115], v234 offset:41216\n"
"v_mfma_f32_16x16x32_bf16 v[136:139], a[44:47], v[116:119], v[136:139]\n"
"ds_read_b128 v[116:119], v235 offset:41216\n"
"s_waitcnt lgkmcnt(9)\n"
"v_mfma_f32_16x16x32_bf16 v[132:135], a[240:243], v[36:39], v[0:3]\n"
"v_add_u32 v232, 0xffff4000, v232\n"
"v_mfma_f32_16x16x32_bf16 v[132:135], a[244:247], v[40:43], v[132:135]\n"
"v_add_u32 v233, 0xffff4000, v233\n"
"v_mfma_f32_16x16x32_bf16 v[132:135], a[248:251], v[44:47], v[132:135]\n"
"v_add_u32 v234, 0xffff4000, v234\n"
"v_mfma_f32_16x16x32_bf16 v[132:135], a[252:255], v[60:63], v[132:135]\n"
"v_add_u32 v235, 0xffff4000, v235\n"
"s_waitcnt lgkmcnt(5)\n"
"v_mfma_f32_16x16x32_bf16 v[132:135], a[48:51], v[64:67], v[132:135]\n"
"s_add_u32 m0, s31, 0xdf00\n"
"v_mfma_f32_16x16x32_bf16 v[132:135], a[52:55], v[68:71], v[132:135]\n"
"global_load_lds_dwordx4 v237, s[52:53] offset:256 nt\n"
"v_mfma_f32_16x16x32_bf16 v[132:135], a[56:59], v[84:87], v[132:135]\n"
"v_cvt_pk_bf16_f32 v210, v136, v137\n"
"v_mfma_f32_16x16x32_bf16 v[132:135], a[60:63], v[88:91], v[132:135]\n"
"v_cvt_pk_bf16_f32 v211, v138, v139\n"
"s_waitcnt lgkmcnt(0)\n"
"v_mfma_f32_16x16x32_bf16 v[132:135], a[64:67], v[92:95], v[132:135]\n"
"ds_read_b128 v[216:219], v246 offset:0\n"
"v_mfma_f32_16x16x32_bf16 v[132:135], a[68:71], v[108:111], v[132:135]\n"
"ds_read_b128 v[220:223], v246 offset:1024\n"
"v_mfma_f32_16x16x32_bf16 v[132:135], a[72:75], v[112:115], v[132:135]\n"
"ds_read_b128 v[224:227], v246 offset:2048\n"
"v_mfma_f32_16x16x32_bf16 v[132:135], a[76:79], v[116:119], v[132:135]\n"
"v_lshlrev_b32 v0, 16, v48\n"
"v_mfma_f32_16x16x32_bf16 v[136:139], a[124:127], v[116:119], v[4:7]\n"
"ds_write_b64 v244, v[210:211] offset:12424\n"
"v_mfma_f32_16x16x32_bf16 v[136:139], a[120:123], v[112:115], v[136:139]\n"
"v_and_b32 v1, 0xffff0000, v48\n"
"v_mfma_f32_16x16x32_bf16 v[136:139], a[116:119], v[108:111], v[136:139]\n"
"v_lshlrev_b32 v2, 16, v49\n"
"v_mfma_f32_16x16x32_bf16 v[136:139], a[112:115], v[92:95], v[136:139]\n"
"v_cvt_pk_bf16_f32 v208, v132, v133\n"
"v_mfma_f32_16x16x32_bf16 v[136:139], a[108:111], v[88:91], v[136:139]\n"
"v_cvt_pk_bf16_f32 v209, v134, v135\n"
"v_mfma_f32_16x16x32_bf16 v[136:139], a[104:107], v[84:87], v[136:139]\n"
"v_and_b32 v3, 0xffff0000, v49\n"
"v_mfma_f32_16x16x32_bf16 v[136:139], a[100:103], v[68:71], v[136:139]\n"
"v_lshlrev_b32 v4, 16, v50\n"
"v_mfma_f32_16x16x32_bf16 v[136:139], a[96:99], v[64:67], v[136:139]\n"
"s_waitcnt lgkmcnt(1)\n"
"v_mfma_f32_16x16x32_bf16 v[136:139], a[92:95], v[60:63], v[136:139]\n"
"v_and_b32 v5, 0xffff0000, v50\n"
"v_mfma_f32_16x16x32_bf16 v[136:139], a[88:91], v[44:47], v[136:139]\n"
"global_store_dwordx4 v247, v[216:219], s[54:55] offset:768 sc1\n"
"ds_write_b64 v244, v[208:209] offset:24576\n"
"v_mfma_f32_16x16x32_bf16 v[136:139], a[84:87], v[40:43], v[136:139]\n"
"s_add_u32 m0, s31, 0xfe00\n"
"v_mfma_f32_16x16x32_bf16 v[136:139], a[80:83], v[36:39], v[136:139]\n"
"global_load_lds_dwordx4 v237, s[52:53] offset:512 nt\n"
"v_mfma_f32_16x16x32_bf16 v[132:135], a[128:131], v[36:39], v[8:11]\n"
"v_lshlrev_b32 v6, 16, v51\n"
"v_mfma_f32_16x16x32_bf16 v[132:135], a[132:135], v[40:43], v[132:135]\n"
"v_and_b32 v7, 0xffff0000, v51\n"
"v_mfma_f32_16x16x32_bf16 v[132:135], a[136:139], v[44:47], v[132:135]\n"
"s_add_u32 s83, s83, s86\n"
"v_mfma_f32_16x16x32_bf16 v[132:135], a[140:143], v[60:63], v[132:135]\n"
"v_cvt_pk_bf16_f32 v210, v136, v137\n"
"v_mfma_f32_16x16x32_bf16 v[132:135], a[144:147], v[64:67], v[132:135]\n"
"v_cvt_pk_bf16_f32 v211, v138, v139\n"
"v_mfma_f32_16x16x32_bf16 v[132:135], a[148:151], v[68:71], v[132:135]\n"
"v_lshlrev_b32 v8, 16, v52\n"
"v_mfma_f32_16x16x32_bf16 v[132:135], a[152:155], v[84:87], v[132:135]\n"
"v_and_b32 v9, 0xffff0000, v52\n"
"v_mfma_f32_16x16x32_bf16 v[132:135], a[156:159], v[88:91], v[132:135]\n"
"v_lshlrev_b32 v10, 16, v53\n"
"v_mfma_f32_16x16x32_bf16 v[132:135], a[160:163], v[92:95], v[132:135]\n"
"v_and_b32 v11, 0xffff0000, v53\n"
"v_mfma_f32_16x16x32_bf16 v[132:135], a[164:167], v[108:111], v[132:135]\n"
"s_memrealtime s[84:85]\n"
"v_mfma_f32_16x16x32_bf16 v[132:135], a[168:171], v[112:115], v[132:135]\n"
"global_store_dwordx4 v252, v[220:223], s[54:55] offset:768 sc1\n"
"ds_write_b64 v244, v[210:211] offset:24584\n"
"v_mfma_f32_16x16x32_bf16 v[132:135], a[172:175], v[116:119], v[132:135]\n"
"s_sub_i32 s88, s88, 6\n"
"v_mfma_f32_16x16x32_bf16 v[136:139], a[220:223], v[116:119], v[12:15]\n"
"s_max_i32 s88, s88, 0\n"
"v_mfma_f32_16x16x32_bf16 v[136:139], a[216:219], v[112:115], v[136:139]\n"
"v_mfma_f32_16x16x32_bf16 v[136:139], a[212:215], v[108:111], v[136:139]\n"
"v_mfma_f32_16x16x32_bf16 v[136:139], a[208:211], v[92:95], v[136:139]\n"
"v_cvt_pk_bf16_f32 v212, v132, v133\n"
"v_mfma_f32_16x16x32_bf16 v[136:139], a[204:207], v[88:91], v[136:139]\n"
"v_mov_b32 v242, 8\n"
"v_mfma_f32_16x16x32_bf16 v[136:139], a[200:203], v[84:87], v[136:139]\n"
"v_cvt_pk_bf16_f32 v213, v134, v135\n"
"v_mfma_f32_16x16x32_bf16 v[136:139], a[196:199], v[68:71], v[136:139]\n"
"s_waitcnt vmcnt(21) lgkmcnt(2)\n"
"ds_write_b32 v240, v242\n"
"v_mfma_f32_16x16x32_bf16 v[136:139], a[192:195], v[64:67], v[136:139]\n"
"v_lshlrev_b32 v12, 16, v54\n"
"v_mfma_f32_16x16x32_bf16 v[136:139], a[188:191], v[60:63], v[136:139]\n"
"v_and_b32 v13, 0xffff0000, v54\n"
"v_mfma_f32_16x16x32_bf16 v[136:139], a[184:187], v[44:47], v[136:139]\n"
"v_lshlrev_b32 v14, 16, v55\n"
"v_mfma_f32_16x16x32_bf16 v[136:139], a[180:183], v[40:43], v[136:139]\n"
"global_store_dwordx4 v253, v[224:227], s[54:55] offset:768 sc1\n"
"ds_write_b64 v245, v[212:213] offset:24576\n"
"v_mfma_f32_16x16x32_bf16 v[136:139], a[176:179], v[36:39], v[136:139]\n"
"s_add_u32 m0, s31, 0xd000\n"
"v_mfma_f32_16x16x32_bf16 v[132:135], a[224:227], v[36:39], v[16:19]\n"
"global_load_lds_dwordx4 v238, s[52:53] offset:0 nt\n"
"v_mfma_f32_16x16x32_bf16 v[132:135], a[228:231], v[40:43], v[132:135]\n"
"v_and_b32 v15, 0xffff0000, v55\n"
"v_mfma_f32_16x16x32_bf16 v[132:135], a[232:235], v[44:47], v[132:135]\n"
"ds_read_b32 v243, v241\n"
"v_mfma_f32_16x16x32_bf16 v[132:135], a[236:239], v[60:63], v[132:135]\n"
"v_cvt_pk_bf16_f32 v214, v136, v137\n"
"v_mfma_f32_16x16x32_bf16 v[132:135], a[0:3], v[64:67], v[132:135]\n"
"v_cvt_pk_bf16_f32 v215, v138, v139\n"
"v_mfma_f32_16x16x32_bf16 v[132:135], a[4:7], v[68:71], v[132:135]\n"
"v_lshlrev_b32 v16, 16, v56\n"
"v_mfma_f32_16x16x32_bf16 v[132:135], a[8:11], v[84:87], v[132:135]\n"
"s_waitcnt lgkmcnt(0)\n"
"v_cmp_gt_u32 s[56:57], 8, v243\n"
"v_mfma_f32_16x16x32_bf16 v[132:135], a[12:15], v[88:91], v[132:135]\n"
"s_sub_u32 s84, s84, s83\n"
"v_mfma_f32_16x16x32_bf16 v[132:135], a[16:19], v[92:95], v[132:135]\n"
"s_or_b64 s[56:57], s[56:57], vcc\n"
"s_cbranch_scc1 LB8\n"
"LB8R:\n"
"v_mfma_f32_16x16x32_bf16 v[132:135], a[20:23], v[108:111], v[132:135]\n"
"s_cmp_lt_i32 s84, s88\n"
"s_cselect_b64 vcc, -1, 0\n"
"v_mfma_f32_16x16x32_bf16 v[132:135], v[140:143], v[112:115], v[132:135]\n"
"ds_write_b64 v245, v[214:215] offset:24584\n"
"v_mfma_f32_16x16x32_bf16 v[132:135], v[156:159], v[116:119], v[132:135]\n"
"v_and_b32 v17, 0xffff0000, v56\n"
"v_mfma_f32_16x16x32_bf16 v[136:139], a[44:47], v[116:119], v[20:23]\n"
"ds_read_b128 v[116:119], v235 offset:16384\n"
"v_mfma_f32_16x16x32_bf16 v[136:139], a[40:43], v[112:115], v[136:139]\n"
"ds_read_b128 v[112:115], v234 offset:16384\n"
"v_mfma_f32_16x16x32_bf16 v[136:139], a[36:39], v[108:111], v[136:139]\n"
"ds_read_b128 v[108:111], v233 offset:16384\n"
"v_mfma_f32_16x16x32_bf16 v[136:139], a[32:35], v[92:95], v[136:139]\n"
"ds_read_b128 v[92:95], v232 offset:16384\n"
"v_cvt_pk_bf16_f32 v208, v132, v133\n"
"v_mfma_f32_16x16x32_bf16 v[136:139], a[28:31], v[88:91], v[136:139]\n"
"ds_read_b128 v[88:91], v235 offset:8192\n"
"v_cvt_pk_bf16_f32 v209, v134, v135\n"
"v_mfma_f32_16x16x32_bf16 v[136:139], a[24:27], v[84:87], v[136:139]\n"
"ds_read_b128 v[84:87], v234 offset:8192\n"
"v_mfma_f32_16x16x32_bf16 v[136:139], v[204:207], v[68:71], v[136:139]\n"
"ds_read_b128 v[68:71], v233 offset:8192\n"
"v_mfma_f32_16x16x32_bf16 v[136:139], v[188:191], v[64:67], v[136:139]\n"
"ds_read_b128 v[64:67], v232 offset:8192\n"
"v_mfma_f32_16x16x32_bf16 v[136:139], v[184:187], v[60:63], v[136:139]\n"
"ds_read_b128 v[60:63], v235 offset:0\n"
"v_mfma_f32_16x16x32_bf16 v[136:139], v[180:183], v[44:47], v[136:139]\n"
"ds_read_b128 v[44:47], v234 offset:0\n"
"v_mfma_f32_16x16x32_bf16 v[136:139], v[164:167], v[40:43], v[136:139]\n"
"ds_read_b128 v[40:43], v233 offset:0\n"
"v_mfma_f32_16x16x32_bf16 v[136:139], v[160:163], v[36:39], v[136:139]\n"
"ds_read_b128 v[36:39], v232 offset:0\n"
"s_waitcnt lgkmcnt(8)\n"
"v_mfma_f32_16x16x32_bf16 v[132:135], a[76:79], v[116:119], v[0:3]\n"
"ds_write_b64 v244, v[208:209] offset:24704\n"
"v_mfma_f32_16x16x32_bf16 v[132:135], a[72:75], v[112:115], v[132:135]\n"
"s_add_u32 m0, s31, 0xef00\n"
"v_mfma_f32_16x16x32_bf16 v[132:135], a[68:71], v[108:111], v[132:135]\n"
"global_load_lds_dwordx4 v238, s[52:53] offset:256 nt\n"
"v_mfma_f32_16x16x32_bf16 v[132:135], a[64:67], v[92:95], v[132:135]\n"
"v_lshlrev_b32 v18, 16, v57\n"
"v_cvt_pk_bf16_f32 v211, v138, v139\n"
"s_waitcnt lgkmcnt(5)\n"
"v_mfma_f32_16x16x32_bf16 v[132:135], a[60:63], v[88:91], v[132:135]\n"
"v_and_b32 v19, 0xffff0000, v57\n"
"v_mfma_f32_16x16x32_bf16 v[132:135], a[56:59], v[84:87], v[132:135]\n"
"v_lshlrev_b32 v20, 16, v58\n"
"v_mfma_f32_16x16x32_bf16 v[132:135], a[52:55], v[68:71], v[132:135]\n"
"v_and_b32 v21, 0xffff0000, v58\n"
"v_mfma_f32_16x16x32_bf16 v[132:135], a[48:51], v[64:67], v[132:135]\n"
"v_lshlrev_b32 v22, 16, v59\n"
"s_waitcnt lgkmcnt(1)\n"
"v_mfma_f32_16x16x32_bf16 v[132:135], a[252:255], v[60:63], v[132:135]\n"
"v_and_b32 v23, 0xffff0000, v59\n"
"v_mfma_f32_16x16x32_bf16 v[132:135], a[248:251], v[44:47], v[132:135]\n"
"v_cvt_pk_bf16_f32 v210, v136, v137\n"
"v_mfma_f32_16x16x32_bf16 v[132:135], a[244:247], v[40:43], v[132:135]\n"
"s_add_u32 s54, s26, s35\n"
"s_addc_u32 s55, s27, 0\n"
"v_mfma_f32_16x16x32_bf16 v[132:135], a[240:243], v[36:39], v[132:135]\n"
"ds_read_b128 v[216:219], v246 offset:12288\n"
"v_mfma_f32_16x16x32_bf16 v[136:139], a[80:83], v[36:39], v[4:7]\n"
"ds_read_b128 v[220:223], v246 offset:13312\n"
"v_mfma_f32_16x16x32_bf16 v[136:139], a[84:87], v[40:43], v[136:139]\n"
"ds_read_b128 v[224:227], v246 offset:14336\n"
"v_mfma_f32_16x16x32_bf16 v[136:139], a[88:91], v[44:47], v[136:139]\n"
"ds_write_b64 v244, v[210:211] offset:24712\n"
"v_mfma_f32_16x16x32_bf16 v[136:139], a[92:95], v[60:63], v[136:139]\n"
"v_cvt_pk_bf16_f32 v208, v132, v133\n"
"v_mfma_f32_16x16x32_bf16 v[136:139], a[96:99], v[64:67], v[136:139]\n"
"v_cvt_pk_bf16_f32 v209, v134, v135\n"
"v_mfma_f32_16x16x32_bf16 v[136:139], a[100:103], v[68:71], v[136:139]\n"
"s_sub_i32 s84, s84, s19\n"
"v_mfma_f32_16x16x32_bf16 v[136:139], a[104:107], v[84:87], v[136:139]\n"
"s_max_i32 s88, s88, s84\n"
"v_mfma_f32_16x16x32_bf16 v[136:139], a[108:111], v[88:91], v[136:139]\n"
"s_waitcnt lgkmcnt(1)\n"
"v_mfma_f32_16x16x32_bf16 v[136:139], a[112:115], v[92:95], v[136:139]\n"
"v_mfma_f32_16x16x32_bf16 v[136:139], a[116:119], v[108:111], v[136:139]\n"
"global_store_dwordx4 v247, v[216:219], s[54:55] offset:0 sc1\n"
"ds_write_b64 v244, v[208:209] offset:0\n"
"v_mfma_f32_16x16x32_bf16 v[136:139], a[120:123], v[112:115], v[136:139]\n"
"v_mfma_f32_16x16x32_bf16 v[136:139], a[124:127], v[116:119], v[136:139]\n"
"s_add_u32 m0, s31, 0x10e00\n"
"v_mfma_f32_16x16x32_bf16 v[132:135], a[172:175], v[116:119], v[8:11]\n"
"global_load_lds_dwordx4 v238, s[52:53] offset:512 nt\n"
"v_mfma_f32_16x16x32_bf16 v[132:135], a[168:171], v[112:115], v[132:135]\n"
"v_mfma_f32_16x16x32_bf16 v[132:135], a[164:167], v[108:111], v[132:135]\n"
"v_mfma_f32_16x16x32_bf16 v[132:135], a[160:163], v[92:95], v[132:135]\n"
"v_cvt_pk_bf16_f32 v210, v136, v137\n"
"v_mfma_f32_16x16x32_bf16 v[132:135], a[156:159], v[88:91], v[132:135]\n"
"v_cvt_pk_bf16_f32 v211, v138, v139\n"
"v_mfma_f32_16x16x32_bf16 v[132:135], a[152:155], v[84:87], v[132:135]\n"
"v_mfma_f32_16x16x32_bf16 v[132:135], a[148:151], v[68:71], v[132:135]\n"
"s_mov_b64 exec, s[10:11]\n"
"s_cbranch_execz LHT7\n"
"LHR7:\n"
"s_mov_b64 exec, -1\n"
"v_mfma_f32_16x16x32_bf16 v[132:135], a[144:147], v[64:67], v[132:135]\n"
"v_mfma_f32_16x16x32_bf16 v[132:135], a[140:143], v[60:63], v[132:135]\n"
"v_mfma_f32_16x16x32_bf16 v[132:135], a[136:139], v[44:47], v[132:135]\n"
"global_store_dwordx4 v252, v[220:223], s[54:55] offset:0 sc1\n"
"ds_write_b64 v244, v[210:211] offset:8\n"
"v_mfma_f32_16x16x32_bf16 v[132:135], a[132:135], v[40:43], v[132:135]\n"
"v_mfma_f32_16x16x32_bf16 v[132:135], a[128:131], v[36:39], v[132:135]\n"
"v_mfma_f32_16x16x32_bf16 v[136:139], a[176:179], v[36:39], v[12:15]\n"
"v_mfma_f32_16x16x32_bf16 v[136:139], a[180:183], v[40:43], v[136:139]\n"
"v_mfma_f32_16x16x32_bf16 v[136:139], a[184:187], v[44:47], v[136:139]\n"
"v_mfma_f32_16x16x32_bf16 v[136:139], a[188:191], v[60:63], v[136:139]\n"
"v_cvt_pk_bf16_f32 v212, v132, v133\n"
"v_mfma_f32_16x16x32_bf16 v[136:139], a[192:195], v[64:67], v[136:139]\n"
"v_mov_b32 v242, 9\n"
"v_mfma_f32_16x16x32_bf16 v[136:139], a[196:199], v[68:71], v[136:139]\n"
"v_cvt_pk_bf16_f32 v213, v134, v135\n"
"v_mfma_f32_16x16x32_bf16 v[136:139], a[200:203], v[84:87], v[136:139]\n"
"s_waitcnt lgkmcnt(2)\n"
"ds_write_b32 v240, v242\n"
"v_mfma_f32_16x16x32_bf16 v[136:139], a[204:207], v[88:91], v[136:139]\n"
"v_mfma_f32_16x16x32_bf16 v[136:139], a[208:211], v[92:95], v[136:139]\n"
"v_mfma_f32_16x16x32_bf16 v[136:139], a[212:215], v[108:111], v[136:139]\n"
"v_mfma_f32_16x16x32_bf16 v[136:139], a[216:219], v[112:115], v[136:139]\n"
"global_store_dwordx4 v253, v[224:227], s[54:55] offset:0 sc1\n"
"ds_write_b64 v245, v[212:213] offset:0\n"
"v_mfma_f32_16x16x32_bf16 v[136:139], a[220:223], v[116:119], v[136:139]\n"
"s_add_u32 s52, s20, s39\n"
"s_addc_u32 s53, s21, 0\n"
"v_mfma_f32_16x16x32_bf16 v[132:135], v[156:159], v[116:119], v[16:19]\n"
"s_add_u32 m0, s31, 0x12000\n"
"v_mfma_f32_16x16x32_bf16 v[132:135], v[140:143], v[112:115], v[132:135]\n"
"global_load_lds_dwordx4 v237, s[52:53] offset:0 nt\n"
"v_mfma_f32_16x16x32_bf16 v[132:135], a[20:23], v[108:111], v[132:135]\n"
"ds_read_b32 v243, v241\n"
"v_mfma_f32_16x16x32_bf16 v[132:135], a[16:19], v[92:95], v[132:135]\n"
"v_cvt_pk_bf16_f32 v214, v136, v137\n"
"v_mfma_f32_16x16x32_bf16 v[132:135], a[12:15], v[88:91], v[132:135]\n"
"v_cvt_pk_bf16_f32 v215, v138, v139\n"
"v_mfma_f32_16x16x32_bf16 v[132:135], a[8:11], v[84:87], v[132:135]\n"
"v_mfma_f32_16x16x32_bf16 v[132:135], a[4:7], v[68:71], v[132:135]\n"
"s_waitcnt lgkmcnt(0)\n"
"v_cmp_gt_u32 s[56:57], 9, v243\n"
"v_mfma_f32_16x16x32_bf16 v[132:135], a[0:3], v[64:67], v[132:135]\n"
"v_mfma_f32_16x16x32_bf16 v[132:135], a[236:239], v[60:63], v[132:135]\n"
"s_or_b64 s[56:57], s[56:57], vcc\n"
"s_cbranch_scc1 LB9\n"
"LB9R:\n"
"v_mfma_f32_16x16x32_bf16 v[132:135], a[232:235], v[44:47], v[132:135]\n"
"ds_write_b64 v245, v[214:215] offset:8\n"
"v_mfma_f32_16x16x32_bf16 v[132:135], a[228:231], v[40:43], v[132:135]\n"
"v_mfma_f32_16x16x32_bf16 v[132:135], a[224:227], v[36:39], v[132:135]\n"
"v_mfma_f32_16x16x32_bf16 v[136:139], v[160:163], v[36:39], v[20:23]\n"
"ds_read_b128 v[36:39], v232 offset:256\n"
"v_mfma_f32_16x16x32_bf16 v[136:139], v[164:167], v[40:43], v[136:139]\n"
"ds_read_b128 v[40:43], v233 offset:256\n"
"v_mfma_f32_16x16x32_bf16 v[136:139], v[180:183], v[44:47], v[136:139]\n"
"ds_read_b128 v[44:47], v234 offset:256\n"
"LHT7:\n"
"s_cbranch_execz LHR7\n"
"v_mfma_f32_16x16x32_bf16 v[136:139], v[184:187], v[60:63], v[136:139]\n"
"ds_read_b128 v[60:63], v235 offset:256\n"
"v_cvt_pk_bf16_f32 v208, v132, v133\n"
"v_mfma_f32_16x16x32_bf16 v[136:139], v[188:191], v[64:67], v[136:139]\n"
"ds_read_b128 v[64:67], v232 offset:8448\n"
"v_cvt_pk_bf16_f32 v209, v134, v135\n"
"v_mfma_f32_16x16x32_bf16 v[136:139], v[204:207], v[68:71], v[136:139]\n"
"ds_read_b128 v[68:71], v233 offset:8448\n"
"v_mfma_f32_16x16x32_bf16 v[136:139], a[24:27], v[84:87], v[136:139]\n"
"ds_read_b128 v[84:87], v234 offset:8448\n"
"v_mfma_f32_16x16x32_bf16 v[136:139], a[28:31], v[88:91], v[136:139]\n"
"ds_read_b128 v[88:91], v235 offset:8448\n"
"v_mfma_f32_16x16x32_bf16 v[136:139], a[32:35], v[92:95], v[136:139]\n"
"ds_read_b128 v[92:95], v232 offset:16640\n"
"v_mfma_f32_16x16x32_bf16 v[136:139], a[36:39], v[108:111], v[136:139]\n"
"ds_read_b128 v[108:111], v233 offset:16640\n"
"v_mfma_f32_16x16x32_bf16 v[136:139], a[40:43], v[112:115], v[136:139]\n"
"ds_read_b128 v[112:115], v234 offset:16640\n"
"v_mfma_f32_16x16x32_bf16 v[136:139], a[44:47], v[116:119], v[136:139]\n"
"ds_read_b128 v[116:119], v235 offset:16640\n"
"s_waitcnt lgkmcnt(8)\n"
"v_mfma_f32_16x16x32_bf16 v[132:135], a[240:243], v[36:39], v[0:3]\n"
"ds_write_b64 v244, v[208:209] offset:128\n"
"v_mfma_f32_16x16x32_bf16 v[132:135], a[244:247], v[40:43], v[132:135]\n"
"s_add_u32 m0, s31, 0x13f00\n"
"v_mfma_f32_16x16x32_bf16 v[132:135], a[248:251], v[44:47], v[132:135]\n"
"global_load_lds_dwordx4 v237, s[52:53] offset:256 nt\n"
"v_mfma_f32_16x16x32_bf16 v[132:135], a[252:255], v[60:63], v[132:135]\n"
"v_cvt_pk_bf16_f32 v210, v136, v137\n"
"s_waitcnt lgkmcnt(5)\n"
"v_mfma_f32_16x16x32_bf16 v[132:135], a[48:51], v[64:67], v[132:135]\n"
"v_cvt_pk_bf16_f32 v211, v138, v139\n"
"v_mfma_f32_16x16x32_bf16 v[132:135], a[52:55], v[68:71], v[132:135]\n"
"v_mfma_f32_16x16x32_bf16 v[132:135], a[56:59], v[84:87], v[132:135]\n"
"ds_read_b128 v[216:219], v246 offset:24576\n"
"v_mfma_f32_16x16x32_bf16 v[132:135], a[60:63], v[88:91], v[132:135]\n"
"ds_read_b128 v[220:223], v246 offset:25600\n"
"s_waitcnt lgkmcnt(3)\n"
"v_mfma_f32_16x16x32_bf16 v[132:135], a[64:67], v[92:95], v[132:135]\n"
"ds_read_b128 v[224:227], v246 offset:26624\n"
"v_mfma_f32_16x16x32_bf16 v[132:135], a[68:71], v[108:111], v[132:135]\n"
"ds_write_b64 v244, v[210:211] offset:136\n"
"v_mfma_f32_16x16x32_bf16 v[132:135], a[72:75], v[112:115], v[132:135]\n"
"v_mfma_f32_16x16x32_bf16 v[132:135], a[76:79], v[116:119], v[132:135]\n"
"v_mfma_f32_16x16x32_bf16 v[136:139], a[124:127], v[116:119], v[4:7]\n"
"s_add_u32 s83, s83, s86\n"
"v_mfma_f32_16x16x32_bf16 v[136:139], a[120:123], v[112:115], v[136:139]\n"
"s_sub_i32 s88, s88, 6\n"
"v_mfma_f32_16x16x32_bf16 v[136:139], a[116:119], v[108:111], v[136:139]\n"
"s_max_i32 s88, s88, 0\n"
"v_mfma_f32_16x16x32_bf16 v[136:139], a[112:115], v[92:95], v[136:139]\n"
"v_cvt_pk_bf16_f32 v208, v132, v133\n"
"v_mfma_f32_16x16x32_bf16 v[136:139], a[108:111], v[88:91], v[136:139]\n"
"v_cvt_pk_bf16_f32 v209, v134, v135\n"
"v_mfma_f32_16x16x32_bf16 v[136:139], a[104:107], v[84:87], v[136:139]\n"
"v_mfma_f32_16x16x32_bf16 v[136:139], a[100:103], v[68:71], v[136:139]\n"
"v_mfma_f32_16x16x32_bf16 v[136:139], a[96:99], v[64:67], v[136:139]\n"
"s_waitcnt lgkmcnt(1)\n"
"v_mfma_f32_16x16x32_bf16 v[136:139], a[92:95], v[60:63], v[136:139]\n"
"v_mfma_f32_16x16x32_bf16 v[136:139], a[88:91], v[44:47], v[136:139]\n"
"global_store_dwordx4 v247, v[216:219], s[54:55] offset:768 sc1\n"
"ds_write_b64 v244, v[208:209] offset:12288\n"
"v_mfma_f32_16x16x32_bf16 v[136:139], a[84:87], v[40:43], v[136:139]\n"
"v_mfma_f32_16x16x32_bf16 v[136:139], a[80:83], v[36:39], v[136:139]\n"
"v_mfma_f32_16x16x32_bf16 v[132:135], a[128:131], v[36:39], v[8:11]\n"
"v_mfma_f32_16x16x32_bf16 v[132:135], a[132:135], v[40:43], v[132:135]\n"
"v_mfma_f32_16x16x32_bf16 v[132:135], a[136:139], v[44:47], v[132:135]\n"
"v_mfma_f32_16x16x32_bf16 v[132:135], a[140:143], v[60:63], v[132:135]\n"
"v_cvt_pk_bf16_f32 v210, v136, v137\n"
"v_mfma_f32_16x16x32_bf16 v[132:135], a[144:147], v[64:67], v[132:135]\n"
"v_cvt_pk_bf16_f32 v211, v138, v139\n"
"v_mfma_f32_16x16x32_bf16 v[132:135], a[148:151], v[68:71], v[132:135]\n"
"s_add_u32 m0, s31, 0x15e00\n"
"v_mfma_f32_16x16x32_bf16 v[132:135], a[152:155], v[84:87], v[132:135]\n"
"global_load_lds_dwordx4 v237, s[52:53] offset:512 nt\n"
"v_mfma_f32_16x16x32_bf16 v[132:135], a[156:159], v[88:91], v[132:135]\n"
"v_mfma_f32_16x16x32_bf16 v[132:135], a[160:163], v[92:95], v[132:135]\n"
"v_mfma_f32_16x16x32_bf16 v[132:135], a[164:167], v[108:111], v[132:135]\n"
"s_memrealtime s[84:85]\n"
"v_mfma_f32_16x16x32_bf16 v[132:135], a[168:171], v[112:115], v[132:135]\n"
"global_store_dwordx4 v252, v[220:223], s[54:55] offset:768 sc1\n"
"ds_write_b64 v244, v[210:211] offset:12296\n"
"v_mfma_f32_16x16x32_bf16 v[132:135], a[172:175], v[116:119], v[132:135]\n"
"v_mfma_f32_16x16x32_bf16 v[136:139], a[220:223], v[116:119], v[12:15]\n"
"v_mfma_f32_16x16x32_bf16 v[136:139], a[216:219], v[112:115], v[136:139]\n"
"v_mfma_f32_16x16x32_bf16 v[136:139], a[212:215], v[108:111], v[136:139]\n"
"v_mfma_f32_16x16x32_bf16 v[136:139], a[208:211], v[92:95], v[136:139]\n"
"v_cvt_pk_bf16_f32 v212, v132, v133\n"
"v_mfma_f32_16x16x32_bf16 v[136:139], a[204:207], v[88:91], v[136:139]\n"
"v_mov_b32 v242, 10\n"
"v_mfma_f32_16x16x32_bf16 v[136:139], a[200:203], v[84:87], v[136:139]\n"
"v_cvt_pk_bf16_f32 v213, v134, v135\n"
"v_mfma_f32_16x16x32_bf16 v[136:139], a[196:199], v[68:71], v[136:139]\n"
"s_waitcnt vmcnt(20) lgkmcnt(2)\n"
"ds_write_b32 v240, v242\n"
"v_mfma_f32_16x16x32_bf16 v[136:139], a[192:195], v[64:67], v[136:139]\n"
"v_mfma_f32_16x16x32_bf16 v[136:139], a[188:191], v[60:63], v[136:139]\n"
"v_mfma_f32_16x16x32_bf16 v[136:139], a[184:187], v[44:47], v[136:139]\n"
"v_mfma_f32_16x16x32_bf16 v[136:139], a[180:183], v[40:43], v[136:139]\n"
"global_store_dwordx4 v253, v[224:227], s[54:55] offset:768 sc1\n"
"ds_write_b64 v245, v[212:213] offset:12288\n"
"v_mfma_f32_16x16x32_bf16 v[136:139], a[176:179], v[36:39], v[136:139]\n"
"s_bitcmp1_b32 s92, 2\n"
"s_cselect_b32 s93, 0x6000, 0\n"
"v_mfma_f32_16x16x32_bf16 v[132:135], a[224:227], v[36:39], v[16:19]\n"
"s_sub_u32 s94, s93, 0xc000\n"
"v_mfma_f32_16x16x32_bf16 v[132:135], a[228:231], v[40:43], v[132:135]\n"
"v_add_u32 v231, s93, v247\n"
"v_mfma_f32_16x16x32_bf16 v[132:135], a[232:235], v[44:47], v[132:135]\n"
"ds_read_b32 v243, v241\n"
"v_mfma_f32_16x16x32_bf16 v[132:135], a[236:239], v[60:63], v[132:135]\n"
"v_cvt_pk_bf16_f32 v214, v136, v137\n"
"v_mfma_f32_16x16x32_bf16 v[132:135], a[0:3], v[64:67], v[132:135]\n"
"v_cvt_pk_bf16_f32 v215, v138, v139\n"
"v_mfma_f32_16x16x32_bf16 v[132:135], a[4:7], v[68:71], v[132:135]\n"
"s_add_u32 m0, s31, 0x13000\n"
"v_mfma_f32_16x16x32_bf16 v[132:135], a[8:11], v[84:87], v[132:135]\n"
"s_waitcnt lgkmcnt(0)\n"
"v_cmp_gt_u32 s[56:57], 10, v243\n"
"v_mfma_f32_16x16x32_bf16 v[132:135], a[12:15], v[88:91], v[132:135]\n"
"s_sub_u32 s84, s84, s83\n"
"v_mfma_f32_16x16x32_bf16 v[132:135], a[16:19], v[92:95], v[132:135]\n"
"s_or_b64 s[56:57], s[56:57], vcc\n"
"s_cbranch_scc1 LB10\n"
"LB10R:\n"
"v_mfma_f32_16x16x32_bf16 v[132:135], a[20:23], v[108:111], v[132:135]\n"
"s_cmp_lt_i32 s84, s88\n"
"s_cselect_b64 vcc, -1, 0\n"
"v_mfma_f32_16x16x32_bf16 v[132:135], v[140:143], v[112:115], v[132:135]\n"
"ds_write_b64 v245, v[214:215] offset:12296\n"
"v_mfma_f32_16x16x32_bf16 v[132:135], v[156:159], v[116:119], v[132:135]\n"
"global_load_lds_dwordx4 v238, s[52:53] offset:0 nt\n"
"v_mfma_f32_16x16x32_bf16 v[136:139], a[44:47], v[116:119], v[20:23]\n"
"ds_read_b128 v[116:119], v235 offset:40960\n"
"v_mfma_f32_16x16x32_bf16 v[136:139], a[40:43], v[112:115], v[136:139]\n"
"ds_read_b128 v[112:115], v234 offset:40960\n"
"v_mfma_f32_16x16x32_bf16 v[136:139], a[36:39], v[108:111], v[136:139]\n"
"ds_read_b128 v[108:111], v233 offset:40960\n"
"v_mfma_f32_16x16x32_bf16 v[136:139], a[32:35], v[92:95], v[136:139]\n"
"ds_read_b128 v[92:95], v232 offset:40960\n"
"v_cvt_pk_bf16_f32 v208, v132, v133\n"
"v_mfma_f32_16x16x32_bf16 v[136:139], a[28:31], v[88:91], v[136:139]\n"
"ds_read_b128 v[88:91], v235 offset:32768\n"
"v_cvt_pk_bf16_f32 v209, v134, v135\n"
"v_mfma_f32_16x16x32_bf16 v[136:139], a[24:27], v[84:87], v[136:139]\n"
"ds_read_b128 v[84:87], v234 offset:32768\n"
"v_mfma_f32_16x16x32_bf16 v[136:139], v[204:207], v[68:71], v[136:139]\n"
"ds_read_b128 v[68:71], v233 offset:32768\n"
"v_mfma_f32_16x16x32_bf16 v[136:139], v[188:191], v[64:67], v[136:139]\n"
"ds_read_b128 v[64:67], v232 offset:32768\n"
"v_mfma_f32_16x16x32_bf16 v[136:139], v[184:187], v[60:63], v[136:139]\n"
"ds_read_b128 v[60:63], v235 offset:24576\n"
"v_mfma_f32_16x16x32_bf16 v[136:139], v[180:183], v[44:47], v[136:139]\n"
"ds_read_b128 v[44:47], v234 offset:24576\n"
"v_mfma_f32_16x16x32_bf16 v[136:139], v[164:167], v[40:43], v[136:139]\n"
"ds_read_b128 v[40:43], v233 offset:24576\n"
"v_mfma_f32_16x16x32_bf16 v[136:139], v[160:163], v[36:39], v[136:139]\n"
"ds_read_b128 v[36:39], v232 offset:24576\n"
"s_waitcnt lgkmcnt(8)\n"
"v_mfma_f32_16x16x32_bf16 v[132:135], a[76:79], v[116:119], v[0:3]\n"
"ds_write_b64 v244, v[208:209] offset:12416\n"
"v_mfma_f32_16x16x32_bf16 v[132:135], a[72:75], v[112:115], v[132:135]\n"
"s_add_u32 s54, s26, s36\n"
"s_addc_u32 s55, s27, 0\n"
"v_mfma_f32_16x16x32_bf16 v[132:135], a[68:71], v[108:111], v[132:135]\n"
"s_sub_i32 s84, s84, s19\n"
"v_mfma_f32_16x16x32_bf16 v[132:135], a[64:67], v[92:95], v[132:135]\n"
"v_cvt_pk_bf16_f32 v210, v136, v137\n"
"s_waitcnt lgkmcnt(5)\n"
"v_mfma_f32_16x16x32_bf16 v[132:135], a[60:63], v[88:91], v[132:135]\n"
"v_cvt_pk_bf16_f32 v211, v138, v139\n"
"v_mfma_f32_16x16x32_bf16 v[132:135], a[56:59], v[84:87], v[132:135]\n"
"s_add_u32 m0, s31, 0x14f00\n"
"v_mfma_f32_16x16x32_bf16 v[132:135], a[52:55], v[68:71], v[132:135]\n"
"ds_read_b128 v[216:219], v246 offset:0\n"
"v_mfma_f32_16x16x32_bf16 v[132:135], a[48:51], v[64:67], v[132:135]\n"
"ds_read_b128 v[220:223], v246 offset:1024\n"
"s_waitcnt lgkmcnt(3)\n"
"v_mfma_f32_16x16x32_bf16 v[132:135], a[252:255], v[60:63], v[132:135]\n"
"ds_read_b128 v[224:227], v246 offset:2048\n"
"v_mfma_f32_16x16x32_bf16 v[132:135], a[248:251], v[44:47], v[132:135]\n"
"ds_write_b64 v244, v[210:211] offset:12424\n"
"v_mfma_f32_16x16x32_bf16 v[132:135], a[244:247], v[40:43], v[132:135]\n"
"global_load_lds_dwordx4 v238, s[52:53] offset:256 nt\n"
"v_mfma_f32_16x16x32_bf16 v[132:135], a[240:243], v[36:39], v[132:135]\n"
"s_max_i32 s88, s88, s84\n"
"v_mfma_f32_16x16x32_bf16 v[136:139], a[80:83], v[36:39], v[4:7]\n"
"v_add_u32 v247, s94, v247\n"
"v_mfma_f32_16x16x32_bf16 v[136:139], a[84:87], v[40:43], v[136:139]\n"
"v_min_u32 v247, v247, v231\n"
"v_mfma_f32_16x16x32_bf16 v[136:139], a[88:91], v[44:47], v[136:139]\n"
"v_add_u32 v231, s93, v252\n"
"v_mfma_f32_16x16x32_bf16 v[136:139], a[92:95], v[60:63], v[136:139]\n"
"v_cvt_pk_bf16_f32 v208, v132, v133\n"
"v_add_u32 v252, s94, v252\n"
"v_mfma_f32_16x16x32_bf16 v[136:139], a[96:99], v[64:67], v[136:139]\n"
"v_cvt_pk_bf16_f32 v209, v134, v135\n"
"v_mfma_f32_16x16x32_bf16 v[136:139], a[100:103], v[68:71], v[136:139]\n"
"v_min_u32 v252, v252, v231\n"
"v_mfma_f32_16x16x32_bf16 v[136:139], a[104:107], v[84:87], v[136:139]\n"
"v_add_u32 v231, s93, v253\n"
"v_mfma_f32_16x16x32_bf16 v[136:139], a[108:111], v[88:91], v[136:139]\n"
"s_waitcnt lgkmcnt(1)\n"
"v_add_u32 v253, s94, v253\n"
"v_mfma_f32_16x16x32_bf16 v[136:139], a[112:115], v[92:95], v[136:139]\n"
"v_min_u32 v253, v253, v231\n"
"v_mfma_f32_16x16x32_bf16 v[136:139], a[116:119], v[108:111], v[136:139]\n"
"global_store_dwordx4 v247, v[216:219], s[54:55] offset:0 sc1\n"
"ds_write_b64 v244, v[208:209] offset:24576\n"
"v_mfma_f32_16x16x32_bf16 v[136:139], a[120:123], v[112:115], v[136:139]\n"
"v_mfma_f32_16x16x32_bf16 v[136:139], a[124:127], v[116:119], v[136:139]\n"
"v_mfma_f32_16x16x32_bf16 v[132:135], a[172:175], v[116:119], v[8:11]\n"
"v_mfma_f32_16x16x32_bf16 v[132:135], a[168:171], v[112:115], v[132:135]\n"
"v_mfma_f32_16x16x32_bf16 v[132:135], a[164:167], v[108:111], v[132:135]\n"
"v_mfma_f32_16x16x32_bf16 v[132:135], a[160:163], v[92:95], v[132:135]\n"
"v_cvt_pk_bf16_f32 v210, v136, v137\n"
"v_mfma_f32_16x16x32_bf16 v[132:135], a[156:159], v[88:91], v[132:135]\n"
"v_cvt_pk_bf16_f32 v211, v138, v139\n"
"v_mfma_f32_16x16x32_bf16 v[132:135], a[152:155], v[84:87], v[132:135]\n"
"v_mfma_f32_16x16x32_bf16 v[132:135], a[148:151], v[68:71], v[132:135]\n"
"v_mfma_f32_16x16x32_bf16 v[132:135], a[144:147], v[64:67], v[132:135]\n"
"v_mfma_f32_16x16x32_bf16 v[132:135], a[140:143], v[60:63], v[132:135]\n"
"v_mfma_f32_16x16x32_bf16 v[132:135], a[136:139], v[44:47], v[132:135]\n"
"global_store_dwordx4 v252, v[220:223], s[54:55] offset:0 sc1\n"
"ds_write_b64 v244, v[210:211] offset:24584\n"
"v_mfma_f32_16x16x32_bf16 v[132:135], a[132:135], v[40:43], v[132:135]\n"
"s_add_u32 m0, s31, 0x16e00\n"
"v_mfma_f32_16x16x32_bf16 v[132:135], a[128:131], v[36:39], v[132:135]\n"
"global_load_lds_dwordx4 v238, s[52:53] offset:512 nt\n"
"v_mfma_f32_16x16x32_bf16 v[136:139], a[176:179], v[36:39], v[12:15]\n"
"s_bitcmp1_b32 s92, 4\n"
"s_cselect_b32 s93, 0x6000, 0\n"
"v_mfma_f32_16x16x32_bf16 v[136:139], a[180:183], v[40:43], v[136:139]\n"
"s_sub_u32 s94, s93, 0xc000\n"
"v_mfma_f32_16x16x32_bf16 v[136:139], a[184:187], v[44:47], v[136:139]\n"
"v_add_u32 v231, s93, v237\n"
"v_mfma_f32_16x16x32_bf16 v[136:139], a[188:191], v[60:63], v[136:139]\n"
"v_cvt_pk_bf16_f32 v212, v132, v133\n"
"v_mfma_f32_16x16x32_bf16 v[136:139], a[192:195], v[64:67], v[136:139]\n"
"v_mov_b32 v242, 11\n"
"v_mfma_f32_16x16x32_bf16 v[136:139], a[196:199], v[68:71], v[136:139]\n"
"v_cvt_pk_bf16_f32 v213, v134, v135\n"
"v_mfma_f32_16x16x32_bf16 v[136:139], a[200:203], v[84:87], v[136:139]\n"
"s_waitcnt lgkmcnt(2)\n"
"ds_write_b32 v240, v242\n"
"v_mfma_f32_16x16x32_bf16 v[136:139], a[204:207], v[88:91], v[136:139]\n"
"v_add_u32 v237, s94, v237\n"
"v_mfma_f32_16x16x32_bf16 v[136:139], a[208:211], v[92:95], v[136:139]\n"
"v_min_u32 v237, v237, v231\n"
"v_mfma_f32_16x16x32_bf16 v[136:139], a[212:215], v[108:111], v[136:139]\n"
"v_add_u32 v231, s93, v238\n"
"v_mfma_f32_16x16x32_bf16 v[136:139], a[216:219], v[112:115], v[136:139]\n"
"global_store_dwordx4 v253, v[224:227], s[54:55] offset:0 sc1\n"
"ds_write_b64 v245, v[212:213] offset:24576\n"
"v_mfma_f32_16x16x32_bf16 v[136:139], a[220:223], v[116:119], v[136:139]\n"
"v_add_u32 v238, s94, v238\n"
"v_mfma_f32_16x16x32_bf16 v[132:135], v[156:159], v[116:119], v[16:19]\n"
"v_min_u32 v238, v238, v231\n"
"v_mfma_f32_16x16x32_bf16 v[132:135], v[140:143], v[112:115], v[132:135]\n"
"v_mfma_f32_16x16x32_bf16 v[132:135], a[20:23], v[108:111], v[132:135]\n"
"ds_read_b32 v243, v241\n"
"v_mfma_f32_16x16x32_bf16 v[132:135], a[16:19], v[92:95], v[132:135]\n"
"v_cvt_pk_bf16_f32 v214, v136, v137\n"
"v_mfma_f32_16x16x32_bf16 v[132:135], a[12:15], v[88:91], v[132:135]\n"
"v_cvt_pk_bf16_f32 v215, v138, v139\n"
"v_mfma_f32_16x16x32_bf16 v[132:135], a[8:11], v[84:87], v[132:135]\n"
"v_mfma_f32_16x16x32_bf16 v[132:135], a[4:7], v[68:71], v[132:135]\n"
"s_waitcnt lgkmcnt(0)\n"
"v_cmp_gt_u32 s[56:57], 11, v243\n"
"v_mfma_f32_16x16x32_bf16 v[132:135], a[0:3], v[64:67], v[132:135]\n"
"s_add_u32 s52, s20, s40\n"
"s_addc_u32 s53, s21, 0\n"
"v_mfma_f32_16x16x32_bf16 v[132:135], a[236:239], v[60:63], v[132:135]\n"
"s_or_b64 s[56:57], s[56:57], vcc\n"
"s_cbranch_scc1 LB11\n"
"LB11R:\n"
"v_mfma_f32_16x16x32_bf16 v[132:135], a[232:235], v[44:47], v[132:135]\n"
"ds_write_b64 v245, v[214:215] offset:24584\n"
"v_mfma_f32_16x16x32_bf16 v[132:135], a[228:231], v[40:43], v[132:135]\n"
"s_add_u32 m0, s31, 0x0\n"
"v_mfma_f32_16x16x32_bf16 v[132:135], a[224:227], v[36:39], v[132:135]\n"
"global_load_lds_dwordx4 v237, s[52:53] offset:0 nt\n"
"v_mfma_f32_16x16x32_bf16 v[136:139], v[160:163], v[36:39], v[20:23]\n"
"ds_read_b128 v[36:39], v232 offset:24832\n"
"v_mfma_f32_16x16x32_bf16 v[136:139], v[164:167], v[40:43], v[136:139]\n"
"ds_read_b128 v[40:43], v233 offset:24832\n"
"v_mfma_f32_16x16x32_bf16 v[136:139], v[180:183], v[44:47], v[136:139]\n"
"ds_read_b128 v[44:47], v234 offset:24832\n"
"v_mfma_f32_16x16x32_bf16 v[136:139], v[184:187], v[60:63], v[136:139]\n"
"ds_read_b128 v[60:63], v235 offset:24832\n"
"v_cvt_pk_bf16_f32 v208, v132, v133\n"
"v_mfma_f32_16x16x32_bf16 v[136:139], v[188:191], v[64:67], v[136:139]\n"
"ds_read_b128 v[64:67], v232 offset:33024\n"
"v_cvt_pk_bf16_f32 v209, v134, v135\n"
"v_mfma_f32_16x16x32_bf16 v[136:139], v[204:207], v[68:71], v[136:139]\n"
"ds_read_b128 v[68:71], v233 offset:33024\n"
"v_mfma_f32_16x16x32_bf16 v[136:139], a[24:27], v[84:87], v[136:139]\n"
"ds_read_b128 v[84:87], v234 offset:33024\n"
"v_mfma_f32_16x16x32_bf16 v[136:139], a[28:31], v[88:91], v[136:139]\n"
"ds_read_b128 v[88:91], v235 offset:33024\n"
"v_mfma_f32_16x16x32_bf16 v[136:139], a[32:35], v[92:95], v[136:139]\n"
"ds_read_b128 v[92:95], v232 offset:41216\n"
"v_mfma_f32_16x16x32_bf16 v[136:139], a[36:39], v[108:111], v[136:139]\n"
"ds_read_b128 v[108:111], v233 offset:41216\n"
"ds_write_b64 v244, v[208:209] offset:24704\n"
"v_mfma_f32_16x16x32_bf16 v[136:139], a[40:43], v[112:115], v[136:139]\n"
"ds_read_b128 v[112:115], v234 offset:41216\n"
"v_mfma_f32_16x16x32_bf16 v[136:139], a[44:47], v[116:119], v[136:139]\n"
"ds_read_b128 v[116:119], v235 offset:41216\n"
"s_waitcnt lgkmcnt(9)\n"
"v_mfma_f32_16x16x32_bf16 v[132:135], a[240:243], v[36:39], v[0:3]\n"
"v_add_u32 v232, 0xc000, v232\n"
"v_mfma_f32_16x16x32_bf16 v[132:135], a[244:247], v[40:43], v[132:135]\n"
"v_add_u32 v233, 0xc000, v233\n"
"v_mfma_f32_16x16x32_bf16 v[132:135], a[248:251], v[44:47], v[132:135]\n"
"v_add_u32 v234, 0xc000, v234\n"
"v_mfma_f32_16x16x32_bf16 v[132:135], a[252:255], v[60:63], v[132:135]\n"
"v_add_u32 v235, 0xc000, v235\n"
"s_waitcnt lgkmcnt(5)\n"
"v_mfma_f32_16x16x32_bf16 v[132:135], a[48:51], v[64:67], v[132:135]\n"
"v_cvt_pk_bf16_f32 v210, v136, v137\n"
"v_mfma_f32_16x16x32_bf16 v[132:135], a[52:55], v[68:71], v[132:135]\n"
"v_cvt_pk_bf16_f32 v211, v138, v139\n"
"v_mfma_f32_16x16x32_bf16 v[132:135], a[56:59], v[84:87], v[132:135]\n"
"ds_read_b128 v[216:219], v246 offset:12288\n"
"v_mfma_f32_16x16x32_bf16 v[132:135], a[60:63], v[88:91], v[132:135]\n"
"ds_read_b128 v[220:223], v246 offset:13312\n"
"s_waitcnt lgkmcnt(2)\n"
"v_mfma_f32_16x16x32_bf16 v[132:135], a[64:67], v[92:95], v[132:135]\n"
"ds_read_b128 v[224:227], v246 offset:14336\n"
"v_mfma_f32_16x16x32_bf16 v[132:135], a[68:71], v[108:111], v[132:135]\n"
"s_add_u32 m0, s31, 0x1f00\n"
"v_mfma_f32_16x16x32_bf16 v[132:135], a[72:75], v[112:115], v[132:135]\n"
"ds_write_b64 v244, v[210:211] offset:24712\n"
"v_mfma_f32_16x16x32_bf16 v[132:135], a[76:79], v[116:119], v[132:135]\n"
"global_load_lds_dwordx4 v237, s[52:53] offset:256 nt\n"
"v_mfma_f32_16x16x32_bf16 v[136:139], a[124:127], v[116:119], v[4:7]\n"
"v_lshlrev_b32 v0, 16, v72\n"
"v_mfma_f32_16x16x32_bf16 v[136:139], a[120:123], v[112:115], v[136:139]\n"
"v_and_b32 v1, 0xffff0000, v72\n"
"v_mfma_f32_16x16x32_bf16 v[136:139], a[116:119], v[108:111], v[136:139]\n"
"v_lshlrev_b32 v2, 16, v73\n"
"v_mfma_f32_16x16x32_bf16 v[136:139], a[112:115], v[92:95], v[136:139]\n"
"v_cvt_pk_bf16_f32 v208, v132, v133\n"
"v_mfma_f32_16x16x32_bf16 v[136:139], a[108:111], v[88:91], v[136:139]\n"
"v_cvt_pk_bf16_f32 v209, v134, v135\n"
"v_mfma_f32_16x16x32_bf16 v[136:139], a[104:107], v[84:87], v[136:139]\n"
"v_and_b32 v3, 0xffff0000, v73\n"
"v_mfma_f32_16x16x32_bf16 v[136:139], a[100:103], v[68:71], v[136:139]\n"
"v_lshlrev_b32 v4, 16, v74\n"
"v_mfma_f32_16x16x32_bf16 v[136:139], a[96:99], v[64:67], v[136:139]\n"
"s_waitcnt lgkmcnt(1)\n"
"v_mfma_f32_16x16x32_bf16 v[136:139], a[92:95], v[60:63], v[136:139]\n"
"v_and_b32 v5, 0xffff0000, v74\n"
"v_mfma_f32_16x16x32_bf16 v[136:139], a[88:91], v[44:47], v[136:139]\n"
"global_store_dwordx4 v247, v[216:219], s[54:55] offset:768 sc1\n"
"ds_write_b64 v244, v[208:209] offset:0\n"
"v_mfma_f32_16x16x32_bf16 v[136:139], a[84:87], v[40:43], v[136:139]\n"
"v_lshlrev_b32 v6, 16, v75\n"
"v_mfma_f32_16x16x32_bf16 v[136:139], a[80:83], v[36:39], v[136:139]\n"
"v_and_b32 v7, 0xffff0000, v75\n"
"v_mfma_f32_16x16x32_bf16 v[132:135], a[128:131], v[36:39], v[8:11]\n"
"s_add_u32 s83, s83, s86\n"
"v_mfma_f32_16x16x32_bf16 v[132:135], a[132:135], v[40:43], v[132:135]\n"
"s_sub_i32 s88, s88, 6\n"
"v_mfma_f32_16x16x32_bf16 v[132:135], a[136:139], v[44:47], v[132:135]\n"
"s_max_i32 s88, s88, 0\n"
"v_mfma_f32_16x16x32_bf16 v[132:135], a[140:143], v[60:63], v[132:135]\n"
"v_cvt_pk_bf16_f32 v210, v136, v137\n"
"v_mfma_f32_16x16x32_bf16 v[132:135], a[144:147], v[64:67], v[132:135]\n"
"v_cvt_pk_bf16_f32 v211, v138, v139\n"
"v_mfma_f32_16x16x32_bf16 v[132:135], a[148:151], v[68:71], v[132:135]\n"
"v_lshlrev_b32 v8, 16, v76\n"
"v_mfma_f32_16x16x32_bf16 v[132:135], a[152:155], v[84:87], v[132:135]\n"
"v_and_b32 v9, 0xffff0000, v76\n"
"v_mfma_f32_16x16x32_bf16 v[132:135], a[156:159], v[88:91], v[132:135]\n"
"v_lshlrev_b32 v10, 16, v77\n"
"v_mfma_f32_16x16x32_bf16 v[132:135], a[160:163], v[92:95], v[132:135]\n"
"v_and_b32 v11, 0xffff0000, v77\n"
"v_mfma_f32_16x16x32_bf16 v[132:135], a[164:167], v[108:111], v[132:135]\n"
"s_memrealtime s[84:85]\n"
"v_mfma_f32_16x16x32_bf16 v[132:135], a[168:171], v[112:115], v[132:135]\n"
"global_store_dwordx4 v252, v[220:223], s[54:55] offset:768 sc1\n"
"ds_write_b64 v244, v[210:211] offset:8\n"
"v_mfma_f32_16x16x32_bf16 v[132:135], a[172:175], v[116:119], v[132:135]\n"
"s_add_u32 m0, s31, 0x3e00\n"
"v_mfma_f32_16x16x32_bf16 v[136:139], a[220:223], v[116:119], v[12:15]\n"
"global_load_lds_dwordx4 v237, s[52:53] offset:512 nt\n"
"v_mfma_f32_16x16x32_bf16 v[136:139], a[216:219], v[112:115], v[136:139]\n"
"v_mfma_f32_16x16x32_bf16 v[136:139], a[212:215], v[108:111], v[136:139]\n"
"v_mfma_f32_16x16x32_bf16 v[136:139], a[208:211], v[92:95], v[136:139]\n"
"v_cvt_pk_bf16_f32 v212, v132, v133\n"
"v_mfma_f32_16x16x32_bf16 v[136:139], a[204:207], v[88:91], v[136:139]\n"
"v_mov_b32 v242, 12\n"
"v_mfma_f32_16x16x32_bf16 v[136:139], a[200:203], v[84:87], v[136:139]\n"
"v_cvt_pk_bf16_f32 v213, v134, v135\n"
"v_mfma_f32_16x16x32_bf16 v[136:139], a[196:199], v[68:71], v[136:139]\n"
"s_waitcnt vmcnt(19) lgkmcnt(2)\n"
"ds_write_b32 v240, v242\n"
"v_mfma_f32_16x16x32_bf16 v[136:139], a[192:195], v[64:67], v[136:139]\n"
"v_lshlrev_b32 v12, 16, v78\n"
"v_mfma_f32_16x16x32_bf16 v[136:139], a[188:191], v[60:63], v[136:139]\n"
"v_and_b32 v13, 0xffff0000, v78\n"
"v_mfma_f32_16x16x32_bf16 v[136:139], a[184:187], v[44:47], v[136:139]\n"
"v_lshlrev_b32 v14, 16, v79\n"
"v_mfma_f32_16x16x32_bf16 v[136:139], a[180:183], v[40:43], v[136:139]\n"
"global_store_dwordx4 v253, v[224:227], s[54:55] offset:768 sc1\n"
"ds_write_b64 v245, v[212:213] offset:0\n"
"v_mfma_f32_16x16x32_bf16 v[136:139], a[176:179], v[36:39], v[136:139]\n"
"v_and_b32 v15, 0xffff0000, v79\n"
"v_mfma_f32_16x16x32_bf16 v[132:135], a[224:227], v[36:39], v[16:19]\n"
"v_mfma_f32_16x16x32_bf16 v[132:135], a[228:231], v[40:43], v[132:135]\n"
"v_mfma_f32_16x16x32_bf16 v[132:135], a[232:235], v[44:47], v[132:135]\n"
"ds_read_b32 v243, v241\n"
"v_mfma_f32_16x16x32_bf16 v[132:135], a[236:239], v[60:63], v[132:135]\n"
"v_cvt_pk_bf16_f32 v214, v136, v137\n"
"v_mfma_f32_16x16x32_bf16 v[132:135], a[0:3], v[64:67], v[132:135]\n"
"v_cvt_pk_bf16_f32 v215, v138, v139\n"
"v_mfma_f32_16x16x32_bf16 v[132:135], a[4:7], v[68:71], v[132:135]\n"
"v_lshlrev_b32 v16, 16, v80\n"
"v_mfma_f32_16x16x32_bf16 v[132:135], a[8:11], v[84:87], v[132:135]\n"
"s_waitcnt lgkmcnt(0)\n"
"v_cmp_gt_u32 s[56:57], 12, v243\n"
"v_mfma_f32_16x16x32_bf16 v[132:135], a[12:15], v[88:91], v[132:135]\n"
"s_sub_u32 s84, s84, s83\n"
"v_mfma_f32_16x16x32_bf16 v[132:135], a[16:19], v[92:95], v[132:135]\n"
"s_or_b64 s[56:57], s[56:57], vcc\n"
"s_cbranch_scc1 LB12\n"
"LB12R:\n"
"v_mfma_f32_16x16x32_bf16 v[132:135], a[20:23], v[108:111], v[132:135]\n"
"s_cmp_lt_i32 s84, s88\n"
"s_cselect_b64 vcc, -1, 0\n"
"v_mfma_f32_16x16x32_bf16 v[132:135], v[140:143], v[112:115], v[132:135]\n"
"ds_write_b64 v245, v[214:215] offset:8\n"
"v_mfma_f32_16x16x32_bf16 v[132:135], v[156:159], v[116:119], v[132:135]\n"
"v_and_b32 v17, 0xffff0000, v80\n"
"v_mfma_f32_16x16x32_bf16 v[136:139], a[44:47], v[116:119], v[20:23]\n"
"ds_read_b128 v[116:119], v235 offset:16384\n"
"v_mfma_f32_16x16x32_bf16 v[136:139], a[40:43], v[112:115], v[136:139]\n"
"ds_read_b128 v[112:115], v234 offset:16384\n"
"v_mfma_f32_16x16x32_bf16 v[136:139], a[36:39], v[108:111], v[136:139]\n"
"ds_read_b128 v[108:111], v233 offset:16384\n"
"v_mfma_f32_16x16x32_bf16 v[136:139], a[32:35], v[92:95], v[136:139]\n"
"ds_read_b128 v[92:95], v232 offset:16384\n"
"v_cvt_pk_bf16_f32 v208, v132, v133\n"
"v_mfma_f32_16x16x32_bf16 v[136:139], a[28:31], v[88:91], v[136:139]\n"
"ds_read_b128 v[88:91], v235 offset:8192\n"
"v_cvt_pk_bf16_f32 v209, v134, v135\n"
"v_mfma_f32_16x16x32_bf16 v[136:139], a[24:27], v[84:87], v[136:139]\n"
"ds_read_b128 v[84:87], v234 offset:8192\n"
"v_mfma_f32_16x16x32_bf16 v[136:139], v[204:207], v[68:71], v[136:139]\n"
"ds_read_b128 v[68:71], v233 offset:8192\n"
"v_mfma_f32_16x16x32_bf16 v[136:139], v[188:191], v[64:67], v[136:139]\n"
"ds_read_b128 v[64:67], v232 offset:8192\n"
"v_mfma_f32_16x16x32_bf16 v[136:139], v[184:187], v[60:63], v[136:139]\n"
"ds_read_b128 v[60:63], v235 offset:0\n"
"v_mfma_f32_16x16x32_bf16 v[136:139], v[180:183], v[44:47], v[136:139]\n"
"ds_read_b128 v[44:47], v234 offset:0\n"
"v_mfma_f32_16x16x32_bf16 v[136:139], v[164:167], v[40:43], v[136:139]\n"
"ds_read_b128 v[40:43], v233 offset:0\n"
"v_mfma_f32_16x16x32_bf16 v[136:139], v[160:163], v[36:39], v[136:139]\n"
"ds_read_b128 v[36:39], v232 offset:0\n"
"s_waitcnt lgkmcnt(8)\n"
"v_mfma_f32_16x16x32_bf16 v[132:135], a[76:79], v[116:119], v[0:3]\n"
"ds_write_b64 v244, v[208:209] offset:128\n"
"v_mfma_f32_16x16x32_bf16 v[132:135], a[72:75], v[112:115], v[132:135]\n"
"s_add_u32 m0, s31, 0x1000\n"
"v_mfma_f32_16x16x32_bf16 v[132:135], a[68:71], v[108:111], v[132:135]\n"
"global_load_lds_dwordx4 v238, s[52:53] offset:0 nt\n"
"v_mfma_f32_16x16x32_bf16 v[132:135], a[64:67], v[92:95], v[132:135]\n"
"v_lshlrev_b32 v18, 16, v81\n"
"v_cvt_pk_bf16_f32 v211, v138, v139\n"
"s_waitcnt lgkmcnt(5)\n"
"v_mfma_f32_16x16x32_bf16 v[132:135], a[60:63], v[88:91], v[132:135]\n"
"v_and_b32 v19, 0xffff0000, v81\n"
"v_mfma_f32_16x16x32_bf16 v[132:135], a[56:59], v[84:87], v[132:135]\n"
"v_lshlrev_b32 v20, 16, v82\n"
"v_mfma_f32_16x16x32_bf16 v[132:135], a[52:55], v[68:71], v[132:135]\n"
"v_and_b32 v21, 0xffff0000, v82\n"
"v_mfma_f32_16x16x32_bf16 v[132:135], a[48:51], v[64:67], v[132:135]\n"
"v_lshlrev_b32 v22, 16, v83\n"
"s_waitcnt lgkmcnt(1)\n"
"v_mfma_f32_16x16x32_bf16 v[132:135], a[252:255], v[60:63], v[132:135]\n"
"v_and_b32 v23, 0xffff0000, v83\n"
"v_mfma_f32_16x16x32_bf16 v[132:135], a[248:251], v[44:47], v[132:135]\n"
"v_cvt_pk_bf16_f32 v210, v136, v137\n"
"v_mfma_f32_16x16x32_bf16 v[132:135], a[244:247], v[40:43], v[132:135]\n"
"s_add_u32 s54, s26, s37\n"
"s_addc_u32 s55, s27, 0\n"
"v_mfma_f32_16x16x32_bf16 v[132:135], a[240:243], v[36:39], v[132:135]\n"
"ds_read_b128 v[216:219], v246 offset:24576\n"
"v_mfma_f32_16x16x32_bf16 v[136:139], a[80:83], v[36:39], v[4:7]\n"
"ds_read_b128 v[220:223], v246 offset:25600\n"
"v_mfma_f32_16x16x32_bf16 v[136:139], a[84:87], v[40:43], v[136:139]\n"
"ds_read_b128 v[224:227], v246 offset:26624\n"
"v_mfma_f32_16x16x32_bf16 v[136:139], a[88:91], v[44:47], v[136:139]\n"
"ds_write_b64 v244, v[210:211] offset:136\n"
"v_mfma_f32_16x16x32_bf16 v[136:139], a[92:95], v[60:63], v[136:139]\n"
"v_cvt_pk_bf16_f32 v208, v132, v133\n"
"v_mfma_f32_16x16x32_bf16 v[136:139], a[96:99], v[64:67], v[136:139]\n"
"v_cvt_pk_bf16_f32 v209, v134, v135\n"
"v_mfma_f32_16x16x32_bf16 v[136:139], a[100:103], v[68:71], v[136:139]\n"
"s_add_u32 m0, s31, 0x2f00\n"
"v_mfma_f32_16x16x32_bf16 v[136:139], a[104:107], v[84:87], v[136:139]\n"
"global_load_lds_dwordx4 v238, s[52:53] offset:256 nt\n"
"v_mfma_f32_16x16x32_bf16 v[136:139], a[108:111], v[88:91], v[136:139]\n"
"s_waitcnt lgkmcnt(1)\n"
"v_mfma_f32_16x16x32_bf16 v[136:139], a[112:115], v[92:95], v[136:139]\n"
"s_sub_i32 s84, s84, s19\n"
"v_mfma_f32_16x16x32_bf16 v[136:139], a[116:119], v[108:111], v[136:139]\n"
"global_store_dwordx4 v247, v[216:219], s[54:55] offset:0 sc1\n"
"ds_write_b64 v244, v[208:209] offset:12288\n"
"v_mfma_f32_16x16x32_bf16 v[136:139], a[120:123], v[112:115], v[136:139]\n"
"s_max_i32 s88, s88, s84\n"
"v_mfma_f32_16x16x32_bf16 v[136:139], a[124:127], v[116:119], v[136:139]\n"
"s_mov_b64 exec, s[4:5]\n"
"s_cbranch_execz LHT8\n"
"LHR8:\n"
"s_mov_b64 exec, -1\n"
"v_mfma_f32_16x16x32_bf16 v[132:135], a[172:175], v[116:119], v[8:11]\n"
"v_mfma_f32_16x16x32_bf16 v[132:135], a[168:171], v[112:115], v[132:135]\n"
"v_mfma_f32_16x16x32_bf16 v[132:135], a[164:167], v[108:111], v[132:135]\n"
"v_mfma_f32_16x16x32_bf16 v[132:135], a[160:163], v[92:95], v[132:135]\n"
"v_cvt_pk_bf16_f32 v210, v136, v137\n"
"v_mfma_f32_16x16x32_bf16 v[132:135], a[156:159], v[88:91], v[132:135]\n"
"v_cvt_pk_bf16_f32 v211, v138, v139\n"
"v_mfma_f32_16x16x32_bf16 v[132:135], a[152:155], v[84:87], v[132:135]\n"
"v_mfma_f32_16x16x32_bf16 v[132:135], a[148:151], v[68:71], v[132:135]\n"
"s_mov_b32 s87, 1\n"
"s_or_b32 s56, s51, s76\n"
"s_cmp_lg_u32 s56, 0\n"
"s_cbranch_scc1 LG1\n"
"v_cmp_ne_u64 s[58:59], s[80:81], v[24:25]\n"
"v_cmp_ne_u64 s[56:57], s[80:81], v[26:27]\n"
"s_mov_b32 s87, 0\n"
"s_nop 3\n"
"s_or_b64 s[56:57], s[56:57], s[58:59]\n"
"s_cbranch_scc1 LG1\n"
"v_mov_b32 v28, 0\n"
"v_mov_b32 v30, s80\n"
"v_mov_b32 v31, s81\n"
"s_mov_b32 s87, 1\n"
"s_mov_b64 exec, 1\n"
"global_store_dwordx2 v28, v[30:31], s[28:29] sc1\n"
"s_mov_b64 exec, -1\n"
"s_nop 4\n"
"LG1:\n"
"v_mfma_f32_16x16x32_bf16 v[132:135], a[144:147], v[64:67], v[132:135]\n"
"v_mfma_f32_16x16x32_bf16 v[132:135], a[140:143], v[60:63], v[132:135]\n"
"v_mfma_f32_16x16x32_bf16 v[132:135], a[136:139], v[44:47], v[132:135]\n"
"global_store_dwordx4 v252, v[220:223], s[54:55] offset:0 sc1\n"
"ds_write_b64 v244, v[210:211] offset:12296\n"
"v_mfma_f32_16x16x32_bf16 v[132:135], a[132:135], v[40:43], v[132:135]\n"
"v_mfma_f32_16x16x32_bf16 v[132:135], a[128:131], v[36:39], v[132:135]\n"
"v_mfma_f32_16x16x32_bf16 v[136:139], a[176:179], v[36:39], v[12:15]\n"
"s_add_u32 m0, s31, 0x4e00\n"
"v_mfma_f32_16x16x32_bf16 v[136:139], a[180:183], v[40:43], v[136:139]\n"
"global_load_lds_dwordx4 v238, s[52:53] offset:512 nt\n"
"v_mfma_f32_16x16x32_bf16 v[136:139], a[184:187], v[44:47], v[136:139]\n"
"v_mfma_f32_16x16x32_bf16 v[136:139], a[188:191], v[60:63], v[136:139]\n"
"v_cvt_pk_bf16_f32 v212, v132, v133\n"
"v_mfma_f32_16x16x32_bf16 v[136:139], a[192:195], v[64:67], v[136:139]\n"
"v_mov_b32 v242, 13\n"
"v_mfma_f32_16x16x32_bf16 v[136:139], a[196:199], v[68:71], v[136:139]\n"
"v_cvt_pk_bf16_f32 v213, v134, v135\n"
"v_mfma_f32_16x16x32_bf16 v[136:139], a[200:203], v[84:87], v[136:139]\n"
"s_waitcnt lgkmcnt(2)\n"
"ds_write_b32 v240, v242\n"
"v_mfma_f32_16x16x32_bf16 v[136:139], a[204:207], v[88:91], v[136:139]\n"
"LHT8:\n"
"s_cbranch_execz LHR8\n"
"v_mfma_f32_16x16x32_bf16 v[136:139], a[208:211], v[92:95], v[136:139]\n"
"v_mfma_f32_16x16x32_bf16 v[136:139], a[212:215], v[108:111], v[136:139]\n"
"v_mfma_f32_16x16x32_bf16 v[136:139], a[216:219], v[112:115], v[136:139]\n"
"global_store_dwordx4 v253, v[224:227], s[54:55] offset:0 sc1\n"
"ds_write_b64 v245, v[212:213] offset:12288\n"
"v_mfma_f32_16x16x32_bf16 v[136:139], a[220:223], v[116:119], v[136:139]\n"
"v_mfma_f32_16x16x32_bf16 v[132:135], v[156:159], v[116:119], v[16:19]\n"
"v_mfma_f32_16x16x32_bf16 v[132:135], v[140:143], v[112:115], v[132:135]\n"
"v_mfma_f32_16x16x32_bf16 v[132:135], a[20:23], v[108:111], v[132:135]\n"
"ds_read_b32 v243, v241\n"
"v_mfma_f32_16x16x32_bf16 v[132:135], a[16:19], v[92:95], v[132:135]\n"
"v_cvt_pk_bf16_f32 v214, v136, v137\n"
"v_mfma_f32_16x16x32_bf16 v[132:135], a[12:15], v[88:91], v[132:135]\n"
"v_cvt_pk_bf16_f32 v215, v138, v139\n"
"v_mfma_f32_16x16x32_bf16 v[132:135], a[8:11], v[84:87], v[132:135]\n"
"v_mfma_f32_16x16x32_bf16 v[132:135], a[4:7], v[68:71], v[132:135]\n"
"s_waitcnt lgkmcnt(0)\n"
"v_cmp_gt_u32 s[56:57], 13, v243\n"
"v_mfma_f32_16x16x32_bf16 v[132:135], a[0:3], v[64:67], v[132:135]\n"
"v_mfma_f32_16x16x32_bf16 v[132:135], a[236:239], v[60:63], v[132:135]\n"
"s_or_b64 s[56:57], s[56:57], vcc\n"
"s_cbranch_scc1 LB13\n"
"LB13R:\n"
"v_mfma_f32_16x16x32_bf16 v[132:135], a[232:235], v[44:47], v[132:135]\n"
"ds_write_b64 v245, v[214:215] offset:12296\n"
"v_mfma_f32_16x16x32_bf16 v[132:135], a[228:231], v[40:43], v[132:135]\n"
"v_mfma_f32_16x16x32_bf16 v[132:135], a[224:227], v[36:39], v[132:135]\n"
"v_mfma_f32_16x16x32_bf16 v[136:139], v[160:163], v[36:39], v[20:23]\n"
"ds_read_b128 v[36:39], v232 offset:256\n"
"v_mfma_f32_16x16x32_bf16 v[136:139], v[164:167], v[40:43], v[136:139]\n"
"ds_read_b128 v[40:43], v233 offset:256\n"
"v_mfma_f32_16x16x32_bf16 v[136:139], v[180:183], v[44:47], v[136:139]\n"
"ds_read_b128 v[44:47], v234 offset:256\n"
"v_mfma_f32_16x16x32_bf16 v[136:139], v[184:187], v[60:63], v[136:139]\n"
"ds_read_b128 v[60:63], v235 offset:256\n"
"v_cvt_pk_bf16_f32 v208, v132, v133\n"
"v_mfma_f32_16x16x32_bf16 v[136:139], v[188:191], v[64:67], v[136:139]\n"
"ds_read_b128 v[64:67], v232 offset:8448\n"
"v_cvt_pk_bf16_f32 v209, v134, v135\n"
"v_mfma_f32_16x16x32_bf16 v[136:139], v[204:207], v[68:71], v[136:139]\n"
"ds_read_b128 v[68:71], v233 offset:8448\n"
"v_mfma_f32_16x16x32_bf16 v[136:139], a[24:27], v[84:87], v[136:139]\n"
"ds_read_b128 v[84:87], v234 offset:8448\n"
"v_mfma_f32_16x16x32_bf16 v[136:139], a[28:31], v[88:91], v[136:139]\n"
"ds_read_b128 v[88:91], v235 offset:8448\n"
"v_mfma_f32_16x16x32_bf16 v[136:139], a[32:35], v[92:95], v[136:139]\n"
"ds_read_b128 v[92:95], v232 offset:16640\n"
"v_mfma_f32_16x16x32_bf16 v[136:139], a[36:39], v[108:111], v[136:139]\n"
"ds_read_b128 v[108:111], v233 offset:16640\n"
"v_mfma_f32_16x16x32_bf16 v[136:139], a[40:43], v[112:115], v[136:139]\n"
"ds_read_b128 v[112:115], v234 offset:16640\n"
"v_mfma_f32_16x16x32_bf16 v[136:139], a[44:47], v[116:119], v[136:139]\n"
"ds_read_b128 v[116:119], v235 offset:16640\n"
"s_waitcnt lgkmcnt(8)\n"
"v_mfma_f32_16x16x32_bf16 v[132:135], a[240:243], v[36:39], v[0:3]\n"
"ds_write_b64 v244, v[208:209] offset:12416\n"
"v_mfma_f32_16x16x32_bf16 v[132:135], a[244:247], v[40:43], v[132:135]\n"
"s_add_u32 s52, s20, s41\n"
"s_addc_u32 s53, s21, 0\n"
"v_mfma_f32_16x16x32_bf16 v[132:135], a[248:251], v[44:47], v[132:135]\n"
"s_add_u32 m0, s31, 0x6000\n"
"v_mfma_f32_16x16x32_bf16 v[132:135], a[252:255], v[60:63], v[132:135]\n"
"global_load_lds_dwordx4 v237, s[52:53] offset:0 nt\n"
"s_waitcnt lgkmcnt(5)\n"
"v_mfma_f32_16x16x32_bf16 v[132:135], a[48:51], v[64:67], v[132:135]\n"
"v_cvt_pk_bf16_f32 v210, v136, v137\n"
"v_mfma_f32_16x16x32_bf16 v[132:135], a[52:55], v[68:71], v[132:135]\n"
"v_cvt_pk_bf16_f32 v211, v138, v139\n"
"v_mfma_f32_16x16x32_bf16 v[132:135], a[56:59], v[84:87], v[132:135]\n"
"ds_read_b128 v[216:219], v246 offset:0\n"
"v_mfma_f32_16x16x32_bf16 v[132:135], a[60:63], v[88:91], v[132:135]\n"
"ds_read_b128 v[220:223], v246 offset:1024\n"
"s_waitcnt lgkmcnt(3)\n"
"v_mfma_f32_16x16x32_bf16 v[132:135], a[64:67], v[92:95], v[132:135]\n"
"ds_read_b128 v[224:227], v246 offset:2048\n"
"v_mfma_f32_16x16x32_bf16 v[132:135], a[68:71], v[108:111], v[132:135]\n"
"v_mfma_f32_16x16x32_bf16 v[132:135], a[72:75], v[112:115], v[132:135]\n"
"ds_write_b64 v244, v[210:211] offset:12424\n"
"v_mfma_f32_16x16x32_bf16 v[132:135], a[76:79], v[116:119], v[132:135]\n"
"v_mfma_f32_16x16x32_bf16 v[136:139], a[124:127], v[116:119], v[4:7]\n"
"s_add_u32 s83, s83, s86\n"
"v_mfma_f32_16x16x32_bf16 v[136:139], a[120:123], v[112:115], v[136:139]\n"
"s_sub_i32 s88, s88, 6\n"
"v_mfma_f32_16x16x32_bf16 v[136:139], a[116:119], v[108:111], v[136:139]\n"
"s_max_i32 s88, s88, 0\n"
"v_mfma_f32_16x16x32_bf16 v[136:139], a[112:115], v[92:95], v[136:139]\n"
"v_cvt_pk_bf16_f32 v208, v132, v133\n"
"v_mfma_f32_16x16x32_bf16 v[136:139], a[108:111], v[88:91], v[136:139]\n"
"v_cvt_pk_bf16_f32 v209, v134, v135\n"
"v_mfma_f32_16x16x32_bf16 v[136:139], a[104:107], v[84:87], v[136:139]\n"
"s_add_u32 m0, s31, 0x7f00\n"
"v_mfma_f32_16x16x32_bf16 v[136:139], a[100:103], v[68:71], v[136:139]\n"
"global_load_lds_dwordx4 v237, s[52:53] offset:256 nt\n"
"v_mfma_f32_16x16x32_bf16 v[136:139], a[96:99], v[64:67], v[136:139]\n"
"s_waitcnt lgkmcnt(1)\n"
"v_mfma_f32_16x16x32_bf16 v[136:139], a[92:95], v[60:63], v[136:139]\n"
"v_mfma_f32_16x16x32_bf16 v[136:139], a[88:91], v[44:47], v[136:139]\n"
"global_store_dwordx4 v247, v[216:219], s[54:55] offset:768 sc1\n"
"ds_write_b64 v244, v[208:209] offset:24576\n"
"v_mfma_f32_16x16x32_bf16 v[136:139], a[84:87], v[40:43], v[136:139]\n"
"v_mfma_f32_16x16x32_bf16 v[136:139], a[80:83], v[36:39], v[136:139]\n"
"v_mfma_f32_16x16x32_bf16 v[132:135], a[128:131], v[36:39], v[8:11]\n"
"v_mfma_f32_16x16x32_bf16 v[132:135], a[132:135], v[40:43], v[132:135]\n"
"v_mfma_f32_16x16x32_bf16 v[132:135], a[136:139], v[44:47], v[132:135]\n"
"v_mfma_f32_16x16x32_bf16 v[132:135], a[140:143], v[60:63], v[132:135]\n"
"v_cvt_pk_bf16_f32 v210, v136, v137\n"
"v_mfma_f32_16x16x32_bf16 v[132:135], a[144:147], v[64:67], v[132:135]\n"
"v_cvt_pk_bf16_f32 v211, v138, v139\n"
"v_mfma_f32_16x16x32_bf16 v[132:135], a[148:151], v[68:71], v[132:135]\n"
"v_mfma_f32_16x16x32_bf16 v[132:135], a[152:155], v[84:87], v[132:135]\n"
"v_mfma_f32_16x16x32_bf16 v[132:135], a[156:159], v[88:91], v[132:135]\n"
"v_mfma_f32_16x16x32_bf16 v[132:135], a[160:163], v[92:95], v[132:135]\n"
"v_mfma_f32_16x16x32_bf16 v[132:135], a[164:167], v[108:111], v[132:135]\n"
"s_memrealtime s[84:85]\n"
"v_mfma_f32_16x16x32_bf16 v[132:135], a[168:171], v[112:115], v[132:135]\n"
"global_store_dwordx4 v252, v[220:223], s[54:55] offset:768 sc1\n"
"ds_write_b64 v244, v[210:211] offset:24584\n"
"v_mfma_f32_16x16x32_bf16 v[132:135], a[172:175], v[116:119], v[132:135]\n"
"v_mfma_f32_16x16x32_bf16 v[136:139], a[220:223], v[116:119], v[12:15]\n"
"v_mfma_f32_16x16x32_bf16 v[136:139], a[216:219], v[112:115], v[136:139]\n"
"v_mfma_f32_16x16x32_bf16 v[136:139], a[212:215], v[108:111], v[136:139]\n"
"v_mfma_f32_16x16x32_bf16 v[136:139], a[208:211], v[92:95], v[136:139]\n"
"v_cvt_pk_bf16_f32 v212, v132, v133\n"
"v_mfma_f32_16x16x32_bf16 v[136:139], a[204:207], v[88:91], v[136:139]\n"
"v_mov_b32 v242, 14\n"
"v_mfma_f32_16x16x32_bf16 v[136:139], a[200:203], v[84:87], v[136:139]\n"
"v_cvt_pk_bf16_f32 v213, v134, v135\n"
"v_mfma_f32_16x16x32_bf16 v[136:139], a[196:199], v[68:71], v[136:139]\n"
"s_waitcnt vmcnt(17) lgkmcnt(2)\n"
"ds_write_b32 v240, v242\n"
"v_mfma_f32_16x16x32_bf16 v[136:139], a[192:195], v[64:67], v[136:139]\n"
"s_add_u32 m0, s31, 0x9e00\n"
"v_mfma_f32_16x16x32_bf16 v[136:139], a[188:191], v[60:63], v[136:139]\n"
"global_load_lds_dwordx4 v237, s[52:53] offset:512 nt\n"
"v_mfma_f32_16x16x32_bf16 v[136:139], a[184:187], v[44:47], v[136:139]\n"
"v_mfma_f32_16x16x32_bf16 v[136:139], a[180:183], v[40:43], v[136:139]\n"
"global_store_dwordx4 v253, v[224:227], s[54:55] offset:768 sc1\n"
"ds_write_b64 v245, v[212:213] offset:24576\n"
"v_mfma_f32_16x16x32_bf16 v[136:139], a[176:179], v[36:39], v[136:139]\n"
"s_bitcmp1_b32 s92, 3\n"
"s_cselect_b32 s93, 0x6000, 0\n"
"v_mfma_f32_16x16x32_bf16 v[132:135], a[224:227], v[36:39], v[16:19]\n"
"s_sub_u32 s94, s93, 0xc000\n"
"v_mfma_f32_16x16x32_bf16 v[132:135], a[228:231], v[40:43], v[132:135]\n"
"v_add_u32 v231, s93, v247\n"
"v_mfma_f32_16x16x32_bf16 v[132:135], a[232:235], v[44:47], v[132:135]\n"
"ds_read_b32 v243, v241\n"
"v_mfma_f32_16x16x32_bf16 v[132:135], a[236:239], v[60:63], v[132:135]\n"
"v_cvt_pk_bf16_f32 v214, v136, v137\n"
"v_mfma_f32_16x16x32_bf16 v[132:135], a[0:3], v[64:67], v[132:135]\n"
"v_cvt_pk_bf16_f32 v215, v138, v139\n"
"v_mfma_f32_16x16x32_bf16 v[132:135], a[4:7], v[68:71], v[132:135]\n"
"v_add_u32 v247, s94, v247\n"
"v_mfma_f32_16x16x32_bf16 v[132:135], a[8:11], v[84:87], v[132:135]\n"
"s_waitcnt lgkmcnt(0)\n"
"v_cmp_gt_u32 s[56:57], 14, v243\n"
"v_mfma_f32_16x16x32_bf16 v[132:135], a[12:15], v[88:91], v[132:135]\n"
"s_sub_u32 s84, s84, s83\n"
"v_mfma_f32_16x16x32_bf16 v[132:135], a[16:19], v[92:95], v[132:135]\n"
"s_or_b64 s[56:57], s[56:57], vcc\n"
"s_cbranch_scc1 LB14\n"
"LB14R:\n"
"v_mfma_f32_16x16x32_bf16 v[132:135], a[20:23], v[108:111], v[132:135]\n"
"s_cmp_lt_i32 s84, s88\n"
"s_cselect_b64 vcc, -1, 0\n"
"v_mfma_f32_16x16x32_bf16 v[132:135], v[140:143], v[112:115], v[132:135]\n"
"ds_write_b64 v245, v[214:215] offset:24584\n"
"v_mfma_f32_16x16x32_bf16 v[132:135], v[156:159], v[116:119], v[132:135]\n"
"v_min_u32 v247, v247, v231\n"
"v_mfma_f32_16x16x32_bf16 v[136:139], a[44:47], v[116:119], v[20:23]\n"
"ds_read_b128 v[116:119], v235 offset:40960\n"
"v_mfma_f32_16x16x32_bf16 v[136:139], a[40:43], v[112:115], v[136:139]\n"
"ds_read_b128 v[112:115], v234 offset:40960\n"
"v_mfma_f32_16x16x32_bf16 v[136:139], a[36:39], v[108:111], v[136:139]\n"
"ds_read_b128 v[108:111], v233 offset:40960\n"
"v_mfma_f32_16x16x32_bf16 v[136:139], a[32:35], v[92:95], v[136:139]\n"
"ds_read_b128 v[92:95], v232 offset:40960\n"
"v_cvt_pk_bf16_f32 v208, v132, v133\n"
"v_mfma_f32_16x16x32_bf16 v[136:139], a[28:31], v[88:91], v[136:139]\n"
"ds_read_b128 v[88:91], v235 offset:32768\n"
"v_cvt_pk_bf16_f32 v209, v134, v135\n"
"v_mfma_f32_16x16x32_bf16 v[136:139], a[24:27], v[84:87], v[136:139]\n"
"ds_read_b128 v[84:87], v234 offset:32768\n"
"v_mfma_f32_16x16x32_bf16 v[136:139], v[204:207], v[68:71], v[136:139]\n"
"ds_read_b128 v[68:71], v233 offset:32768\n"
"v_mfma_f32_16x16x32_bf16 v[136:139], v[188:191], v[64:67], v[136:139]\n"
"ds_read_b128 v[64:67], v232 offset:32768\n"
"v_mfma_f32_16x16x32_bf16 v[136:139], v[184:187], v[60:63], v[136:139]\n"
"ds_read_b128 v[60:63], v235 offset:24576\n"
"v_mfma_f32_16x16x32_bf16 v[136:139], v[180:183], v[44:47], v[136:139]\n"
"ds_read_b128 v[44:47], v234 offset:24576\n"
"v_mfma_f32_16x16x32_bf16 v[136:139], v[164:167], v[40:43], v[136:139]\n"
"ds_read_b128 v[40:43], v233 offset:24576\n"
"v_mfma_f32_16x16x32_bf16 v[136:139], v[160:163], v[36:39], v[136:139]\n"
"ds_read_b128 v[36:39], v232 offset:24576\n"
"s_waitcnt lgkmcnt(8)\n"
"v_mfma_f32_16x16x32_bf16 v[132:135], a[76:79], v[116:119], v[0:3]\n"
"ds_write_b64 v244, v[208:209] offset:24704\n"
"v_mfma_f32_16x16x32_bf16 v[132:135], a[72:75], v[112:115], v[132:135]\n"
"s_add_u32 m0, s31, 0x7000\n"
"v_mfma_f32_16x16x32_bf16 v[132:135], a[68:71], v[108:111], v[132:135]\n"
"global_load_lds_dwordx4 v238, s[52:53] offset:0 nt\n"
"v_mfma_f32_16x16x32_bf16 v[132:135], a[64:67], v[92:95], v[132:135]\n"
"v_cvt_pk_bf16_f32 v210, v136, v137\n"
"s_waitcnt lgkmcnt(5)\n"
"v_mfma_f32_16x16x32_bf16 v[132:135], a[60:63], v[88:91], v[132:135]\n"
"v_cvt_pk_bf16_f32 v211, v138, v139\n"
"v_mfma_f32_16x16x32_bf16 v[132:135], a[56:59], v[84:87], v[132:135]\n"
"s_add_u32 s54, s26, s38\n"
"s_addc_u32 s55, s27, 0\n"
"v_mfma_f32_16x16x32_bf16 v[132:135], a[52:55], v[68:71], v[132:135]\n"
"ds_read_b128 v[216:219], v246 offset:12288\n"
"v_mfma_f32_16x16x32_bf16 v[132:135], a[48:51], v[64:67], v[132:135]\n"
"ds_read_b128 v[220:223], v246 offset:13312\n"
"s_waitcnt lgkmcnt(3)\n"
"v_mfma_f32_16x16x32_bf16 v[132:135], a[252:255], v[60:63], v[132:135]\n"
"ds_read_b128 v[224:227], v246 offset:14336\n"
"v_mfma_f32_16x16x32_bf16 v[132:135], a[248:251], v[44:47], v[132:135]\n"
"ds_write_b64 v244, v[210:211] offset:24712\n"
"v_mfma_f32_16x16x32_bf16 v[132:135], a[244:247], v[40:43], v[132:135]\n"
"s_sub_i32 s84, s84, s19\n"
"v_mfma_f32_16x16x32_bf16 v[132:135], a[240:243], v[36:39], v[132:135]\n"
"s_max_i32 s88, s88, s84\n"
"v_mfma_f32_16x16x32_bf16 v[136:139], a[80:83], v[36:39], v[4:7]\n"
"v_add_u32 v231, s93, v252\n"
"v_mfma_f32_16x16x32_bf16 v[136:139], a[84:87], v[40:43], v[136:139]\n"
"v_add_u32 v252, s94, v252\n"
"v_mfma_f32_16x16x32_bf16 v[136:139], a[88:91], v[44:47], v[136:139]\n"
"v_min_u32 v252, v252, v231\n"
"v_mfma_f32_16x16x32_bf16 v[136:139], a[92:95], v[60:63], v[136:139]\n"
"v_cvt_pk_bf16_f32 v208, v132, v133\n"
"v_mfma_f32_16x16x32_bf16 v[136:139], a[96:99], v[64:67], v[136:139]\n"
"v_cvt_pk_bf16_f32 v209, v134, v135\n"
"v_mfma_f32_16x16x32_bf16 v[136:139], a[100:103], v[68:71], v[136:139]\n"
"v_add_u32 v231, s93, v253\n"
"v_mfma_f32_16x16x32_bf16 v[136:139], a[104:107], v[84:87], v[136:139]\n"
"v_add_u32 v253, s94, v253\n"
"v_mfma_f32_16x16x32_bf16 v[136:139], a[108:111], v[88:91], v[136:139]\n"
"s_waitcnt lgkmcnt(1)\n"
"v_mfma_f32_16x16x32_bf16 v[136:139], a[112:115], v[92:95], v[136:139]\n"
"v_min_u32 v253, v253, v231\n"
"v_mfma_f32_16x16x32_bf16 v[136:139], a[116:119], v[108:111], v[136:139]\n"
"global_store_dwordx4 v247, v[216:219], s[54:55] offset:0 sc1\n"
"ds_write_b64 v244, v[208:209] offset:0\n"
"v_mfma_f32_16x16x32_bf16 v[136:139], a[120:123], v[112:115], v[136:139]\n"
"s_add_u32 m0, s31, 0x8f00\n"
"v_mfma_f32_16x16x32_bf16 v[136:139], a[124:127], v[116:119], v[136:139]\n"
"global_load_lds_dwordx4 v238, s[52:53] offset:256 nt\n"
"v_mfma_f32_16x16x32_bf16 v[132:135], a[172:175], v[116:119], v[8:11]\n"
"v_mfma_f32_16x16x32_bf16 v[132:135], a[168:171], v[112:115], v[132:135]\n"
"v_mfma_f32_16x16x32_bf16 v[132:135], a[164:167], v[108:111], v[132:135]\n"
"v_mfma_f32_16x16x32_bf16 v[132:135], a[160:163], v[92:95], v[132:135]\n"
"v_cvt_pk_bf16_f32 v210, v136, v137\n"
"v_mfma_f32_16x16x32_bf16 v[132:135], a[156:159], v[88:91], v[132:135]\n"
"v_cvt_pk_bf16_f32 v211, v138, v139\n"
"v_mfma_f32_16x16x32_bf16 v[132:135], a[152:155], v[84:87], v[132:135]\n"
"v_mfma_f32_16x16x32_bf16 v[132:135], a[148:151], v[68:71], v[132:135]\n"
"v_mfma_f32_16x16x32_bf16 v[132:135], a[144:147], v[64:67], v[132:135]\n"
"v_mfma_f32_16x16x32_bf16 v[132:135], a[140:143], v[60:63], v[132:135]\n"
"v_mfma_f32_16x16x32_bf16 v[132:135], a[136:139], v[44:47], v[132:135]\n"
"global_store_dwordx4 v252, v[220:223], s[54:55] offset:0 sc1\n"
"ds_write_b64 v244, v[210:211] offset:8\n"
"v_mfma_f32_16x16x32_bf16 v[132:135], a[132:135], v[40:43], v[132:135]\n"
"v_mfma_f32_16x16x32_bf16 v[132:135], a[128:131], v[36:39], v[132:135]\n"
"v_mfma_f32_16x16x32_bf16 v[136:139], a[176:179], v[36:39], v[12:15]\n"
"v_mfma_f32_16x16x32_bf16 v[136:139], a[180:183], v[40:43], v[136:139]\n"
"v_mfma_f32_16x16x32_bf16 v[136:139], a[184:187], v[44:47], v[136:139]\n"
"v_mfma_f32_16x16x32_bf16 v[136:139], a[188:191], v[60:63], v[136:139]\n"
"v_cvt_pk_bf16_f32 v212, v132, v133\n"
"v_mfma_f32_16x16x32_bf16 v[136:139], a[192:195], v[64:67], v[136:139]\n"
"v_mov_b32 v242, 15\n"
"v_mfma_f32_16x16x32_bf16 v[136:139], a[196:199], v[68:71], v[136:139]\n"
"v_cvt_pk_bf16_f32 v213, v134, v135\n"
"v_mfma_f32_16x16x32_bf16 v[136:139], a[200:203], v[84:87], v[136:139]\n"
"s_waitcnt lgkmcnt(2)\n"
"ds_write_b32 v240, v242\n"
"v_mfma_f32_16x16x32_bf16 v[136:139], a[204:207], v[88:91], v[136:139]\n"
"v_mfma_f32_16x16x32_bf16 v[136:139], a[208:211], v[92:95], v[136:139]\n"
"v_mfma_f32_16x16x32_bf16 v[136:139], a[212:215], v[108:111], v[136:139]\n"
"v_mfma_f32_16x16x32_bf16 v[136:139], a[216:219], v[112:115], v[136:139]\n"
"global_store_dwordx4 v253, v[224:227], s[54:55] offset:0 sc1\n"
"ds_write_b64 v245, v[212:213] offset:0\n"
"v_mfma_f32_16x16x32_bf16 v[136:139], a[220:223], v[116:119], v[136:139]\n"
"s_add_u32 m0, s31, 0xae00\n"
"v_mfma_f32_16x16x32_bf16 v[132:135], v[156:159], v[116:119], v[16:19]\n"
"global_load_lds_dwordx4 v238, s[52:53] offset:512 nt\n"
"v_mfma_f32_16x16x32_bf16 v[132:135], v[140:143], v[112:115], v[132:135]\n"
"s_bitcmp1_b32 s92, 5\n"
"s_cselect_b32 s93, 0x6000, 0\n"
"v_mfma_f32_16x16x32_bf16 v[132:135], a[20:23], v[108:111], v[132:135]\n"
"ds_read_b32 v243, v241\n"
"v_mfma_f32_16x16x32_bf16 v[132:135], a[16:19], v[92:95], v[132:135]\n"
"v_cvt_pk_bf16_f32 v214, v136, v137\n"
"v_mfma_f32_16x16x32_bf16 v[132:135], a[12:15], v[88:91], v[132:135]\n"
"v_cvt_pk_bf16_f32 v215, v138, v139\n"
"v_mfma_f32_16x16x32_bf16 v[132:135], a[8:11], v[84:87], v[132:135]\n"
"s_sub_u32 s94, s93, 0xc000\n"
"v_mfma_f32_16x16x32_bf16 v[132:135], a[4:7], v[68:71], v[132:135]\n"
"s_waitcnt lgkmcnt(0)\n"
"v_cmp_gt_u32 s[56:57], 15, v243\n"
"v_mfma_f32_16x16x32_bf16 v[132:135], a[0:3], v[64:67], v[132:135]\n"
"v_add_u32 v231, s93, v237\n"
"v_mfma_f32_16x16x32_bf16 v[132:135], a[236:239], v[60:63], v[132:135]\n"
"s_or_b64 s[56:57], s[56:57], vcc\n"
"s_cbranch_scc1 LB15\n"
"LB15R:\n"
"v_mfma_f32_16x16x32_bf16 v[132:135], a[232:235], v[44:47], v[132:135]\n"
"ds_write_b64 v245, v[214:215] offset:8\n"
"v_mfma_f32_16x16x32_bf16 v[132:135], a[228:231], v[40:43], v[132:135]\n"
"v_add_u32 v237, s94, v237\n"
"v_mfma_f32_16x16x32_bf16 v[132:135], a[224:227], v[36:39], v[132:135]\n"
"v_min_u32 v237, v237, v231\n"
"v_mfma_f32_16x16x32_bf16 v[136:139], v[160:163], v[36:39], v[20:23]\n"
"ds_read_b128 v[36:39], v232 offset:24832\n"
"v_add_u32 v231, s93, v238\n"
"v_mfma_f32_16x16x32_bf16 v[136:139], v[164:167], v[40:43], v[136:139]\n"
"ds_read_b128 v[40:43], v233 offset:24832\n"
"v_add_u32 v238, s94, v238\n"
"v_mfma_f32_16x16x32_bf16 v[136:139], v[180:183], v[44:47], v[136:139]\n"
"ds_read_b128 v[44:47], v234 offset:24832\n"
"v_min_u32 v238, v238, v231\n"
"v_mfma_f32_16x16x32_bf16 v[136:139], v[184:187], v[60:63], v[136:139]\n"
"ds_read_b128 v[60:63], v235 offset:24832\n"
"v_cvt_pk_bf16_f32 v208, v132, v133\n"
"v_mfma_f32_16x16x32_bf16 v[136:139], v[188:191], v[64:67], v[136:139]\n"
"ds_read_b128 v[64:67], v232 offset:33024\n"
"v_cvt_pk_bf16_f32 v209, v134, v135\n"
"v_mfma_f32_16x16x32_bf16 v[136:139], v[204:207], v[68:71], v[136:139]\n"
"ds_read_b128 v[68:71], v233 offset:33024\n"
"v_mfma_f32_16x16x32_bf16 v[136:139], a[24:27], v[84:87], v[136:139]\n"
"ds_read_b128 v[84:87], v234 offset:33024\n"
"v_mfma_f32_16x16x32_bf16 v[136:139], a[28:31], v[88:91], v[136:139]\n"
"ds_read_b128 v[88:91], v235 offset:33024\n"
"v_mfma_f32_16x16x32_bf16 v[136:139], a[32:35], v[92:95], v[136:139]\n"
"ds_read_b128 v[92:95], v232 offset:41216\n"
"v_mfma_f32_16x16x32_bf16 v[136:139], a[36:39], v[108:111], v[136:139]\n"
"ds_read_b128 v[108:111], v233 offset:41216\n"
"ds_write_b64 v244, v[208:209] offset:128\n"
"v_mfma_f32_16x16x32_bf16 v[136:139], a[40:43], v[112:115], v[136:139]\n"
"ds_read_b128 v[112:115], v234 offset:41216\n"
"v_mfma_f32_16x16x32_bf16 v[136:139], a[44:47], v[116:119], v[136:139]\n"
"ds_read_b128 v[116:119], v235 offset:41216\n"
"s_waitcnt lgkmcnt(9)\n"
"v_mfma_f32_16x16x32_bf16 v[132:135], a[240:243], v[36:39], v[0:3]\n"
"v_add_u32 v232, 0xffff4000, v232\n"
"v_mfma_f32_16x16x32_bf16 v[132:135], a[244:247], v[40:43], v[132:135]\n"
"v_add_u32 v233, 0xffff4000, v233\n"
"v_mfma_f32_16x16x32_bf16 v[132:135], a[248:251], v[44:47], v[132:135]\n"
"v_add_u32 v234, 0xffff4000, v234\n"
"v_mfma_f32_16x16x32_bf16 v[132:135], a[252:255], v[60:63], v[132:135]\n"
"v_add_u32 v235, 0xffff4000, v235\n"
"s_waitcnt lgkmcnt(5)\n"
"v_mfma_f32_16x16x32_bf16 v[132:135], a[48:51], v[64:67], v[132:135]\n"
"s_add_u32 s52, s20, s42\n"
"s_addc_u32 s53, s21, 0\n"
"v_mfma_f32_16x16x32_bf16 v[132:135], a[52:55], v[68:71], v[132:135]\n"
"s_add_u32 m0, s31, 0xc000\n"
"v_mfma_f32_16x16x32_bf16 v[132:135], a[56:59], v[84:87], v[132:135]\n"
"global_load_lds_dwordx4 v237, s[52:53] offset:0 nt\n"
"v_mfma_f32_16x16x32_bf16 v[132:135], a[60:63], v[88:91], v[132:135]\n"
"v_cvt_pk_bf16_f32 v210, v136, v137\n"
"s_waitcnt lgkmcnt(0)\n"
"v_mfma_f32_16x16x32_bf16 v[132:135], a[64:67], v[92:95], v[132:135]\n"
"v_cvt_pk_bf16_f32 v211, v138, v139\n"
"v_mfma_f32_16x16x32_bf16 v[132:135], a[68:71], v[108:111], v[132:135]\n"
"ds_read_b128 v[216:219], v246 offset:24576\n"
"v_mfma_f32_16x16x32_bf16 v[132:135], a[72:75], v[112:115], v[132:135]\n"
"ds_read_b128 v[220:223], v246 offset:25600\n"
"v_mfma_f32_16x16x32_bf16 v[132:135], a[76:79], v[116:119], v[132:135]\n"
"ds_read_b128 v[224:227], v246 offset:26624\n"
"v_mfma_f32_16x16x32_bf16 v[136:139], a[124:127], v[116:119], v[4:7]\n"
"v_lshlrev_b32 v0, 16, v96\n"
"v_mfma_f32_16x16x32_bf16 v[136:139], a[120:123], v[112:115], v[136:139]\n"
"ds_write_b64 v244, v[210:211] offset:136\n"
"v_mfma_f32_16x16x32_bf16 v[136:139], a[116:119], v[108:111], v[136:139]\n"
"v_and_b32 v1, 0xffff0000, v96\n"
"v_mfma_f32_16x16x32_bf16 v[136:139], a[112:115], v[92:95], v[136:139]\n"
"v_cvt_pk_bf16_f32 v208, v132, v133\n"
"v_mfma_f32_16x16x32_bf16 v[136:139], a[108:111], v[88:91], v[136:139]\n"
"v_cvt_pk_bf16_f32 v209, v134, v135\n"
"v_mfma_f32_16x16x32_bf16 v[136:139], a[104:107], v[84:87], v[136:139]\n"
"v_lshlrev_b32 v2, 16, v97\n"
"v_mfma_f32_16x16x32_bf16 v[136:139], a[100:103], v[68:71], v[136:139]\n"
"v_and_b32 v3, 0xffff0000, v97\n"
"v_mfma_f32_16x16x32_bf16 v[136:139], a[96:99], v[64:67], v[136:139]\n"
"s_waitcnt lgkmcnt(1)\n"
"v_mfma_f32_16x16x32_bf16 v[136:139], a[92:95], v[60:63], v[136:139]\n"
"v_lshlrev_b32 v4, 16, v98\n"
"v_mfma_f32_16x16x32_bf16 v[136:139], a[88:91], v[44:47], v[136:139]\n"
"global_store_dwordx4 v247, v[216:219], s[54:55] offset:768 sc1\n"
"ds_write_b64 v244, v[208:209] offset:12288\n"
"v_mfma_f32_16x16x32_bf16 v[136:139], a[84:87], v[40:43], v[136:139]\n"
"s_add_u32 m0, s31, 0xdf00\n"
"v_mfma_f32_16x16x32_bf16 v[136:139], a[80:83], v[36:39], v[136:139]\n"
"global_load_lds_dwordx4 v237, s[52:53] offset:256 nt\n"
"v_mfma_f32_16x16x32_bf16 v[132:135], a[128:131], v[36:39], v[8:11]\n"
"v_and_b32 v5, 0xffff0000, v98\n"
"v_mfma_f32_16x16x32_bf16 v[132:135], a[132:135], v[40:43], v[132:135]\n"
"v_lshlrev_b32 v6, 16, v99\n"
"v_mfma_f32_16x16x32_bf16 v[132:135], a[136:139], v[44:47], v[132:135]\n"
"v_and_b32 v7, 0xffff0000, v99\n"
"v_mfma_f32_16x16x32_bf16 v[132:135], a[140:143], v[60:63], v[132:135]\n"
"v_cvt_pk_bf16_f32 v210, v136, v137\n"
"v_mfma_f32_16x16x32_bf16 v[132:135], a[144:147], v[64:67], v[132:135]\n"
"v_cvt_pk_bf16_f32 v211, v138, v139\n"
"v_mfma_f32_16x16x32_bf16 v[132:135], a[148:151], v[68:71], v[132:135]\n"
"v_lshlrev_b32 v8, 16, v100\n"
"v_mfma_f32_16x16x32_bf16 v[132:135], a[152:155], v[84:87], v[132:135]\n"
"v_and_b32 v9, 0xffff0000, v100\n"
"v_mfma_f32_16x16x32_bf16 v[132:135], a[156:159], v[88:91], v[132:135]\n"
"v_lshlrev_b32 v10, 16, v101\n"
"v_mfma_f32_16x16x32_bf16 v[132:135], a[160:163], v[92:95], v[132:135]\n"
"v_and_b32 v11, 0xffff0000, v101\n"
"v_mfma_f32_16x16x32_bf16 v[132:135], a[164:167], v[108:111], v[132:135]\n"
"s_memrealtime s[84:85]\n"
"v_mfma_f32_16x16x32_bf16 v[132:135], a[168:171], v[112:115], v[132:135]\n"
"global_store_dwordx4 v252, v[220:223], s[54:55] offset:768 sc1\n"
"ds_write_b64 v244, v[210:211] offset:12296\n"
"v_mfma_f32_16x16x32_bf16 v[132:135], a[172:175], v[116:119], v[132:135]\n"
"s_add_u32 s83, s83, s86\n"
"v_mfma_f32_16x16x32_bf16 v[136:139], a[220:223], v[116:119], v[12:15]\n"
"s_sub_i32 s88, s88, 6\n"
"v_mfma_f32_16x16x32_bf16 v[136:139], a[216:219], v[112:115], v[136:139]\n"
"s_max_i32 s88, s88, 0\n"
"v_mfma_f32_16x16x32_bf16 v[136:139], a[212:215], v[108:111], v[136:139]\n"
"s_mov_b64 exec, s[6:7]\n"
"s_cbranch_execz LHT9\n"
"LHR9:\n"
"s_mov_b64 exec, -1\n"
"v_mfma_f32_16x16x32_bf16 v[136:139], a[208:211], v[92:95], v[136:139]\n"
"v_cvt_pk_bf16_f32 v212, v132, v133\n"
"v_mfma_f32_16x16x32_bf16 v[136:139], a[204:207], v[88:91], v[136:139]\n"
"v_mov_b32 v242, 16\n"
"v_mfma_f32_16x16x32_bf16 v[136:139], a[200:203], v[84:87], v[136:139]\n"
"v_cvt_pk_bf16_f32 v213, v134, v135\n"
"v_mfma_f32_16x16x32_bf16 v[136:139], a[196:199], v[68:71], v[136:139]\n"
"s_waitcnt vmcnt(17) lgkmcnt(2)\n"
"ds_write_b32 v240, v242\n"
"v_mfma_f32_16x16x32_bf16 v[136:139], a[192:195], v[64:67], v[136:139]\n"
"v_lshlrev_b32 v12, 16, v102\n"
"v_mfma_f32_16x16x32_bf16 v[136:139], a[188:191], v[60:63], v[136:139]\n"
"v_and_b32 v13, 0xffff0000, v102\n"
"v_mfma_f32_16x16x32_bf16 v[136:139], a[184:187], v[44:47], v[136:139]\n"
"v_lshlrev_b32 v14, 16, v103\n"
"v_mfma_f32_16x16x32_bf16 v[136:139], a[180:183], v[40:43], v[136:139]\n"
"global_store_dwordx4 v253, v[224:227], s[54:55] offset:768 sc1\n"
"ds_write_b64 v245, v[212:213] offset:12288\n"
"v_mfma_f32_16x16x32_bf16 v[136:139], a[176:179], v[36:39], v[136:139]\n"
"s_add_u32 m0, s31, 0xfe00\n"
"v_mfma_f32_16x16x32_bf16 v[132:135], a[224:227], v[36:39], v[16:19]\n"
"global_load_lds_dwordx4 v237, s[52:53] offset:512 nt\n"
"v_mfma_f32_16x16x32_bf16 v[132:135], a[228:231], v[40:43], v[132:135]\n"
"v_and_b32 v15, 0xffff0000, v103\n"
"v_mfma_f32_16x16x32_bf16 v[132:135], a[232:235], v[44:47], v[132:135]\n"
"ds_read_b32 v243, v241\n"
"v_mfma_f32_16x16x32_bf16 v[132:135], a[236:239], v[60:63], v[132:135]\n"
"v_cvt_pk_bf16_f32 v214, v136, v137\n"
"v_mfma_f32_16x16x32_bf16 v[132:135], a[0:3], v[64:67], v[132:135]\n"
"v_cvt_pk_bf16_f32 v215, v138, v139\n"
"v_mfma_f32_16x16x32_bf16 v[132:135], a[4:7], v[68:71], v[132:135]\n"
"v_lshlrev_b32 v16, 16, v104\n"
"v_mfma_f32_16x16x32_bf16 v[132:135], a[8:11], v[84:87], v[132:135]\n"
"s_waitcnt lgkmcnt(0)\n"
"v_cmp_gt_u32 s[56:57], 16, v243\n"
"v_mfma_f32_16x16x32_bf16 v[132:135], a[12:15], v[88:91], v[132:135]\n"
"s_sub_u32 s84, s84, s83\n"
"v_mfma_f32_16x16x32_bf16 v[132:135], a[16:19], v[92:95], v[132:135]\n"
"s_or_b64 s[56:57], s[56:57], vcc\n"
"s_cbranch_scc1 LB16\n"
"LB16R:\n"
"v_mfma_f32_16x16x32_bf16 v[132:135], a[20:23], v[108:111], v[132:135]\n"
"s_cmp_lt_i32 s84, s88\n"
"s_cselect_b64 vcc, -1, 0\n"
"v_mfma_f32_16x16x32_bf16 v[132:135], v[140:143], v[112:115], v[132:135]\n"
"ds_write_b64 v245, v[214:215] offset:12296\n"
"v_mfma_f32_16x16x32_bf16 v[132:135], v[156:159], v[116:119], v[132:135]\n"
"v_and_b32 v17, 0xffff0000, v104\n"
"v_mfma_f32_16x16x32_bf16 v[136:139], a[44:47], v[116:119], v[20:23]\n"
"ds_read_b128 v[116:119], v235 offset:16384\n"
"v_mfma_f32_16x16x32_bf16 v[136:139], a[40:43], v[112:115], v[136:139]\n"
"ds_read_b128 v[112:115], v234 offset:16384\n"
"v_mfma_f32_16x16x32_bf16 v[136:139], a[36:39], v[108:111], v[136:139]\n"
"ds_read_b128 v[108:111], v233 offset:16384\n"
"v_mfma_f32_16x16x32_bf16 v[136:139], a[32:35], v[92:95], v[136:139]\n"
"ds_read_b128 v[92:95], v232 offset:16384\n"
"v_cvt_pk_bf16_f32 v208, v132, v133\n"
"v_mfma_f32_16x16x32_bf16 v[136:139], a[28:31], v[88:91], v[136:139]\n"
"ds_read_b128 v[88:91], v235 offset:8192\n"
"v_cvt_pk_bf16_f32 v209, v134, v135\n"
"v_mfma_f32_16x16x32_bf16 v[136:139], a[24:27], v[84:87], v[136:139]\n"
"ds_read_b128 v[84:87], v234 offset:8192\n"
"v_mfma_f32_16x16x32_bf16 v[136:139], v[204:207], v[68:71], v[136:139]\n"
"ds_read_b128 v[68:71], v233 offset:8192\n"
"v_mfma_f32_16x16x32_bf16 v[136:139], v[188:191], v[64:67], v[136:139]\n"
"ds_read_b128 v[64:67], v232 offset:8192\n"
"v_mfma_f32_16x16x32_bf16 v[136:139], v[184:187], v[60:63], v[136:139]\n"
"ds_read_b128 v[60:63], v235 offset:0\n"
"v_mfma_f32_16x16x32_bf16 v[136:139], v[180:183], v[44:47], v[136:139]\n"
"ds_read_b128 v[44:47], v234 offset:0\n"
"v_mfma_f32_16x16x32_bf16 v[136:139], v[164:167], v[40:43], v[136:139]\n"
"ds_read_b128 v[40:43], v233 offset:0\n"
"v_mfma_f32_16x16x32_bf16 v[136:139], v[160:163], v[36:39], v[136:139]\n"
"ds_read_b128 v[36:39], v232 offset:0\n"
"s_waitcnt lgkmcnt(8)\n"
"v_mfma_f32_16x16x32_bf16 v[132:135], a[76:79], v[116:119], v[0:3]\n"
"ds_write_b64 v244, v[208:209] offset:12416\n"
"v_mfma_f32_16x16x32_bf16 v[132:135], a[72:75], v[112:115], v[132:135]\n"
"s_add_u32 m0, s31, 0xd000\n"
"v_mfma_f32_16x16x32_bf16 v[132:135], a[68:71], v[108:111], v[132:135]\n"
"global_load_lds_dwordx4 v238, s[52:53] offset:0 nt\n"
"v_mfma_f32_16x16x32_bf16 v[132:135], a[64:67], v[92:95], v[132:135]\n"
"v_lshlrev_b32 v18, 16, v105\n"
"v_cvt_pk_bf16_f32 v211, v138, v139\n"
"s_waitcnt lgkmcnt(5)\n"
"v_mfma_f32_16x16x32_bf16 v[132:135], a[60:63], v[88:91], v[132:135]\n"
"v_and_b32 v19, 0xffff0000, v105\n"
"v_mfma_f32_16x16x32_bf16 v[132:135], a[56:59], v[84:87], v[132:135]\n"
"v_lshlrev_b32 v20, 16, v106\n"
"v_mfma_f32_16x16x32_bf16 v[132:135], a[52:55], v[68:71], v[132:135]\n"
"v_and_b32 v21, 0xffff0000, v106\n"
"v_mfma_f32_16x16x32_bf16 v[132:135], a[48:51], v[64:67], v[132:135]\n"
"v_lshlrev_b32 v22, 16, v107\n"
"s_waitcnt lgkmcnt(1)\n"
"v_mfma_f32_16x16x32_bf16 v[132:135], a[252:255], v[60:63], v[132:135]\n"
"v_and_b32 v23, 0xffff0000, v107\n"
"v_mfma_f32_16x16x32_bf16 v[132:135], a[248:251], v[44:47], v[132:135]\n"
"v_cvt_pk_bf16_f32 v210, v136, v137\n"
"v_mfma_f32_16x16x32_bf16 v[132:135], a[244:247], v[40:43], v[132:135]\n"
"s_add_u32 s54, s26, s39\n"
"s_addc_u32 s55, s27, 0\n"
"v_mfma_f32_16x16x32_bf16 v[132:135], a[240:243], v[36:39], v[132:135]\n"
"ds_read_b128 v[216:219], v246 offset:0\n"
"v_mfma_f32_16x16x32_bf16 v[136:139], a[80:83], v[36:39], v[4:7]\n"
"ds_read_b128 v[220:223], v246 offset:1024\n"
"v_mfma_f32_16x16x32_bf16 v[136:139], a[84:87], v[40:43], v[136:139]\n"
"ds_read_b128 v[224:227], v246 offset:2048\n"
"v_mfma_f32_16x16x32_bf16 v[136:139], a[88:91], v[44:47], v[136:139]\n"
"ds_write_b64 v244, v[210:211] offset:12424\n"
"v_mfma_f32_16x16x32_bf16 v[136:139], a[92:95], v[60:63], v[136:139]\n"
"v_cvt_pk_bf16_f32 v208, v132, v133\n"
"v_mfma_f32_16x16x32_bf16 v[136:139], a[96:99], v[64:67], v[136:139]\n"
"v_cvt_pk_bf16_f32 v209, v134, v135\n"
"v_mfma_f32_16x16x32_bf16 v[136:139], a[100:103], v[68:71], v[136:139]\n"
"s_sub_i32 s84, s84, s19\n"
"v_mfma_f32_16x16x32_bf16 v[136:139], a[104:107], v[84:87], v[136:139]\n"
"s_max_i32 s88, s88, s84\n"
"v_mfma_f32_16x16x32_bf16 v[136:139], a[108:111], v[88:91], v[136:139]\n"
"s_waitcnt lgkmcnt(1)\n"
"v_mfma_f32_16x16x32_bf16 v[136:139], a[112:115], v[92:95], v[136:139]\n"
"v_mfma_f32_16x16x32_bf16 v[136:139], a[116:119], v[108:111], v[136:139]\n"
"global_store_dwordx4 v247, v[216:219], s[54:55] offset:0 sc1\n"
"ds_write_b64 v244, v[208:209] offset:24576\n"
"v_mfma_f32_16x16x32_bf16 v[136:139], a[120:123], v[112:115], v[136:139]\n"
"v_mfma_f32_16x16x32_bf16 v[136:139], a[124:127], v[116:119], v[136:139]\n"
"v_mfma_f32_16x16x32_bf16 v[132:135], a[172:175], v[116:119], v[8:11]\n"
"v_mfma_f32_16x16x32_bf16 v[132:135], a[168:171], v[112:115], v[132:135]\n"
"s_add_u32 m0, s31, 0xef00\n"
"v_mfma_f32_16x16x32_bf16 v[132:135], a[164:167], v[108:111], v[132:135]\n"
"global_load_lds_dwordx4 v238, s[52:53] offset:256 nt\n"
"v_mfma_f32_16x16x32_bf16 v[132:135], a[160:163], v[92:95], v[132:135]\n"
"v_cvt_pk_bf16_f32 v210, v136, v137\n"
"v_mfma_f32_16x16x32_bf16 v[132:135], a[156:159], v[88:91], v[132:135]\n"
"v_cvt_pk_bf16_f32 v211, v138, v139\n"
"v_mfma_f32_16x16x32_bf16 v[132:135], a[152:155], v[84:87], v[132:135]\n"
"LHT9:\n"
"s_cbranch_execz LHR9\n"
"v_mfma_f32_16x16x32_bf16 v[132:135], a[148:151], v[68:71], v[132:135]\n"
"v_mfma_f32_16x16x32_bf16 v[132:135], a[144:147], v[64:67], v[132:135]\n"
"v_mfma_f32_16x16x32_bf16 v[132:135], a[140:143], v[60:63], v[132:135]\n"
"v_mfma_f32_16x16x32_bf16 v[132:135], a[136:139], v[44:47], v[132:135]\n"
"global_store_dwordx4 v252, v[220:223], s[54:55] offset:0 sc1\n"
"ds_write_b64 v244, v[210:211] offset:24584\n"
"v_mfma_f32_16x16x32_bf16 v[132:135], a[132:135], v[40:43], v[132:135]\n"
"v_mfma_f32_16x16x32_bf16 v[132:135], a[128:131], v[36:39], v[132:135]\n"
"v_mfma_f32_16x16x32_bf16 v[136:139], a[176:179], v[36:39], v[12:15]\n"
"v_mfma_f32_16x16x32_bf16 v[136:139], a[180:183], v[40:43], v[136:139]\n"
"v_mfma_f32_16x16x32_bf16 v[136:139], a[184:187], v[44:47], v[136:139]\n"
"v_mfma_f32_16x16x32_bf16 v[136:139], a[188:191], v[60:63], v[136:139]\n"
"v_cvt_pk_bf16_f32 v212, v132, v133\n"
"v_mfma_f32_16x16x32_bf16 v[136:139], a[192:195], v[64:67], v[136:139]\n"
"v_mov_b32 v242, 17\n"
"v_mfma_f32_16x16x32_bf16 v[136:139], a[196:199], v[68:71], v[136:139]\n"
"v_cvt_pk_bf16_f32 v213, v134, v135\n"
"v_mfma_f32_16x16x32_bf16 v[136:139], a[200:203], v[84:87], v[136:139]\n"
"s_waitcnt lgkmcnt(2)\n"
"ds_write_b32 v240, v242\n"
"v_mfma_f32_16x16x32_bf16 v[136:139], a[204:207], v[88:91], v[136:139]\n"
"v_mfma_f32_16x16x32_bf16 v[136:139], a[208:211], v[92:95], v[136:139]\n"
"v_mfma_f32_16x16x32_bf16 v[136:139], a[212:215], v[108:111], v[136:139]\n"
"v_mfma_f32_16x16x32_bf16 v[136:139], a[216:219], v[112:115], v[136:139]\n"
"global_store_dwordx4 v253, v[224:227], s[54:55] offset:0 sc1\n"
"ds_write_b64 v245, v[212:213] offset:24576\n"
"v_mfma_f32_16x16x32_bf16 v[136:139], a[220:223], v[116:119], v[136:139]\n"
"v_mfma_f32_16x16x32_bf16 v[132:135], v[156:159], v[116:119], v[16:19]\n"
"v_mfma_f32_16x16x32_bf16 v[132:135], v[140:143], v[112:115], v[132:135]\n"
"v_mfma_f32_16x16x32_bf16 v[132:135], a[20:23], v[108:111], v[132:135]\n"
"ds_read_b32 v243, v241\n"
"v_mfma_f32_16x16x32_bf16 v[132:135], a[16:19], v[92:95], v[132:135]\n"
"v_cvt_pk_bf16_f32 v214, v136, v137\n"
"v_mfma_f32_16x16x32_bf16 v[132:135], a[12:15], v[88:91], v[132:135]\n"
"v_cvt_pk_bf16_f32 v215, v138, v139\n"
"v_mfma_f32_16x16x32_bf16 v[132:135], a[8:11], v[84:87], v[132:135]\n"
"s_add_u32 m0, s31, 0x10e00\n"
"v_mfma_f32_16x16x32_bf16 v[132:135], a[4:7], v[68:71], v[132:135]\n"
"s_waitcnt lgkmcnt(0)\n"
"v_cmp_gt_u32 s[56:57], 17, v243\n"
"v_mfma_f32_16x16x32_bf16 v[132:135], a[0:3], v[64:67], v[132:135]\n"
"global_load_lds_dwordx4 v238, s[52:53] offset:512 nt\n"
"v_mfma_f32_16x16x32_bf16 v[132:135], a[236:239], v[60:63], v[132:135]\n"
"s_or_b64 s[56:57], s[56:57], vcc\n"
"s_cbranch_scc1 LB17\n"
"LB17R:\n"
"v_mfma_f32_16x16x32_bf16 v[132:135], a[232:235], v[44:47], v[132:135]\n"
"ds_write_b64 v245, v[214:215] offset:24584\n"
"v_mfma_f32_16x16x32_bf16 v[132:135], a[228:231], v[40:43], v[132:135]\n"
"v_mfma_f32_16x16x32_bf16 v[132:135], a[224:227], v[36:39], v[132:135]\n"
"v_mfma_f32_16x16x32_bf16 v[136:139], v[160:163], v[36:39], v[20:23]\n"
"ds_read_b128 v[36:39], v232 offset:256\n"
"v_mfma_f32_16x16x32_bf16 v[136:139], v[164:167], v[40:43], v[136:139]\n"
"ds_read_b128 v[40:43], v233 offset:256\n"
"v_mfma_f32_16x16x32_bf16 v[136:139], v[180:183], v[44:47], v[136:139]\n"
"ds_read_b128 v[44:47], v234 offset:256\n"
"v_mfma_f32_16x16x32_bf16 v[136:139], v[184:187], v[60:63], v[136:139]\n"
"ds_read_b128 v[60:63], v235 offset:256\n"
"v_cvt_pk_bf16_f32 v208, v132, v133\n"
"v_mfma_f32_16x16x32_bf16 v[136:139], v[188:191], v[64:67], v[136:139]\n"
"ds_read_b128 v[64:67], v232 offset:8448\n"
"v_cvt_pk_bf16_f32 v209, v134, v135\n"
"v_mfma_f32_16x16x32_bf16 v[136:139], v[204:207], v[68:71], v[136:139]\n"
"ds_read_b128 v[68:71], v233 offset:8448\n"
"v_mfma_f32_16x16x32_bf16 v[136:139], a[24:27], v[84:87], v[136:139]\n"
"ds_read_b128 v[84:87], v234 offset:8448\n"
"v_mfma_f32_16x16x32_bf16 v[136:139], a[28:31], v[88:91], v[136:139]\n"
"ds_read_b128 v[88:91], v235 offset:8448\n"
"v_mfma_f32_16x16x32_bf16 v[136:139], a[32:35], v[92:95], v[136:139]\n"
"ds_read_b128 v[92:95], v232 offset:16640\n"
"v_mfma_f32_16x16x32_bf16 v[136:139], a[36:39], v[108:111], v[136:139]\n"
"ds_read_b128 v[108:111], v233 offset:16640\n"
"v_mfma_f32_16x16x32_bf16 v[136:139], a[40:43], v[112:115], v[136:139]\n"
"ds_read_b128 v[112:115], v234 offset:16640\n"
"v_mfma_f32_16x16x32_bf16 v[136:139], a[44:47], v[116:119], v[136:139]\n"
"ds_read_b128 v[116:119], v235 offset:16640\n"
"s_waitcnt lgkmcnt(8)\n"
"v_mfma_f32_16x16x32_bf16 v[132:135], a[240:243], v[36:39], v[0:3]\n"
"ds_write_b64 v244, v[208:209] offset:24704\n"
"v_mfma_f32_16x16x32_bf16 v[132:135], a[244:247], v[40:43], v[132:135]\n"
"v_mfma_f32_16x16x32_bf16 v[132:135], a[248:251], v[44:47], v[132:135]\n"
"s_add_u32 s52, s20, s43\n"
"s_addc_u32 s53, s21, 0\n"
"v_mfma_f32_16x16x32_bf16 v[132:135], a[252:255], v[60:63], v[132:135]\n"
"v_cvt_pk_bf16_f32 v210, v136, v137\n"
"s_waitcnt lgkmcnt(5)\n"
"v_mfma_f32_16x16x32_bf16 v[132:135], a[48:51], v[64:67], v[132:135]\n"
"v_cvt_pk_bf16_f32 v211, v138, v139\n"
"v_mfma_f32_16x16x32_bf16 v[132:135], a[52:55], v[68:71], v[132:135]\n"
"s_add_u32 m0, s31, 0x12000\n"
"v_mfma_f32_16x16x32_bf16 v[132:135], a[56:59], v[84:87], v[132:135]\n"
"ds_read_b128 v[216:219], v246 offset:12288\n"
"v_mfma_f32_16x16x32_bf16 v[132:135], a[60:63], v[88:91], v[132:135]\n"
"ds_read_b128 v[220:223], v246 offset:13312\n"
"s_waitcnt lgkmcnt(3)\n"
"v_mfma_f32_16x16x32_bf16 v[132:135], a[64:67], v[92:95], v[132:135]\n"
"ds_read_b128 v[224:227], v246 offset:14336\n"
"v_mfma_f32_16x16x32_bf16 v[132:135], a[68:71], v[108:111], v[132:135]\n"
"ds_write_b64 v244, v[210:211] offset:24712\n"
"v_mfma_f32_16x16x32_bf16 v[132:135], a[72:75], v[112:115], v[132:135]\n"
"global_load_lds_dwordx4 v237, s[52:53] offset:0 nt\n"
"v_mfma_f32_16x16x32_bf16 v[132:135], a[76:79], v[116:119], v[132:135]\n"
"v_mfma_f32_16x16x32_bf16 v[136:139], a[124:127], v[116:119], v[4:7]\n"
"s_add_u32 s83, s83, s86\n"
"v_mfma_f32_16x16x32_bf16 v[136:139], a[120:123], v[112:115], v[136:139]\n"
"s_sub_i32 s88, s88, 6\n"
"v_mfma_f32_16x16x32_bf16 v[136:139], a[116:119], v[108:111], v[136:139]\n"
"s_max_i32 s88, s88, 0\n"
"v_mfma_f32_16x16x32_bf16 v[136:139], a[112:115], v[92:95], v[136:139]\n"
"v_cvt_pk_bf16_f32 v208, v132, v133\n"
"v_mfma_f32_16x16x32_bf16 v[136:139], a[108:111], v[88:91], v[136:139]\n"
"v_cvt_pk_bf16_f32 v209, v134, v135\n"
"v_mfma_f32_16x16x32_bf16 v[136:139], a[104:107], v[84:87], v[136:139]\n"
"v_mfma_f32_16x16x32_bf16 v[136:139], a[100:103], v[68:71], v[136:139]\n"
"v_mfma_f32_16x16x32_bf16 v[136:139], a[96:99], v[64:67], v[136:139]\n"
"s_waitcnt lgkmcnt(1)\n"
"v_mfma_f32_16x16x32_bf16 v[136:139], a[92:95], v[60:63], v[136:139]\n"
"v_mfma_f32_16x16x32_bf16 v[136:139], a[88:91], v[44:47], v[136:139]\n"
"global_store_dwordx4 v247, v[216:219], s[54:55] offset:768 sc1\n"
"ds_write_b64 v244, v[208:209] offset:0\n"
"v_mfma_f32_16x16x32_bf16 v[136:139], a[84:87], v[40:43], v[136:139]\n"
"v_mfma_f32_16x16x32_bf16 v[136:139], a[80:83], v[36:39], v[136:139]\n"
"v_mfma_f32_16x16x32_bf16 v[132:135], a[128:131], v[36:39], v[8:11]\n"
"v_mfma_f32_16x16x32_bf16 v[132:135], a[132:135], v[40:43], v[132:135]\n"
"v_mfma_f32_16x16x32_bf16 v[132:135], a[136:139], v[44:47], v[132:135]\n"
"v_mfma_f32_16x16x32_bf16 v[132:135], a[140:143], v[60:63], v[132:135]\n"
"v_cvt_pk_bf16_f32 v210, v136, v137\n"
"v_mfma_f32_16x16x32_bf16 v[132:135], a[144:147], v[64:67], v[132:135]\n"
"v_cvt_pk_bf16_f32 v211, v138, v139\n"
"v_mfma_f32_16x16x32_bf16 v[132:135], a[148:151], v[68:71], v[132:135]\n"
"s_add_u32 m0, s31, 0x13f00\n"
"v_mfma_f32_16x16x32_bf16 v[132:135], a[152:155], v[84:87], v[132:135]\n"
"global_load_lds_dwordx4 v237, s[52:53] offset:256 nt\n"
"v_mfma_f32_16x16x32_bf16 v[132:135], a[156:159], v[88:91], v[132:135]\n"
"v_mfma_f32_16x16x32_bf16 v[132:135], a[160:163], v[92:95], v[132:135]\n"
"v_mfma_f32_16x16x32_bf16 v[132:135], a[164:167], v[108:111], v[132:135]\n"
"s_memrealtime s[84:85]\n"
"v_mfma_f32_16x16x32_bf16 v[132:135], a[168:171], v[112:115], v[132:135]\n"
"global_store_dwordx4 v252, v[220:223], s[54:55] offset:768 sc1\n"
"ds_write_b64 v244, v[210:211] offset:8\n"
"v_mfma_f32_16x16x32_bf16 v[132:135], a[172:175], v[116:119], v[132:135]\n"
"v_mfma_f32_16x16x32_bf16 v[136:139], a[220:223], v[116:119], v[12:15]\n"
"v_mfma_f32_16x16x32_bf16 v[136:139], a[216:219], v[112:115], v[136:139]\n"
"v_mfma_f32_16x16x32_bf16 v[136:139], a[212:215], v[108:111], v[136:139]\n"
"v_mfma_f32_16x16x32_bf16 v[136:139], a[208:211], v[92:95], v[136:139]\n"
"v_cvt_pk_bf16_f32 v212, v132, v133\n"
"v_mfma_f32_16x16x32_bf16 v[136:139], a[204:207], v[88:91], v[136:139]\n"
"v_mov_b32 v242, 18\n"
"v_mfma_f32_16x16x32_bf16 v[136:139], a[200:203], v[84:87], v[136:139]\n"
"v_cvt_pk_bf16_f32 v213, v134, v135\n"
"v_mfma_f32_16x16x32_bf16 v[136:139], a[196:199], v[68:71], v[136:139]\n"
"s_waitcnt vmcnt(16) lgkmcnt(2)\n"
"ds_write_b32 v240, v242\n"
"v_mfma_f32_16x16x32_bf16 v[136:139], a[192:195], v[64:67], v[136:139]\n"
"v_mfma_f32_16x16x32_bf16 v[136:139], a[188:191], v[60:63], v[136:139]\n"
"v_mfma_f32_16x16x32_bf16 v[136:139], a[184:187], v[44:47], v[136:139]\n"
"v_mfma_f32_16x16x32_bf16 v[136:139], a[180:183], v[40:43], v[136:139]\n"
"global_store_dwordx4 v253, v[224:227], s[54:55] offset:768 sc1\n"
"ds_write_b64 v245, v[212:213] offset:0\n"
"v_mfma_f32_16x16x32_bf16 v[136:139], a[176:179], v[36:39], v[136:139]\n"
"s_bitcmp1_b32 s92, 4\n"
"s_cselect_b32 s93, 0x6000, 0\n"
"v_mfma_f32_16x16x32_bf16 v[132:135], a[224:227], v[36:39], v[16:19]\n"
"s_sub_u32 s94, s93, 0xc000\n"
"v_mfma_f32_16x16x32_bf16 v[132:135], a[228:231], v[40:43], v[132:135]\n"
"v_add_u32 v231, s93, v247\n"
"v_mfma_f32_16x16x32_bf16 v[132:135], a[232:235], v[44:47], v[132:135]\n"
"ds_read_b32 v243, v241\n"
"v_mfma_f32_16x16x32_bf16 v[132:135], a[236:239], v[60:63], v[132:135]\n"
"v_cvt_pk_bf16_f32 v214, v136, v137\n"
"v_mfma_f32_16x16x32_bf16 v[132:135], a[0:3], v[64:67], v[132:135]\n"
"v_cvt_pk_bf16_f32 v215, v138, v139\n"
"v_mfma_f32_16x16x32_bf16 v[132:135], a[4:7], v[68:71], v[132:135]\n"
"s_add_u32 m0, s31, 0x15e00\n"
"v_mfma_f32_16x16x32_bf16 v[132:135], a[8:11], v[84:87], v[132:135]\n"
"s_waitcnt lgkmcnt(0)\n"
"v_cmp_gt_u32 s[56:57], 18, v243\n"
"v_mfma_f32_16x16x32_bf16 v[132:135], a[12:15], v[88:91], v[132:135]\n"
"s_sub_u32 s84, s84, s83\n"
"v_mfma_f32_16x16x32_bf16 v[132:135], a[16:19], v[92:95], v[132:135]\n"
"s_or_b64 s[56:57], s[56:57], vcc\n"
"s_cbranch_scc1 LB18\n"
"LB18R:\n"
"v_mfma_f32_16x16x32_bf16 v[132:135], a[20:23], v[108:111], v[132:135]\n"
"s_cmp_lt_i32 s84, s88\n"
"s_cselect_b64 vcc, -1, 0\n"
"v_mfma_f32_16x16x32_bf16 v[132:135], v[140:143], v[112:115], v[132:135]\n"
"ds_write_b64 v245, v[214:215] offset:8\n"
"v_mfma_f32_16x16x32_bf16 v[132:135], v[156:159], v[116:119], v[132:135]\n"
"global_load_lds_dwordx4 v237, s[52:53] offset:512 nt\n"
"v_mfma_f32_16x16x32_bf16 v[136:139], a[44:47], v[116:119], v[20:23]\n"
"ds_read_b128 v[116:119], v235 offset:40960\n"
"v_mfma_f32_16x16x32_bf16 v[136:139], a[40:43], v[112:115], v[136:139]\n"
"ds_read_b128 v[112:115], v234 offset:40960\n"
"v_mfma_f32_16x16x32_bf16 v[136:139], a[36:39], v[108:111], v[136:139]\n"
"ds_read_b128 v[108:111], v233 offset:40960\n"
"v_mfma_f32_16x16x32_bf16 v[136:139], a[32:35], v[92:95], v[136:139]\n"
"ds_read_b128 v[92:95], v232 offset:40960\n"
"v_cvt_pk_bf16_f32 v208, v132, v133\n"
"v_mfma_f32_16x16x32_bf16 v[136:139], a[28:31], v[88:91], v[136:139]\n"
"ds_read_b128 v[88:91], v235 offset:32768\n"
"v_cvt_pk_bf16_f32 v209, v134, v135\n"
"v_mfma_f32_16x16x32_bf16 v[136:139], a[24:27], v[84:87], v[136:139]\n"
"ds_read_b128 v[84:87], v234 offset:32768\n"
"v_mfma_f32_16x16x32_bf16 v[136:139], v[204:207], v[68:71], v[136:139]\n"
"ds_read_b128 v[68:71], v233 offset:32768\n"
"v_mfma_f32_16x16x32_bf16 v[136:139], v[188:191], v[64:67], v[136:139]\n"
"ds_read_b128 v[64:67], v232 offset:32768\n"
"v_mfma_f32_16x16x32_bf16 v[136:139], v[184:187], v[60:63], v[136:139]\n"
"ds_read_b128 v[60:63], v235 offset:24576\n"
"v_mfma_f32_16x16x32_bf16 v[136:139], v[180:183], v[44:47], v[136:139]\n"
"ds_read_b128 v[44:47], v234 offset:24576\n"
"v_mfma_f32_16x16x32_bf16 v[136:139], v[164:167], v[40:43], v[136:139]\n"
"ds_read_b128 v[40:43], v233 offset:24576\n"
"v_mfma_f32_16x16x32_bf16 v[136:139], v[160:163], v[36:39], v[136:139]\n"
"ds_read_b128 v[36:39], v232 offset:24576\n"
"s_waitcnt lgkmcnt(8)\n"
"v_mfma_f32_16x16x32_bf16 v[132:135], a[76:79], v[116:119], v[0:3]\n"
"ds_write_b64 v244, v[208:209] offset:128\n"
"v_mfma_f32_16x16x32_bf16 v[132:135], a[72:75], v[112:115], v[132:135]\n"
"s_add_u32 s54, s26, s40\n"
"s_addc_u32 s55, s27, 0\n"
"v_mfma_f32_16x16x32_bf16 v[132:135], a[68:71], v[108:111], v[132:135]\n"
"s_sub_i32 s84, s84, s19\n"
"v_mfma_f32_16x16x32_bf16 v[132:135], a[64:67], v[92:95], v[132:135]\n"
"v_cvt_pk_bf16_f32 v210, v136, v137\n"
"s_waitcnt lgkmcnt(5)\n"
"v_mfma_f32_16x16x32_bf16 v[132:135], a[60:63], v[88:91], v[132:135]\n"
"v_cvt_pk_bf16_f32 v211, v138, v139\n"
"v_mfma_f32_16x16x32_bf16 v[132:135], a[56:59], v[84:87], v[132:135]\n"
"s_max_i32 s88, s88, s84\n"
"v_mfma_f32_16x16x32_bf16 v[132:135], a[52:55], v[68:71], v[132:135]\n"
"ds_read_b128 v[216:219], v246 offset:24576\n"
"v_mfma_f32_16x16x32_bf16 v[132:135], a[48:51], v[64:67], v[132:135]\n"
"ds_read_b128 v[220:223], v246 offset:25600\n"
"s_waitcnt lgkmcnt(3)\n"
"v_mfma_f32_16x16x32_bf16 v[132:135], a[252:255], v[60:63], v[132:135]\n"
"ds_read_b128 v[224:227], v246 offset:26624\n"
"v_mfma_f32_16x16x32_bf16 v[132:135], a[248:251], v[44:47], v[132:135]\n"
"ds_write_b64 v244, v[210:211] offset:136\n"
"v_mfma_f32_16x16x32_bf16 v[132:135], a[244:247], v[40:43], v[132:135]\n"
"s_add_u32 m0, s31, 0x13000\n"
"v_mfma_f32_16x16x32_bf16 v[132:135], a[240:243], v[36:39], v[132:135]\n"
"global_load_lds_dwordx4 v238, s[52:53] offset:0 nt\n"
"v_mfma_f32_16x16x32_bf16 v[136:139], a[80:83], v[36:39], v[4:7]\n"
"v_add_u32 v247, s94, v247\n"
"v_mfma_f32_16x16x32_bf16 v[136:139], a[84:87], v[40:43], v[136:139]\n"
"v_min_u32 v247, v247, v231\n"
"v_mfma_f32_16x16x32_bf16 v[136:139], a[88:91], v[44:47], v[136:139]\n"
"v_add_u32 v231, s93, v252\n"
"v_mfma_f32_16x16x32_bf16 v[136:139], a[92:95], v[60:63], v[136:139]\n"
"v_cvt_pk_bf16_f32 v208, v132, v133\n"
"v_add_u32 v252, s94, v252\n"
"v_mfma_f32_16x16x32_bf16 v[136:139], a[96:99], v[64:67], v[136:139]\n"
"v_cvt_pk_bf16_f32 v209, v134, v135\n"
"v_mfma_f32_16x16x32_bf16 v[136:139], a[100:103], v[68:71], v[136:139]\n"
"v_min_u32 v252, v252, v231\n"
"v_mfma_f32_16x16x32_bf16 v[136:139], a[104:107], v[84:87], v[136:139]\n"
"v_add_u32 v231, s93, v253\n"
"v_mfma_f32_16x16x32_bf16 v[136:139], a[108:111], v[88:91], v[136:139]\n"
"s_waitcnt lgkmcnt(1)\n"
"v_add_u32 v253, s94, v253\n"
"v_mfma_f32_16x16x32_bf16 v[136:139], a[112:115], v[92:95], v[136:139]\n"
"v_min_u32 v253, v253, v231\n"
"v_mfma_f32_16x16x32_bf16 v[136:139], a[116:119], v[108:111], v[136:139]\n"
"global_store_dwordx4 v247, v[216:219], s[54:55] offset:0 sc1\n"
"ds_write_b64 v244, v[208:209] offset:12288\n"
"v_mfma_f32_16x16x32_bf16 v[136:139], a[120:123], v[112:115], v[136:139]\n"
"v_mfma_f32_16x16x32_bf16 v[136:139], a[124:127], v[116:119], v[136:139]\n"
"v_mfma_f32_16x16x32_bf16 v[132:135], a[172:175], v[116:119], v[8:11]\n"
"v_mfma_f32_16x16x32_bf16 v[132:135], a[168:171], v[112:115], v[132:135]\n"
"v_mfma_f32_16x16x32_bf16 v[132:135], a[164:167], v[108:111], v[132:135]\n"
"v_mfma_f32_16x16x32_bf16 v[132:135], a[160:163], v[92:95], v[132:135]\n"
"v_cvt_pk_bf16_f32 v210, v136, v137\n"
"v_mfma_f32_16x16x32_bf16 v[132:135], a[156:159], v[88:91], v[132:135]\n"
"v_cvt_pk_bf16_f32 v211, v138, v139\n"
"v_mfma_f32_16x16x32_bf16 v[132:135], a[152:155], v[84:87], v[132:135]\n"
"v_mfma_f32_16x16x32_bf16 v[132:135], a[148:151], v[68:71], v[132:135]\n"
"v_mfma_f32_16x16x32_bf16 v[132:135], a[144:147], v[64:67], v[132:135]\n"
"v_mfma_f32_16x16x32_bf16 v[132:135], a[140:143], v[60:63], v[132:135]\n"
"v_mfma_f32_16x16x32_bf16 v[132:135], a[136:139], v[44:47], v[132:135]\n"
"global_store_dwordx4 v252, v[220:223], s[54:55] offset:0 sc1\n"
"ds_write_b64 v244, v[210:211] offset:12296\n"
"v_mfma_f32_16x16x32_bf16 v[132:135], a[132:135], v[40:43], v[132:135]\n"
"s_add_u32 m0, s31, 0x14f00\n"
"v_mfma_f32_16x16x32_bf16 v[132:135], a[128:131], v[36:39], v[132:135]\n"
"global_load_lds_dwordx4 v238, s[52:53] offset:256 nt\n"
"v_mfma_f32_16x16x32_bf16 v[136:139], a[176:179], v[36:39], v[12:15]\n"
"v_mfma_f32_16x16x32_bf16 v[136:139], a[180:183], v[40:43], v[136:139]\n"
"v_mfma_f32_16x16x32_bf16 v[136:139], a[184:187], v[44:47], v[136:139]\n"
"v_mfma_f32_16x16x32_bf16 v[136:139], a[188:191], v[60:63], v[136:139]\n"
"v_cvt_pk_bf16_f32 v212, v132, v133\n"
"v_mfma_f32_16x16x32_bf16 v[136:139], a[192:195], v[64:67], v[136:139]\n"
"v_mov_b32 v242, 19\n"
"v_mfma_f32_16x16x32_bf16 v[136:139], a[196:199], v[68:71], v[136:139]\n"
"v_cvt_pk_bf16_f32 v213, v134, v135\n"
"v_mfma_f32_16x16x32_bf16 v[136:139], a[200:203], v[84:87], v[136:139]\n"
"s_waitcnt lgkmcnt(2)\n"
"ds_write_b32 v240, v242\n"
"v_mfma_f32_16x16x32_bf16 v[136:139], a[204:207], v[88:91], v[136:139]\n"
"v_mfma_f32_16x16x32_bf16 v[136:139], a[208:211], v[92:95], v[136:139]\n"
"v_mfma_f32_16x16x32_bf16 v[136:139], a[212:215], v[108:111], v[136:139]\n"
"v_mfma_f32_16x16x32_bf16 v[136:139], a[216:219], v[112:115], v[136:139]\n"
"global_store_dwordx4 v253, v[224:227], s[54:55] offset:0 sc1\n"
"ds_write_b64 v245, v[212:213] offset:12288\n"
"v_mfma_f32_16x16x32_bf16 v[136:139], a[220:223], v[116:119], v[136:139]\n"
"v_mfma_f32_16x16x32_bf16 v[132:135], v[156:159], v[116:119], v[16:19]\n"
"v_mfma_f32_16x16x32_bf16 v[132:135], v[140:143], v[112:115], v[132:135]\n"
"v_mfma_f32_16x16x32_bf16 v[132:135], a[20:23], v[108:111], v[132:135]\n"
"ds_read_b32 v243, v241\n"
"v_mfma_f32_16x16x32_bf16 v[132:135], a[16:19], v[92:95], v[132:135]\n"
"v_cvt_pk_bf16_f32 v214, v136, v137\n"
"v_mfma_f32_16x16x32_bf16 v[132:135], a[12:15], v[88:91], v[132:135]\n"
"v_cvt_pk_bf16_f32 v215, v138, v139\n"
"v_mfma_f32_16x16x32_bf16 v[132:135], a[8:11], v[84:87], v[132:135]\n"
"v_mfma_f32_16x16x32_bf16 v[132:135], a[4:7], v[68:71], v[132:135]\n"
"s_waitcnt lgkmcnt(0)\n"
"v_cmp_gt_u32 s[56:57], 19, v243\n"
"v_mfma_f32_16x16x32_bf16 v[132:135], a[0:3], v[64:67], v[132:135]\n"
"v_mfma_f32_16x16x32_bf16 v[132:135], a[236:239], v[60:63], v[132:135]\n"
"s_or_b64 s[56:57], s[56:57], vcc\n"
"s_cbranch_scc1 LB19\n"
"LB19R:\n"
"v_mfma_f32_16x16x32_bf16 v[132:135], a[232:235], v[44:47], v[132:135]\n"
"ds_write_b64 v245, v[214:215] offset:12296\n"
"v_mfma_f32_16x16x32_bf16 v[132:135], a[228:231], v[40:43], v[132:135]\n"
"v_mfma_f32_16x16x32_bf16 v[132:135], a[224:227], v[36:39], v[132:135]\n"
"v_mfma_f32_16x16x32_bf16 v[136:139], v[160:163], v[36:39], v[20:23]\n"
"ds_read_b128 v[36:39], v232 offset:24832\n"
"v_mfma_f32_16x16x32_bf16 v[136:139], v[164:167], v[40:43], v[136:139]\n"
"ds_read_b128 v[40:43], v233 offset:24832\n"
"v_mfma_f32_16x16x32_bf16 v[136:139], v[180:183], v[44:47], v[136:139]\n"
"ds_read_b128 v[44:47], v234 offset:24832\n"
"v_mfma_f32_16x16x32_bf16 v[136:139], v[184:187], v[60:63], v[136:139]\n"
"ds_read_b128 v[60:63], v235 offset:24832\n"
"v_cvt_pk_bf16_f32 v208, v132, v133\n"
"v_mfma_f32_16x16x32_bf16 v[136:139], v[188:191], v[64:67], v[136:139]\n"
"ds_read_b128 v[64:67], v232 offset:33024\n"
"v_cvt_pk_bf16_f32 v209, v134, v135\n"
"v_mfma_f32_16x16x32_bf16 v[136:139], v[204:207], v[68:71], v[136:139]\n"
"ds_read_b128 v[68:71], v233 offset:33024\n"
"v_mfma_f32_16x16x32_bf16 v[136:139], a[24:27], v[84:87], v[136:139]\n"
"ds_read_b128 v[84:87], v234 offset:33024\n"
"v_mfma_f32_16x16x32_bf16 v[136:139], a[28:31], v[88:91], v[136:139]\n"
"ds_read_b128 v[88:91], v235 offset:33024\n"
"v_mfma_f32_16x16x32_bf16 v[136:139], a[32:35], v[92:95], v[136:139]\n"
"ds_read_b128 v[92:95], v232 offset:41216\n"
"v_mfma_f32_16x16x32_bf16 v[136:139], a[36:39], v[108:111], v[136:139]\n"
"ds_read_b128 v[108:111], v233 offset:41216\n"
"ds_write_b64 v244, v[208:209] offset:12416\n"
"v_mfma_f32_16x16x32_bf16 v[136:139], a[40:43], v[112:115], v[136:139]\n"
"ds_read_b128 v[112:115], v234 offset:41216\n"
"v_mfma_f32_16x16x32_bf16 v[136:139], a[44:47], v[116:119], v[136:139]\n"
"ds_read_b128 v[116:119], v235 offset:41216\n"
"s_waitcnt lgkmcnt(9)\n"
"v_mfma_f32_16x16x32_bf16 v[132:135], a[240:243], v[36:39], v[0:3]\n"
"v_add_u32 v232, 0xc000, v232\n"
"v_mfma_f32_16x16x32_bf16 v[132:135], a[244:247], v[40:43], v[132:135]\n"
"v_add_u32 v233, 0xc000, v233\n"
"v_mfma_f32_16x16x32_bf16 v[132:135], a[248:251], v[44:47], v[132:135]\n"
"v_add_u32 v234, 0xc000, v234\n"
"v_mfma_f32_16x16x32_bf16 v[132:135], a[252:255], v[60:63], v[132:135]\n"
"v_add_u32 v235, 0xc000, v235\n"
"s_waitcnt lgkmcnt(5)\n"
"v_mfma_f32_16x16x32_bf16 v[132:135], a[48:51], v[64:67], v[132:135]\n"
"s_add_u32 m0, s31, 0x16e00\n"
"v_mfma_f32_16x16x32_bf16 v[132:135], a[52:55], v[68:71], v[132:135]\n"
"global_load_lds_dwordx4 v238, s[52:53] offset:512 nt\n"
"v_mfma_f32_16x16x32_bf16 v[132:135], a[56:59], v[84:87], v[132:135]\n"
"v_cvt_pk_bf16_f32 v210, v136, v137\n"
"s_bitcmp1_b32 s92, 6\n"
"s_cselect_b32 s93, 0x6000, 0\n"
"v_mfma_f32_16x16x32_bf16 v[132:135], a[60:63], v[88:91], v[132:135]\n"
"v_cvt_pk_bf16_f32 v211, v138, v139\n"
"s_sub_u32 s94, s93, 0xc000\n"
"s_waitcnt lgkmcnt(0)\n"
"v_mfma_f32_16x16x32_bf16 v[132:135], a[64:67], v[92:95], v[132:135]\n"
"ds_read_b128 v[216:219], v246 offset:0\n"
"v_add_u32 v231, s93, v237\n"
"v_mfma_f32_16x16x32_bf16 v[132:135], a[68:71], v[108:111], v[132:135]\n"
"ds_read_b128 v[220:223], v246 offset:1024\n"
"v_add_u32 v237, s94, v237\n"
"v_mfma_f32_16x16x32_bf16 v[132:135], a[72:75], v[112:115], v[132:135]\n"
"ds_read_b128 v[224:227], v246 offset:2048\n"
"v_min_u32 v237, v237, v231\n"
"v_mfma_f32_16x16x32_bf16 v[132:135], a[76:79], v[116:119], v[132:135]\n"
"s_add_u32 s52, s20, s44\n"
"s_addc_u32 s53, s21, 0\n"
"v_add_u32 v231, s93, v238\n"
"v_mfma_f32_16x16x32_bf16 v[136:139], a[124:127], v[116:119], v[4:7]\n"
"ds_write_b64 v244, v[210:211] offset:12424\n"
"v_add_u32 v238, s94, v238\n"
"v_mfma_f32_16x16x32_bf16 v[136:139], a[120:123], v[112:115], v[136:139]\n"
"s_add_u32 m0, s31, 0x0\n"
"v_min_u32 v238, v238, v231\n"
"v_mfma_f32_16x16x32_bf16 v[136:139], a[116:119], v[108:111], v[136:139]\n"
"global_load_lds_dwordx4 v237, s[52:53] offset:0 nt\n"
"v_mfma_f32_16x16x32_bf16 v[136:139], a[112:115], v[92:95], v[136:139]\n"
"v_cvt_pk_bf16_f32 v208, v132, v133\n"
"v_mfma_f32_16x16x32_bf16 v[136:139], a[108:111], v[88:91], v[136:139]\n"
"v_cvt_pk_bf16_f32 v209, v134, v135\n"
"v_mfma_f32_16x16x32_bf16 v[136:139], a[104:107], v[84:87], v[136:139]\n"
"v_lshlrev_b32 v0, 16, v120\n"
"v_mfma_f32_16x16x32_bf16 v[136:139], a[100:103], v[68:71], v[136:139]\n"
"v_and_b32 v1, 0xffff0000, v120\n"
"v_mfma_f32_16x16x32_bf16 v[136:139], a[96:99], v[64:67], v[136:139]\n"
"s_waitcnt lgkmcnt(1)\n"
"v_mfma_f32_16x16x32_bf16 v[136:139], a[92:95], v[60:63], v[136:139]\n"
"v_lshlrev_b32 v2, 16, v121\n"
"v_mfma_f32_16x16x32_bf16 v[136:139], a[88:91], v[44:47], v[136:139]\n"
"global_store_dwordx4 v247, v[216:219], s[54:55] offset:768 sc1\n"
"ds_write_b64 v244, v[208:209] offset:24576\n"
"v_mfma_f32_16x16x32_bf16 v[136:139], a[84:87], v[40:43], v[136:139]\n"
"v_and_b32 v3, 0xffff0000, v121\n"
"v_mfma_f32_16x16x32_bf16 v[136:139], a[80:83], v[36:39], v[136:139]\n"
"v_lshlrev_b32 v4, 16, v122\n"
"v_mfma_f32_16x16x32_bf16 v[132:135], a[128:131], v[36:39], v[8:11]\n"
"v_and_b32 v5, 0xffff0000, v122\n"
"v_mfma_f32_16x16x32_bf16 v[132:135], a[132:135], v[40:43], v[132:135]\n"
"v_lshlrev_b32 v6, 16, v123\n"
"v_mfma_f32_16x16x32_bf16 v[132:135], a[136:139], v[44:47], v[132:135]\n"
"v_and_b32 v7, 0xffff0000, v123\n"
"v_mfma_f32_16x16x32_bf16 v[132:135], a[140:143], v[60:63], v[132:135]\n"
"v_cvt_pk_bf16_f32 v210, v136, v137\n"
"v_mfma_f32_16x16x32_bf16 v[132:135], a[144:147], v[64:67], v[132:135]\n"
"v_cvt_pk_bf16_f32 v211, v138, v139\n"
"v_mfma_f32_16x16x32_bf16 v[132:135], a[148:151], v[68:71], v[132:135]\n"
"v_lshlrev_b32 v8, 16, v124\n"
"v_mfma_f32_16x16x32_bf16 v[132:135], a[152:155], v[84:87], v[132:135]\n"
"v_and_b32 v9, 0xffff0000, v124\n"
"v_mfma_f32_16x16x32_bf16 v[132:135], a[156:159], v[88:91], v[132:135]\n"
"v_lshlrev_b32 v10, 16, v125\n"
"v_mfma_f32_16x16x32_bf16 v[132:135], a[160:163], v[92:95], v[132:135]\n"
"v_and_b32 v11, 0xffff0000, v125\n"
"v_mfma_f32_16x16x32_bf16 v[132:135], a[164:167], v[108:111], v[132:135]\n"
"s_memrealtime s[84:85]\n"
"v_mfma_f32_16x16x32_bf16 v[132:135], a[168:171], v[112:115], v[132:135]\n"
"global_store_dwordx4 v252, v[220:223], s[54:55] offset:768 sc1\n"
"ds_write_b64 v244, v[210:211] offset:24584\n"
"v_mfma_f32_16x16x32_bf16 v[132:135], a[172:175], v[116:119], v[132:135]\n"
"s_add_u32 m0, s31, 0x1f00\n"
"v_mfma_f32_16x16x32_bf16 v[136:139], a[220:223], v[116:119], v[12:15]\n"
"global_load_lds_dwordx4 v237, s[52:53] offset:256 nt\n"
"v_mfma_f32_16x16x32_bf16 v[136:139], a[216:219], v[112:115], v[136:139]\n"
"s_add_u32 s83, s83, s86\n"
"v_mfma_f32_16x16x32_bf16 v[136:139], a[212:215], v[108:111], v[136:139]\n"
"s_sub_i32 s88, s88, 6\n"
"v_mfma_f32_16x16x32_bf16 v[136:139], a[208:211], v[92:95], v[136:139]\n"
"v_cvt_pk_bf16_f32 v212, v132, v133\n"
"v_mfma_f32_16x16x32_bf16 v[136:139], a[204:207], v[88:91], v[136:139]\n"
"v_mov_b32 v242, 20\n"
"v_mfma_f32_16x16x32_bf16 v[136:139], a[200:203], v[84:87], v[136:139]\n"
"v_cvt_pk_bf16_f32 v213, v134, v135\n"
"v_mfma_f32_16x16x32_bf16 v[136:139], a[196:199], v[68:71], v[136:139]\n"
"s_waitcnt vmcnt(16) lgkmcnt(2)\n"
"ds_write_b32 v240, v242\n"
"v_mfma_f32_16x16x32_bf16 v[136:139], a[192:195], v[64:67], v[136:139]\n"
"v_lshlrev_b32 v12, 16, v126\n"
"v_mfma_f32_16x16x32_bf16 v[136:139], a[188:191], v[60:63], v[136:139]\n"
"v_and_b32 v13, 0xffff0000, v126\n"
"v_mfma_f32_16x16x32_bf16 v[136:139], a[184:187], v[44:47], v[136:139]\n"
"v_lshlrev_b32 v14, 16, v127\n"
"v_mfma_f32_16x16x32_bf16 v[136:139], a[180:183], v[40:43], v[136:139]\n"
"global_store_dwordx4 v253, v[224:227], s[54:55] offset:768 sc1\n"
"ds_write_b64 v245, v[212:213] offset:24576\n"
"v_mfma_f32_16x16x32_bf16 v[136:139], a[176:179], v[36:39], v[136:139]\n"
"v_and_b32 v15, 0xffff0000, v127\n"
"v_mfma_f32_16x16x32_bf16 v[132:135], a[224:227], v[36:39], v[16:19]\n"
"s_max_i32 s88, s88, 0\n"
"v_mfma_f32_16x16x32_bf16 v[132:135], a[228:231], v[40:43], v[132:135]\n"
"s_mov_b64 exec, s[8:9]\n"
"s_cbranch_execz LHT10\n"
"LHR10:\n"
"s_mov_b64 exec, -1\n"
"v_mfma_f32_16x16x32_bf16 v[132:135], a[232:235], v[44:47], v[132:135]\n"
"ds_read_b32 v243, v241\n"
"v_mfma_f32_16x16x32_bf16 v[132:135], a[236:239], v[60:63], v[132:135]\n"
"v_cvt_pk_bf16_f32 v214, v136, v137\n"
"v_mfma_f32_16x16x32_bf16 v[132:135], a[0:3], v[64:67], v[132:135]\n"
"v_cvt_pk_bf16_f32 v215, v138, v139\n"
"v_mfma_f32_16x16x32_bf16 v[132:135], a[4:7], v[68:71], v[132:135]\n"
"v_lshlrev_b32 v16, 16, v128\n"
"v_mfma_f32_16x16x32_bf16 v[132:135], a[8:11], v[84:87], v[132:135]\n"
"s_waitcnt lgkmcnt(0)\n"
"v_cmp_gt_u32 s[56:57], 20, v243\n"
"v_mfma_f32_16x16x32_bf16 v[132:135], a[12:15], v[88:91], v[132:135]\n"
"s_sub_u32 s84, s84, s83\n"
"v_mfma_f32_16x16x32_bf16 v[132:135], a[16:19], v[92:95], v[132:135]\n"
"s_or_b64 s[56:57], s[56:57], vcc\n"
"s_cbranch_scc1 LB20\n"
"LB20R:\n"
"v_mfma_f32_16x16x32_bf16 v[132:135], a[20:23], v[108:111], v[132:135]\n"
"s_cmp_lt_i32 s84, s88\n"
"s_cselect_b64 vcc, -1, 0\n"
"v_mfma_f32_16x16x32_bf16 v[132:135], v[140:143], v[112:115], v[132:135]\n"
"ds_write_b64 v245, v[214:215] offset:24584\n"
"v_mfma_f32_16x16x32_bf16 v[132:135], v[156:159], v[116:119], v[132:135]\n"
"v_and_b32 v17, 0xffff0000, v128\n"
"v_mfma_f32_16x16x32_bf16 v[136:139], a[44:47], v[116:119], v[20:23]\n"
"ds_read_b128 v[116:119], v235 offset:16384\n"
"v_mfma_f32_16x16x32_bf16 v[136:139], a[40:43], v[112:115], v[136:139]\n"
"ds_read_b128 v[112:115], v234 offset:16384\n"
"v_mfma_f32_16x16x32_bf16 v[136:139], a[36:39], v[108:111], v[136:139]\n"
"ds_read_b128 v[108:111], v233 offset:16384\n"
"v_mfma_f32_16x16x32_bf16 v[136:139], a[32:35], v[92:95], v[136:139]\n"
"ds_read_b128 v[92:95], v232 offset:16384\n"
"v_cvt_pk_bf16_f32 v208, v132, v133\n"
"v_mfma_f32_16x16x32_bf16 v[136:139], a[28:31], v[88:91], v[136:139]\n"
"ds_read_b128 v[88:91], v235 offset:8192\n"
"v_cvt_pk_bf16_f32 v209, v134, v135\n"
"v_mfma_f32_16x16x32_bf16 v[136:139], a[24:27], v[84:87], v[136:139]\n"
"ds_read_b128 v[84:87], v234 offset:8192\n"
"v_mfma_f32_16x16x32_bf16 v[136:139], v[204:207], v[68:71], v[136:139]\n"
"ds_read_b128 v[68:71], v233 offset:8192\n"
"v_mfma_f32_16x16x32_bf16 v[136:139], v[188:191], v[64:67], v[136:139]\n"
"ds_read_b128 v[64:67], v232 offset:8192\n"
"v_mfma_f32_16x16x32_bf16 v[136:139], v[184:187], v[60:63], v[136:139]\n"
"ds_read_b128 v[60:63], v235 offset:0\n"
"v_mfma_f32_16x16x32_bf16 v[136:139], v[180:183], v[44:47], v[136:139]\n"
"ds_read_b128 v[44:47], v234 offset:0\n"
"v_mfma_f32_16x16x32_bf16 v[136:139], v[164:167], v[40:43], v[136:139]\n"
"ds_read_b128 v[40:43], v233 offset:0\n"
"v_mfma_f32_16x16x32_bf16 v[136:139], v[160:163], v[36:39], v[136:139]\n"
"ds_read_b128 v[36:39], v232 offset:0\n"
"s_waitcnt lgkmcnt(8)\n"
"v_mfma_f32_16x16x32_bf16 v[132:135], a[76:79], v[116:119], v[0:3]\n"
"ds_write_b64 v244, v[208:209] offset:24704\n"
"v_mfma_f32_16x16x32_bf16 v[132:135], a[72:75], v[112:115], v[132:135]\n"
"s_add_u32 m0, s31, 0x3e00\n"
"v_mfma_f32_16x16x32_bf16 v[132:135], a[68:71], v[108:111], v[132:135]\n"
"global_load_lds_dwordx4 v237, s[52:53] offset:512 nt\n"
"v_mfma_f32_16x16x32_bf16 v[132:135], a[64:67], v[92:95], v[132:135]\n"
"v_lshlrev_b32 v18, 16, v129\n"
"v_cvt_pk_bf16_f32 v211, v138, v139\n"
"s_waitcnt lgkmcnt(5)\n"
"v_mfma_f32_16x16x32_bf16 v[132:135], a[60:63], v[88:91], v[132:135]\n"
"v_and_b32 v19, 0xffff0000, v129\n"
"v_mfma_f32_16x16x32_bf16 v[132:135], a[56:59], v[84:87], v[132:135]\n"
"v_lshlrev_b32 v20, 16, v130\n"
"v_mfma_f32_16x16x32_bf16 v[132:135], a[52:55], v[68:71], v[132:135]\n"
"v_and_b32 v21, 0xffff0000, v130\n"
"v_mfma_f32_16x16x32_bf16 v[132:135], a[48:51], v[64:67], v[132:135]\n"
"v_lshlrev_b32 v22, 16, v131\n"
"s_waitcnt lgkmcnt(1)\n"
"v_mfma_f32_16x16x32_bf16 v[132:135], a[252:255], v[60:63], v[132:135]\n"
"v_and_b32 v23, 0xffff0000, v131\n"
"v_mfma_f32_16x16x32_bf16 v[132:135], a[248:251], v[44:47], v[132:135]\n"
"v_cvt_pk_bf16_f32 v210, v136, v137\n"
"v_mfma_f32_16x16x32_bf16 v[132:135], a[244:247], v[40:43], v[132:135]\n"
"s_add_u32 s54, s26, s41\n"
"s_addc_u32 s55, s27, 0\n"
"v_mfma_f32_16x16x32_bf16 v[132:135], a[240:243], v[36:39], v[132:135]\n"
"ds_read_b128 v[216:219], v246 offset:12288\n"
"v_mfma_f32_16x16x32_bf16 v[136:139], a[80:83], v[36:39], v[4:7]\n"
"ds_read_b128 v[220:223], v246 offset:13312\n"
"v_mfma_f32_16x16x32_bf16 v[136:139], a[84:87], v[40:43], v[136:139]\n"
"ds_read_b128 v[224:227], v246 offset:14336\n"
"LHT10:\n"
"s_cbranch_execz LHR10\n"
"v_mfma_f32_16x16x32_bf16 v[136:139], a[88:91], v[44:47], v[136:139]\n"
"ds_write_b64 v244, v[210:211] offset:24712\n"
"v_mfma_f32_16x16x32_bf16 v[136:139], a[92:95], v[60:63], v[136:139]\n"
"v_cvt_pk_bf16_f32 v208, v132, v133\n"
"v_mfma_f32_16x16x32_bf16 v[136:139], a[96:99], v[64:67], v[136:139]\n"
"v_cvt_pk_bf16_f32 v209, v134, v135\n"
"v_mfma_f32_16x16x32_bf16 v[136:139], a[100:103], v[68:71], v[136:139]\n"
"s_add_u32 m0, s31, 0x1000\n"
"v_mfma_f32_16x16x32_bf16 v[136:139], a[104:107], v[84:87], v[136:139]\n"
"global_load_lds_dwordx4 v238, s[52:53] offset:0 nt\n"
"v_mfma_f32_16x16x32_bf16 v[136:139], a[108:111], v[88:91], v[136:139]\n"
"s_waitcnt lgkmcnt(1)\n"
"v_mfma_f32_16x16x32_bf16 v[136:139], a[112:115], v[92:95], v[136:139]\n"
"s_sub_i32 s84, s84, s19\n"
"v_mfma_f32_16x16x32_bf16 v[136:139], a[116:119], v[108:111], v[136:139]\n"
"global_store_dwordx4 v247, v[216:219], s[54:55] offset:0 sc1\n"
"ds_write_b64 v244, v[208:209] offset:0\n"
"v_mfma_f32_16x16x32_bf16 v[136:139], a[120:123], v[112:115], v[136:139]\n"
"s_max_i32 s88, s88, s84\n"
"v_mfma_f32_16x16x32_bf16 v[136:139], a[124:127], v[116:119], v[136:139]\n"
"v_mfma_f32_16x16x32_bf16 v[132:135], a[172:175], v[116:119], v[8:11]\n"
"v_mfma_f32_16x16x32_bf16 v[132:135], a[168:171], v[112:115], v[132:135]\n"
"v_mfma_f32_16x16x32_bf16 v[132:135], a[164:167], v[108:111], v[132:135]\n"
"v_mfma_f32_16x16x32_bf16 v[132:135], a[160:163], v[92:95], v[132:135]\n"
"v_cvt_pk_bf16_f32 v210, v136, v137\n"
"v_mfma_f32_16x16x32_bf16 v[132:135], a[156:159], v[88:91], v[132:135]\n"
"v_cvt_pk_bf16_f32 v211, v138, v139\n"
"v_mfma_f32_16x16x32_bf16 v[132:135], a[152:155], v[84:87], v[132:135]\n"
"v_mfma_f32_16x16x32_bf16 v[132:135], a[148:151], v[68:71], v[132:135]\n"
"v_mfma_f32_16x16x32_bf16 v[132:135], a[144:147], v[64:67], v[132:135]\n"
"v_mfma_f32_16x16x32_bf16 v[132:135], a[140:143], v[60:63], v[132:135]\n"
"v_mfma_f32_16x16x32_bf16 v[132:135], a[136:139], v[44:47], v[132:135]\n"
"global_store_dwordx4 v252, v[220:223], s[54:55] offset:0 sc1\n"
"ds_write_b64 v244, v[210:211] offset:8\n"
"v_mfma_f32_16x16x32_bf16 v[132:135], a[132:135], v[40:43], v[132:135]\n"
"v_mfma_f32_16x16x32_bf16 v[132:135], a[128:131], v[36:39], v[132:135]\n"
"v_mfma_f32_16x16x32_bf16 v[136:139], a[176:179], v[36:39], v[12:15]\n"
"v_mfma_f32_16x16x32_bf16 v[136:139], a[180:183], v[40:43], v[136:139]\n"
"v_mfma_f32_16x16x32_bf16 v[136:139], a[184:187], v[44:47], v[136:139]\n"
"s_add_u32 m0, s31, 0x2f00\n"
"v_mfma_f32_16x16x32_bf16 v[136:139], a[188:191], v[60:63], v[136:139]\n"
"v_cvt_pk_bf16_f32 v212, v132, v133\n"
"v_mfma_f32_16x16x32_bf16 v[136:139], a[192:195], v[64:67], v[136:139]\n"
"v_mov_b32 v242, 21\n"
"v_mfma_f32_16x16x32_bf16 v[136:139], a[196:199], v[68:71], v[136:139]\n"
"v_cvt_pk_bf16_f32 v213, v134, v135\n"
"v_mfma_f32_16x16x32_bf16 v[136:139], a[200:203], v[84:87], v[136:139]\n"
"s_waitcnt lgkmcnt(2)\n"
"ds_write_b32 v240, v242\n"
"v_mfma_f32_16x16x32_bf16 v[136:139], a[204:207], v[88:91], v[136:139]\n"
"global_load_lds_dwordx4 v238, s[52:53] offset:256 nt\n"
"v_mfma_f32_16x16x32_bf16 v[136:139], a[208:211], v[92:95], v[136:139]\n"
"v_mfma_f32_16x16x32_bf16 v[136:139], a[212:215], v[108:111], v[136:139]\n"
"v_mfma_f32_16x16x32_bf16 v[136:139], a[216:219], v[112:115], v[136:139]\n"
"global_store_dwordx4 v253, v[224:227], s[54:55] offset:0 sc1\n"
"ds_write_b64 v245, v[212:213] offset:0\n"
"v_mfma_f32_16x16x32_bf16 v[136:139], a[220:223], v[116:119], v[136:139]\n"
"v_mfma_f32_16x16x32_bf16 v[132:135], v[156:159], v[116:119], v[16:19]\n"
"v_mfma_f32_16x16x32_bf16 v[132:135], v[140:143], v[112:115], v[132:135]\n"
"v_mfma_f32_16x16x32_bf16 v[132:135], a[20:23], v[108:111], v[132:135]\n"
"ds_read_b32 v243, v241\n"
"v_mfma_f32_16x16x32_bf16 v[132:135], a[16:19], v[92:95], v[132:135]\n"
"v_cvt_pk_bf16_f32 v214, v136, v137\n"
"v_mfma_f32_16x16x32_bf16 v[132:135], a[12:15], v[88:91], v[132:135]\n"
"v_cvt_pk_bf16_f32 v215, v138, v139\n"
"v_mfma_f32_16x16x32_bf16 v[132:135], a[8:11], v[84:87], v[132:135]\n"
"v_mfma_f32_16x16x32_bf16 v[132:135], a[4:7], v[68:71], v[132:135]\n"
"s_waitcnt lgkmcnt(0)\n"
"v_cmp_gt_u32 s[56:57], 21, v243\n"
"v_mfma_f32_16x16x32_bf16 v[132:135], a[0:3], v[64:67], v[132:135]\n"
"v_mfma_f32_16x16x32_bf16 v[132:135], a[236:239], v[60:63], v[132:135]\n"
"s_or_b64 s[56:57], s[56:57], vcc\n"
"s_cbranch_scc1 LB21\n"
"LB21R:\n"
"v_mfma_f32_16x16x32_bf16 v[132:135], a[232:235], v[44:47], v[132:135]\n"
"ds_write_b64 v245, v[214:215] offset:8\n"
"v_mfma_f32_16x16x32_bf16 v[132:135], a[228:231], v[40:43], v[132:135]\n"
"v_mfma_f32_16x16x32_bf16 v[132:135], a[224:227], v[36:39], v[132:135]\n"
"v_mfma_f32_16x16x32_bf16 v[136:139], v[160:163], v[36:39], v[20:23]\n"
"ds_read_b128 v[36:39], v232 offset:256\n"
"v_mfma_f32_16x16x32_bf16 v[136:139], v[164:167], v[40:43], v[136:139]\n"
"ds_read_b128 v[40:43], v233 offset:256\n"
"v_mfma_f32_16x16x32_bf16 v[136:139], v[180:183], v[44:47], v[136:139]\n"
"ds_read_b128 v[44:47], v234 offset:256\n"
"v_mfma_f32_16x16x32_bf16 v[136:139], v[184:187], v[60:63], v[136:139]\n"
"ds_read_b128 v[60:63], v235 offset:256\n"
"v_cvt_pk_bf16_f32 v208, v132, v133\n"
"v_mfma_f32_16x16x32_bf16 v[136:139], v[188:191], v[64:67], v[136:139]\n"
"ds_read_b128 v[64:67], v232 offset:8448\n"
"v_cvt_pk_bf16_f32 v209, v134, v135\n"
"v_mfma_f32_16x16x32_bf16 v[136:139], v[204:207], v[68:71], v[136:139]\n"
"ds_read_b128 v[68:71], v233 offset:8448\n"
"v_mfma_f32_16x16x32_bf16 v[136:139], a[24:27], v[84:87], v[136:139]\n"
"ds_read_b128 v[84:87], v234 offset:8448\n"
"v_mfma_f32_16x16x32_bf16 v[136:139], a[28:31], v[88:91], v[136:139]\n"
"ds_read_b128 v[88:91], v235 offset:8448\n"
"v_mfma_f32_16x16x32_bf16 v[136:139], a[32:35], v[92:95], v[136:139]\n"
"ds_read_b128 v[92:95], v232 offset:16640\n"
"v_mfma_f32_16x16x32_bf16 v[136:139], a[36:39], v[108:111], v[136:139]\n"
"ds_read_b128 v[108:111], v233 offset:16640\n"
"v_mfma_f32_16x16x32_bf16 v[136:139], a[40:43], v[112:115], v[136:139]\n"
"ds_read_b128 v[112:115], v234 offset:16640\n"
"v_mfma_f32_16x16x32_bf16 v[136:139], a[44:47], v[116:119], v[136:139]\n"
"ds_read_b128 v[116:119], v235 offset:16640\n"
"s_waitcnt lgkmcnt(8)\n"
"v_mfma_f32_16x16x32_bf16 v[132:135], a[240:243], v[36:39], v[0:3]\n"
"ds_write_b64 v244, v[208:209] offset:128\n"
"v_mfma_f32_16x16x32_bf16 v[132:135], a[244:247], v[40:43], v[132:135]\n"
"s_add_u32 m0, s31, 0x4e00\n"
"v_mfma_f32_16x16x32_bf16 v[132:135], a[248:251], v[44:47], v[132:135]\n"
"global_load_lds_dwordx4 v238, s[52:53] offset:512 nt\n"
"v_mfma_f32_16x16x32_bf16 v[132:135], a[252:255], v[60:63], v[132:135]\n"
"v_cvt_pk_bf16_f32 v210, v136, v137\n"
"s_waitcnt lgkmcnt(5)\n"
"v_mfma_f32_16x16x32_bf16 v[132:135], a[48:51], v[64:67], v[132:135]\n"
"v_cvt_pk_bf16_f32 v211, v138, v139\n"
"v_mfma_f32_16x16x32_bf16 v[132:135], a[52:55], v[68:71], v[132:135]\n"
"v_mfma_f32_16x16x32_bf16 v[132:135], a[56:59], v[84:87], v[132:135]\n"
"ds_read_b128 v[216:219], v246 offset:24576\n"
"v_mfma_f32_16x16x32_bf16 v[132:135], a[60:63], v[88:91], v[132:135]\n"
"ds_read_b128 v[220:223], v246 offset:25600\n"
"s_waitcnt lgkmcnt(3)\n"
"v_mfma_f32_16x16x32_bf16 v[132:135], a[64:67], v[92:95], v[132:135]\n"
"ds_read_b128 v[224:227], v246 offset:26624\n"
"v_mfma_f32_16x16x32_bf16 v[132:135], a[68:71], v[108:111], v[132:135]\n"
"ds_write_b64 v244, v[210:211] offset:136\n"
"v_mfma_f32_16x16x32_bf16 v[132:135], a[72:75], v[112:115], v[132:135]\n"
"v_mfma_f32_16x16x32_bf16 v[132:135], a[76:79], v[116:119], v[132:135]\n"
"v_mfma_f32_16x16x32_bf16 v[136:139], a[124:127], v[116:119], v[4:7]\n"
"s_add_u32 s83, s83, s86\n"
"v_mfma_f32_16x16x32_bf16 v[136:139], a[120:123], v[112:115], v[136:139]\n"
"s_sub_i32 s88, s88, 6\n"
"v_mfma_f32_16x16x32_bf16 v[136:139], a[116:119], v[108:111], v[136:139]\n"
"s_max_i32 s88, s88, 0\n"
"v_mfma_f32_16x16x32_bf16 v[136:139], a[112:115], v[92:95], v[136:139]\n"
"v_cvt_pk_bf16_f32 v208, v132, v133\n"
"v_mfma_f32_16x16x32_bf16 v[136:139], a[108:111], v[88:91], v[136:139]\n"
"v_cvt_pk_bf16_f32 v209, v134, v135\n"
"v_mfma_f32_16x16x32_bf16 v[136:139], a[104:107], v[84:87], v[136:139]\n"
"s_add_u32 s52, s20, s45\n"
"s_addc_u32 s53, s21, 0\n"
"v_mfma_f32_16x16x32_bf16 v[136:139], a[100:103], v[68:71], v[136:139]\n"
"s_add_u32 m0, s31, 0x6000\n"
"v_mfma_f32_16x16x32_bf16 v[136:139], a[96:99], v[64:67], v[136:139]\n"
"s_waitcnt lgkmcnt(1)\n"
"v_mfma_f32_16x16x32_bf16 v[136:139], a[92:95], v[60:63], v[136:139]\n"
"global_load_lds_dwordx4 v237, s[52:53] offset:0 nt\n"
"v_mfma_f32_16x16x32_bf16 v[136:139], a[88:91], v[44:47], v[136:139]\n"
"global_store_dwordx4 v247, v[216:219], s[54:55] offset:768 sc1\n"
"ds_write_b64 v244, v[208:209] offset:12288\n"
"v_mfma_f32_16x16x32_bf16 v[136:139], a[84:87], v[40:43], v[136:139]\n"
"v_mfma_f32_16x16x32_bf16 v[136:139], a[80:83], v[36:39], v[136:139]\n"
"v_mfma_f32_16x16x32_bf16 v[132:135], a[128:131], v[36:39], v[8:11]\n"
"v_mfma_f32_16x16x32_bf16 v[132:135], a[132:135], v[40:43], v[132:135]\n"
"v_mfma_f32_16x16x32_bf16 v[132:135], a[136:139], v[44:47], v[132:135]\n"
"v_mfma_f32_16x16x32_bf16 v[132:135], a[140:143], v[60:63], v[132:135]\n"
"v_cvt_pk_bf16_f32 v210, v136, v137\n"
"v_mfma_f32_16x16x32_bf16 v[132:135], a[144:147], v[64:67], v[132:135]\n"
"v_cvt_pk_bf16_f32 v211, v138, v139\n"
"v_mfma_f32_16x16x32_bf16 v[132:135], a[148:151], v[68:71], v[132:135]\n"
"v_mfma_f32_16x16x32_bf16 v[132:135], a[152:155], v[84:87], v[132:135]\n"
"v_mfma_f32_16x16x32_bf16 v[132:135], a[156:159], v[88:91], v[132:135]\n"
"v_mfma_f32_16x16x32_bf16 v[132:135], a[160:163], v[92:95], v[132:135]\n"
"v_mfma_f32_16x16x32_bf16 v[132:135], a[164:167], v[108:111], v[132:135]\n"
"s_memrealtime s[84:85]\n"
"v_mfma_f32_16x16x32_bf16 v[132:135], a[168:171], v[112:115], v[132:135]\n"
"global_store_dwordx4 v252, v[220:223], s[54:55] offset:768 sc1\n"
"ds_write_b64 v244, v[210:211] offset:12296\n"
"v_mfma_f32_16x16x32_bf16 v[132:135], a[172:175], v[116:119], v[132:135]\n"
"v_mfma_f32_16x16x32_bf16 v[136:139], a[220:223], v[116:119], v[12:15]\n"
"v_mfma_f32_16x16x32_bf16 v[136:139], a[216:219], v[112:115], v[136:139]\n"
"v_mfma_f32_16x16x32_bf16 v[136:139], a[212:215], v[108:111], v[136:139]\n"
"v_mfma_f32_16x16x32_bf16 v[136:139], a[208:211], v[92:95], v[136:139]\n"
"v_cvt_pk_bf16_f32 v212, v132, v133\n"
"v_mfma_f32_16x16x32_bf16 v[136:139], a[204:207], v[88:91], v[136:139]\n"
"v_mov_b32 v242, 22\n"
"v_mfma_f32_16x16x32_bf16 v[136:139], a[200:203], v[84:87], v[136:139]\n"
"v_cvt_pk_bf16_f32 v213, v134, v135\n"
"v_mfma_f32_16x16x32_bf16 v[136:139], a[196:199], v[68:71], v[136:139]\n"
"s_waitcnt vmcnt(15) lgkmcnt(2)\n"
"ds_write_b32 v240, v242\n"
"v_mfma_f32_16x16x32_bf16 v[136:139], a[192:195], v[64:67], v[136:139]\n"
"s_add_u32 m0, s31, 0x7f00\n"
"v_mfma_f32_16x16x32_bf16 v[136:139], a[188:191], v[60:63], v[136:139]\n"
"global_load_lds_dwordx4 v237, s[52:53] offset:256 nt\n"
"v_mfma_f32_16x16x32_bf16 v[136:139], a[184:187], v[44:47], v[136:139]\n"
"v_mfma_f32_16x16x32_bf16 v[136:139], a[180:183], v[40:43], v[136:139]\n"
"global_store_dwordx4 v253, v[224:227], s[54:55] offset:768 sc1\n"
"ds_write_b64 v245, v[212:213] offset:12288\n"
"v_mfma_f32_16x16x32_bf16 v[136:139], a[176:179], v[36:39], v[136:139]\n"
"s_bitcmp1_b32 s92, 5\n"
"s_cselect_b32 s93, 0x6000, 0\n"
"v_mfma_f32_16x16x32_bf16 v[132:135], a[224:227], v[36:39], v[16:19]\n"
"s_sub_u32 s94, s93, 0xc000\n"
"v_mfma_f32_16x16x32_bf16 v[132:135], a[228:231], v[40:43], v[132:135]\n"
"v_add_u32 v231, s93, v247\n"
"v_mfma_f32_16x16x32_bf16 v[132:135], a[232:235], v[44:47], v[132:135]\n"
"ds_read_b32 v243, v241\n"
"v_mfma_f32_16x16x32_bf16 v[132:135], a[236:239], v[60:63], v[132:135]\n"
"v_cvt_pk_bf16_f32 v214, v136, v137\n"
"v_mfma_f32_16x16x32_bf16 v[132:135], a[0:3], v[64:67], v[132:135]\n"
"v_cvt_pk_bf16_f32 v215, v138, v139\n"
"v_mfma_f32_16x16x32_bf16 v[132:135], a[4:7], v[68:71], v[132:135]\n"
"v_add_u32 v247, s94, v247\n"
"v_mfma_f32_16x16x32_bf16 v[132:135], a[8:11], v[84:87], v[132:135]\n"
"s_waitcnt lgkmcnt(0)\n"
"v_cmp_gt_u32 s[56:57], 22, v243\n"
"v_mfma_f32_16x16x32_bf16 v[132:135], a[12:15], v[88:91], v[132:135]\n"
"s_sub_u32 s84, s84, s83\n"
"v_mfma_f32_16x16x32_bf16 v[132:135], a[16:19], v[92:95], v[132:135]\n"
"s_or_b64 s[56:57], s[56:57], vcc\n"
"s_cbranch_scc1 LB22\n"
"LB22R:\n"
"v_mfma_f32_16x16x32_bf16 v[132:135], a[20:23], v[108:111], v[132:135]\n"
"s_cmp_lt_i32 s84, s88\n"
"s_cselect_b64 vcc, -1, 0\n"
"v_mfma_f32_16x16x32_bf16 v[132:135], v[140:143], v[112:115], v[132:135]\n"
"ds_write_b64 v245, v[214:215] offset:12296\n"
"v_mfma_f32_16x16x32_bf16 v[132:135], v[156:159], v[116:119], v[132:135]\n"
"v_min_u32 v247, v247, v231\n"
"v_mfma_f32_16x16x32_bf16 v[136:139], a[44:47], v[116:119], v[20:23]\n"
"ds_read_b128 v[116:119], v235 offset:40960\n"
"v_mfma_f32_16x16x32_bf16 v[136:139], a[40:43], v[112:115], v[136:139]\n"
"ds_read_b128 v[112:115], v234 offset:40960\n"
"v_mfma_f32_16x16x32_bf16 v[136:139], a[36:39], v[108:111], v[136:139]\n"
"ds_read_b128 v[108:111], v233 offset:40960\n"
"v_mfma_f32_16x16x32_bf16 v[136:139], a[32:35], v[92:95], v[136:139]\n"
"ds_read_b128 v[92:95], v232 offset:40960\n"
"v_cvt_pk_bf16_f32 v208, v132, v133\n"
"v_mfma_f32_16x16x32_bf16 v[136:139], a[28:31], v[88:91], v[136:139]\n"
"ds_read_b128 v[88:91], v235 offset:32768\n"
"v_cvt_pk_bf16_f32 v209, v134, v135\n"
"v_mfma_f32_16x16x32_bf16 v[136:139], a[24:27], v[84:87], v[136:139]\n"
"ds_read_b128 v[84:87], v234 offset:32768\n"
"v_mfma_f32_16x16x32_bf16 v[136:139], v[204:207], v[68:71], v[136:139]\n"
"ds_read_b128 v[68:71], v233 offset:32768\n"
"v_mfma_f32_16x16x32_bf16 v[136:139], v[188:191], v[64:67], v[136:139]\n"
"ds_read_b128 v[64:67], v232 offset:32768\n"
"v_mfma_f32_16x16x32_bf16 v[136:139], v[184:187], v[60:63], v[136:139]\n"
"ds_read_b128 v[60:63], v235 offset:24576\n"
"v_mfma_f32_16x16x32_bf16 v[136:139], v[180:183], v[44:47], v[136:139]\n"
"ds_read_b128 v[44:47], v234 offset:24576\n"
"v_mfma_f32_16x16x32_bf16 v[136:139], v[164:167], v[40:43], v[136:139]\n"
"ds_read_b128 v[40:43], v233 offset:24576\n"
"v_mfma_f32_16x16x32_bf16 v[136:139], v[160:163], v[36:39], v[136:139]\n"
"ds_read_b128 v[36:39], v232 offset:24576\n"
"s_waitcnt lgkmcnt(8)\n"
"v_mfma_f32_16x16x32_bf16 v[132:135], a[76:79], v[116:119], v[0:3]\n"
"ds_write_b64 v244, v[208:209] offset:12416\n"
"v_mfma_f32_16x16x32_bf16 v[132:135], a[72:75], v[112:115], v[132:135]\n"
"s_add_u32 m0, s31, 0x9e00\n"
"v_mfma_f32_16x16x32_bf16 v[132:135], a[68:71], v[108:111], v[132:135]\n"
"global_load_lds_dwordx4 v237, s[52:53] offset:512 nt\n"
"v_mfma_f32_16x16x32_bf16 v[132:135], a[64:67], v[92:95], v[132:135]\n"
"v_cvt_pk_bf16_f32 v210, v136, v137\n"
"s_waitcnt lgkmcnt(5)\n"
"v_mfma_f32_16x16x32_bf16 v[132:135], a[60:63], v[88:91], v[132:135]\n"
"v_cvt_pk_bf16_f32 v211, v138, v139\n"
"v_mfma_f32_16x16x32_bf16 v[132:135], a[56:59], v[84:87], v[132:135]\n"
"s_add_u32 s54, s26, s42\n"
"s_addc_u32 s55, s27, 0\n"
"v_mfma_f32_16x16x32_bf16 v[132:135], a[52:55], v[68:71], v[132:135]\n"
"ds_read_b128 v[216:219], v246 offset:0\n"
"v_mfma_f32_16x16x32_bf16 v[132:135], a[48:51], v[64:67], v[132:135]\n"
"ds_read_b128 v[220:223], v246 offset:1024\n"
"s_waitcnt lgkmcnt(3)\n"
"v_mfma_f32_16x16x32_bf16 v[132:135], a[252:255], v[60:63], v[132:135]\n"
"ds_read_b128 v[224:227], v246 offset:2048\n"
"v_mfma_f32_16x16x32_bf16 v[132:135], a[248:251], v[44:47], v[132:135]\n"
"ds_write_b64 v244, v[210:211] offset:12424\n"
"v_mfma_f32_16x16x32_bf16 v[132:135], a[244:247], v[40:43], v[132:135]\n"
"s_sub_i32 s84, s84, s19\n"
"v_mfma_f32_16x16x32_bf16 v[132:135], a[240:243], v[36:39], v[132:135]\n"
"s_max_i32 s88, s88, s84\n"
"v_mfma_f32_16x16x32_bf16 v[136:139], a[80:83], v[36:39], v[4:7]\n"
"v_add_u32 v231, s93, v252\n"
"v_mfma_f32_16x16x32_bf16 v[136:139], a[84:87], v[40:43], v[136:139]\n"
"v_add_u32 v252, s94, v252\n"
"v_mfma_f32_16x16x32_bf16 v[136:139], a[88:91], v[44:47], v[136:139]\n"
"v_min_u32 v252, v252, v231\n"
"v_mfma_f32_16x16x32_bf16 v[136:139], a[92:95], v[60:63], v[136:139]\n"
"v_cvt_pk_bf16_f32 v208, v132, v133\n"
"v_mfma_f32_16x16x32_bf16 v[136:139], a[96:99], v[64:67], v[136:139]\n"
"v_cvt_pk_bf16_f32 v209, v134, v135\n"
"v_mfma_f32_16x16x32_bf16 v[136:139], a[100:103], v[68:71], v[136:139]\n"
"v_add_u32 v231, s93, v253\n"
"v_mfma_f32_16x16x32_bf16 v[136:139], a[104:107], v[84:87], v[136:139]\n"
"v_add_u32 v253, s94, v253\n"
"v_mfma_f32_16x16x32_bf16 v[136:139], a[108:111], v[88:91], v[136:139]\n"
"s_waitcnt lgkmcnt(1)\n"
"v_mfma_f32_16x16x32_bf16 v[136:139], a[112:115], v[92:95], v[136:139]\n"
"v_min_u32 v253, v253, v231\n"
"v_mfma_f32_16x16x32_bf16 v[136:139], a[116:119], v[108:111], v[136:139]\n"
"global_store_dwordx4 v247, v[216:219], s[54:55] offset:0 sc1\n"
"ds_write_b64 v244, v[208:209] offset:24576\n"
"v_mfma_f32_16x16x32_bf16 v[136:139], a[120:123], v[112:115], v[136:139]\n"
"s_add_u32 m0, s31, 0x7000\n"
"v_mfma_f32_16x16x32_bf16 v[136:139], a[124:127], v[116:119], v[136:139]\n"
"global_load_lds_dwordx4 v238, s[52:53] offset:0 nt\n"
"v_mfma_f32_16x16x32_bf16 v[132:135], a[172:175], v[116:119], v[8:11]\n"
"v_mfma_f32_16x16x32_bf16 v[132:135], a[168:171], v[112:115], v[132:135]\n"
"v_mfma_f32_16x16x32_bf16 v[132:135], a[164:167], v[108:111], v[132:135]\n"
"v_mfma_f32_16x16x32_bf16 v[132:135], a[160:163], v[92:95], v[132:135]\n"
"v_cvt_pk_bf16_f32 v210, v136, v137\n"
"v_mfma_f32_16x16x32_bf16 v[132:135], a[156:159], v[88:91], v[132:135]\n"
"v_cvt_pk_bf16_f32 v211, v138, v139\n"
"v_mfma_f32_16x16x32_bf16 v[132:135], a[152:155], v[84:87], v[132:135]\n"
"v_mfma_f32_16x16x32_bf16 v[132:135], a[148:151], v[68:71], v[132:135]\n"
"v_mfma_f32_16x16x32_bf16 v[132:135], a[144:147], v[64:67], v[132:135]\n"
"v_mfma_f32_16x16x32_bf16 v[132:135], a[140:143], v[60:63], v[132:135]\n"
"v_mfma_f32_16x16x32_bf16 v[132:135], a[136:139], v[44:47], v[132:135]\n"
"global_store_dwordx4 v252, v[220:223], s[54:55] offset:0 sc1\n"
"ds_write_b64 v244, v[210:211] offset:24584\n"
"v_mfma_f32_16x16x32_bf16 v[132:135], a[132:135], v[40:43], v[132:135]\n"
"v_mfma_f32_16x16x32_bf16 v[132:135], a[128:131], v[36:39], v[132:135]\n"
"v_mfma_f32_16x16x32_bf16 v[136:139], a[176:179], v[36:39], v[12:15]\n"
"v_mfma_f32_16x16x32_bf16 v[136:139], a[180:183], v[40:43], v[136:139]\n"
"v_mfma_f32_16x16x32_bf16 v[136:139], a[184:187], v[44:47], v[136:139]\n"
"v_mfma_f32_16x16x32_bf16 v[136:139], a[188:191], v[60:63], v[136:139]\n"
"v_cvt_pk_bf16_f32 v212, v132, v133\n"
"v_mfma_f32_16x16x32_bf16 v[136:139], a[192:195], v[64:67], v[136:139]\n"
"v_mov_b32 v242, 23\n"
"v_mfma_f32_16x16x32_bf16 v[136:139], a[196:199], v[68:71], v[136:139]\n"
"v_cvt_pk_bf16_f32 v213, v134, v135\n"
"v_mfma_f32_16x16x32_bf16 v[136:139], a[200:203], v[84:87], v[136:139]\n"
"s_waitcnt lgkmcnt(2)\n"
"ds_write_b32 v240, v242\n"
"v_mfma_f32_16x16x32_bf16 v[136:139], a[204:207], v[88:91], v[136:139]\n"
"v_mfma_f32_16x16x32_bf16 v[136:139], a[208:211], v[92:95], v[136:139]\n"
"v_mfma_f32_16x16x32_bf16 v[136:139], a[212:215], v[108:111], v[136:139]\n"
"v_mfma_f32_16x16x32_bf16 v[136:139], a[216:219], v[112:115], v[136:139]\n"
"global_store_dwordx4 v253, v[224:227], s[54:55] offset:0 sc1\n"
"ds_write_b64 v245, v[212:213] offset:24576\n"
"v_mfma_f32_16x16x32_bf16 v[136:139], a[220:223], v[116:119], v[136:139]\n"
"s_add_u32 m0, s31, 0x8f00\n"
"v_mfma_f32_16x16x32_bf16 v[132:135], v[156:159], v[116:119], v[16:19]\n"
"global_load_lds_dwordx4 v238, s[52:53] offset:256 nt\n"
"v_mfma_f32_16x16x32_bf16 v[132:135], v[140:143], v[112:115], v[132:135]\n"
"v_mfma_f32_16x16x32_bf16 v[132:135], a[20:23], v[108:111], v[132:135]\n"
"ds_read_b32 v243, v241\n"
"v_mfma_f32_16x16x32_bf16 v[132:135], a[16:19], v[92:95], v[132:135]\n"
"v_cvt_pk_bf16_f32 v214, v136, v137\n"
"v_mfma_f32_16x16x32_bf16 v[132:135], a[12:15], v[88:91], v[132:135]\n"
"v_cvt_pk_bf16_f32 v215, v138, v139\n"
"v_mfma_f32_16x16x32_bf16 v[132:135], a[8:11], v[84:87], v[132:135]\n"
"v_mfma_f32_16x16x32_bf16 v[132:135], a[4:7], v[68:71], v[132:135]\n"
"s_waitcnt lgkmcnt(0)\n"
"v_cmp_gt_u32 s[56:57], 23, v243\n"
"v_mfma_f32_16x16x32_bf16 v[132:135], a[0:3], v[64:67], v[132:135]\n"
"v_mfma_f32_16x16x32_bf16 v[132:135], a[236:239], v[60:63], v[132:135]\n"
"s_or_b64 s[56:57], s[56:57], vcc\n"
"s_cbranch_scc1 LB23\n"
"LB23R:\n"
"v_mfma_f32_16x16x32_bf16 v[132:135], a[232:235], v[44:47], v[132:135]\n"
"ds_write_b64 v245, v[214:215] offset:24584\n"
"v_mfma_f32_16x16x32_bf16 v[132:135], a[228:231], v[40:43], v[132:135]\n"
"v_mfma_f32_16x16x32_bf16 v[132:135], a[224:227], v[36:39], v[132:135]\n"
"v_mfma_f32_16x16x32_bf16 v[136:139], v[160:163], v[36:39], v[20:23]\n"
"ds_read_b128 v[36:39], v232 offset:24832\n"
"v_mfma_f32_16x16x32_bf16 v[136:139], v[164:167], v[40:43], v[136:139]\n"
"ds_read_b128 v[40:43], v233 offset:24832\n"
"v_mfma_f32_16x16x32_bf16 v[136:139], v[180:183], v[44:47], v[136:139]\n"
"ds_read_b128 v[44:47], v234 offset:24832\n"
"v_mfma_f32_16x16x32_bf16 v[136:139], v[184:187], v[60:63], v[136:139]\n"
"ds_read_b128 v[60:63], v235 offset:24832\n"
"v_cvt_pk_bf16_f32 v208, v132, v133\n"
"v_mfma_f32_16x16x32_bf16 v[136:139], v[188:191], v[64:67], v[136:139]\n"
"ds_read_b128 v[64:67], v232 offset:33024\n"
"v_cvt_pk_bf16_f32 v209, v134, v135\n"
"v_mfma_f32_16x16x32_bf16 v[136:139], v[204:207], v[68:71], v[136:139]\n"
"ds_read_b128 v[68:71], v233 offset:33024\n"
"v_mfma_f32_16x16x32_bf16 v[136:139], a[24:27], v[84:87], v[136:139]\n"
"ds_read_b128 v[84:87], v234 offset:33024\n"
"v_mfma_f32_16x16x32_bf16 v[136:139], a[28:31], v[88:91], v[136:139]\n"
"ds_read_b128 v[88:91], v235 offset:33024\n"
"v_mfma_f32_16x16x32_bf16 v[136:139], a[32:35], v[92:95], v[136:139]\n"
"ds_read_b128 v[92:95], v232 offset:41216\n"
"v_mfma_f32_16x16x32_bf16 v[136:139], a[36:39], v[108:111], v[136:139]\n"
"ds_read_b128 v[108:111], v233 offset:41216\n"
"ds_write_b64 v244, v[208:209] offset:24704\n"
"v_mfma_f32_16x16x32_bf16 v[136:139], a[40:43], v[112:115], v[136:139]\n"
"ds_read_b128 v[112:115], v234 offset:41216\n"
"v_mfma_f32_16x16x32_bf16 v[136:139], a[44:47], v[116:119], v[136:139]\n"
"ds_read_b128 v[116:119], v235 offset:41216\n"
"s_waitcnt lgkmcnt(9)\n"
"v_mfma_f32_16x16x32_bf16 v[132:135], a[240:243], v[36:39], v[0:3]\n"
"v_add_u32 v232, 0xffff4000, v232\n"
"v_mfma_f32_16x16x32_bf16 v[132:135], a[244:247], v[40:43], v[132:135]\n"
"v_add_u32 v233, 0xffff4000, v233\n"
"v_mfma_f32_16x16x32_bf16 v[132:135], a[248:251], v[44:47], v[132:135]\n"
"v_add_u32 v234, 0xffff4000, v234\n"
"v_mfma_f32_16x16x32_bf16 v[132:135], a[252:255], v[60:63], v[132:135]\n"
"v_add_u32 v235, 0xffff4000, v235\n"
"s_waitcnt lgkmcnt(5)\n"
"v_mfma_f32_16x16x32_bf16 v[132:135], a[48:51], v[64:67], v[132:135]\n"
"s_add_u32 m0, s31, 0xae00\n"
"v_mfma_f32_16x16x32_bf16 v[132:135], a[52:55], v[68:71], v[132:135]\n"
"global_load_lds_dwordx4 v238, s[52:53] offset:512 nt\n"
"v_mfma_f32_16x16x32_bf16 v[132:135], a[56:59], v[84:87], v[132:135]\n"
"v_cvt_pk_bf16_f32 v210, v136, v137\n"
"s_bitcmp1_b32 s92, 7\n"
"s_cselect_b32 s93, 0x6000, 0\n"
"v_mfma_f32_16x16x32_bf16 v[132:135], a[60:63], v[88:91], v[132:135]\n"
"v_cvt_pk_bf16_f32 v211, v138, v139\n"
"s_waitcnt lgkmcnt(0)\n"
"v_mfma_f32_16x16x32_bf16 v[132:135], a[64:67], v[92:95], v[132:135]\n"
"ds_read_b128 v[216:219], v246 offset:12288\n"
"s_sub_u32 s94, s93, 0xc000\n"
"v_mfma_f32_16x16x32_bf16 v[132:135], a[68:71], v[108:111], v[132:135]\n"
"ds_read_b128 v[220:223], v246 offset:13312\n"
"v_add_u32 v231, s93, v237\n"
"v_mfma_f32_16x16x32_bf16 v[132:135], a[72:75], v[112:115], v[132:135]\n"
"ds_read_b128 v[224:227], v246 offset:14336\n"
"v_add_u32 v237, s94, v237\n"
"v_mfma_f32_16x16x32_bf16 v[132:135], a[76:79], v[116:119], v[132:135]\n"
"v_lshlrev_b32 v0, 16, v144\n"
"v_min_u32 v237, v237, v231\n"
"v_mfma_f32_16x16x32_bf16 v[136:139], a[124:127], v[116:119], v[4:7]\n"
"ds_write_b64 v244, v[210:211] offset:24712\n"
"v_add_u32 v231, s93, v238\n"
"v_mfma_f32_16x16x32_bf16 v[136:139], a[120:123], v[112:115], v[136:139]\n"
"v_and_b32 v1, 0xffff0000, v144\n"
"v_add_u32 v238, s94, v238\n"
"v_mfma_f32_16x16x32_bf16 v[136:139], a[116:119], v[108:111], v[136:139]\n"
"v_lshlrev_b32 v2, 16, v145\n"
"v_min_u32 v238, v238, v231\n"
"v_mfma_f32_16x16x32_bf16 v[136:139], a[112:115], v[92:95], v[136:139]\n"
"v_cvt_pk_bf16_f32 v208, v132, v133\n"
"v_mfma_f32_16x16x32_bf16 v[136:139], a[108:111], v[88:91], v[136:139]\n"
"v_cvt_pk_bf16_f32 v209, v134, v135\n"
"v_mfma_f32_16x16x32_bf16 v[136:139], a[104:107], v[84:87], v[136:139]\n"
"v_and_b32 v3, 0xffff0000, v145\n"
"v_mfma_f32_16x16x32_bf16 v[136:139], a[100:103], v[68:71], v[136:139]\n"
"v_lshlrev_b32 v4, 16, v146\n"
"v_mfma_f32_16x16x32_bf16 v[136:139], a[96:99], v[64:67], v[136:139]\n"
"s_waitcnt lgkmcnt(1)\n"
"v_mfma_f32_16x16x32_bf16 v[136:139], a[92:95], v[60:63], v[136:139]\n"
"v_and_b32 v5, 0xffff0000, v146\n"
"v_mfma_f32_16x16x32_bf16 v[136:139], a[88:91], v[44:47], v[136:139]\n"
"global_store_dwordx4 v247, v[216:219], s[54:55] offset:768 sc1\n"
"ds_write_b64 v244, v[208:209] offset:0\n"
"v_mfma_f32_16x16x32_bf16 v[136:139], a[84:87], v[40:43], v[136:139]\n"
"s_add_u32 s52, s20, s46\n"
"s_addc_u32 s53, s21, 0\n"
"v_mfma_f32_16x16x32_bf16 v[136:139], a[80:83], v[36:39], v[136:139]\n"
"s_add_u32 m0, s31, 0xc000\n"
"v_mfma_f32_16x16x32_bf16 v[132:135], a[128:131], v[36:39], v[8:11]\n"
"global_load_lds_dwordx4 v237, s[52:53] offset:0 nt\n"
"v_mfma_f32_16x16x32_bf16 v[132:135], a[132:135], v[40:43], v[132:135]\n"
"v_lshlrev_b32 v6, 16, v147\n"
"v_mfma_f32_16x16x32_bf16 v[132:135], a[136:139], v[44:47], v[132:135]\n"
"v_and_b32 v7, 0xffff0000, v147\n"
"v_mfma_f32_16x16x32_bf16 v[132:135], a[140:143], v[60:63], v[132:135]\n"
"v_cvt_pk_bf16_f32 v210, v136, v137\n"
"v_mfma_f32_16x16x32_bf16 v[132:135], a[144:147], v[64:67], v[132:135]\n"
"v_cvt_pk_bf16_f32 v211, v138, v139\n"
"v_mfma_f32_16x16x32_bf16 v[132:135], a[148:151], v[68:71], v[132:135]\n"
"v_lshlrev_b32 v8, 16, v148\n"
"v_mfma_f32_16x16x32_bf16 v[132:135], a[152:155], v[84:87], v[132:135]\n"
"v_and_b32 v9, 0xffff0000, v148\n"
"v_mfma_f32_16x16x32_bf16 v[132:135], a[156:159], v[88:91], v[132:135]\n"
"v_lshlrev_b32 v10, 16, v149\n"
"v_mfma_f32_16x16x32_bf16 v[132:135], a[160:163], v[92:95], v[132:135]\n"
"v_and_b32 v11, 0xffff0000, v149\n"
"v_mfma_f32_16x16x32_bf16 v[132:135], a[164:167], v[108:111], v[132:135]\n"
"s_memrealtime s[84:85]\n"
"v_mfma_f32_16x16x32_bf16 v[132:135], a[168:171], v[112:115], v[132:135]\n"
"global_store_dwordx4 v252, v[220:223], s[54:55] offset:768 sc1\n"
"ds_write_b64 v244, v[210:211] offset:8\n"
"v_mfma_f32_16x16x32_bf16 v[132:135], a[172:175], v[116:119], v[132:135]\n"
"s_add_u32 s83, s83, s86\n"
"v_mfma_f32_16x16x32_bf16 v[136:139], a[220:223], v[116:119], v[12:15]\n"
"s_sub_i32 s88, s88, 6\n"
"v_mfma_f32_16x16x32_bf16 v[136:139], a[216:219], v[112:115], v[136:139]\n"
"s_max_i32 s88, s88, 0\n"
"v_mfma_f32_16x16x32_bf16 v[136:139], a[212:215], v[108:111], v[136:139]\n"
"s_mov_b64 exec, s[10:11]\n"
"s_cbranch_execz LHT11\n"
"LHR11:\n"
"s_mov_b64 exec, -1\n"
"v_mfma_f32_16x16x32_bf16 v[136:139], a[208:211], v[92:95], v[136:139]\n"
"v_cvt_pk_bf16_f32 v212, v132, v133\n"
"v_mfma_f32_16x16x32_bf16 v[136:139], a[204:207], v[88:91], v[136:139]\n"
"v_mov_b32 v242, 24\n"
"v_mfma_f32_16x16x32_bf16 v[136:139], a[200:203], v[84:87], v[136:139]\n"
"v_cvt_pk_bf16_f32 v213, v134, v135\n"
"v_mfma_f32_16x16x32_bf16 v[136:139], a[196:199], v[68:71], v[136:139]\n"
"s_waitcnt vmcnt(15) lgkmcnt(2)\n"
"ds_write_b32 v240, v242\n"
"v_mfma_f32_16x16x32_bf16 v[136:139], a[192:195], v[64:67], v[136:139]\n"
"v_lshlrev_b32 v12, 16, v150\n"
"v_mfma_f32_16x16x32_bf16 v[136:139], a[188:191], v[60:63], v[136:139]\n"
"v_and_b32 v13, 0xffff0000, v150\n"
"v_mfma_f32_16x16x32_bf16 v[136:139], a[184:187], v[44:47], v[136:139]\n"
"v_lshlrev_b32 v14, 16, v151\n"
"v_mfma_f32_16x16x32_bf16 v[136:139], a[180:183], v[40:43], v[136:139]\n"
"global_store_dwordx4 v253, v[224:227], s[54:55] offset:768 sc1\n"
"ds_write_b64 v245, v[212:213] offset:0\n"
"v_mfma_f32_16x16x32_bf16 v[136:139], a[176:179], v[36:39], v[136:139]\n"
"v_and_b32 v15, 0xffff0000, v151\n"
"v_mfma_f32_16x16x32_bf16 v[132:135], a[224:227], v[36:39], v[16:19]\n"
"s_add_u32 m0, s31, 0xdf00\n"
"v_mfma_f32_16x16x32_bf16 v[132:135], a[228:231], v[40:43], v[132:135]\n"
"global_load_lds_dwordx4 v237, s[52:53] offset:256 nt\n"
"v_mfma_f32_16x16x32_bf16 v[132:135], a[232:235], v[44:47], v[132:135]\n"
"ds_read_b32 v243, v241\n"
"v_mfma_f32_16x16x32_bf16 v[132:135], a[236:239], v[60:63], v[132:135]\n"
"v_cvt_pk_bf16_f32 v214, v136, v137\n"
"v_mfma_f32_16x16x32_bf16 v[132:135], a[0:3], v[64:67], v[132:135]\n"
"v_cvt_pk_bf16_f32 v215, v138, v139\n"
"v_mfma_f32_16x16x32_bf16 v[132:135], a[4:7], v[68:71], v[132:135]\n"
"v_lshlrev_b32 v16, 16, v152\n"
"v_mfma_f32_16x16x32_bf16 v[132:135], a[8:11], v[84:87], v[132:135]\n"
"s_waitcnt lgkmcnt(0)\n"
"v_cmp_gt_u32 s[56:57], 24, v243\n"
"v_mfma_f32_16x16x32_bf16 v[132:135], a[12:15], v[88:91], v[132:135]\n"
"s_sub_u32 s84, s84, s83\n"
"v_mfma_f32_16x16x32_bf16 v[132:135], a[16:19], v[92:95], v[132:135]\n"
"s_or_b64 s[56:57], s[56:57], vcc\n"
"s_cbranch_scc1 LB24\n"
"LB24R:\n"
"v_mfma_f32_16x16x32_bf16 v[132:135], a[20:23], v[108:111], v[132:135]\n"
"s_cmp_lt_i32 s84, s88\n"
"s_cselect_b64 vcc, -1, 0\n"
"v_mfma_f32_16x16x32_bf16 v[132:135], v[140:143], v[112:115], v[132:135]\n"
"ds_write_b64 v245, v[214:215] offset:8\n"
"v_mfma_f32_16x16x32_bf16 v[132:135], v[156:159], v[116:119], v[132:135]\n"
"v_and_b32 v17, 0xffff0000, v152\n"
"v_mfma_f32_16x16x32_bf16 v[136:139], a[44:47], v[116:119], v[20:23]\n"
"ds_read_b128 v[116:119], v235 offset:16384\n"
"v_mfma_f32_16x16x32_bf16 v[136:139], a[40:43], v[112:115], v[136:139]\n"
"ds_read_b128 v[112:115], v234 offset:16384\n"
"v_mfma_f32_16x16x32_bf16 v[136:139], a[36:39], v[108:111], v[136:139]\n"
"ds_read_b128 v[108:111], v233 offset:16384\n"
"v_mfma_f32_16x16x32_bf16 v[136:139], a[32:35], v[92:95], v[136:139]\n"
"ds_read_b128 v[92:95], v232 offset:16384\n"
"v_cvt_pk_bf16_f32 v208, v132, v133\n"
"v_mfma_f32_16x16x32_bf16 v[136:139], a[28:31], v[88:91], v[136:139]\n"
"ds_read_b128 v[88:91], v235 offset:8192\n"
"v_cvt_pk_bf16_f32 v209, v134, v135\n"
"v_mfma_f32_16x16x32_bf16 v[136:139], a[24:27], v[84:87], v[136:139]\n"
"ds_read_b128 v[84:87], v234 offset:8192\n"
"v_mfma_f32_16x16x32_bf16 v[136:139], v[204:207], v[68:71], v[136:139]\n"
"ds_read_b128 v[68:71], v233 offset:8192\n"
"v_mfma_f32_16x16x32_bf16 v[136:139], v[188:191], v[64:67], v[136:139]\n"
"ds_read_b128 v[64:67], v232 offset:8192\n"
"v_mfma_f32_16x16x32_bf16 v[136:139], v[184:187], v[60:63], v[136:139]\n"
"ds_read_b128 v[60:63], v235 offset:0\n"
"v_mfma_f32_16x16x32_bf16 v[136:139], v[180:183], v[44:47], v[136:139]\n"
"ds_read_b128 v[44:47], v234 offset:0\n"
"v_mfma_f32_16x16x32_bf16 v[136:139], v[164:167], v[40:43], v[136:139]\n"
"ds_read_b128 v[40:43], v233 offset:0\n"
"v_mfma_f32_16x16x32_bf16 v[136:139], v[160:163], v[36:39], v[136:139]\n"
"ds_read_b128 v[36:39], v232 offset:0\n"
"s_waitcnt lgkmcnt(8)\n"
"v_mfma_f32_16x16x32_bf16 v[132:135], a[76:79], v[116:119], v[0:3]\n"
"ds_write_b64 v244, v[208:209] offset:128\n"
"v_mfma_f32_16x16x32_bf16 v[132:135], a[72:75], v[112:115], v[132:135]\n"
"v_lshlrev_b32 v18, 16, v153\n"
"LHT11:\n"
"s_cbranch_execz LHR11\n"
"v_mfma_f32_16x16x32_bf16 v[132:135], a[68:71], v[108:111], v[132:135]\n"
"v_and_b32 v19, 0xffff0000, v153\n"
"v_mfma_f32_16x16x32_bf16 v[132:135], a[64:67], v[92:95], v[132:135]\n"
"v_lshlrev_b32 v20, 16, v154\n"
"s_waitcnt lgkmcnt(5)\n"
"v_mfma_f32_16x16x32_bf16 v[132:135], a[60:63], v[88:91], v[132:135]\n"
"v_and_b32 v21, 0xffff0000, v154\n"
"v_mfma_f32_16x16x32_bf16 v[132:135], a[56:59], v[84:87], v[132:135]\n"
"v_lshlrev_b32 v22, 16, v155\n"
"v_mfma_f32_16x16x32_bf16 v[132:135], a[52:55], v[68:71], v[132:135]\n"
"v_and_b32 v23, 0xffff0000, v155\n"
"v_mfma_f32_16x16x32_bf16 v[132:135], a[48:51], v[64:67], v[132:135]\n"
"v_cvt_pk_bf16_f32 v210, v136, v137\n"
"s_waitcnt lgkmcnt(1)\n"
"v_mfma_f32_16x16x32_bf16 v[132:135], a[252:255], v[60:63], v[132:135]\n"
"v_cvt_pk_bf16_f32 v211, v138, v139\n"
"v_mfma_f32_16x16x32_bf16 v[132:135], a[248:251], v[44:47], v[132:135]\n"
"s_add_u32 s54, s26, s43\n"
"s_addc_u32 s55, s27, 0\n"
"v_mfma_f32_16x16x32_bf16 v[132:135], a[244:247], v[40:43], v[132:135]\n"
"ds_read_b128 v[216:219], v246 offset:24576\n"
"v_mfma_f32_16x16x32_bf16 v[132:135], a[240:243], v[36:39], v[132:135]\n"
"ds_read_b128 v[220:223], v246 offset:25600\n"
"v_mfma_f32_16x16x32_bf16 v[136:139], a[80:83], v[36:39], v[4:7]\n"
"ds_read_b128 v[224:227], v246 offset:26624\n"
"v_mfma_f32_16x16x32_bf16 v[136:139], a[84:87], v[40:43], v[136:139]\n"
"ds_write_b64 v244, v[210:211] offset:136\n"
"v_mfma_f32_16x16x32_bf16 v[136:139], a[88:91], v[44:47], v[136:139]\n"
"s_add_u32 m0, s31, 0xfe00\n"
"v_mfma_f32_16x16x32_bf16 v[136:139], a[92:95], v[60:63], v[136:139]\n"
"v_cvt_pk_bf16_f32 v208, v132, v133\n"
"v_mfma_f32_16x16x32_bf16 v[136:139], a[96:99], v[64:67], v[136:139]\n"
"v_cvt_pk_bf16_f32 v209, v134, v135\n"
"v_mfma_f32_16x16x32_bf16 v[136:139], a[100:103], v[68:71], v[136:139]\n"
"global_load_lds_dwordx4 v237, s[52:53] offset:512 nt\n"
"v_mfma_f32_16x16x32_bf16 v[136:139], a[104:107], v[84:87], v[136:139]\n"
"s_sub_i32 s84, s84, s19\n"
"v_mfma_f32_16x16x32_bf16 v[136:139], a[108:111], v[88:91], v[136:139]\n"
"s_waitcnt lgkmcnt(1)\n"
"v_mfma_f32_16x16x32_bf16 v[136:139], a[112:115], v[92:95], v[136:139]\n"
"s_max_i32 s88, s88, s84\n"
"v_mfma_f32_16x16x32_bf16 v[136:139], a[116:119], v[108:111], v[136:139]\n"
"global_store_dwordx4 v247, v[216:219], s[54:55] offset:0 sc1\n"
"ds_write_b64 v244, v[208:209] offset:12288\n"
"v_mfma_f32_16x16x32_bf16 v[136:139], a[120:123], v[112:115], v[136:139]\n"
"v_mfma_f32_16x16x32_bf16 v[136:139], a[124:127], v[116:119], v[136:139]\n"
"v_mfma_f32_16x16x32_bf16 v[132:135], a[172:175], v[116:119], v[8:11]\n"
"v_mfma_f32_16x16x32_bf16 v[132:135], a[168:171], v[112:115], v[132:135]\n"
"v_mfma_f32_16x16x32_bf16 v[132:135], a[164:167], v[108:111], v[132:135]\n"
"s_add_u32 m0, s31, 0xd000\n"
"v_mfma_f32_16x16x32_bf16 v[132:135], a[160:163], v[92:95], v[132:135]\n"
"v_cvt_pk_bf16_f32 v210, v136, v137\n"
"v_mfma_f32_16x16x32_bf16 v[132:135], a[156:159], v[88:91], v[132:135]\n"
"v_cvt_pk_bf16_f32 v211, v138, v139\n"
"v_mfma_f32_16x16x32_bf16 v[132:135], a[152:155], v[84:87], v[132:135]\n"
"global_load_lds_dwordx4 v238, s[52:53] offset:0 nt\n"
"v_mfma_f32_16x16x32_bf16 v[132:135], a[148:151], v[68:71], v[132:135]\n"
"v_mfma_f32_16x16x32_bf16 v[132:135], a[144:147], v[64:67], v[132:135]\n"
"v_mfma_f32_16x16x32_bf16 v[132:135], a[140:143], v[60:63], v[132:135]\n"
"v_mfma_f32_16x16x32_bf16 v[132:135], a[136:139], v[44:47], v[132:135]\n"
"global_store_dwordx4 v252, v[220:223], s[54:55] offset:0 sc1\n"
"ds_write_b64 v244, v[210:211] offset:12296\n"
"v_mfma_f32_16x16x32_bf16 v[132:135], a[132:135], v[40:43], v[132:135]\n"
"v_mfma_f32_16x16x32_bf16 v[132:135], a[128:131], v[36:39], v[132:135]\n"
"v_mfma_f32_16x16x32_bf16 v[136:139], a[176:179], v[36:39], v[12:15]\n"
"v_mfma_f32_16x16x32_bf16 v[136:139], a[180:183], v[40:43], v[136:139]\n"
"v_mfma_f32_16x16x32_bf16 v[136:139], a[184:187], v[44:47], v[136:139]\n"
"v_mfma_f32_16x16x32_bf16 v[136:139], a[188:191], v[60:63], v[136:139]\n"
"v_cvt_pk_bf16_f32 v212, v132, v133\n"
"v_mfma_f32_16x16x32_bf16 v[136:139], a[192:195], v[64:67], v[136:139]\n"
"v_mov_b32 v242, 25\n"
"v_mfma_f32_16x16x32_bf16 v[136:139], a[196:199], v[68:71], v[136:139]\n"
"v_cvt_pk_bf16_f32 v213, v134, v135\n"
"v_mfma_f32_16x16x32_bf16 v[136:139], a[200:203], v[84:87], v[136:139]\n"
"s_waitcnt lgkmcnt(2)\n"
"ds_write_b32 v240, v242\n"
"v_mfma_f32_16x16x32_bf16 v[136:139], a[204:207], v[88:91], v[136:139]\n"
"v_mfma_f32_16x16x32_bf16 v[136:139], a[208:211], v[92:95], v[136:139]\n"
"v_mfma_f32_16x16x32_bf16 v[136:139], a[212:215], v[108:111], v[136:139]\n"
"v_mfma_f32_16x16x32_bf16 v[136:139], a[216:219], v[112:115], v[136:139]\n"
"global_store_dwordx4 v253, v[224:227], s[54:55] offset:0 sc1\n"
"ds_write_b64 v245, v[212:213] offset:12288\n"
"v_mfma_f32_16x16x32_bf16 v[136:139], a[220:223], v[116:119], v[136:139]\n"
"v_mfma_f32_16x16x32_bf16 v[132:135], v[156:159], v[116:119], v[16:19]\n"
"v_mfma_f32_16x16x32_bf16 v[132:135], v[140:143], v[112:115], v[132:135]\n"
"v_mfma_f32_16x16x32_bf16 v[132:135], a[20:23], v[108:111], v[132:135]\n"
"ds_read_b32 v243, v241\n"
"v_mfma_f32_16x16x32_bf16 v[132:135], a[16:19], v[92:95], v[132:135]\n"
"v_cvt_pk_bf16_f32 v214, v136, v137\n"
"v_mfma_f32_16x16x32_bf16 v[132:135], a[12:15], v[88:91], v[132:135]\n"
"v_cvt_pk_bf16_f32 v215, v138, v139\n"
"v_mfma_f32_16x16x32_bf16 v[132:135], a[8:11], v[84:87], v[132:135]\n"
"s_add_u32 m0, s31, 0xef00\n"
"v_mfma_f32_16x16x32_bf16 v[132:135], a[4:7], v[68:71], v[132:135]\n"
"s_waitcnt lgkmcnt(0)\n"
"v_cmp_gt_u32 s[56:57], 25, v243\n"
"v_mfma_f32_16x16x32_bf16 v[132:135], a[0:3], v[64:67], v[132:135]\n"
"global_load_lds_dwordx4 v238, s[52:53] offset:256 nt\n"
"v_mfma_f32_16x16x32_bf16 v[132:135], a[236:239], v[60:63], v[132:135]\n"
"s_or_b64 s[56:57], s[56:57], vcc\n"
"s_cbranch_scc1 LB25\n"
"LB25R:\n"
"v_mfma_f32_16x16x32_bf16 v[132:135], a[232:235], v[44:47], v[132:135]\n"
"ds_write_b64 v245, v[214:215] offset:12296\n"
"v_mfma_f32_16x16x32_bf16 v[132:135], a[228:231], v[40:43], v[132:135]\n"
"v_mfma_f32_16x16x32_bf16 v[132:135], a[224:227], v[36:39], v[132:135]\n"
"v_mfma_f32_16x16x32_bf16 v[136:139], v[160:163], v[36:39], v[20:23]\n"
"ds_read_b128 v[36:39], v232 offset:256\n"
"v_mfma_f32_16x16x32_bf16 v[136:139], v[164:167], v[40:43], v[136:139]\n"
"ds_read_b128 v[40:43], v233 offset:256\n"
"v_mfma_f32_16x16x32_bf16 v[136:139], v[180:183], v[44:47], v[136:139]\n"
"ds_read_b128 v[44:47], v234 offset:256\n"
"v_mfma_f32_16x16x32_bf16 v[136:139], v[184:187], v[60:63], v[136:139]\n"
"ds_read_b128 v[60:63], v235 offset:256\n"
"v_cvt_pk_bf16_f32 v208, v132, v133\n"
"v_mfma_f32_16x16x32_bf16 v[136:139], v[188:191], v[64:67], v[136:139]\n"
"ds_read_b128 v[64:67], v232 offset:8448\n"
"v_cvt_pk_bf16_f32 v209, v134, v135\n"
"v_mfma_f32_16x16x32_bf16 v[136:139], v[204:207], v[68:71], v[136:139]\n"
"ds_read_b128 v[68:71], v233 offset:8448\n"
"v_mfma_f32_16x16x32_bf16 v[136:139], a[24:27], v[84:87], v[136:139]\n"
"ds_read_b128 v[84:87], v234 offset:8448\n"
"v_mfma_f32_16x16x32_bf16 v[136:139], a[28:31], v[88:91], v[136:139]\n"
"ds_read_b128 v[88:91], v235 offset:8448\n"
"v_mfma_f32_16x16x32_bf16 v[136:139], a[32:35], v[92:95], v[136:139]\n"
"ds_read_b128 v[92:95], v232 offset:16640\n"
"v_mfma_f32_16x16x32_bf16 v[136:139], a[36:39], v[108:111], v[136:139]\n"
"ds_read_b128 v[108:111], v233 offset:16640\n"
"v_mfma_f32_16x16x32_bf16 v[136:139], a[40:43], v[112:115], v[136:139]\n"
"ds_read_b128 v[112:115], v234 offset:16640\n"
"v_mfma_f32_16x16x32_bf16 v[136:139], a[44:47], v[116:119], v[136:139]\n"
"ds_read_b128 v[116:119], v235 offset:16640\n"
"s_waitcnt lgkmcnt(8)\n"
"v_mfma_f32_16x16x32_bf16 v[132:135], a[240:243], v[36:39], v[0:3]\n"
"ds_write_b64 v244, v[208:209] offset:12416\n"
"v_mfma_f32_16x16x32_bf16 v[132:135], a[244:247], v[40:43], v[132:135]\n"
"v_mfma_f32_16x16x32_bf16 v[132:135], a[248:251], v[44:47], v[132:135]\n"
"v_mfma_f32_16x16x32_bf16 v[132:135], a[252:255], v[60:63], v[132:135]\n"
"v_cvt_pk_bf16_f32 v210, v136, v137\n"
"s_waitcnt lgkmcnt(5)\n"
"v_mfma_f32_16x16x32_bf16 v[132:135], a[48:51], v[64:67], v[132:135]\n"
"v_cvt_pk_bf16_f32 v211, v138, v139\n"
"v_mfma_f32_16x16x32_bf16 v[132:135], a[52:55], v[68:71], v[132:135]\n"
"s_add_u32 m0, s31, 0x10e00\n"
"v_mfma_f32_16x16x32_bf16 v[132:135], a[56:59], v[84:87], v[132:135]\n"
"ds_read_b128 v[216:219], v246 offset:0\n"
"v_mfma_f32_16x16x32_bf16 v[132:135], a[60:63], v[88:91], v[132:135]\n"
"ds_read_b128 v[220:223], v246 offset:1024\n"
"s_waitcnt lgkmcnt(3)\n"
"v_mfma_f32_16x16x32_bf16 v[132:135], a[64:67], v[92:95], v[132:135]\n"
"ds_read_b128 v[224:227], v246 offset:2048\n"
"v_mfma_f32_16x16x32_bf16 v[132:135], a[68:71], v[108:111], v[132:135]\n"
"ds_write_b64 v244, v[210:211] offset:12424\n"
"v_mfma_f32_16x16x32_bf16 v[132:135], a[72:75], v[112:115], v[132:135]\n"
"global_load_lds_dwordx4 v238, s[52:53] offset:512 nt\n"
"v_mfma_f32_16x16x32_bf16 v[132:135], a[76:79], v[116:119], v[132:135]\n"
"v_mfma_f32_16x16x32_bf16 v[136:139], a[124:127], v[116:119], v[4:7]\n"
"s_add_u32 s83, s83, s86\n"
"v_mfma_f32_16x16x32_bf16 v[136:139], a[120:123], v[112:115], v[136:139]\n"
"s_sub_i32 s88, s88, 6\n"
"v_mfma_f32_16x16x32_bf16 v[136:139], a[116:119], v[108:111], v[136:139]\n"
"s_max_i32 s88, s88, 0\n"
"v_mfma_f32_16x16x32_bf16 v[136:139], a[112:115], v[92:95], v[136:139]\n"
"v_cvt_pk_bf16_f32 v208, v132, v133\n"
"v_mfma_f32_16x16x32_bf16 v[136:139], a[108:111], v[88:91], v[136:139]\n"
"v_cvt_pk_bf16_f32 v209, v134, v135\n"
"v_mfma_f32_16x16x32_bf16 v[136:139], a[104:107], v[84:87], v[136:139]\n"
"v_mfma_f32_16x16x32_bf16 v[136:139], a[100:103], v[68:71], v[136:139]\n"
"v_mfma_f32_16x16x32_bf16 v[136:139], a[96:99], v[64:67], v[136:139]\n"
"s_waitcnt lgkmcnt(1)\n"
"v_mfma_f32_16x16x32_bf16 v[136:139], a[92:95], v[60:63], v[136:139]\n"
"v_mfma_f32_16x16x32_bf16 v[136:139], a[88:91], v[44:47], v[136:139]\n"
"global_store_dwordx4 v247, v[216:219], s[54:55] offset:768 sc1\n"
"ds_write_b64 v244, v[208:209] offset:24576\n"
"v_mfma_f32_16x16x32_bf16 v[136:139], a[84:87], v[40:43], v[136:139]\n"
"v_mfma_f32_16x16x32_bf16 v[136:139], a[80:83], v[36:39], v[136:139]\n"
"v_mfma_f32_16x16x32_bf16 v[132:135], a[128:131], v[36:39], v[8:11]\n"
"v_mfma_f32_16x16x32_bf16 v[132:135], a[132:135], v[40:43], v[132:135]\n"
"v_mfma_f32_16x16x32_bf16 v[132:135], a[136:139], v[44:47], v[132:135]\n"
"v_mfma_f32_16x16x32_bf16 v[132:135], a[140:143], v[60:63], v[132:135]\n"
"v_cvt_pk_bf16_f32 v210, v136, v137\n"
"v_mfma_f32_16x16x32_bf16 v[132:135], a[144:147], v[64:67], v[132:135]\n"
"v_cvt_pk_bf16_f32 v211, v138, v139\n"
"v_mfma_f32_16x16x32_bf16 v[132:135], a[148:151], v[68:71], v[132:135]\n"
"s_add_u32 s52, s20, s47\n"
"s_addc_u32 s53, s21, 0\n"
"v_mfma_f32_16x16x32_bf16 v[132:135], a[152:155], v[84:87], v[132:135]\n"
"s_add_u32 m0, s31, 0x12000\n"
"v_mfma_f32_16x16x32_bf16 v[132:135], a[156:159], v[88:91], v[132:135]\n"
"global_load_lds_dwordx4 v237, s[52:53] offset:0 nt\n"
"v_mfma_f32_16x16x32_bf16 v[132:135], a[160:163], v[92:95], v[132:135]\n"
"v_mfma_f32_16x16x32_bf16 v[132:135], a[164:167], v[108:111], v[132:135]\n"
"s_memrealtime s[84:85]\n"
"v_mfma_f32_16x16x32_bf16 v[132:135], a[168:171], v[112:115], v[132:135]\n"
"global_store_dwordx4 v252, v[220:223], s[54:55] offset:768 sc1\n"
"ds_write_b64 v244, v[210:211] offset:24584\n"
"v_mfma_f32_16x16x32_bf16 v[132:135], a[172:175], v[116:119], v[132:135]\n"
"v_mfma_f32_16x16x32_bf16 v[136:139], a[220:223], v[116:119], v[12:15]\n"
"v_mfma_f32_16x16x32_bf16 v[136:139], a[216:219], v[112:115], v[136:139]\n"
"v_mfma_f32_16x16x32_bf16 v[136:139], a[212:215], v[108:111], v[136:139]\n"
"v_mfma_f32_16x16x32_bf16 v[136:139], a[208:211], v[92:95], v[136:139]\n"
"v_cvt_pk_bf16_f32 v212, v132, v133\n"
"v_mfma_f32_16x16x32_bf16 v[136:139], a[204:207], v[88:91], v[136:139]\n"
"v_mov_b32 v242, 26\n"
"v_mfma_f32_16x16x32_bf16 v[136:139], a[200:203], v[84:87], v[136:139]\n"
"v_cvt_pk_bf16_f32 v213, v134, v135\n"
"v_mfma_f32_16x16x32_bf16 v[136:139], a[196:199], v[68:71], v[136:139]\n"
"s_waitcnt vmcnt(15) lgkmcnt(2)\n"
"ds_write_b32 v240, v242\n"
"v_mfma_f32_16x16x32_bf16 v[136:139], a[192:195], v[64:67], v[136:139]\n"
"v_mfma_f32_16x16x32_bf16 v[136:139], a[188:191], v[60:63], v[136:139]\n"
"v_mfma_f32_16x16x32_bf16 v[136:139], a[184:187], v[44:47], v[136:139]\n"
"v_mfma_f32_16x16x32_bf16 v[136:139], a[180:183], v[40:43], v[136:139]\n"
"global_store_dwordx4 v253, v[224:227], s[54:55] offset:768 sc1\n"
"ds_write_b64 v245, v[212:213] offset:24576\n"
"v_mfma_f32_16x16x32_bf16 v[136:139], a[176:179], v[36:39], v[136:139]\n"
"s_bitcmp1_b32 s92, 6\n"
"s_cselect_b32 s93, 0x6000, 0\n"
"v_mfma_f32_16x16x32_bf16 v[132:135], a[224:227], v[36:39], v[16:19]\n"
"s_sub_u32 s94, s93, 0xc000\n"
"v_mfma_f32_16x16x32_bf16 v[132:135], a[228:231], v[40:43], v[132:135]\n"
"v_add_u32 v231, s93, v247\n"
"v_mfma_f32_16x16x32_bf16 v[132:135], a[232:235], v[44:47], v[132:135]\n"
"ds_read_b32 v243, v241\n"
"v_mfma_f32_16x16x32_bf16 v[132:135], a[236:239], v[60:63], v[132:135]\n"
"v_cvt_pk_bf16_f32 v214, v136, v137\n"
"v_mfma_f32_16x16x32_bf16 v[132:135], a[0:3], v[64:67], v[132:135]\n"
"v_cvt_pk_bf16_f32 v215, v138, v139\n"
"v_mfma_f32_16x16x32_bf16 v[132:135], a[4:7], v[68:71], v[132:135]\n"
"v_add_u32 v247, s94, v247\n"
"v_mfma_f32_16x16x32_bf16 v[132:135], a[8:11], v[84:87], v[132:135]\n"
"s_waitcnt lgkmcnt(0)\n"
"v_cmp_gt_u32 s[56:57], 26, v243\n"
"v_mfma_f32_16x16x32_bf16 v[132:135], a[12:15], v[88:91], v[132:135]\n"
"s_sub_u32 s84, s84, s83\n"
"v_mfma_f32_16x16x32_bf16 v[132:135], a[16:19], v[92:95], v[132:135]\n"
"s_or_b64 s[56:57], s[56:57], vcc\n"
"s_cbranch_scc1 LB26\n"
"LB26R:\n"
"v_mfma_f32_16x16x32_bf16 v[132:135], a[20:23], v[108:111], v[132:135]\n"
"s_cmp_lt_i32 s84, s88\n"
"s_cselect_b64 vcc, -1, 0\n"
"v_mfma_f32_16x16x32_bf16 v[132:135], v[140:143], v[112:115], v[132:135]\n"
"ds_write_b64 v245, v[214:215] offset:24584\n"
"v_mfma_f32_16x16x32_bf16 v[132:135], v[156:159], v[116:119], v[132:135]\n"
"s_add_u32 m0, s31, 0x13f00\n"
"v_mfma_f32_16x16x32_bf16 v[136:139], a[44:47], v[116:119], v[20:23]\n"
"ds_read_b128 v[116:119], v235 offset:40960\n"
"v_mfma_f32_16x16x32_bf16 v[136:139], a[40:43], v[112:115], v[136:139]\n"
"ds_read_b128 v[112:115], v234 offset:40960\n"
"v_mfma_f32_16x16x32_bf16 v[136:139], a[36:39], v[108:111], v[136:139]\n"
"ds_read_b128 v[108:111], v233 offset:40960\n"
"v_mfma_f32_16x16x32_bf16 v[136:139], a[32:35], v[92:95], v[136:139]\n"
"ds_read_b128 v[92:95], v232 offset:40960\n"
"v_cvt_pk_bf16_f32 v208, v132, v133\n"
"v_mfma_f32_16x16x32_bf16 v[136:139], a[28:31], v[88:91], v[136:139]\n"
"ds_read_b128 v[88:91], v235 offset:32768\n"
"v_cvt_pk_bf16_f32 v209, v134, v135\n"
"v_mfma_f32_16x16x32_bf16 v[136:139], a[24:27], v[84:87], v[136:139]\n"
"ds_read_b128 v[84:87], v234 offset:32768\n"
"v_mfma_f32_16x16x32_bf16 v[136:139], v[204:207], v[68:71], v[136:139]\n"
"ds_read_b128 v[68:71], v233 offset:32768\n"
"v_mfma_f32_16x16x32_bf16 v[136:139], v[188:191], v[64:67], v[136:139]\n"
"ds_read_b128 v[64:67], v232 offset:32768\n"
"v_mfma_f32_16x16x32_bf16 v[136:139], v[184:187], v[60:63], v[136:139]\n"
"ds_read_b128 v[60:63], v235 offset:24576\n"
"v_mfma_f32_16x16x32_bf16 v[136:139], v[180:183], v[44:47], v[136:139]\n"
"ds_read_b128 v[44:47], v234 offset:24576\n"
"v_mfma_f32_16x16x32_bf16 v[136:139], v[164:167], v[40:43], v[136:139]\n"
"ds_read_b128 v[40:43], v233 offset:24576\n"
"v_mfma_f32_16x16x32_bf16 v[136:139], v[160:163], v[36:39], v[136:139]\n"
"ds_read_b128 v[36:39], v232 offset:24576\n"
"s_waitcnt lgkmcnt(8)\n"
"v_mfma_f32_16x16x32_bf16 v[132:135], a[76:79], v[116:119], v[0:3]\n"
"ds_write_b64 v244, v[208:209] offset:24704\n"
"v_mfma_f32_16x16x32_bf16 v[132:135], a[72:75], v[112:115], v[132:135]\n"
"global_load_lds_dwordx4 v237, s[52:53] offset:256 nt\n"
"v_mfma_f32_16x16x32_bf16 v[132:135], a[68:71], v[108:111], v[132:135]\n"
"s_add_u32 s54, s26, s44\n"
"s_addc_u32 s55, s27, 0\n"
"v_mfma_f32_16x16x32_bf16 v[132:135], a[64:67], v[92:95], v[132:135]\n"
"v_cvt_pk_bf16_f32 v210, v136, v137\n"
"s_waitcnt lgkmcnt(5)\n"
"v_mfma_f32_16x16x32_bf16 v[132:135], a[60:63], v[88:91], v[132:135]\n"
"v_cvt_pk_bf16_f32 v211, v138, v139\n"
"v_mfma_f32_16x16x32_bf16 v[132:135], a[56:59], v[84:87], v[132:135]\n"
"s_sub_i32 s84, s84, s19\n"
"v_mfma_f32_16x16x32_bf16 v[132:135], a[52:55], v[68:71], v[132:135]\n"
"ds_read_b128 v[216:219], v246 offset:12288\n"
"v_mfma_f32_16x16x32_bf16 v[132:135], a[48:51], v[64:67], v[132:135]\n"
"ds_read_b128 v[220:223], v246 offset:13312\n"
"s_waitcnt lgkmcnt(3)\n"
"v_mfma_f32_16x16x32_bf16 v[132:135], a[252:255], v[60:63], v[132:135]\n"
"ds_read_b128 v[224:227], v246 offset:14336\n"
"v_mfma_f32_16x16x32_bf16 v[132:135], a[248:251], v[44:47], v[132:135]\n"
"ds_write_b64 v244, v[210:211] offset:24712\n"
"v_mfma_f32_16x16x32_bf16 v[132:135], a[244:247], v[40:43], v[132:135]\n"
"s_add_u32 m0, s31, 0x15e00\n"
"v_mfma_f32_16x16x32_bf16 v[132:135], a[240:243], v[36:39], v[132:135]\n"
"global_load_lds_dwordx4 v237, s[52:53] offset:512 nt\n"
"v_mfma_f32_16x16x32_bf16 v[136:139], a[80:83], v[36:39], v[4:7]\n"
"s_max_i32 s88, s88, s84\n"
"v_mfma_f32_16x16x32_bf16 v[136:139], a[84:87], v[40:43], v[136:139]\n"
"v_min_u32 v247, v247, v231\n"
"v_mfma_f32_16x16x32_bf16 v[136:139], a[88:91], v[44:47], v[136:139]\n"
"v_add_u32 v231, s93, v252\n"
"v_mfma_f32_16x16x32_bf16 v[136:139], a[92:95], v[60:63], v[136:139]\n"
"v_cvt_pk_bf16_f32 v208, v132, v133\n"
"v_add_u32 v252, s94, v252\n"
"v_mfma_f32_16x16x32_bf16 v[136:139], a[96:99], v[64:67], v[136:139]\n"
"v_cvt_pk_bf16_f32 v209, v134, v135\n"
"v_mfma_f32_16x16x32_bf16 v[136:139], a[100:103], v[68:71], v[136:139]\n"
"v_min_u32 v252, v252, v231\n"
"v_mfma_f32_16x16x32_bf16 v[136:139], a[104:107], v[84:87], v[136:139]\n"
"v_add_u32 v231, s93, v253\n"
"v_mfma_f32_16x16x32_bf16 v[136:139], a[108:111], v[88:91], v[136:139]\n"
"s_waitcnt lgkmcnt(1)\n"
"v_add_u32 v253, s94, v253\n"
"v_mfma_f32_16x16x32_bf16 v[136:139], a[112:115], v[92:95], v[136:139]\n"
"v_min_u32 v253, v253, v231\n"
"v_mfma_f32_16x16x32_bf16 v[136:139], a[116:119], v[108:111], v[136:139]\n"
"global_store_dwordx4 v247, v[216:219], s[54:55] offset:0 sc1\n"
"ds_write_b64 v244, v[208:209] offset:0\n"
"v_mfma_f32_16x16x32_bf16 v[136:139], a[120:123], v[112:115], v[136:139]\n"
"v_mfma_f32_16x16x32_bf16 v[136:139], a[124:127], v[116:119], v[136:139]\n"
"v_mfma_f32_16x16x32_bf16 v[132:135], a[172:175], v[116:119], v[8:11]\n"
"v_mfma_f32_16x16x32_bf16 v[132:135], a[168:171], v[112:115], v[132:135]\n"
"v_mfma_f32_16x16x32_bf16 v[132:135], a[164:167], v[108:111], v[132:135]\n"
"v_mfma_f32_16x16x32_bf16 v[132:135], a[160:163], v[92:95], v[132:135]\n"
"v_cvt_pk_bf16_f32 v210, v136, v137\n"
"v_mfma_f32_16x16x32_bf16 v[132:135], a[156:159], v[88:91], v[132:135]\n"
"v_cvt_pk_bf16_f32 v211, v138, v139\n"
"v_mfma_f32_16x16x32_bf16 v[132:135], a[152:155], v[84:87], v[132:135]\n"
"v_mfma_f32_16x16x32_bf16 v[132:135], a[148:151], v[68:71], v[132:135]\n"
"v_mfma_f32_16x16x32_bf16 v[132:135], a[144:147], v[64:67], v[132:135]\n"
"v_mfma_f32_16x16x32_bf16 v[132:135], a[140:143], v[60:63], v[132:135]\n"
"v_mfma_f32_16x16x32_bf16 v[132:135], a[136:139], v[44:47], v[132:135]\n"
"global_store_dwordx4 v252, v[220:223], s[54:55] offset:0 sc1\n"
"ds_write_b64 v244, v[210:211] offset:8\n"
"v_mfma_f32_16x16x32_bf16 v[132:135], a[132:135], v[40:43], v[132:135]\n"
"s_add_u32 m0, s31, 0x13000\n"
"v_mfma_f32_16x16x32_bf16 v[132:135], a[128:131], v[36:39], v[132:135]\n"
"global_load_lds_dwordx4 v238, s[52:53] offset:0 nt\n"
"v_mfma_f32_16x16x32_bf16 v[136:139], a[176:179], v[36:39], v[12:15]\n"
"v_mfma_f32_16x16x32_bf16 v[136:139], a[180:183], v[40:43], v[136:139]\n"
"v_mfma_f32_16x16x32_bf16 v[136:139], a[184:187], v[44:47], v[136:139]\n"
"v_mfma_f32_16x16x32_bf16 v[136:139], a[188:191], v[60:63], v[136:139]\n"
"v_cvt_pk_bf16_f32 v212, v132, v133\n"
"v_mfma_f32_16x16x32_bf16 v[136:139], a[192:195], v[64:67], v[136:139]\n"
"v_mov_b32 v242, 27\n"
"v_mfma_f32_16x16x32_bf16 v[136:139], a[196:199], v[68:71], v[136:139]\n"
"v_cvt_pk_bf16_f32 v213, v134, v135\n"
"v_mfma_f32_16x16x32_bf16 v[136:139], a[200:203], v[84:87], v[136:139]\n"
"s_waitcnt lgkmcnt(2)\n"
"ds_write_b32 v240, v242\n"
"v_mfma_f32_16x16x32_bf16 v[136:139], a[204:207], v[88:91], v[136:139]\n"
"v_mfma_f32_16x16x32_bf16 v[136:139], a[208:211], v[92:95], v[136:139]\n"
"v_mfma_f32_16x16x32_bf16 v[136:139], a[212:215], v[108:111], v[136:139]\n"
"v_mfma_f32_16x16x32_bf16 v[136:139], a[216:219], v[112:115], v[136:139]\n"
"global_store_dwordx4 v253, v[224:227], s[54:55] offset:0 sc1\n"
"ds_write_b64 v245, v[212:213] offset:0\n"
"v_mfma_f32_16x16x32_bf16 v[136:139], a[220:223], v[116:119], v[136:139]\n"
"v_mfma_f32_16x16x32_bf16 v[132:135], v[156:159], v[116:119], v[16:19]\n"
"v_mfma_f32_16x16x32_bf16 v[132:135], v[140:143], v[112:115], v[132:135]\n"
"v_mfma_f32_16x16x32_bf16 v[132:135], a[20:23], v[108:111], v[132:135]\n"
"ds_read_b32 v243, v241\n"
"v_mfma_f32_16x16x32_bf16 v[132:135], a[16:19], v[92:95], v[132:135]\n"
"v_cvt_pk_bf16_f32 v214, v136, v137\n"
"v_mfma_f32_16x16x32_bf16 v[132:135], a[12:15], v[88:91], v[132:135]\n"
"v_cvt_pk_bf16_f32 v215, v138, v139\n"
"v_mfma_f32_16x16x32_bf16 v[132:135], a[8:11], v[84:87], v[132:135]\n"
"v_mfma_f32_16x16x32_bf16 v[132:135], a[4:7], v[68:71], v[132:135]\n"
"s_waitcnt lgkmcnt(0)\n"
"v_cmp_gt_u32 s[56:57], 27, v243\n"
"v_mfma_f32_16x16x32_bf16 v[132:135], a[0:3], v[64:67], v[132:135]\n"
"v_mfma_f32_16x16x32_bf16 v[132:135], a[236:239], v[60:63], v[132:135]\n"
"s_or_b64 s[56:57], s[56:57], vcc\n"
"s_cbranch_scc1 LB27\n"
"LB27R:\n"
"v_mfma_f32_16x16x32_bf16 v[132:135], a[232:235], v[44:47], v[132:135]\n"
"ds_write_b64 v245, v[214:215] offset:8\n"
"v_mfma_f32_16x16x32_bf16 v[132:135], a[228:231], v[40:43], v[132:135]\n"
"v_mfma_f32_16x16x32_bf16 v[132:135], a[224:227], v[36:39], v[132:135]\n"
"v_mfma_f32_16x16x32_bf16 v[136:139], v[160:163], v[36:39], v[20:23]\n"
"ds_read_b128 v[36:39], v232 offset:24832\n"
"v_mfma_f32_16x16x32_bf16 v[136:139], v[164:167], v[40:43], v[136:139]\n"
"ds_read_b128 v[40:43], v233 offset:24832\n"
"v_mfma_f32_16x16x32_bf16 v[136:139], v[180:183], v[44:47], v[136:139]\n"
"ds_read_b128 v[44:47], v234 offset:24832\n"
"v_mfma_f32_16x16x32_bf16 v[136:139], v[184:187], v[60:63], v[136:139]\n"
"ds_read_b128 v[60:63], v235 offset:24832\n"
"v_cvt_pk_bf16_f32 v208, v132, v133\n"
"v_mfma_f32_16x16x32_bf16 v[136:139], v[188:191], v[64:67], v[136:139]\n"
"ds_read_b128 v[64:67], v232 offset:33024\n"
"v_cvt_pk_bf16_f32 v209, v134, v135\n"
"v_mfma_f32_16x16x32_bf16 v[136:139], v[204:207], v[68:71], v[136:139]\n"
"ds_read_b128 v[68:71], v233 offset:33024\n"
"v_mfma_f32_16x16x32_bf16 v[136:139], a[24:27], v[84:87], v[136:139]\n"
"ds_read_b128 v[84:87], v234 offset:33024\n"
"v_mfma_f32_16x16x32_bf16 v[136:139], a[28:31], v[88:91], v[136:139]\n"
"ds_read_b128 v[88:91], v235 offset:33024\n"
"v_mfma_f32_16x16x32_bf16 v[136:139], a[32:35], v[92:95], v[136:139]\n"
"ds_read_b128 v[92:95], v232 offset:41216\n"
"v_mfma_f32_16x16x32_bf16 v[136:139], a[36:39], v[108:111], v[136:139]\n"
"ds_read_b128 v[108:111], v233 offset:41216\n"
"ds_write_b64 v244, v[208:209] offset:128\n"
"v_mfma_f32_16x16x32_bf16 v[136:139], a[40:43], v[112:115], v[136:139]\n"
"ds_read_b128 v[112:115], v234 offset:41216\n"
"v_mfma_f32_16x16x32_bf16 v[136:139], a[44:47], v[116:119], v[136:139]\n"
"ds_read_b128 v[116:119], v235 offset:41216\n"
"s_waitcnt lgkmcnt(9)\n"
"v_mfma_f32_16x16x32_bf16 v[132:135], a[240:243], v[36:39], v[0:3]\n"
"v_add_u32 v232, 0xc000, v232\n"
"v_mfma_f32_16x16x32_bf16 v[132:135], a[244:247], v[40:43], v[132:135]\n"
"v_add_u32 v233, 0xc000, v233\n"
"v_mfma_f32_16x16x32_bf16 v[132:135], a[248:251], v[44:47], v[132:135]\n"
"v_add_u32 v234, 0xc000, v234\n"
"v_mfma_f32_16x16x32_bf16 v[132:135], a[252:255], v[60:63], v[132:135]\n"
"v_add_u32 v235, 0xc000, v235\n"
"s_waitcnt lgkmcnt(5)\n"
"v_mfma_f32_16x16x32_bf16 v[132:135], a[48:51], v[64:67], v[132:135]\n"
"s_add_u32 m0, s31, 0x14f00\n"
"v_mfma_f32_16x16x32_bf16 v[132:135], a[52:55], v[68:71], v[132:135]\n"
"global_load_lds_dwordx4 v238, s[52:53] offset:256 nt\n"
"v_mfma_f32_16x16x32_bf16 v[132:135], a[56:59], v[84:87], v[132:135]\n"
"v_cvt_pk_bf16_f32 v210, v136, v137\n"
"v_mfma_f32_16x16x32_bf16 v[132:135], a[60:63], v[88:91], v[132:135]\n"
"v_cvt_pk_bf16_f32 v211, v138, v139\n"
"s_waitcnt lgkmcnt(0)\n"
"v_mfma_f32_16x16x32_bf16 v[132:135], a[64:67], v[92:95], v[132:135]\n"
"ds_read_b128 v[216:219], v246 offset:24576\n"
"v_mfma_f32_16x16x32_bf16 v[132:135], a[68:71], v[108:111], v[132:135]\n"
"ds_read_b128 v[220:223], v246 offset:25600\n"
"v_mfma_f32_16x16x32_bf16 v[132:135], a[72:75], v[112:115], v[132:135]\n"
"ds_read_b128 v[224:227], v246 offset:26624\n"
"v_mfma_f32_16x16x32_bf16 v[132:135], a[76:79], v[116:119], v[132:135]\n"
"s_add_u32 m0, s31, 0x16e00\n"
"v_mfma_f32_16x16x32_bf16 v[136:139], a[124:127], v[116:119], v[4:7]\n"
"ds_write_b64 v244, v[210:211] offset:136\n"
"v_mfma_f32_16x16x32_bf16 v[136:139], a[120:123], v[112:115], v[136:139]\n"
"global_load_lds_dwordx4 v238, s[52:53] offset:512 nt\n"
"v_mfma_f32_16x16x32_bf16 v[136:139], a[116:119], v[108:111], v[136:139]\n"
"v_lshlrev_b32 v0, 16, v168\n"
"s_bitcmp1_b32 s92, 8\n"
"s_cselect_b32 s93, 0x6000, 0\n"
"v_mfma_f32_16x16x32_bf16 v[136:139], a[112:115], v[92:95], v[136:139]\n"
"v_cvt_pk_bf16_f32 v208, v132, v133\n"
"s_sub_u32 s94, s93, 0xc000\n"
"v_mfma_f32_16x16x32_bf16 v[136:139], a[108:111], v[88:91], v[136:139]\n"
"v_cvt_pk_bf16_f32 v209, v134, v135\n"
"v_add_u32 v231, s93, v237\n"
"v_mfma_f32_16x16x32_bf16 v[136:139], a[104:107], v[84:87], v[136:139]\n"
"v_and_b32 v1, 0xffff0000, v168\n"
"v_add_u32 v237, s94, v237\n"
"v_mfma_f32_16x16x32_bf16 v[136:139], a[100:103], v[68:71], v[136:139]\n"
"v_lshlrev_b32 v2, 16, v169\n"
"v_min_u32 v237, v237, v231\n"
"v_mfma_f32_16x16x32_bf16 v[136:139], a[96:99], v[64:67], v[136:139]\n"
"s_waitcnt lgkmcnt(1)\n"
"v_add_u32 v231, s93, v238\n"
"v_mfma_f32_16x16x32_bf16 v[136:139], a[92:95], v[60:63], v[136:139]\n"
"v_and_b32 v3, 0xffff0000, v169\n"
"v_add_u32 v238, s94, v238\n"
"v_mfma_f32_16x16x32_bf16 v[136:139], a[88:91], v[44:47], v[136:139]\n"
"global_store_dwordx4 v247, v[216:219], s[54:55] offset:768 sc1\n"
"ds_write_b64 v244, v[208:209] offset:12288\n"
"v_mfma_f32_16x16x32_bf16 v[136:139], a[84:87], v[40:43], v[136:139]\n"
"v_lshlrev_b32 v4, 16, v170\n"
"v_min_u32 v238, v238, v231\n"
"v_mfma_f32_16x16x32_bf16 v[136:139], a[80:83], v[36:39], v[136:139]\n"
"v_and_b32 v5, 0xffff0000, v170\n"
"v_mfma_f32_16x16x32_bf16 v[132:135], a[128:131], v[36:39], v[8:11]\n"
"v_lshlrev_b32 v6, 16, v171\n"
"v_mfma_f32_16x16x32_bf16 v[132:135], a[132:135], v[40:43], v[132:135]\n"
"v_and_b32 v7, 0xffff0000, v171\n"
"v_mfma_f32_16x16x32_bf16 v[132:135], a[136:139], v[44:47], v[132:135]\n"
"s_add_u32 s83, s83, s86\n"
"v_mfma_f32_16x16x32_bf16 v[132:135], a[140:143], v[60:63], v[132:135]\n"
"v_cvt_pk_bf16_f32 v210, v136, v137\n"
"v_mfma_f32_16x16x32_bf16 v[132:135], a[144:147], v[64:67], v[132:135]\n"
"v_cvt_pk_bf16_f32 v211, v138, v139\n"
"v_mfma_f32_16x16x32_bf16 v[132:135], a[148:151], v[68:71], v[132:135]\n"
"v_lshlrev_b32 v8, 16, v172\n"
"v_mfma_f32_16x16x32_bf16 v[132:135], a[152:155], v[84:87], v[132:135]\n"
"v_and_b32 v9, 0xffff0000, v172\n"
"v_mfma_f32_16x16x32_bf16 v[132:135], a[156:159], v[88:91], v[132:135]\n"
"v_lshlrev_b32 v10, 16, v173\n"
"v_mfma_f32_16x16x32_bf16 v[132:135], a[160:163], v[92:95], v[132:135]\n"
"v_and_b32 v11, 0xffff0000, v173\n"
"s_mov_b64 exec, s[4:5]\n"
"s_cbranch_execz LHT12\n"
"LHR12:\n"
"s_mov_b64 exec, -1\n"
"v_mfma_f32_16x16x32_bf16 v[132:135], a[164:167], v[108:111], v[132:135]\n"
"s_memrealtime s[84:85]\n"
"v_mfma_f32_16x16x32_bf16 v[132:135], a[168:171], v[112:115], v[132:135]\n"
"global_store_dwordx4 v252, v[220:223], s[54:55] offset:768 sc1\n"
"ds_write_b64 v244, v[210:211] offset:12296\n"
"v_mfma_f32_16x16x32_bf16 v[132:135], a[172:175], v[116:119], v[132:135]\n"
"s_add_u32 s52, s20, s48\n"
"s_addc_u32 s53, s21, 0\n"
"v_mfma_f32_16x16x32_bf16 v[136:139], a[220:223], v[116:119], v[12:15]\n"
"s_add_u32 m0, s31, 0x0\n"
"v_mfma_f32_16x16x32_bf16 v[136:139], a[216:219], v[112:115], v[136:139]\n"
"global_load_lds_dwordx4 v237, s[52:53] offset:0 nt\n"
"v_mfma_f32_16x16x32_bf16 v[136:139], a[212:215], v[108:111], v[136:139]\n"
"s_sub_i32 s88, s88, 6\n"
"v_mfma_f32_16x16x32_bf16 v[136:139], a[208:211], v[92:95], v[136:139]\n"
"v_cvt_pk_bf16_f32 v212, v132, v133\n"
"v_mfma_f32_16x16x32_bf16 v[136:139], a[204:207], v[88:91], v[136:139]\n"
"v_mov_b32 v242, 28\n"
"v_mfma_f32_16x16x32_bf16 v[136:139], a[200:203], v[84:87], v[136:139]\n"
"v_cvt_pk_bf16_f32 v213, v134, v135\n"
"v_mfma_f32_16x16x32_bf16 v[136:139], a[196:199], v[68:71], v[136:139]\n"
"s_waitcnt vmcnt(15) lgkmcnt(2)\n"
"ds_write_b32 v240, v242\n"
"v_mfma_f32_16x16x32_bf16 v[136:139], a[192:195], v[64:67], v[136:139]\n"
"v_lshlrev_b32 v12, 16, v174\n"
"v_mfma_f32_16x16x32_bf16 v[136:139], a[188:191], v[60:63], v[136:139]\n"
"v_and_b32 v13, 0xffff0000, v174\n"
"v_mfma_f32_16x16x32_bf16 v[136:139], a[184:187], v[44:47], v[136:139]\n"
"v_lshlrev_b32 v14, 16, v175\n"
"v_mfma_f32_16x16x32_bf16 v[136:139], a[180:183], v[40:43], v[136:139]\n"
"global_store_dwordx4 v253, v[224:227], s[54:55] offset:768 sc1\n"
"ds_write_b64 v245, v[212:213] offset:12288\n"
"v_mfma_f32_16x16x32_bf16 v[136:139], a[176:179], v[36:39], v[136:139]\n"
"v_and_b32 v15, 0xffff0000, v175\n"
"v_mfma_f32_16x16x32_bf16 v[132:135], a[224:227], v[36:39], v[16:19]\n"
"s_max_i32 s88, s88, 0\n"
"v_mfma_f32_16x16x32_bf16 v[132:135], a[228:231], v[40:43], v[132:135]\n"
"v_mfma_f32_16x16x32_bf16 v[132:135], a[232:235], v[44:47], v[132:135]\n"
"ds_read_b32 v243, v241\n"
"v_mfma_f32_16x16x32_bf16 v[132:135], a[236:239], v[60:63], v[132:135]\n"
"v_cvt_pk_bf16_f32 v214, v136, v137\n"
"v_mfma_f32_16x16x32_bf16 v[132:135], a[0:3], v[64:67], v[132:135]\n"
"v_cvt_pk_bf16_f32 v215, v138, v139\n"
"v_mfma_f32_16x16x32_bf16 v[132:135], a[4:7], v[68:71], v[132:135]\n"
"v_lshlrev_b32 v16, 16, v176\n"
"v_mfma_f32_16x16x32_bf16 v[132:135], a[8:11], v[84:87], v[132:135]\n"
"s_waitcnt lgkmcnt(0)\n"
"v_cmp_gt_u32 s[56:57], 28, v243\n"
"v_mfma_f32_16x16x32_bf16 v[132:135], a[12:15], v[88:91], v[132:135]\n"
"s_sub_u32 s84, s84, s83\n"
"v_mfma_f32_16x16x32_bf16 v[132:135], a[16:19], v[92:95], v[132:135]\n"
"s_or_b64 s[56:57], s[56:57], vcc\n"
"s_cbranch_scc1 LB28\n"
"LB28R:\n"
"v_mfma_f32_16x16x32_bf16 v[132:135], a[20:23], v[108:111], v[132:135]\n"
"s_cmp_lt_i32 s84, s88\n"
"s_cselect_b64 vcc, -1, 0\n"
"v_mfma_f32_16x16x32_bf16 v[132:135], v[140:143], v[112:115], v[132:135]\n"
"ds_write_b64 v245, v[214:215] offset:12296\n"
"v_mfma_f32_16x16x32_bf16 v[132:135], v[156:159], v[116:119], v[132:135]\n"
"v_and_b32 v17, 0xffff0000, v176\n"
"v_mfma_f32_16x16x32_bf16 v[136:139], a[44:47], v[116:119], v[20:23]\n"
"ds_read_b128 v[116:119], v235 offset:16384\n"
"v_mfma_f32_16x16x32_bf16 v[136:139], a[40:43], v[112:115], v[136:139]\n"
"ds_read_b128 v[112:115], v234 offset:16384\n"
"v_mfma_f32_16x16x32_bf16 v[136:139], a[36:39], v[108:111], v[136:139]\n"
"ds_read_b128 v[108:111], v233 offset:16384\n"
"v_mfma_f32_16x16x32_bf16 v[136:139], a[32:35], v[92:95], v[136:139]\n"
"ds_read_b128 v[92:95], v232 offset:16384\n"
"s_cmp_gt_i32 s84, 38\n"
"s_cbranch_scc1 LEARLY\n"
"LHT12:\n"
"s_cbranch_execz LHR12\n"
"v_mfma_f32_16x16x32_bf16 v[136:139], a[28:31], v[88:91], v[136:139]\n"
"ds_read_b128 v[88:91], v235 offset:8192\n"
"v_cvt_pk_bf16_f32 v208, v132, v133\n"
"v_mfma_f32_16x16x32_bf16 v[136:139], a[24:27], v[84:87], v[136:139]\n"
"ds_read_b128 v[84:87], v234 offset:8192\n"
"v_cvt_pk_bf16_f32 v209, v134, v135\n"
"v_mfma_f32_16x16x32_bf16 v[136:139], v[204:207], v[68:71], v[136:139]\n"
"ds_read_b128 v[68:71], v233 offset:8192\n"
"v_mfma_f32_16x16x32_bf16 v[136:139], v[188:191], v[64:67], v[136:139]\n"
"ds_read_b128 v[64:67], v232 offset:8192\n"
"v_mfma_f32_16x16x32_bf16 v[136:139], v[184:187], v[60:63], v[136:139]\n"
"ds_read_b128 v[60:63], v235 offset:0\n"
"v_mfma_f32_16x16x32_bf16 v[136:139], v[180:183], v[44:47], v[136:139]\n"
"ds_read_b128 v[44:47], v234 offset:0\n"
"v_mfma_f32_16x16x32_bf16 v[136:139], v[164:167], v[40:43], v[136:139]\n"
"ds_read_b128 v[40:43], v233 offset:0\n"
"v_mfma_f32_16x16x32_bf16 v[136:139], v[160:163], v[36:39], v[136:139]\n"
"ds_read_b128 v[36:39], v232 offset:0\n"
"s_waitcnt lgkmcnt(8)\n"
"v_mfma_f32_16x16x32_bf16 v[132:135], a[76:79], v[116:119], v[0:3]\n"
"ds_write_b64 v244, v[208:209] offset:12416\n"
"v_mfma_f32_16x16x32_bf16 v[132:135], a[72:75], v[112:115], v[132:135]\n"
"s_add_u32 m0, s31, 0x1f00\n"
"v_mfma_f32_16x16x32_bf16 v[132:135], a[68:71], v[108:111], v[132:135]\n"
"global_load_lds_dwordx4 v237, s[52:53] offset:256 nt\n"
"v_mfma_f32_16x16x32_bf16 v[132:135], a[64:67], v[92:95], v[132:135]\n"
"v_lshlrev_b32 v18, 16, v177\n"
"v_cvt_pk_bf16_f32 v211, v138, v139\n"
"s_waitcnt lgkmcnt(5)\n"
"v_mfma_f32_16x16x32_bf16 v[132:135], a[60:63], v[88:91], v[132:135]\n"
"v_and_b32 v19, 0xffff0000, v177\n"
"v_mfma_f32_16x16x32_bf16 v[132:135], a[56:59], v[84:87], v[132:135]\n"
"v_lshlrev_b32 v20, 16, v178\n"
"v_mfma_f32_16x16x32_bf16 v[132:135], a[52:55], v[68:71], v[132:135]\n"
"v_and_b32 v21, 0xffff0000, v178\n"
"v_mfma_f32_16x16x32_bf16 v[132:135], a[48:51], v[64:67], v[132:135]\n"
"v_lshlrev_b32 v22, 16, v179\n"
"s_waitcnt lgkmcnt(1)\n"
"v_mfma_f32_16x16x32_bf16 v[132:135], a[252:255], v[60:63], v[132:135]\n"
"v_and_b32 v23, 0xffff0000, v179\n"
"v_mfma_f32_16x16x32_bf16 v[132:135], a[248:251], v[44:47], v[132:135]\n"
"v_cvt_pk_bf16_f32 v210, v136, v137\n"
"v_mfma_f32_16x16x32_bf16 v[132:135], a[244:247], v[40:43], v[132:135]\n"
"s_add_u32 s54, s26, s45\n"
"s_addc_u32 s55, s27, 0\n"
"v_mfma_f32_16x16x32_bf16 v[132:135], a[240:243], v[36:39], v[132:135]\n"
"ds_read_b128 v[216:219], v246 offset:0\n"
"v_mfma_f32_16x16x32_bf16 v[136:139], a[80:83], v[36:39], v[4:7]\n"
"ds_read_b128 v[220:223], v246 offset:1024\n"
"v_mfma_f32_16x16x32_bf16 v[136:139], a[84:87], v[40:43], v[136:139]\n"
"ds_read_b128 v[224:227], v246 offset:2048\n"
"v_mfma_f32_16x16x32_bf16 v[136:139], a[88:91], v[44:47], v[136:139]\n"
"ds_write_b64 v244, v[210:211] offset:12424\n"
"v_mfma_f32_16x16x32_bf16 v[136:139], a[92:95], v[60:63], v[136:139]\n"
"v_cvt_pk_bf16_f32 v208, v132, v133\n"
"v_mfma_f32_16x16x32_bf16 v[136:139], a[96:99], v[64:67], v[136:139]\n"
"v_cvt_pk_bf16_f32 v209, v134, v135\n"
"v_mfma_f32_16x16x32_bf16 v[136:139], a[100:103], v[68:71], v[136:139]\n"
"s_add_u32 m0, s31, 0x3e00\n"
"v_mfma_f32_16x16x32_bf16 v[136:139], a[104:107], v[84:87], v[136:139]\n"
"global_load_lds_dwordx4 v237, s[52:53] offset:512 nt\n"
"v_mfma_f32_16x16x32_bf16 v[136:139], a[108:111], v[88:91], v[136:139]\n"
"s_waitcnt lgkmcnt(1)\n"
"v_mfma_f32_16x16x32_bf16 v[136:139], a[112:115], v[92:95], v[136:139]\n"
"s_sub_i32 s84, s84, s19\n"
"v_mfma_f32_16x16x32_bf16 v[136:139], a[116:119], v[108:111], v[136:139]\n"
"global_store_dwordx4 v247, v[216:219], s[54:55] offset:0 sc1\n"
"ds_write_b64 v244, v[208:209] offset:24576\n"
"v_mfma_f32_16x16x32_bf16 v[136:139], a[120:123], v[112:115], v[136:139]\n"
"s_max_i32 s88, s88, s84\n"
"v_mfma_f32_16x16x32_bf16 v[136:139], a[124:127], v[116:119], v[136:139]\n"
"v_mfma_f32_16x16x32_bf16 v[132:135], a[172:175], v[116:119], v[8:11]\n"
"v_mfma_f32_16x16x32_bf16 v[132:135], a[168:171], v[112:115], v[132:135]\n"
"v_mfma_f32_16x16x32_bf16 v[132:135], a[164:167], v[108:111], v[132:135]\n"
"v_mfma_f32_16x16x32_bf16 v[132:135], a[160:163], v[92:95], v[132:135]\n"
"v_cvt_pk_bf16_f32 v210, v136, v137\n"
"v_mfma_f32_16x16x32_bf16 v[132:135], a[156:159], v[88:91], v[132:135]\n"
"v_cvt_pk_bf16_f32 v211, v138, v139\n"
"v_mfma_f32_16x16x32_bf16 v[132:135], a[152:155], v[84:87], v[132:135]\n"
"v_mfma_f32_16x16x32_bf16 v[132:135], a[148:151], v[68:71], v[132:135]\n"
"v_mfma_f32_16x16x32_bf16 v[132:135], a[144:147], v[64:67], v[132:135]\n"
"v_mfma_f32_16x16x32_bf16 v[132:135], a[140:143], v[60:63], v[132:135]\n"
"v_mfma_f32_16x16x32_bf16 v[132:135], a[136:139], v[44:47], v[132:135]\n"
"global_store_dwordx4 v252, v[220:223], s[54:55] offset:0 sc1\n"
"ds_write_b64 v244, v[210:211] offset:24584\n"
"v_mfma_f32_16x16x32_bf16 v[132:135], a[132:135], v[40:43], v[132:135]\n"
"v_mfma_f32_16x16x32_bf16 v[132:135], a[128:131], v[36:39], v[132:135]\n"
"v_mfma_f32_16x16x32_bf16 v[136:139], a[176:179], v[36:39], v[12:15]\n"
"v_mfma_f32_16x16x32_bf16 v[136:139], a[180:183], v[40:43], v[136:139]\n"
"v_mfma_f32_16x16x32_bf16 v[136:139], a[184:187], v[44:47], v[136:139]\n"
"v_mfma_f32_16x16x32_bf16 v[136:139], a[188:191], v[60:63], v[136:139]\n"
"v_cvt_pk_bf16_f32 v212, v132, v133\n"
"v_mfma_f32_16x16x32_bf16 v[136:139], a[192:195], v[64:67], v[136:139]\n"
"v_mov_b32 v242, 29\n"
"v_mfma_f32_16x16x32_bf16 v[136:139], a[196:199], v[68:71], v[136:139]\n"
"v_cvt_pk_bf16_f32 v213, v134, v135\n"
"v_mfma_f32_16x16x32_bf16 v[136:139], a[200:203], v[84:87], v[136:139]\n"
"s_waitcnt lgkmcnt(2)\n"
"ds_write_b32 v240, v242\n"
"v_mfma_f32_16x16x32_bf16 v[136:139], a[204:207], v[88:91], v[136:139]\n"
"s_add_u32 m0, s31, 0x1000\n"
"v_mfma_f32_16x16x32_bf16 v[136:139], a[208:211], v[92:95], v[136:139]\n"
"global_load_lds_dwordx4 v238, s[52:53] offset:0 nt\n"
"v_mfma_f32_16x16x32_bf16 v[136:139], a[212:215], v[108:111], v[136:139]\n"
"v_mfma_f32_16x16x32_bf16 v[136:139], a[216:219], v[112:115], v[136:139]\n"
"global_store_dwordx4 v253, v[224:227], s[54:55] offset:0 sc1\n"
"ds_write_b64 v245, v[212:213] offset:24576\n"
"v_mfma_f32_16x16x32_bf16 v[136:139], a[220:223], v[116:119], v[136:139]\n"
"v_mfma_f32_16x16x32_bf16 v[132:135], v[156:159], v[116:119], v[16:19]\n"
"v_mfma_f32_16x16x32_bf16 v[132:135], v[140:143], v[112:115], v[132:135]\n"
"v_mfma_f32_16x16x32_bf16 v[132:135], a[20:23], v[108:111], v[132:135]\n"
"ds_read_b32 v243, v241\n"
"v_mfma_f32_16x16x32_bf16 v[132:135], a[16:19], v[92:95], v[132:135]\n"
"v_cvt_pk_bf16_f32 v214, v136, v137\n"
"v_mfma_f32_16x16x32_bf16 v[132:135], a[12:15], v[88:91], v[132:135]\n"
"v_cvt_pk_bf16_f32 v215, v138, v139\n"
"v_mfma_f32_16x16x32_bf16 v[132:135], a[8:11], v[84:87], v[132:135]\n"
"v_mfma_f32_16x16x32_bf16 v[132:135], a[4:7], v[68:71], v[132:135]\n"
"s_waitcnt lgkmcnt(0)\n"
"v_cmp_gt_u32 s[56:57], 29, v243\n"
"v_mfma_f32_16x16x32_bf16 v[132:135], a[0:3], v[64:67], v[132:135]\n"
"v_mfma_f32_16x16x32_bf16 v[132:135], a[236:239], v[60:63], v[132:135]\n"
"s_or_b64 s[56:57], s[56:57], vcc\n"
"s_cbranch_scc1 LB29\n"
"LB29R:\n"
"v_mfma_f32_16x16x32_bf16 v[132:135], a[232:235], v[44:47], v[132:135]\n"
"ds_write_b64 v245, v[214:215] offset:24584\n"
"v_mfma_f32_16x16x32_bf16 v[132:135], a[228:231], v[40:43], v[132:135]\n"
"v_mfma_f32_16x16x32_bf16 v[132:135], a[224:227], v[36:39], v[132:135]\n"
"v_mfma_f32_16x16x32_bf16 v[136:139], v[160:163], v[36:39], v[20:23]\n"
"ds_read_b128 v[36:39], v232 offset:256\n"
"v_mfma_f32_16x16x32_bf16 v[136:139], v[164:167], v[40:43], v[136:139]\n"
"ds_read_b128 v[40:43], v233 offset:256\n"
"v_mfma_f32_16x16x32_bf16 v[136:139], v[180:183], v[44:47], v[136:139]\n"
"ds_read_b128 v[44:47], v234 offset:256\n"
"v_mfma_f32_16x16x32_bf16 v[136:139], v[184:187], v[60:63], v[136:139]\n"
"ds_read_b128 v[60:63], v235 offset:256\n"
"v_cvt_pk_bf16_f32 v208, v132, v133\n"
"v_mfma_f32_16x16x32_bf16 v[136:139], v[188:191], v[64:67], v[136:139]\n"
"ds_read_b128 v[64:67], v232 offset:8448\n"
"v_cvt_pk_bf16_f32 v209, v134, v135\n"
"v_mfma_f32_16x16x32_bf16 v[136:139], v[204:207], v[68:71], v[136:139]\n"
"ds_read_b128 v[68:71], v233 offset:8448\n"
"v_mfma_f32_16x16x32_bf16 v[136:139], a[24:27], v[84:87], v[136:139]\n"
"ds_read_b128 v[84:87], v234 offset:8448\n"
"v_mfma_f32_16x16x32_bf16 v[136:139], a[28:31], v[88:91], v[136:139]\n"
"ds_read_b128 v[88:91], v235 offset:8448\n"
"v_mfma_f32_16x16x32_bf16 v[136:139], a[32:35], v[92:95], v[136:139]\n"
"ds_read_b128 v[92:95], v232 offset:16640\n"
"v_mfma_f32_16x16x32_bf16 v[136:139], a[36:39], v[108:111], v[136:139]\n"
"ds_read_b128 v[108:111], v233 offset:16640\n"
"v_mfma_f32_16x16x32_bf16 v[136:139], a[40:43], v[112:115], v[136:139]\n"
"ds_read_b128 v[112:115], v234 offset:16640\n"
"v_mfma_f32_16x16x32_bf16 v[136:139], a[44:47], v[116:119], v[136:139]\n"
"ds_read_b128 v[116:119], v235 offset:16640\n"
"s_waitcnt lgkmcnt(8)\n"
"v_mfma_f32_16x16x32_bf16 v[132:135], a[240:243], v[36:39], v[0:3]\n"
"ds_write_b64 v244, v[208:209] offset:24704\n"
"v_mfma_f32_16x16x32_bf16 v[132:135], a[244:247], v[40:43], v[132:135]\n"
"s_add_u32 m0, s31, 0x2f00\n"
"v_mfma_f32_16x16x32_bf16 v[132:135], a[248:251], v[44:47], v[132:135]\n"
"global_load_lds_dwordx4 v238, s[52:53] offset:256 nt\n"
"v_mfma_f32_16x16x32_bf16 v[132:135], a[252:255], v[60:63], v[132:135]\n"
"v_cvt_pk_bf16_f32 v210, v136, v137\n"
"s_waitcnt lgkmcnt(5)\n"
"v_mfma_f32_16x16x32_bf16 v[132:135], a[48:51], v[64:67], v[132:135]\n"
"v_cvt_pk_bf16_f32 v211, v138, v139\n"
"v_mfma_f32_16x16x32_bf16 v[132:135], a[52:55], v[68:71], v[132:135]\n"
"v_mfma_f32_16x16x32_bf16 v[132:135], a[56:59], v[84:87], v[132:135]\n"
"ds_read_b128 v[216:219], v246 offset:12288\n"
"v_mfma_f32_16x16x32_bf16 v[132:135], a[60:63], v[88:91], v[132:135]\n"
"ds_read_b128 v[220:223], v246 offset:13312\n"
"s_waitcnt lgkmcnt(3)\n"
"v_mfma_f32_16x16x32_bf16 v[132:135], a[64:67], v[92:95], v[132:135]\n"
"ds_read_b128 v[224:227], v246 offset:14336\n"
"v_mfma_f32_16x16x32_bf16 v[132:135], a[68:71], v[108:111], v[132:135]\n"
"ds_write_b64 v244, v[210:211] offset:24712\n"
"v_mfma_f32_16x16x32_bf16 v[132:135], a[72:75], v[112:115], v[132:135]\n"
"v_mfma_f32_16x16x32_bf16 v[132:135], a[76:79], v[116:119], v[132:135]\n"
"v_mfma_f32_16x16x32_bf16 v[136:139], a[124:127], v[116:119], v[4:7]\n"
"s_add_u32 s83, s83, s86\n"
"v_mfma_f32_16x16x32_bf16 v[136:139], a[120:123], v[112:115], v[136:139]\n"
"s_sub_i32 s88, s88, 6\n"
"v_mfma_f32_16x16x32_bf16 v[136:139], a[116:119], v[108:111], v[136:139]\n"
"s_max_i32 s88, s88, 0\n"
"v_mfma_f32_16x16x32_bf16 v[136:139], a[112:115], v[92:95], v[136:139]\n"
"v_cvt_pk_bf16_f32 v208, v132, v133\n"
"v_mfma_f32_16x16x32_bf16 v[136:139], a[108:111], v[88:91], v[136:139]\n"
"v_cvt_pk_bf16_f32 v209, v134, v135\n"
"v_mfma_f32_16x16x32_bf16 v[136:139], a[104:107], v[84:87], v[136:139]\n"
"v_mfma_f32_16x16x32_bf16 v[136:139], a[100:103], v[68:71], v[136:139]\n"
"v_mfma_f32_16x16x32_bf16 v[136:139], a[96:99], v[64:67], v[136:139]\n"
"s_waitcnt lgkmcnt(1)\n"
"v_mfma_f32_16x16x32_bf16 v[136:139], a[92:95], v[60:63], v[136:139]\n"
"v_mfma_f32_16x16x32_bf16 v[136:139], a[88:91], v[44:47], v[136:139]\n"
"global_store_dwordx4 v247, v[216:219], s[54:55] offset:768 sc1\n"
"ds_write_b64 v244, v[208:209] offset:0\n"
"v_mfma_f32_16x16x32_bf16 v[136:139], a[84:87], v[40:43], v[136:139]\n"
"s_add_u32 m0, s31, 0x4e00\n"
"v_mfma_f32_16x16x32_bf16 v[136:139], a[80:83], v[36:39], v[136:139]\n"
"global_load_lds_dwordx4 v238, s[52:53] offset:512 nt\n"
"v_mfma_f32_16x16x32_bf16 v[132:135], a[128:131], v[36:39], v[8:11]\n"
"v_mfma_f32_16x16x32_bf16 v[132:135], a[132:135], v[40:43], v[132:135]\n"
"v_mfma_f32_16x16x32_bf16 v[132:135], a[136:139], v[44:47], v[132:135]\n"
"v_mfma_f32_16x16x32_bf16 v[132:135], a[140:143], v[60:63], v[132:135]\n"
"v_cvt_pk_bf16_f32 v210, v136, v137\n"
"v_mfma_f32_16x16x32_bf16 v[132:135], a[144:147], v[64:67], v[132:135]\n"
"v_cvt_pk_bf16_f32 v211, v138, v139\n"
"v_mfma_f32_16x16x32_bf16 v[132:135], a[148:151], v[68:71], v[132:135]\n"
"v_mfma_f32_16x16x32_bf16 v[132:135], a[152:155], v[84:87], v[132:135]\n"
"v_mfma_f32_16x16x32_bf16 v[132:135], a[156:159], v[88:91], v[132:135]\n"
"v_mfma_f32_16x16x32_bf16 v[132:135], a[160:163], v[92:95], v[132:135]\n"
"v_mfma_f32_16x16x32_bf16 v[132:135], a[164:167], v[108:111], v[132:135]\n"
"s_memrealtime s[84:85]\n"
"v_mfma_f32_16x16x32_bf16 v[132:135], a[168:171], v[112:115], v[132:135]\n"
"global_store_dwordx4 v252, v[220:223], s[54:55] offset:768 sc1\n"
"ds_write_b64 v244, v[210:211] offset:8\n"
"v_mfma_f32_16x16x32_bf16 v[132:135], a[172:175], v[116:119], v[132:135]\n"
"v_mfma_f32_16x16x32_bf16 v[136:139], a[220:223], v[116:119], v[12:15]\n"
"v_mfma_f32_16x16x32_bf16 v[136:139], a[216:219], v[112:115], v[136:139]\n"
"v_mfma_f32_16x16x32_bf16 v[136:139], a[212:215], v[108:111], v[136:139]\n"
"v_mfma_f32_16x16x32_bf16 v[136:139], a[208:211], v[92:95], v[136:139]\n"
"v_cvt_pk_bf16_f32 v212, v132, v133\n"
"v_mfma_f32_16x16x32_bf16 v[136:139], a[204:207], v[88:91], v[136:139]\n"
"v_mov_b32 v242, 30\n"
"v_mfma_f32_16x16x32_bf16 v[136:139], a[200:203], v[84:87], v[136:139]\n"
"v_cvt_pk_bf16_f32 v213, v134, v135\n"
"v_mfma_f32_16x16x32_bf16 v[136:139], a[196:199], v[68:71], v[136:139]\n"
"s_waitcnt vmcnt(14) lgkmcnt(2)\n"
"ds_write_b32 v240, v242\n"
"v_mfma_f32_16x16x32_bf16 v[136:139], a[192:195], v[64:67], v[136:139]\n"
"v_mfma_f32_16x16x32_bf16 v[136:139], a[188:191], v[60:63], v[136:139]\n"
"v_mfma_f32_16x16x32_bf16 v[136:139], a[184:187], v[44:47], v[136:139]\n"
"v_mfma_f32_16x16x32_bf16 v[136:139], a[180:183], v[40:43], v[136:139]\n"
"global_store_dwordx4 v253, v[224:227], s[54:55] offset:768 sc1\n"
"ds_write_b64 v245, v[212:213] offset:0\n"
"v_mfma_f32_16x16x32_bf16 v[136:139], a[176:179], v[36:39], v[136:139]\n"
"s_add_u32 s52, s20, s49\n"
"s_addc_u32 s53, s21, 0\n"
"v_mfma_f32_16x16x32_bf16 v[132:135], a[224:227], v[36:39], v[16:19]\n"
"s_add_u32 m0, s31, 0x6000\n"
"v_mfma_f32_16x16x32_bf16 v[132:135], a[228:231], v[40:43], v[132:135]\n"
"global_load_lds_dwordx4 v237, s[52:53] offset:0 nt\n"
"v_mfma_f32_16x16x32_bf16 v[132:135], a[232:235], v[44:47], v[132:135]\n"
"ds_read_b32 v243, v241\n"
"v_mfma_f32_16x16x32_bf16 v[132:135], a[236:239], v[60:63], v[132:135]\n"
"v_cvt_pk_bf16_f32 v214, v136, v137\n"
"v_mfma_f32_16x16x32_bf16 v[132:135], a[0:3], v[64:67], v[132:135]\n"
"v_cvt_pk_bf16_f32 v215, v138, v139\n"
"v_mfma_f32_16x16x32_bf16 v[132:135], a[4:7], v[68:71], v[132:135]\n"
"s_bitcmp1_b32 s92, 7\n"
"s_cselect_b32 s93, 0x6000, 0\n"
"v_mfma_f32_16x16x32_bf16 v[132:135], a[8:11], v[84:87], v[132:135]\n"
"s_waitcnt lgkmcnt(0)\n"
"v_cmp_gt_u32 s[56:57], 30, v243\n"
"v_mfma_f32_16x16x32_bf16 v[132:135], a[12:15], v[88:91], v[132:135]\n"
"s_sub_u32 s84, s84, s83\n"
"v_mfma_f32_16x16x32_bf16 v[132:135], a[16:19], v[92:95], v[132:135]\n"
"s_or_b64 s[56:57], s[56:57], vcc\n"
"s_cbranch_scc1 LB30\n"
"LB30R:\n"
"v_mfma_f32_16x16x32_bf16 v[132:135], a[20:23], v[108:111], v[132:135]\n"
"s_cmp_lt_i32 s84, s88\n"
"s_cselect_b64 vcc, -1, 0\n"
"v_mfma_f32_16x16x32_bf16 v[132:135], v[140:143], v[112:115], v[132:135]\n"
"ds_write_b64 v245, v[214:215] offset:8\n"
"v_mfma_f32_16x16x32_bf16 v[132:135], v[156:159], v[116:119], v[132:135]\n"
"s_sub_u32 s94, s93, 0xc000\n"
"v_mfma_f32_16x16x32_bf16 v[136:139], a[44:47], v[116:119], v[20:23]\n"
"ds_read_b128 v[116:119], v235 offset:40960\n"
"v_mfma_f32_16x16x32_bf16 v[136:139], a[40:43], v[112:115], v[136:139]\n"
"ds_read_b128 v[112:115], v234 offset:40960\n"
"v_mfma_f32_16x16x32_bf16 v[136:139], a[36:39], v[108:111], v[136:139]\n"
"ds_read_b128 v[108:111], v233 offset:40960\n"
"v_mfma_f32_16x16x32_bf16 v[136:139], a[32:35], v[92:95], v[136:139]\n"
"ds_read_b128 v[92:95], v232 offset:40960\n"
"v_cvt_pk_bf16_f32 v208, v132, v133\n"
"v_mfma_f32_16x16x32_bf16 v[136:139], a[28:31], v[88:91], v[136:139]\n"
"ds_read_b128 v[88:91], v235 offset:32768\n"
"v_cvt_pk_bf16_f32 v209, v134, v135\n"
"v_mfma_f32_16x16x32_bf16 v[136:139], a[24:27], v[84:87], v[136:139]\n"
"ds_read_b128 v[84:87], v234 offset:32768\n"
"v_mfma_f32_16x16x32_bf16 v[136:139], v[204:207], v[68:71], v[136:139]\n"
"ds_read_b128 v[68:71], v233 offset:32768\n"
"v_mfma_f32_16x16x32_bf16 v[136:139], v[188:191], v[64:67], v[136:139]\n"
"ds_read_b128 v[64:67], v232 offset:32768\n"
"v_mfma_f32_16x16x32_bf16 v[136:139], v[184:187], v[60:63], v[136:139]\n"
"ds_read_b128 v[60:63], v235 offset:24576\n"
"v_mfma_f32_16x16x32_bf16 v[136:139], v[180:183], v[44:47], v[136:139]\n"
"ds_read_b128 v[44:47], v234 offset:24576\n"
"v_mfma_f32_16x16x32_bf16 v[136:139], v[164:167], v[40:43], v[136:139]\n"
"ds_read_b128 v[40:43], v233 offset:24576\n"
"v_mfma_f32_16x16x32_bf16 v[136:139], v[160:163], v[36:39], v[136:139]\n"
"ds_read_b128 v[36:39], v232 offset:24576\n"
"s_waitcnt lgkmcnt(8)\n"
"v_mfma_f32_16x16x32_bf16 v[132:135], a[76:79], v[116:119], v[0:3]\n"
"ds_write_b64 v244, v[208:209] offset:128\n"
"v_mfma_f32_16x16x32_bf16 v[132:135], a[72:75], v[112:115], v[132:135]\n"
"s_add_u32 m0, s31, 0x7f00\n"
"v_mfma_f32_16x16x32_bf16 v[132:135], a[68:71], v[108:111], v[132:135]\n"
"global_load_lds_dwordx4 v237, s[52:53] offset:256 nt\n"
"v_mfma_f32_16x16x32_bf16 v[132:135], a[64:67], v[92:95], v[132:135]\n"
"v_cvt_pk_bf16_f32 v210, v136, v137\n"
"s_waitcnt lgkmcnt(5)\n"
"v_mfma_f32_16x16x32_bf16 v[132:135], a[60:63], v[88:91], v[132:135]\n"
"v_cvt_pk_bf16_f32 v211, v138, v139\n"
"v_mfma_f32_16x16x32_bf16 v[132:135], a[56:59], v[84:87], v[132:135]\n"
"s_add_u32 s54, s26, s46\n"
"s_addc_u32 s55, s27, 0\n"
"v_mfma_f32_16x16x32_bf16 v[132:135], a[52:55], v[68:71], v[132:135]\n"
"ds_read_b128 v[216:219], v246 offset:24576\n"
"v_mfma_f32_16x16x32_bf16 v[132:135], a[48:51], v[64:67], v[132:135]\n"
"ds_read_b128 v[220:223], v246 offset:25600\n"
"s_waitcnt lgkmcnt(3)\n"
"v_mfma_f32_16x16x32_bf16 v[132:135], a[252:255], v[60:63], v[132:135]\n"
"ds_read_b128 v[224:227], v246 offset:26624\n"
"v_mfma_f32_16x16x32_bf16 v[132:135], a[248:251], v[44:47], v[132:135]\n"
"ds_write_b64 v244, v[210:211] offset:136\n"
"v_mfma_f32_16x16x32_bf16 v[132:135], a[244:247], v[40:43], v[132:135]\n"
"s_sub_i32 s84, s84, s19\n"
"v_mfma_f32_16x16x32_bf16 v[132:135], a[240:243], v[36:39], v[132:135]\n"
"s_max_i32 s88, s88, s84\n"
"v_mfma_f32_16x16x32_bf16 v[136:139], a[80:83], v[36:39], v[4:7]\n"
"v_add_u32 v231, s93, v247\n"
"v_mfma_f32_16x16x32_bf16 v[136:139], a[84:87], v[40:43], v[136:139]\n"
"v_add_u32 v247, s94, v247\n"
"v_mfma_f32_16x16x32_bf16 v[136:139], a[88:91], v[44:47], v[136:139]\n"
"v_min_u32 v247, v247, v231\n"
"v_mfma_f32_16x16x32_bf16 v[136:139], a[92:95], v[60:63], v[136:139]\n"
"v_cvt_pk_bf16_f32 v208, v132, v133\n"
"v_add_u32 v231, s93, v252\n"
"v_mfma_f32_16x16x32_bf16 v[136:139], a[96:99], v[64:67], v[136:139]\n"
"v_cvt_pk_bf16_f32 v209, v134, v135\n"
"v_add_u32 v252, s94, v252\n"
"v_mfma_f32_16x16x32_bf16 v[136:139], a[100:103], v[68:71], v[136:139]\n"
"v_min_u32 v252, v252, v231\n"
"v_mfma_f32_16x16x32_bf16 v[136:139], a[104:107], v[84:87], v[136:139]\n"
"v_add_u32 v231, s93, v253\n"
"v_mfma_f32_16x16x32_bf16 v[136:139], a[108:111], v[88:91], v[136:139]\n"
"s_waitcnt lgkmcnt(1)\n"
"v_add_u32 v253, s94, v253\n"
"v_mfma_f32_16x16x32_bf16 v[136:139], a[112:115], v[92:95], v[136:139]\n"
"v_min_u32 v253, v253, v231\n"
"v_mfma_f32_16x16x32_bf16 v[136:139], a[116:119], v[108:111], v[136:139]\n"
"global_store_dwordx4 v247, v[216:219], s[54:55] offset:0 sc1\n"
"ds_write_b64 v244, v[208:209] offset:12288\n"
"v_mfma_f32_16x16x32_bf16 v[136:139], a[120:123], v[112:115], v[136:139]\n"
"s_add_u32 m0, s31, 0x9e00\n"
"v_mfma_f32_16x16x32_bf16 v[136:139], a[124:127], v[116:119], v[136:139]\n"
"global_load_lds_dwordx4 v237, s[52:53] offset:512 nt\n"
"v_mfma_f32_16x16x32_bf16 v[132:135], a[172:175], v[116:119], v[8:11]\n"
"v_mfma_f32_16x16x32_bf16 v[132:135], a[168:171], v[112:115], v[132:135]\n"
"v_mfma_f32_16x16x32_bf16 v[132:135], a[164:167], v[108:111], v[132:135]\n"
"v_mfma_f32_16x16x32_bf16 v[132:135], a[160:163], v[92:95], v[132:135]\n"
"v_cvt_pk_bf16_f32 v210, v136, v137\n"
"v_mfma_f32_16x16x32_bf16 v[132:135], a[156:159], v[88:91], v[132:135]\n"
"v_cvt_pk_bf16_f32 v211, v138, v139\n"
"v_mfma_f32_16x16x32_bf16 v[132:135], a[152:155], v[84:87], v[132:135]\n"
"v_mfma_f32_16x16x32_bf16 v[132:135], a[148:151], v[68:71], v[132:135]\n"
"v_mfma_f32_16x16x32_bf16 v[132:135], a[144:147], v[64:67], v[132:135]\n"
"v_mfma_f32_16x16x32_bf16 v[132:135], a[140:143], v[60:63], v[132:135]\n"
"v_mfma_f32_16x16x32_bf16 v[132:135], a[136:139], v[44:47], v[132:135]\n"
"global_store_dwordx4 v252, v[220:223], s[54:55] offset:0 sc1\n"
"ds_write_b64 v244, v[210:211] offset:12296\n"
"v_mfma_f32_16x16x32_bf16 v[132:135], a[132:135], v[40:43], v[132:135]\n"
"v_mfma_f32_16x16x32_bf16 v[132:135], a[128:131], v[36:39], v[132:135]\n"
"v_mfma_f32_16x16x32_bf16 v[136:139], a[176:179], v[36:39], v[12:15]\n"
"v_mfma_f32_16x16x32_bf16 v[136:139], a[180:183], v[40:43], v[136:139]\n"
"v_mfma_f32_16x16x32_bf16 v[136:139], a[184:187], v[44:47], v[136:139]\n"
"v_mfma_f32_16x16x32_bf16 v[136:139], a[188:191], v[60:63], v[136:139]\n"
"v_cvt_pk_bf16_f32 v212, v132, v133\n"
"v_mfma_f32_16x16x32_bf16 v[136:139], a[192:195], v[64:67], v[136:139]\n"
"v_mov_b32 v242, 31\n"
"v_mfma_f32_16x16x32_bf16 v[136:139], a[196:199], v[68:71], v[136:139]\n"
"v_cvt_pk_bf16_f32 v213, v134, v135\n"
"v_mfma_f32_16x16x32_bf16 v[136:139], a[200:203], v[84:87], v[136:139]\n"
"s_waitcnt lgkmcnt(2)\n"
"ds_write_b32 v240, v242\n"
"v_mfma_f32_16x16x32_bf16 v[136:139], a[204:207], v[88:91], v[136:139]\n"
"v_mfma_f32_16x16x32_bf16 v[136:139], a[208:211], v[92:95], v[136:139]\n"
"v_mfma_f32_16x16x32_bf16 v[136:139], a[212:215], v[108:111], v[136:139]\n"
"v_mfma_f32_16x16x32_bf16 v[136:139], a[216:219], v[112:115], v[136:139]\n"
"global_store_dwordx4 v253, v[224:227], s[54:55] offset:0 sc1\n"
"ds_write_b64 v245, v[212:213] offset:12288\n"
"v_mfma_f32_16x16x32_bf16 v[136:139], a[220:223], v[116:119], v[136:139]\n"
"s_add_u32 m0, s31, 0x7000\n"
"v_mfma_f32_16x16x32_bf16 v[132:135], v[156:159], v[116:119], v[16:19]\n"
"global_load_lds_dwordx4 v238, s[52:53] offset:0 nt\n"
"v_mfma_f32_16x16x32_bf16 v[132:135], v[140:143], v[112:115], v[132:135]\n"
"v_mfma_f32_16x16x32_bf16 v[132:135], a[20:23], v[108:111], v[132:135]\n"
"ds_read_b32 v243, v241\n"
"v_mfma_f32_16x16x32_bf16 v[132:135], a[16:19], v[92:95], v[132:135]\n"
"v_cvt_pk_bf16_f32 v214, v136, v137\n"
"v_mfma_f32_16x16x32_bf16 v[132:135], a[12:15], v[88:91], v[132:135]\n"
"v_cvt_pk_bf16_f32 v215, v138, v139\n"
"v_mfma_f32_16x16x32_bf16 v[132:135], a[8:11], v[84:87], v[132:135]\n"
"v_mfma_f32_16x16x32_bf16 v[132:135], a[4:7], v[68:71], v[132:135]\n"
"s_waitcnt lgkmcnt(0)\n"
"v_cmp_gt_u32 s[56:57], 31, v243\n"
"v_mfma_f32_16x16x32_bf16 v[132:135], a[0:3], v[64:67], v[132:135]\n"
"v_mfma_f32_16x16x32_bf16 v[132:135], a[236:239], v[60:63], v[132:135]\n"
"s_or_b64 s[56:57], s[56:57], vcc\n"
"s_cbranch_scc1 LB31\n"
"LB31R:\n"
"v_mfma_f32_16x16x32_bf16 v[132:135], a[232:235], v[44:47], v[132:135]\n"
"ds_write_b64 v245, v[214:215] offset:12296\n"
"v_mfma_f32_16x16x32_bf16 v[132:135], a[228:231], v[40:43], v[132:135]\n"
"v_mfma_f32_16x16x32_bf16 v[132:135], a[224:227], v[36:39], v[132:135]\n"
"v_mfma_f32_16x16x32_bf16 v[136:139], v[160:163], v[36:39], v[20:23]\n"
"ds_read_b128 v[36:39], v232 offset:24832\n"
"v_mfma_f32_16x16x32_bf16 v[136:139], v[164:167], v[40:43], v[136:139]\n"
"ds_read_b128 v[40:43], v233 offset:24832\n"
"v_mfma_f32_16x16x32_bf16 v[136:139], v[180:183], v[44:47], v[136:139]\n"
"ds_read_b128 v[44:47], v234 offset:24832\n"
"v_mfma_f32_16x16x32_bf16 v[136:139], v[184:187], v[60:63], v[136:139]\n"
"ds_read_b128 v[60:63], v235 offset:24832\n"
"v_cvt_pk_bf16_f32 v208, v132, v133\n"
"v_mfma_f32_16x16x32_bf16 v[136:139], v[188:191], v[64:67], v[136:139]\n"
"ds_read_b128 v[64:67], v232 offset:33024\n"
"v_cvt_pk_bf16_f32 v209, v134, v135\n"
"v_mfma_f32_16x16x32_bf16 v[136:139], v[204:207], v[68:71], v[136:139]\n"
"ds_read_b128 v[68:71], v233 offset:33024\n"
"v_mfma_f32_16x16x32_bf16 v[136:139], a[24:27], v[84:87], v[136:139]\n"
"ds_read_b128 v[84:87], v234 offset:33024\n"
"v_mfma_f32_16x16x32_bf16 v[136:139], a[28:31], v[88:91], v[136:139]\n"
"ds_read_b128 v[88:91], v235 offset:33024\n"
"v_mfma_f32_16x16x32_bf16 v[136:139], a[32:35], v[92:95], v[136:139]\n"
"ds_read_b128 v[92:95], v232 offset:41216\n"
"v_mfma_f32_16x16x32_bf16 v[136:139], a[36:39], v[108:111], v[136:139]\n"
"ds_read_b128 v[108:111], v233 offset:41216\n"
"ds_write_b64 v244, v[208:209] offset:12416\n"
"v_mfma_f32_16x16x32_bf16 v[136:139], a[40:43], v[112:115], v[136:139]\n"
"ds_read_b128 v[112:115], v234 offset:41216\n"
"v_mfma_f32_16x16x32_bf16 v[136:139], a[44:47], v[116:119], v[136:139]\n"
"ds_read_b128 v[116:119], v235 offset:41216\n"
"s_waitcnt lgkmcnt(9)\n"
"v_mfma_f32_16x16x32_bf16 v[132:135], a[240:243], v[36:39], v[0:3]\n"
"v_add_u32 v232, 0xffff4000, v232\n"
"v_mfma_f32_16x16x32_bf16 v[132:135], a[244:247], v[40:43], v[132:135]\n"
"v_add_u32 v233, 0xffff4000, v233\n"
"v_mfma_f32_16x16x32_bf16 v[132:135], a[248:251], v[44:47], v[132:135]\n"
"v_add_u32 v234, 0xffff4000, v234\n"
"v_mfma_f32_16x16x32_bf16 v[132:135], a[252:255], v[60:63], v[132:135]\n"
"v_add_u32 v235, 0xffff4000, v235\n"
"s_waitcnt lgkmcnt(5)\n"
"v_mfma_f32_16x16x32_bf16 v[132:135], a[48:51], v[64:67], v[132:135]\n"
"s_add_u32 m0, s31, 0x8f00\n"
"v_mfma_f32_16x16x32_bf16 v[132:135], a[52:55], v[68:71], v[132:135]\n"
"global_load_lds_dwordx4 v238, s[52:53] offset:256 nt\n"
"v_mfma_f32_16x16x32_bf16 v[132:135], a[56:59], v[84:87], v[132:135]\n"
"v_cvt_pk_bf16_f32 v210, v136, v137\n"
"v_mfma_f32_16x16x32_bf16 v[132:135], a[60:63], v[88:91], v[132:135]\n"
"v_cvt_pk_bf16_f32 v211, v138, v139\n"
"s_waitcnt lgkmcnt(0)\n"
"v_mfma_f32_16x16x32_bf16 v[132:135], a[64:67], v[92:95], v[132:135]\n"
"ds_read_b128 v[216:219], v246 offset:0\n"
"v_mfma_f32_16x16x32_bf16 v[132:135], a[68:71], v[108:111], v[132:135]\n"
"ds_read_b128 v[220:223], v246 offset:1024\n"
"v_mfma_f32_16x16x32_bf16 v[132:135], a[72:75], v[112:115], v[132:135]\n"
"ds_read_b128 v[224:227], v246 offset:2048\n"
"v_mfma_f32_16x16x32_bf16 v[132:135], a[76:79], v[116:119], v[132:135]\n"
"v_lshlrev_b32 v0, 16, v192\n"
"v_mfma_f32_16x16x32_bf16 v[136:139], a[124:127], v[116:119], v[4:7]\n"
"ds_write_b64 v244, v[210:211] offset:12424\n"
"v_mfma_f32_16x16x32_bf16 v[136:139], a[120:123], v[112:115], v[136:139]\n"
"v_and_b32 v1, 0xffff0000, v192\n"
"v_mfma_f32_16x16x32_bf16 v[136:139], a[116:119], v[108:111], v[136:139]\n"
"v_lshlrev_b32 v2, 16, v193\n"
"v_mfma_f32_16x16x32_bf16 v[136:139], a[112:115], v[92:95], v[136:139]\n"
"v_cvt_pk_bf16_f32 v208, v132, v133\n"
"v_mfma_f32_16x16x32_bf16 v[136:139], a[108:111], v[88:91], v[136:139]\n"
"v_cvt_pk_bf16_f32 v209, v134, v135\n"
"v_mfma_f32_16x16x32_bf16 v[136:139], a[104:107], v[84:87], v[136:139]\n"
"v_and_b32 v3, 0xffff0000, v193\n"
"v_mfma_f32_16x16x32_bf16 v[136:139], a[100:103], v[68:71], v[136:139]\n"
"v_lshlrev_b32 v4, 16, v194\n"
"v_mfma_f32_16x16x32_bf16 v[136:139], a[96:99], v[64:67], v[136:139]\n"
"s_waitcnt lgkmcnt(1)\n"
"v_mfma_f32_16x16x32_bf16 v[136:139], a[92:95], v[60:63], v[136:139]\n"
"v_and_b32 v5, 0xffff0000, v194\n"
"v_mfma_f32_16x16x32_bf16 v[136:139], a[88:91], v[44:47], v[136:139]\n"
"global_store_dwordx4 v247, v[216:219], s[54:55] offset:768 sc1\n"
"ds_write_b64 v244, v[208:209] offset:24576\n"
"v_mfma_f32_16x16x32_bf16 v[136:139], a[84:87], v[40:43], v[136:139]\n"
"v_lshlrev_b32 v6, 16, v195\n"
"v_mfma_f32_16x16x32_bf16 v[136:139], a[80:83], v[36:39], v[136:139]\n"
"v_and_b32 v7, 0xffff0000, v195\n"
"v_mfma_f32_16x16x32_bf16 v[132:135], a[128:131], v[36:39], v[8:11]\n"
"s_add_u32 m0, s31, 0xae00\n"
"v_mfma_f32_16x16x32_bf16 v[132:135], a[132:135], v[40:43], v[132:135]\n"
"global_load_lds_dwordx4 v238, s[52:53] offset:512 nt\n"
"v_mfma_f32_16x16x32_bf16 v[132:135], a[136:139], v[44:47], v[132:135]\n"
"s_add_u32 s83, s83, s86\n"
"v_mfma_f32_16x16x32_bf16 v[132:135], a[140:143], v[60:63], v[132:135]\n"
"v_cvt_pk_bf16_f32 v210, v136, v137\n"
"v_mfma_f32_16x16x32_bf16 v[132:135], a[144:147], v[64:67], v[132:135]\n"
"v_cvt_pk_bf16_f32 v211, v138, v139\n"
"v_mfma_f32_16x16x32_bf16 v[132:135], a[148:151], v[68:71], v[132:135]\n"
"v_lshlrev_b32 v8, 16, v196\n"
"v_mfma_f32_16x16x32_bf16 v[132:135], a[152:155], v[84:87], v[132:135]\n"
"v_and_b32 v9, 0xffff0000, v196\n"
"v_mfma_f32_16x16x32_bf16 v[132:135], a[156:159], v[88:91], v[132:135]\n"
"v_lshlrev_b32 v10, 16, v197\n"
"v_mfma_f32_16x16x32_bf16 v[132:135], a[160:163], v[92:95], v[132:135]\n"
"v_and_b32 v11, 0xffff0000, v197\n"
"v_mfma_f32_16x16x32_bf16 v[132:135], a[164:167], v[108:111], v[132:135]\n"
"s_memrealtime s[84:85]\n"
"v_mfma_f32_16x16x32_bf16 v[132:135], a[168:171], v[112:115], v[132:135]\n"
"global_store_dwordx4 v252, v[220:223], s[54:55] offset:768 sc1\n"
"ds_write_b64 v244, v[210:211] offset:24584\n"
"v_mfma_f32_16x16x32_bf16 v[132:135], a[172:175], v[116:119], v[132:135]\n"
"s_sub_i32 s88, s88, 6\n"
"v_mfma_f32_16x16x32_bf16 v[136:139], a[220:223], v[116:119], v[12:15]\n"
"s_max_i32 s88, s88, 0\n"
"v_mfma_f32_16x16x32_bf16 v[136:139], a[216:219], v[112:115], v[136:139]\n"
"s_mov_b64 exec, s[6:7]\n"
"s_cbranch_execz LHT13\n"
"LHR13:\n"
"s_mov_b64 exec, -1\n"
"v_mfma_f32_16x16x32_bf16 v[136:139], a[212:215], v[108:111], v[136:139]\n"
"v_mfma_f32_16x16x32_bf16 v[136:139], a[208:211], v[92:95], v[136:139]\n"
"v_cvt_pk_bf16_f32 v212, v132, v133\n"
"v_mfma_f32_16x16x32_bf16 v[136:139], a[204:207], v[88:91], v[136:139]\n"
"v_mov_b32 v242, 32\n"
"v_mfma_f32_16x16x32_bf16 v[136:139], a[200:203], v[84:87], v[136:139]\n"
"v_cvt_pk_bf16_f32 v213, v134, v135\n"
"v_mfma_f32_16x16x32_bf16 v[136:139], a[196:199], v[68:71], v[136:139]\n"
"s_waitcnt vmcnt(13) lgkmcnt(2)\n"
"ds_write_b32 v240, v242\n"
"v_mfma_f32_16x16x32_bf16 v[136:139], a[192:195], v[64:67], v[136:139]\n"
"v_lshlrev_b32 v12, 16, v198\n"
"v_mfma_f32_16x16x32_bf16 v[136:139], a[188:191], v[60:63], v[136:139]\n"
"v_and_b32 v13, 0xffff0000, v198\n"
"v_mfma_f32_16x16x32_bf16 v[136:139], a[184:187], v[44:47], v[136:139]\n"
"v_lshlrev_b32 v14, 16, v199\n"
"v_mfma_f32_16x16x32_bf16 v[136:139], a[180:183], v[40:43], v[136:139]\n"
"global_store_dwordx4 v253, v[224:227], s[54:55] offset:768 sc1\n"
"ds_write_b64 v245, v[212:213] offset:24576\n"
"v_mfma_f32_16x16x32_bf16 v[136:139], a[176:179], v[36:39], v[136:139]\n"
"v_and_b32 v15, 0xffff0000, v199\n"
"v_mfma_f32_16x16x32_bf16 v[132:135], a[224:227], v[36:39], v[16:19]\n"
"v_mfma_f32_16x16x32_bf16 v[132:135], a[228:231], v[40:43], v[132:135]\n"
"v_mfma_f32_16x16x32_bf16 v[132:135], a[232:235], v[44:47], v[132:135]\n"
"ds_read_b32 v243, v241\n"
"v_mfma_f32_16x16x32_bf16 v[132:135], a[236:239], v[60:63], v[132:135]\n"
"v_cvt_pk_bf16_f32 v214, v136, v137\n"
"v_mfma_f32_16x16x32_bf16 v[132:135], a[0:3], v[64:67], v[132:135]\n"
"v_cvt_pk_bf16_f32 v215, v138, v139\n"
"v_mfma_f32_16x16x32_bf16 v[132:135], a[4:7], v[68:71], v[132:135]\n"
"v_lshlrev_b32 v16, 16, v200\n"
"v_mfma_f32_16x16x32_bf16 v[132:135], a[8:11], v[84:87], v[132:135]\n"
"s_waitcnt lgkmcnt(0)\n"
"v_cmp_gt_u32 s[56:57], 32, v243\n"
"v_mfma_f32_16x16x32_bf16 v[132:135], a[12:15], v[88:91], v[132:135]\n"
"s_sub_u32 s84, s84, s83\n"
"LHT13:\n"
"s_cbranch_execz LHR13\n"
"v_mfma_f32_16x16x32_bf16 v[132:135], a[16:19], v[92:95], v[132:135]\n"
"s_or_b64 s[56:57], s[56:57], vcc\n"
"s_cbranch_scc1 LB32\n"
"LB32R:\n"
"v_mfma_f32_16x16x32_bf16 v[132:135], a[20:23], v[108:111], v[132:135]\n"
"s_cmp_lt_i32 s84, s88\n"
"s_cselect_b64 vcc, -1, 0\n"
"v_mfma_f32_16x16x32_bf16 v[132:135], v[140:143], v[112:115], v[132:135]\n"
"ds_write_b64 v245, v[214:215] offset:24584\n"
"v_mfma_f32_16x16x32_bf16 v[132:135], v[156:159], v[116:119], v[132:135]\n"
"v_and_b32 v17, 0xffff0000, v200\n"
"v_mfma_f32_16x16x32_bf16 v[136:139], a[44:47], v[116:119], v[20:23]\n"
"ds_read_b128 v[116:119], v235 offset:16384\n"
"v_mfma_f32_16x16x32_bf16 v[136:139], a[40:43], v[112:115], v[136:139]\n"
"ds_read_b128 v[112:115], v234 offset:16384\n"
"v_mfma_f32_16x16x32_bf16 v[136:139], a[36:39], v[108:111], v[136:139]\n"
"ds_read_b128 v[108:111], v233 offset:16384\n"
"v_mfma_f32_16x16x32_bf16 v[136:139], a[32:35], v[92:95], v[136:139]\n"
"ds_read_b128 v[92:95], v232 offset:16384\n"
"v_cvt_pk_bf16_f32 v208, v132, v133\n"
"v_mfma_f32_16x16x32_bf16 v[136:139], a[28:31], v[88:91], v[136:139]\n"
"ds_read_b128 v[88:91], v235 offset:8192\n"
"v_cvt_pk_bf16_f32 v209, v134, v135\n"
"v_mfma_f32_16x16x32_bf16 v[136:139], a[24:27], v[84:87], v[136:139]\n"
"ds_read_b128 v[84:87], v234 offset:8192\n"
"v_mfma_f32_16x16x32_bf16 v[136:139], v[204:207], v[68:71], v[136:139]\n"
"ds_read_b128 v[68:71], v233 offset:8192\n"
"v_mfma_f32_16x16x32_bf16 v[136:139], v[188:191], v[64:67], v[136:139]\n"
"ds_read_b128 v[64:67], v232 offset:8192\n"
"v_mfma_f32_16x16x32_bf16 v[136:139], v[184:187], v[60:63], v[136:139]\n"
"ds_read_b128 v[60:63], v235 offset:0\n"
"v_mfma_f32_16x16x32_bf16 v[136:139], v[180:183], v[44:47], v[136:139]\n"
"ds_read_b128 v[44:47], v234 offset:0\n"
"v_mfma_f32_16x16x32_bf16 v[136:139], v[164:167], v[40:43], v[136:139]\n"
"ds_read_b128 v[40:43], v233 offset:0\n"
"v_mfma_f32_16x16x32_bf16 v[136:139], v[160:163], v[36:39], v[136:139]\n"
"ds_read_b128 v[36:39], v232 offset:0\n"
"s_waitcnt lgkmcnt(8)\n"
"v_mfma_f32_16x16x32_bf16 v[132:135], a[76:79], v[116:119], v[0:3]\n"
"ds_write_b64 v244, v[208:209] offset:24704\n"
"v_mfma_f32_16x16x32_bf16 v[132:135], a[72:75], v[112:115], v[132:135]\n"
"v_lshlrev_b32 v18, 16, v201\n"
"v_mfma_f32_16x16x32_bf16 v[132:135], a[68:71], v[108:111], v[132:135]\n"
"v_and_b32 v19, 0xffff0000, v201\n"
"v_mfma_f32_16x16x32_bf16 v[132:135], a[64:67], v[92:95], v[132:135]\n"
"v_lshlrev_b32 v20, 16, v202\n"
"s_waitcnt lgkmcnt(5)\n"
"v_mfma_f32_16x16x32_bf16 v[132:135], a[60:63], v[88:91], v[132:135]\n"
"v_and_b32 v21, 0xffff0000, v202\n"
"v_mfma_f32_16x16x32_bf16 v[132:135], a[56:59], v[84:87], v[132:135]\n"
"v_lshlrev_b32 v22, 16, v203\n"
"v_mfma_f32_16x16x32_bf16 v[132:135], a[52:55], v[68:71], v[132:135]\n"
"v_and_b32 v23, 0xffff0000, v203\n"
"v_mfma_f32_16x16x32_bf16 v[132:135], a[48:51], v[64:67], v[132:135]\n"
"v_cvt_pk_bf16_f32 v210, v136, v137\n"
"s_waitcnt lgkmcnt(1)\n"
"v_mfma_f32_16x16x32_bf16 v[132:135], a[252:255], v[60:63], v[132:135]\n"
"v_cvt_pk_bf16_f32 v211, v138, v139\n"
"v_mfma_f32_16x16x32_bf16 v[132:135], a[248:251], v[44:47], v[132:135]\n"
"s_add_u32 s54, s26, s47\n"
"s_addc_u32 s55, s27, 0\n"
"v_mfma_f32_16x16x32_bf16 v[132:135], a[244:247], v[40:43], v[132:135]\n"
"ds_read_b128 v[216:219], v246 offset:12288\n"
"v_mfma_f32_16x16x32_bf16 v[132:135], a[240:243], v[36:39], v[132:135]\n"
"ds_read_b128 v[220:223], v246 offset:13312\n"
"v_mfma_f32_16x16x32_bf16 v[136:139], a[80:83], v[36:39], v[4:7]\n"
"ds_read_b128 v[224:227], v246 offset:14336\n"
"v_mfma_f32_16x16x32_bf16 v[136:139], a[84:87], v[40:43], v[136:139]\n"
"ds_write_b64 v244, v[210:211] offset:24712\n"
"v_mfma_f32_16x16x32_bf16 v[136:139], a[88:91], v[44:47], v[136:139]\n"
"s_sub_i32 s84, s84, s19\n"
"v_mfma_f32_16x16x32_bf16 v[136:139], a[92:95], v[60:63], v[136:139]\n"
"v_cvt_pk_bf16_f32 v208, v132, v133\n"
"v_mfma_f32_16x16x32_bf16 v[136:139], a[96:99], v[64:67], v[136:139]\n"
"v_cvt_pk_bf16_f32 v209, v134, v135\n"
"v_mfma_f32_16x16x32_bf16 v[136:139], a[100:103], v[68:71], v[136:139]\n"
"s_max_i32 s88, s88, s84\n"
"v_mfma_f32_16x16x32_bf16 v[136:139], a[104:107], v[84:87], v[136:139]\n"
"v_mfma_f32_16x16x32_bf16 v[136:139], a[108:111], v[88:91], v[136:139]\n"
"s_waitcnt lgkmcnt(1)\n"
"v_mfma_f32_16x16x32_bf16 v[136:139], a[112:115], v[92:95], v[136:139]\n"
"v_mfma_f32_16x16x32_bf16 v[136:139], a[116:119], v[108:111], v[136:139]\n"
"global_store_dwordx4 v247, v[216:219], s[54:55] offset:0 sc1\n"
"ds_write_b64 v244, v[208:209] offset:0\n"
"v_mfma_f32_16x16x32_bf16 v[136:139], a[120:123], v[112:115], v[136:139]\n"
"v_mfma_f32_16x16x32_bf16 v[136:139], a[124:127], v[116:119], v[136:139]\n"
"v_mfma_f32_16x16x32_bf16 v[132:135], a[172:175], v[116:119], v[8:11]\n"
"v_mfma_f32_16x16x32_bf16 v[132:135], a[168:171], v[112:115], v[132:135]\n"
"v_mfma_f32_16x16x32_bf16 v[132:135], a[164:167], v[108:111], v[132:135]\n"
"v_mfma_f32_16x16x32_bf16 v[132:135], a[160:163], v[92:95], v[132:135]\n"
"v_cvt_pk_bf16_f32 v210, v136, v137\n"
"v_mfma_f32_16x16x32_bf16 v[132:135], a[156:159], v[88:91], v[132:135]\n"
"v_cvt_pk_bf16_f32 v211, v138, v139\n"
"v_mfma_f32_16x16x32_bf16 v[132:135], a[152:155], v[84:87], v[132:135]\n"
"v_mfma_f32_16x16x32_bf16 v[132:135], a[148:151], v[68:71], v[132:135]\n"
"v_mfma_f32_16x16x32_bf16 v[132:135], a[144:147], v[64:67], v[132:135]\n"
"v_mfma_f32_16x16x32_bf16 v[132:135], a[140:143], v[60:63], v[132:135]\n"
"v_mfma_f32_16x16x32_bf16 v[132:135], a[136:139], v[44:47], v[132:135]\n"
"global_store_dwordx4 v252, v[220:223], s[54:55] offset:0 sc1\n"
"ds_write_b64 v244, v[210:211] offset:8\n"
"v_mfma_f32_16x16x32_bf16 v[132:135], a[132:135], v[40:43], v[132:135]\n"
"v_mfma_f32_16x16x32_bf16 v[132:135], a[128:131], v[36:39], v[132:135]\n"
"v_mfma_f32_16x16x32_bf16 v[136:139], a[176:179], v[36:39], v[12:15]\n"
"v_mfma_f32_16x16x32_bf16 v[136:139], a[180:183], v[40:43], v[136:139]\n"
"v_mfma_f32_16x16x32_bf16 v[136:139], a[184:187], v[44:47], v[136:139]\n"
"v_mfma_f32_16x16x32_bf16 v[136:139], a[188:191], v[60:63], v[136:139]\n"
"v_cvt_pk_bf16_f32 v212, v132, v133\n"
"v_mfma_f32_16x16x32_bf16 v[136:139], a[192:195], v[64:67], v[136:139]\n"
"v_mov_b32 v242, 33\n"
"v_mfma_f32_16x16x32_bf16 v[136:139], a[196:199], v[68:71], v[136:139]\n"
"v_cvt_pk_bf16_f32 v213, v134, v135\n"
"v_mfma_f32_16x16x32_bf16 v[136:139], a[200:203], v[84:87], v[136:139]\n"
"s_waitcnt lgkmcnt(2)\n"
"ds_write_b32 v240, v242\n"
"v_mfma_f32_16x16x32_bf16 v[136:139], a[204:207], v[88:91], v[136:139]\n"
"v_mfma_f32_16x16x32_bf16 v[136:139], a[208:211], v[92:95], v[136:139]\n"
"v_mfma_f32_16x16x32_bf16 v[136:139], a[212:215], v[108:111], v[136:139]\n"
"v_mfma_f32_16x16x32_bf16 v[136:139], a[216:219], v[112:115], v[136:139]\n"
"global_store_dwordx4 v253, v[224:227], s[54:55] offset:0 sc1\n"
"ds_write_b64 v245, v[212:213] offset:0\n"
"v_mfma_f32_16x16x32_bf16 v[136:139], a[220:223], v[116:119], v[136:139]\n"
"v_mfma_f32_16x16x32_bf16 v[132:135], v[156:159], v[116:119], v[16:19]\n"
"v_mfma_f32_16x16x32_bf16 v[132:135], v[140:143], v[112:115], v[132:135]\n"
"v_mfma_f32_16x16x32_bf16 v[132:135], a[20:23], v[108:111], v[132:135]\n"
"ds_read_b32 v243, v241\n"
"v_mfma_f32_16x16x32_bf16 v[132:135], a[16:19], v[92:95], v[132:135]\n"
"v_cvt_pk_bf16_f32 v214, v136, v137\n"
"v_mfma_f32_16x16x32_bf16 v[132:135], a[12:15], v[88:91], v[132:135]\n"
"v_cvt_pk_bf16_f32 v215, v138, v139\n"
"v_mfma_f32_16x16x32_bf16 v[132:135], a[8:11], v[84:87], v[132:135]\n"
"v_mfma_f32_16x16x32_bf16 v[132:135], a[4:7], v[68:71], v[132:135]\n"
"s_waitcnt lgkmcnt(0)\n"
"v_cmp_gt_u32 s[56:57], 33, v243\n"
"v_mfma_f32_16x16x32_bf16 v[132:135], a[0:3], v[64:67], v[132:135]\n"
"v_mfma_f32_16x16x32_bf16 v[132:135], a[236:239], v[60:63], v[132:135]\n"
"s_or_b64 s[56:57], s[56:57], vcc\n"
"s_cbranch_scc1 LB33\n"
"LB33R:\n"
"v_mfma_f32_16x16x32_bf16 v[132:135], a[232:235], v[44:47], v[132:135]\n"
"ds_write_b64 v245, v[214:215] offset:8\n"
"v_mfma_f32_16x16x32_bf16 v[132:135], a[228:231], v[40:43], v[132:135]\n"
"v_mfma_f32_16x16x32_bf16 v[132:135], a[224:227], v[36:39], v[132:135]\n"
"v_mfma_f32_16x16x32_bf16 v[136:139], v[160:163], v[36:39], v[20:23]\n"
"ds_read_b128 v[36:39], v232 offset:256\n"
"v_mfma_f32_16x16x32_bf16 v[136:139], v[164:167], v[40:43], v[136:139]\n"
"ds_read_b128 v[40:43], v233 offset:256\n"
"v_mfma_f32_16x16x32_bf16 v[136:139], v[180:183], v[44:47], v[136:139]\n"
"ds_read_b128 v[44:47], v234 offset:256\n"
"v_mfma_f32_16x16x32_bf16 v[136:139], v[184:187], v[60:63], v[136:139]\n"
"ds_read_b128 v[60:63], v235 offset:256\n"
"v_cvt_pk_bf16_f32 v208, v132, v133\n"
"v_mfma_f32_16x16x32_bf16 v[136:139], v[188:191], v[64:67], v[136:139]\n"
"ds_read_b128 v[64:67], v232 offset:8448\n"
"v_cvt_pk_bf16_f32 v209, v134, v135\n"
"v_mfma_f32_16x16x32_bf16 v[136:139], v[204:207], v[68:71], v[136:139]\n"
"ds_read_b128 v[68:71], v233 offset:8448\n"
"v_mfma_f32_16x16x32_bf16 v[136:139], a[24:27], v[84:87], v[136:139]\n"
"ds_read_b128 v[84:87], v234 offset:8448\n"
"v_mfma_f32_16x16x32_bf16 v[136:139], a[28:31], v[88:91], v[136:139]\n"
"ds_read_b128 v[88:91], v235 offset:8448\n"
"v_mfma_f32_16x16x32_bf16 v[136:139], a[32:35], v[92:95], v[136:139]\n"
"ds_read_b128 v[92:95], v232 offset:16640\n"
"v_mfma_f32_16x16x32_bf16 v[136:139], a[36:39], v[108:111], v[136:139]\n"
"ds_read_b128 v[108:111], v233 offset:16640\n"
"v_mfma_f32_16x16x32_bf16 v[136:139], a[40:43], v[112:115], v[136:139]\n"
"ds_read_b128 v[112:115], v234 offset:16640\n"
"v_mfma_f32_16x16x32_bf16 v[136:139], a[44:47], v[116:119], v[136:139]\n"
"ds_read_b128 v[116:119], v235 offset:16640\n"
"s_waitcnt lgkmcnt(8)\n"
"v_mfma_f32_16x16x32_bf16 v[132:135], a[240:243], v[36:39], v[0:3]\n"
"ds_write_b64 v244, v[208:209] offset:128\n"
"v_mfma_f32_16x16x32_bf16 v[132:135], a[244:247], v[40:43], v[132:135]\n"
"v_mfma_f32_16x16x32_bf16 v[132:135], a[248:251], v[44:47], v[132:135]\n"
"v_mfma_f32_16x16x32_bf16 v[132:135], a[252:255], v[60:63], v[132:135]\n"
"v_cvt_pk_bf16_f32 v210, v136, v137\n"
"s_waitcnt lgkmcnt(5)\n"
"v_mfma_f32_16x16x32_bf16 v[132:135], a[48:51], v[64:67], v[132:135]\n"
"v_cvt_pk_bf16_f32 v211, v138, v139\n"
"v_mfma_f32_16x16x32_bf16 v[132:135], a[52:55], v[68:71], v[132:135]\n"
"v_mfma_f32_16x16x32_bf16 v[132:135], a[56:59], v[84:87], v[132:135]\n"
"ds_read_b128 v[216:219], v246 offset:24576\n"
"v_mfma_f32_16x16x32_bf16 v[132:135], a[60:63], v[88:91], v[132:135]\n"
"ds_read_b128 v[220:223], v246 offset:25600\n"
"s_waitcnt lgkmcnt(3)\n"
"v_mfma_f32_16x16x32_bf16 v[132:135], a[64:67], v[92:95], v[132:135]\n"
"ds_read_b128 v[224:227], v246 offset:26624\n"
"v_mfma_f32_16x16x32_bf16 v[132:135], a[68:71], v[108:111], v[132:135]\n"
"ds_write_b64 v244, v[210:211] offset:136\n"
"v_mfma_f32_16x16x32_bf16 v[132:135], a[72:75], v[112:115], v[132:135]\n"
"v_mfma_f32_16x16x32_bf16 v[132:135], a[76:79], v[116:119], v[132:135]\n"
"v_mfma_f32_16x16x32_bf16 v[136:139], a[124:127], v[116:119], v[4:7]\n"
"v_mfma_f32_16x16x32_bf16 v[136:139], a[120:123], v[112:115], v[136:139]\n"
"v_mfma_f32_16x16x32_bf16 v[136:139], a[116:119], v[108:111], v[136:139]\n"
"v_mfma_f32_16x16x32_bf16 v[136:139], a[112:115], v[92:95], v[136:139]\n"
"v_cvt_pk_bf16_f32 v208, v132, v133\n"
"v_mfma_f32_16x16x32_bf16 v[136:139], a[108:111], v[88:91], v[136:139]\n"
"v_cvt_pk_bf16_f32 v209, v134, v135\n"
"v_mfma_f32_16x16x32_bf16 v[136:139], a[104:107], v[84:87], v[136:139]\n"
"v_mfma_f32_16x16x32_bf16 v[136:139], a[100:103], v[68:71], v[136:139]\n"
"v_mfma_f32_16x16x32_bf16 v[136:139], a[96:99], v[64:67], v[136:139]\n"
"s_waitcnt lgkmcnt(1)\n"
"v_mfma_f32_16x16x32_bf16 v[136:139], a[92:95], v[60:63], v[136:139]\n"
"v_mfma_f32_16x16x32_bf16 v[136:139], a[88:91], v[44:47], v[136:139]\n"
"global_store_dwordx4 v247, v[216:219], s[54:55] offset:768 sc1\n"
"ds_write_b64 v244, v[208:209] offset:12288\n"
"v_mfma_f32_16x16x32_bf16 v[136:139], a[84:87], v[40:43], v[136:139]\n"
"v_mfma_f32_16x16x32_bf16 v[136:139], a[80:83], v[36:39], v[136:139]\n"
"v_mfma_f32_16x16x32_bf16 v[132:135], a[128:131], v[36:39], v[8:11]\n"
"v_mfma_f32_16x16x32_bf16 v[132:135], a[132:135], v[40:43], v[132:135]\n"
"v_mfma_f32_16x16x32_bf16 v[132:135], a[136:139], v[44:47], v[132:135]\n"
"v_mfma_f32_16x16x32_bf16 v[132:135], a[140:143], v[60:63], v[132:135]\n"
"v_cvt_pk_bf16_f32 v210, v136, v137\n"
"v_mfma_f32_16x16x32_bf16 v[132:135], a[144:147], v[64:67], v[132:135]\n"
"v_cvt_pk_bf16_f32 v211, v138, v139\n"
"v_mfma_f32_16x16x32_bf16 v[132:135], a[148:151], v[68:71], v[132:135]\n"
"v_mfma_f32_16x16x32_bf16 v[132:135], a[152:155], v[84:87], v[132:135]\n"
"v_mfma_f32_16x16x32_bf16 v[132:135], a[156:159], v[88:91], v[132:135]\n"
"v_mfma_f32_16x16x32_bf16 v[132:135], a[160:163], v[92:95], v[132:135]\n"
"v_mfma_f32_16x16x32_bf16 v[132:135], a[164:167], v[108:111], v[132:135]\n"
"global_store_dwordx4 v252, v[220:223], s[54:55] offset:768 sc1\n"
"ds_write_b64 v244, v[210:211] offset:12296\n"
"v_mfma_f32_16x16x32_bf16 v[132:135], a[168:171], v[112:115], v[132:135]\n"
"v_mfma_f32_16x16x32_bf16 v[132:135], a[172:175], v[116:119], v[132:135]\n"
"v_mfma_f32_16x16x32_bf16 v[136:139], a[220:223], v[116:119], v[12:15]\n"
"v_mfma_f32_16x16x32_bf16 v[136:139], a[216:219], v[112:115], v[136:139]\n"
"v_mfma_f32_16x16x32_bf16 v[136:139], a[212:215], v[108:111], v[136:139]\n"
"v_mfma_f32_16x16x32_bf16 v[136:139], a[208:211], v[92:95], v[136:139]\n"
"v_cvt_pk_bf16_f32 v212, v132, v133\n"
"v_mfma_f32_16x16x32_bf16 v[136:139], a[204:207], v[88:91], v[136:139]\n"
"v_mov_b32 v242, 34\n"
"v_mfma_f32_16x16x32_bf16 v[136:139], a[200:203], v[84:87], v[136:139]\n"
"v_cvt_pk_bf16_f32 v213, v134, v135\n"
"v_mfma_f32_16x16x32_bf16 v[136:139], a[196:199], v[68:71], v[136:139]\n"
"s_waitcnt vmcnt(7) lgkmcnt(2)\n"
"ds_write_b32 v240, v242\n"
"v_mfma_f32_16x16x32_bf16 v[136:139], a[192:195], v[64:67], v[136:139]\n"
"v_mfma_f32_16x16x32_bf16 v[136:139], a[188:191], v[60:63], v[136:139]\n"
"v_mfma_f32_16x16x32_bf16 v[136:139], a[184:187], v[44:47], v[136:139]\n"
"v_mfma_f32_16x16x32_bf16 v[136:139], a[180:183], v[40:43], v[136:139]\n"
"global_store_dwordx4 v253, v[224:227], s[54:55] offset:768 sc1\n"
"ds_write_b64 v245, v[212:213] offset:12288\n"
"v_mfma_f32_16x16x32_bf16 v[136:139], a[176:179], v[36:39], v[136:139]\n"
"s_bitcmp1_b32 s92, 8\n"
"s_cselect_b32 s93, 0x6000, 0\n"
"v_mfma_f32_16x16x32_bf16 v[132:135], a[224:227], v[36:39], v[16:19]\n"
"s_sub_u32 s94, s93, 0xc000\n"
"v_mfma_f32_16x16x32_bf16 v[132:135], a[228:231], v[40:43], v[132:135]\n"
"v_add_u32 v231, s93, v247\n"
"v_mfma_f32_16x16x32_bf16 v[132:135], a[232:235], v[44:47], v[132:135]\n"
"ds_read_b32 v243, v241\n"
"v_mfma_f32_16x16x32_bf16 v[132:135], a[236:239], v[60:63], v[132:135]\n"
"v_cvt_pk_bf16_f32 v214, v136, v137\n"
"v_mfma_f32_16x16x32_bf16 v[132:135], a[0:3], v[64:67], v[132:135]\n"
"v_cvt_pk_bf16_f32 v215, v138, v139\n"
"v_mfma_f32_16x16x32_bf16 v[132:135], a[4:7], v[68:71], v[132:135]\n"
"v_add_u32 v247, s94, v247\n"
"v_mfma_f32_16x16x32_bf16 v[132:135], a[8:11], v[84:87], v[132:135]\n"
"s_waitcnt lgkmcnt(0)\n"
"v_cmp_gt_u32 s[56:57], 34, v243\n"
"v_mfma_f32_16x16x32_bf16 v[132:135], a[12:15], v[88:91], v[132:135]\n"
"v_min_u32 v247, v247, v231\n"
"v_mfma_f32_16x16x32_bf16 v[132:135], a[16:19], v[92:95], v[132:135]\n"
"s_or_b64 s[56:57], s[56:57], vcc\n"
"s_cbranch_scc1 LB34\n"
"LB34R:\n"
"v_mfma_f32_16x16x32_bf16 v[132:135], a[20:23], v[108:111], v[132:135]\n"
"ds_write_b64 v245, v[214:215] offset:12296\n"
"v_mfma_f32_16x16x32_bf16 v[132:135], v[140:143], v[112:115], v[132:135]\n"
"v_add_u32 v231, s93, v252\n"
"v_mfma_f32_16x16x32_bf16 v[132:135], v[156:159], v[116:119], v[132:135]\n"
"v_add_u32 v252, s94, v252\n"
"v_mfma_f32_16x16x32_bf16 v[136:139], a[44:47], v[116:119], v[20:23]\n"
"ds_read_b128 v[116:119], v235 offset:40960\n"
"v_mfma_f32_16x16x32_bf16 v[136:139], a[40:43], v[112:115], v[136:139]\n"
"ds_read_b128 v[112:115], v234 offset:40960\n"
"v_mfma_f32_16x16x32_bf16 v[136:139], a[36:39], v[108:111], v[136:139]\n"
"ds_read_b128 v[108:111], v233 offset:40960\n"
"v_mfma_f32_16x16x32_bf16 v[136:139], a[32:35], v[92:95], v[136:139]\n"
"ds_read_b128 v[92:95], v232 offset:40960\n"
"v_cvt_pk_bf16_f32 v208, v132, v133\n"
"v_mfma_f32_16x16x32_bf16 v[136:139], a[28:31], v[88:91], v[136:139]\n"
"ds_read_b128 v[88:91], v235 offset:32768\n"
"v_cvt_pk_bf16_f32 v209, v134, v135\n"
"v_mfma_f32_16x16x32_bf16 v[136:139], a[24:27], v[84:87], v[136:139]\n"
"ds_read_b128 v[84:87], v234 offset:32768\n"
"v_mfma_f32_16x16x32_bf16 v[136:139], v[204:207], v[68:71], v[136:139]\n"
"ds_read_b128 v[68:71], v233 offset:32768\n"
"v_mfma_f32_16x16x32_bf16 v[136:139], v[188:191], v[64:67], v[136:139]\n"
"ds_read_b128 v[64:67], v232 offset:32768\n"
"v_mfma_f32_16x16x32_bf16 v[136:139], v[184:187], v[60:63], v[136:139]\n"
"ds_read_b128 v[60:63], v235 offset:24576\n"
"v_mfma_f32_16x16x32_bf16 v[136:139], v[180:183], v[44:47], v[136:139]\n"
"ds_read_b128 v[44:47], v234 offset:24576\n"
"v_mfma_f32_16x16x32_bf16 v[136:139], v[164:167], v[40:43], v[136:139]\n"
"ds_read_b128 v[40:43], v233 offset:24576\n"
"v_mfma_f32_16x16x32_bf16 v[136:139], v[160:163], v[36:39], v[136:139]\n"
"ds_read_b128 v[36:39], v232 offset:24576\n"
"s_waitcnt lgkmcnt(8)\n"
"v_mfma_f32_16x16x32_bf16 v[132:135], a[76:79], v[116:119], v[0:3]\n"
"ds_write_b64 v244, v[208:209] offset:12416\n"
"v_mfma_f32_16x16x32_bf16 v[132:135], a[72:75], v[112:115], v[132:135]\n"
"s_add_u32 s54, s26, s48\n"
"s_addc_u32 s55, s27, 0\n"
"v_mfma_f32_16x16x32_bf16 v[132:135], a[68:71], v[108:111], v[132:135]\n"
"v_min_u32 v252, v252, v231\n"
"v_mfma_f32_16x16x32_bf16 v[132:135], a[64:67], v[92:95], v[132:135]\n"
"v_cvt_pk_bf16_f32 v210, v136, v137\n"
"s_waitcnt lgkmcnt(5)\n"
"v_mfma_f32_16x16x32_bf16 v[132:135], a[60:63], v[88:91], v[132:135]\n"
"v_cvt_pk_bf16_f32 v211, v138, v139\n"
"v_mfma_f32_16x16x32_bf16 v[132:135], a[56:59], v[84:87], v[132:135]\n"
"v_add_u32 v231, s93, v253\n"
"v_mfma_f32_16x16x32_bf16 v[132:135], a[52:55], v[68:71], v[132:135]\n"
"ds_read_b128 v[216:219], v246 offset:0\n"
"v_mfma_f32_16x16x32_bf16 v[132:135], a[48:51], v[64:67], v[132:135]\n"
"ds_read_b128 v[220:223], v246 offset:1024\n"
"s_waitcnt lgkmcnt(3)\n"
"v_mfma_f32_16x16x32_bf16 v[132:135], a[252:255], v[60:63], v[132:135]\n"
"ds_read_b128 v[224:227], v246 offset:2048\n"
"v_mfma_f32_16x16x32_bf16 v[132:135], a[248:251], v[44:47], v[132:135]\n"
"ds_write_b64 v244, v[210:211] offset:12424\n"
"v_mfma_f32_16x16x32_bf16 v[132:135], a[244:247], v[40:43], v[132:135]\n"
"v_add_u32 v253, s94, v253\n"
"v_mfma_f32_16x16x32_bf16 v[132:135], a[240:243], v[36:39], v[132:135]\n"
"v_min_u32 v253, v253, v231\n"
"v_mfma_f32_16x16x32_bf16 v[136:139], a[80:83], v[36:39], v[4:7]\n"
"v_mfma_f32_16x16x32_bf16 v[136:139], a[84:87], v[40:43], v[136:139]\n"
"v_mfma_f32_16x16x32_bf16 v[136:139], a[88:91], v[44:47], v[136:139]\n"
"v_mfma_f32_16x16x32_bf16 v[136:139], a[92:95], v[60:63], v[136:139]\n"
"v_cvt_pk_bf16_f32 v208, v132, v133\n"
"v_mfma_f32_16x16x32_bf16 v[136:139], a[96:99], v[64:67], v[136:139]\n"
"v_cvt_pk_bf16_f32 v209, v134, v135\n"
"v_mfma_f32_16x16x32_bf16 v[136:139], a[100:103], v[68:71], v[136:139]\n"
"v_mfma_f32_16x16x32_bf16 v[136:139], a[104:107], v[84:87], v[136:139]\n"
"v_mfma_f32_16x16x32_bf16 v[136:139], a[108:111], v[88:91], v[136:139]\n"
"s_waitcnt lgkmcnt(1)\n"
"v_mfma_f32_16x16x32_bf16 v[136:139], a[112:115], v[92:95], v[136:139]\n"
"v_mfma_f32_16x16x32_bf16 v[136:139], a[116:119], v[108:111], v[136:139]\n"
"global_store_dwordx4 v247, v[216:219], s[54:55] offset:0 sc1\n"
"ds_write_b64 v244, v[208:209] offset:24576\n"
"v_mfma_f32_16x16x32_bf16 v[136:139], a[120:123], v[112:115], v[136:139]\n"
"v_mfma_f32_16x16x32_bf16 v[136:139], a[124:127], v[116:119], v[136:139]\n"
"v_mfma_f32_16x16x32_bf16 v[132:135], a[172:175], v[116:119], v[8:11]\n"
"v_mfma_f32_16x16x32_bf16 v[132:135], a[168:171], v[112:115], v[132:135]\n"
"v_mfma_f32_16x16x32_bf16 v[132:135], a[164:167], v[108:111], v[132:135]\n"
"v_mfma_f32_16x16x32_bf16 v[132:135], a[160:163], v[92:95], v[132:135]\n"
"v_cvt_pk_bf16_f32 v210, v136, v137\n"
"v_mfma_f32_16x16x32_bf16 v[132:135], a[156:159], v[88:91], v[132:135]\n"
"v_cvt_pk_bf16_f32 v211, v138, v139\n"
"v_mfma_f32_16x16x32_bf16 v[132:135], a[152:155], v[84:87], v[132:135]\n"
"v_mfma_f32_16x16x32_bf16 v[132:135], a[148:151], v[68:71], v[132:135]\n"
"v_mfma_f32_16x16x32_bf16 v[132:135], a[144:147], v[64:67], v[132:135]\n"
"v_mfma_f32_16x16x32_bf16 v[132:135], a[140:143], v[60:63], v[132:135]\n"
"v_mfma_f32_16x16x32_bf16 v[132:135], a[136:139], v[44:47], v[132:135]\n"
"global_store_dwordx4 v252, v[220:223], s[54:55] offset:0 sc1\n"
"ds_write_b64 v244, v[210:211] offset:24584\n"
"v_mfma_f32_16x16x32_bf16 v[132:135], a[132:135], v[40:43], v[132:135]\n"
"v_mfma_f32_16x16x32_bf16 v[132:135], a[128:131], v[36:39], v[132:135]\n"
"v_mfma_f32_16x16x32_bf16 v[136:139], a[176:179], v[36:39], v[12:15]\n"
"v_mfma_f32_16x16x32_bf16 v[136:139], a[180:183], v[40:43], v[136:139]\n"
"v_mfma_f32_16x16x32_bf16 v[136:139], a[184:187], v[44:47], v[136:139]\n"
"v_mfma_f32_16x16x32_bf16 v[136:139], a[188:191], v[60:63], v[136:139]\n"
"v_cvt_pk_bf16_f32 v212, v132, v133\n"
"v_mfma_f32_16x16x32_bf16 v[136:139], a[192:195], v[64:67], v[136:139]\n"
"v_mov_b32 v242, 35\n"
"v_mfma_f32_16x16x32_bf16 v[136:139], a[196:199], v[68:71], v[136:139]\n"
"v_cvt_pk_bf16_f32 v213, v134, v135\n"
"v_mfma_f32_16x16x32_bf16 v[136:139], a[200:203], v[84:87], v[136:139]\n"
"s_waitcnt lgkmcnt(2)\n"
"ds_write_b32 v240, v242\n"
"v_mfma_f32_16x16x32_bf16 v[136:139], a[204:207], v[88:91], v[136:139]\n"
"v_mfma_f32_16x16x32_bf16 v[136:139], a[208:211], v[92:95], v[136:139]\n"
"v_mfma_f32_16x16x32_bf16 v[136:139], a[212:215], v[108:111], v[136:139]\n"
"v_mfma_f32_16x16x32_bf16 v[136:139], a[216:219], v[112:115], v[136:139]\n"
"global_store_dwordx4 v253, v[224:227], s[54:55] offset:0 sc1\n"
"ds_write_b64 v245, v[212:213] offset:24576\n"
"v_mfma_f32_16x16x32_bf16 v[136:139], a[220:223], v[116:119], v[136:139]\n"
"v_mfma_f32_16x16x32_bf16 v[132:135], v[156:159], v[116:119], v[16:19]\n"
"v_mfma_f32_16x16x32_bf16 v[132:135], v[140:143], v[112:115], v[132:135]\n"
"v_mfma_f32_16x16x32_bf16 v[132:135], a[20:23], v[108:111], v[132:135]\n"
"ds_read_b32 v243, v241\n"
"v_mfma_f32_16x16x32_bf16 v[132:135], a[16:19], v[92:95], v[132:135]\n"
"v_cvt_pk_bf16_f32 v214, v136, v137\n"
"v_mfma_f32_16x16x32_bf16 v[132:135], a[12:15], v[88:91], v[132:135]\n"
"v_cvt_pk_bf16_f32 v215, v138, v139\n"
"v_mfma_f32_16x16x32_bf16 v[132:135], a[8:11], v[84:87], v[132:135]\n"
"v_mfma_f32_16x16x32_bf16 v[132:135], a[4:7], v[68:71], v[132:135]\n"
"s_waitcnt lgkmcnt(0)\n"
"v_cmp_gt_u32 s[56:57], 35, v243\n"
"v_mfma_f32_16x16x32_bf16 v[132:135], a[0:3], v[64:67], v[132:135]\n"
"v_mfma_f32_16x16x32_bf16 v[132:135], a[236:239], v[60:63], v[132:135]\n"
"s_cmp_lg_u64 s[56:57], 0\n"
"s_cbranch_scc1 LB35\n"
"LB35R:\n"
"v_mfma_f32_16x16x32_bf16 v[132:135], a[232:235], v[44:47], v[132:135]\n"
"ds_write_b64 v245, v[214:215] offset:24584\n"
"v_mfma_f32_16x16x32_bf16 v[132:135], a[228:231], v[40:43], v[132:135]\n"
"v_mfma_f32_16x16x32_bf16 v[132:135], a[224:227], v[36:39], v[132:135]\n"
"v_mfma_f32_16x16x32_bf16 v[136:139], v[160:163], v[36:39], v[20:23]\n"
"ds_read_b128 v[36:39], v232 offset:24832\n"
"v_mfma_f32_16x16x32_bf16 v[136:139], v[164:167], v[40:43], v[136:139]\n"
"ds_read_b128 v[40:43], v233 offset:24832\n"
"v_mfma_f32_16x16x32_bf16 v[136:139], v[180:183], v[44:47], v[136:139]\n"
"ds_read_b128 v[44:47], v234 offset:24832\n"
"v_mfma_f32_16x16x32_bf16 v[136:139], v[184:187], v[60:63], v[136:139]\n"
"ds_read_b128 v[60:63], v235 offset:24832\n"
"v_cvt_pk_bf16_f32 v208, v132, v133\n"
"v_mfma_f32_16x16x32_bf16 v[136:139], v[188:191], v[64:67], v[136:139]\n"
"ds_read_b128 v[64:67], v232 offset:33024\n"
"v_cvt_pk_bf16_f32 v209, v134, v135\n"
"v_mfma_f32_16x16x32_bf16 v[136:139], v[204:207], v[68:71], v[136:139]\n"
"ds_read_b128 v[68:71], v233 offset:33024\n"
"v_mfma_f32_16x16x32_bf16 v[136:139], a[24:27], v[84:87], v[136:139]\n"
"ds_read_b128 v[84:87], v234 offset:33024\n"
"v_mfma_f32_16x16x32_bf16 v[136:139], a[28:31], v[88:91], v[136:139]\n"
"ds_read_b128 v[88:91], v235 offset:33024\n"
"v_mfma_f32_16x16x32_bf16 v[136:139], a[32:35], v[92:95], v[136:139]\n"
"ds_read_b128 v[92:95], v232 offset:41216\n"
"v_mfma_f32_16x16x32_bf16 v[136:139], a[36:39], v[108:111], v[136:139]\n"
"ds_read_b128 v[108:111], v233 offset:41216\n"
"v_mfma_f32_16x16x32_bf16 v[136:139], a[40:43], v[112:115], v[136:139]\n"
"ds_read_b128 v[112:115], v234 offset:41216\n"
"v_mfma_f32_16x16x32_bf16 v[136:139], a[44:47], v[116:119], v[136:139]\n"
"ds_read_b128 v[116:119], v235 offset:41216\n"
"s_waitcnt lgkmcnt(8)\n"
"v_mfma_f32_16x16x32_bf16 v[132:135], a[240:243], v[36:39], v[0:3]\n"
"ds_write_b64 v244, v[208:209] offset:24704\n"
"v_mfma_f32_16x16x32_bf16 v[132:135], a[244:247], v[40:43], v[132:135]\n"
"v_mfma_f32_16x16x32_bf16 v[132:135], a[248:251], v[44:47], v[132:135]\n"
"v_mfma_f32_16x16x32_bf16 v[132:135], a[252:255], v[60:63], v[132:135]\n"
"v_cvt_pk_bf16_f32 v210, v136, v137\n"
"s_waitcnt lgkmcnt(5)\n"
"v_mfma_f32_16x16x32_bf16 v[132:135], a[48:51], v[64:67], v[132:135]\n"
"v_cvt_pk_bf16_f32 v211, v138, v139\n"
"v_mfma_f32_16x16x32_bf16 v[132:135], a[52:55], v[68:71], v[132:135]\n"
"v_mfma_f32_16x16x32_bf16 v[132:135], a[56:59], v[84:87], v[132:135]\n"
"ds_read_b128 v[216:219], v246 offset:12288\n"
"v_mfma_f32_16x16x32_bf16 v[132:135], a[60:63], v[88:91], v[132:135]\n"
"ds_read_b128 v[220:223], v246 offset:13312\n"
"s_waitcnt lgkmcnt(3)\n"
"v_mfma_f32_16x16x32_bf16 v[132:135], a[64:67], v[92:95], v[132:135]\n"
"ds_read_b128 v[224:227], v246 offset:14336\n"
"v_mfma_f32_16x16x32_bf16 v[132:135], a[68:71], v[108:111], v[132:135]\n"
"ds_write_b64 v244, v[210:211] offset:24712\n"
"v_mfma_f32_16x16x32_bf16 v[132:135], a[72:75], v[112:115], v[132:135]\n"
"v_mfma_f32_16x16x32_bf16 v[132:135], a[76:79], v[116:119], v[132:135]\n"
"v_mfma_f32_16x16x32_bf16 v[136:139], a[124:127], v[116:119], v[4:7]\n"
"v_mfma_f32_16x16x32_bf16 v[136:139], a[120:123], v[112:115], v[136:139]\n"
"v_mfma_f32_16x16x32_bf16 v[136:139], a[116:119], v[108:111], v[136:139]\n"
"v_mfma_f32_16x16x32_bf16 v[136:139], a[112:115], v[92:95], v[136:139]\n"
"v_cvt_pk_bf16_f32 v208, v132, v133\n"
"v_mfma_f32_16x16x32_bf16 v[136:139], a[108:111], v[88:91], v[136:139]\n"
"v_cvt_pk_bf16_f32 v209, v134, v135\n"
"v_mfma_f32_16x16x32_bf16 v[136:139], a[104:107], v[84:87], v[136:139]\n"
"v_mfma_f32_16x16x32_bf16 v[136:139], a[100:103], v[68:71], v[136:139]\n"
"v_mfma_f32_16x16x32_bf16 v[136:139], a[96:99], v[64:67], v[136:139]\n"
"s_waitcnt lgkmcnt(1)\n"
"v_mfma_f32_16x16x32_bf16 v[136:139], a[92:95], v[60:63], v[136:139]\n"
"v_mfma_f32_16x16x32_bf16 v[136:139], a[88:91], v[44:47], v[136:139]\n"
"global_store_dwordx4 v247, v[216:219], s[54:55] offset:768 sc1\n"
"ds_write_b64 v244, v[208:209] offset:0\n"
"v_mfma_f32_16x16x32_bf16 v[136:139], a[84:87], v[40:43], v[136:139]\n"
"v_mfma_f32_16x16x32_bf16 v[136:139], a[80:83], v[36:39], v[136:139]\n"
"v_mfma_f32_16x16x32_bf16 v[132:135], a[128:131], v[36:39], v[8:11]\n"
"v_mfma_f32_16x16x32_bf16 v[132:135], a[132:135], v[40:43], v[132:135]\n"
"v_mfma_f32_16x16x32_bf16 v[132:135], a[136:139], v[44:47], v[132:135]\n"
"v_mfma_f32_16x16x32_bf16 v[132:135], a[140:143], v[60:63], v[132:135]\n"
"v_cvt_pk_bf16_f32 v210, v136, v137\n"
"v_mfma_f32_16x16x32_bf16 v[132:135], a[144:147], v[64:67], v[132:135]\n"
"v_cvt_pk_bf16_f32 v211, v138, v139\n"
"v_mfma_f32_16x16x32_bf16 v[132:135], a[148:151], v[68:71], v[132:135]\n"
"v_mfma_f32_16x16x32_bf16 v[132:135], a[152:155], v[84:87], v[132:135]\n"
"v_mfma_f32_16x16x32_bf16 v[132:135], a[156:159], v[88:91], v[132:135]\n"
"v_mfma_f32_16x16x32_bf16 v[132:135], a[160:163], v[92:95], v[132:135]\n"
"v_mfma_f32_16x16x32_bf16 v[132:135], a[164:167], v[108:111], v[132:135]\n"
"global_store_dwordx4 v252, v[220:223], s[54:55] offset:768 sc1\n"
"ds_write_b64 v244, v[210:211] offset:8\n"
"v_mfma_f32_16x16x32_bf16 v[132:135], a[168:171], v[112:115], v[132:135]\n"
"v_mfma_f32_16x16x32_bf16 v[132:135], a[172:175], v[116:119], v[132:135]\n"
"v_mfma_f32_16x16x32_bf16 v[136:139], a[220:223], v[116:119], v[12:15]\n"
"v_mfma_f32_16x16x32_bf16 v[136:139], a[216:219], v[112:115], v[136:139]\n"
"v_mfma_f32_16x16x32_bf16 v[136:139], a[212:215], v[108:111], v[136:139]\n"
"v_mfma_f32_16x16x32_bf16 v[136:139], a[208:211], v[92:95], v[136:139]\n"
"v_cvt_pk_bf16_f32 v212, v132, v133\n"
"v_mfma_f32_16x16x32_bf16 v[136:139], a[204:207], v[88:91], v[136:139]\n"
"v_mov_b32 v242, 36\n"
"v_mfma_f32_16x16x32_bf16 v[136:139], a[200:203], v[84:87], v[136:139]\n"
"v_cvt_pk_bf16_f32 v213, v134, v135\n"
"v_mfma_f32_16x16x32_bf16 v[136:139], a[196:199], v[68:71], v[136:139]\n"
"s_waitcnt lgkmcnt(2)\n"
"ds_write_b32 v240, v242\n"
"v_mfma_f32_16x16x32_bf16 v[136:139], a[192:195], v[64:67], v[136:139]\n"
"v_mfma_f32_16x16x32_bf16 v[136:139], a[188:191], v[60:63], v[136:139]\n"
"v_mfma_f32_16x16x32_bf16 v[136:139], a[184:187], v[44:47], v[136:139]\n"
"v_mfma_f32_16x16x32_bf16 v[136:139], a[180:183], v[40:43], v[136:139]\n"
"global_store_dwordx4 v253, v[224:227], s[54:55] offset:768 sc1\n"
"ds_write_b64 v245, v[212:213] offset:0\n"
"v_mfma_f32_16x16x32_bf16 v[136:139], a[176:179], v[36:39], v[136:139]\n"
"v_mfma_f32_16x16x32_bf16 v[132:135], a[224:227], v[36:39], v[16:19]\n"
"v_mfma_f32_16x16x32_bf16 v[132:135], a[228:231], v[40:43], v[132:135]\n"
"v_mfma_f32_16x16x32_bf16 v[132:135], a[232:235], v[44:47], v[132:135]\n"
"ds_read_b32 v243, v241\n"
"v_mfma_f32_16x16x32_bf16 v[132:135], a[236:239], v[60:63], v[132:135]\n"
"v_cvt_pk_bf16_f32 v214, v136, v137\n"
"v_mfma_f32_16x16x32_bf16 v[132:135], a[0:3], v[64:67], v[132:135]\n"
"v_cvt_pk_bf16_f32 v215, v138, v139\n"
"v_mfma_f32_16x16x32_bf16 v[132:135], a[4:7], v[68:71], v[132:135]\n"
"v_mfma_f32_16x16x32_bf16 v[132:135], a[8:11], v[84:87], v[132:135]\n"
"s_waitcnt lgkmcnt(0)\n"
"v_cmp_gt_u32 s[56:57], 36, v243\n"
"v_mfma_f32_16x16x32_bf16 v[132:135], a[12:15], v[88:91], v[132:135]\n"
"v_mfma_f32_16x16x32_bf16 v[132:135], a[16:19], v[92:95], v[132:135]\n"
"s_cmp_lg_u64 s[56:57], 0\n"
"s_cbranch_scc1 LB36\n"
"LB36R:\n"
"v_mfma_f32_16x16x32_bf16 v[132:135], a[20:23], v[108:111], v[132:135]\n"
"ds_write_b64 v245, v[214:215] offset:8\n"
"v_mfma_f32_16x16x32_bf16 v[132:135], v[140:143], v[112:115], v[132:135]\n"
"v_mfma_f32_16x16x32_bf16 v[132:135], v[156:159], v[116:119], v[132:135]\n"
"v_mfma_f32_16x16x32_bf16 v[136:139], a[44:47], v[116:119], v[20:23]\n"
"v_mfma_f32_16x16x32_bf16 v[136:139], a[40:43], v[112:115], v[136:139]\n"
"v_mfma_f32_16x16x32_bf16 v[136:139], a[36:39], v[108:111], v[136:139]\n"
"v_mfma_f32_16x16x32_bf16 v[136:139], a[32:35], v[92:95], v[136:139]\n"
"v_cvt_pk_bf16_f32 v208, v132, v133\n"
"v_mfma_f32_16x16x32_bf16 v[136:139], a[28:31], v[88:91], v[136:139]\n"
"v_cvt_pk_bf16_f32 v209, v134, v135\n"
"v_mfma_f32_16x16x32_bf16 v[136:139], a[24:27], v[84:87], v[136:139]\n"
"v_mfma_f32_16x16x32_bf16 v[136:139], v[204:207], v[68:71], v[136:139]\n"
"v_mfma_f32_16x16x32_bf16 v[136:139], v[188:191], v[64:67], v[136:139]\n"
"v_mfma_f32_16x16x32_bf16 v[136:139], v[184:187], v[60:63], v[136:139]\n"
"v_mfma_f32_16x16x32_bf16 v[136:139], v[180:183], v[44:47], v[136:139]\n"
"ds_write_b64 v244, v[208:209] offset:128\n"
"v_mfma_f32_16x16x32_bf16 v[136:139], v[164:167], v[40:43], v[136:139]\n"
"v_mfma_f32_16x16x32_bf16 v[136:139], v[160:163], v[36:39], v[136:139]\n"
"s_add_u32 s54, s26, s49\n"
"s_addc_u32 s55, s27, 0\n"
"ds_read_b128 v[216:219], v246 offset:24576\n"
"ds_read_b128 v[220:223], v246 offset:25600\n"
"ds_read_b128 v[224:227], v246 offset:26624\n"
"s_nop 3\n"
"v_cvt_pk_bf16_f32 v210, v136, v137\n"
"v_cvt_pk_bf16_f32 v211, v138, v139\n"
"ds_write_b128 v244, v[208:211] offset:128\n"
"s_waitcnt lgkmcnt(3)\n"
"global_store_dwordx4 v247, v[216:219], s[54:55] offset:0 sc1\n"
"s_waitcnt lgkmcnt(2)\n"
"global_store_dwordx4 v252, v[220:223], s[54:55] offset:0 sc1\n"
"s_waitcnt lgkmcnt(1)\n"
"global_store_dwordx4 v253, v[224:227], s[54:55] offset:0 sc1\n"
"s_waitcnt lgkmcnt(0)\n"
"s_barrier\n"
"ds_read_b128 v[216:219], v246 offset:0\n"
"ds_read_b128 v[220:223], v246 offset:1024\n"
"ds_read_b128 v[224:227], v246 offset:2048\n"
"s_waitcnt lgkmcnt(2)\n"
"global_store_dwordx4 v247, v[216:219], s[54:55] offset:768 sc1\n"
"s_waitcnt lgkmcnt(1)\n"
"global_store_dwordx4 v252, v[220:223], s[54:55] offset:768 sc1\n"
"s_waitcnt lgkmcnt(0)\n"
"global_store_dwordx4 v253, v[224:227], s[54:55] offset:768 sc1\n"
"LFIN:\n"
"s_cmp_lg_u32 s87, 0\n"
"s_cbranch_scc1 LE1\n"
"v_min_u32 v0, 0x370, v239\n"
"v_add_u32 v0, 0x2480, v0\n"
"s_mov_b32 s82, 0x40000\n"
"LE0:\n"
"global_load_dwordx4 v[4:7], v0, s[28:29] sc1\n"
"s_sub_u32 s82, s82, 1\n"
"s_cbranch_scc1 LE1\n"
"s_waitcnt vmcnt(0)\n"
"v_cmp_ne_u64 vcc, s[80:81], v[4:5]\n"
"v_cmp_ne_u64 s[56:57], s[80:81], v[6:7]\n"
"s_nop 3\n"
"s_or_b64 vcc, vcc, s[56:57]\n"
"s_cbranch_scc1 LE0\n"
"v_mov_b32 v0, 0\n"
"v_mov_b32 v2, s80\n"
"v_mov_b32 v3, s81\n"
"s_mov_b64 exec, 1\n"
"global_store_dwordx2 v0, v[2:3], s[28:29] sc1\n"
"s_mov_b64 exec, -1\n"
"LE1:\n"
"s_waitcnt vmcnt(0)\n"
"s_branch LCEND\n"
"LEARLY:\n"
"v_mfma_f32_16x16x32_bf16 v[136:139], a[28:31], v[88:91], v[136:139]\n"
"ds_read_b128 v[88:91], v235 offset:8192\n"
"v_cvt_pk_bf16_f32 v208, v132, v133\n"
"v_mfma_f32_16x16x32_bf16 v[136:139], a[24:27], v[84:87], v[136:139]\n"
"ds_read_b128 v[84:87], v234 offset:8192\n"
"v_cvt_pk_bf16_f32 v209, v134, v135\n"
"v_mfma_f32_16x16x32_bf16 v[136:139], v[204:207], v[68:71], v[136:139]\n"
"ds_read_b128 v[68:71], v233 offset:8192\n"
"v_mfma_f32_16x16x32_bf16 v[136:139], v[188:191], v[64:67], v[136:139]\n"
"ds_read_b128 v[64:67], v232 offset:8192\n"
"v_mfma_f32_16x16x32_bf16 v[136:139], v[184:187], v[60:63], v[136:139]\n"
"ds_read_b128 v[60:63], v235 offset:0\n"
"v_mfma_f32_16x16x32_bf16 v[136:139], v[180:183], v[44:47], v[136:139]\n"
"ds_read_b128 v[44:47], v234 offset:0\n"
"v_mfma_f32_16x16x32_bf16 v[136:139], v[164:167], v[40:43], v[136:139]\n"
"ds_read_b128 v[40:43], v233 offset:0\n"
"v_mfma_f32_16x16x32_bf16 v[136:139], v[160:163], v[36:39], v[136:139]\n"
"ds_read_b128 v[36:39], v232 offset:0\n"
"s_waitcnt lgkmcnt(8)\n"
"v_mfma_f32_16x16x32_bf16 v[132:135], a[76:79], v[116:119], v[0:3]\n"
"ds_write_b64 v244, v[208:209] offset:12416\n"
"v_mfma_f32_16x16x32_bf16 v[132:135], a[72:75], v[112:115], v[132:135]\n"
"s_add_u32 m0, s31, 0x1f00\n"
"v_mfma_f32_16x16x32_bf16 v[132:135], a[68:71], v[108:111], v[132:135]\n"
"global_load_lds_dwordx4 v237, s[52:53] offset:256 nt\n"
"v_mfma_f32_16x16x32_bf16 v[132:135], a[64:67], v[92:95], v[132:135]\n"
"v_lshlrev_b32 v18, 16, v177\n"
"v_cvt_pk_bf16_f32 v211, v138, v139\n"
"s_waitcnt lgkmcnt(5)\n"
"v_mfma_f32_16x16x32_bf16 v[132:135], a[60:63], v[88:91], v[132:135]\n"
"v_and_b32 v19, 0xffff0000, v177\n"
"v_mfma_f32_16x16x32_bf16 v[132:135], a[56:59], v[84:87], v[132:135]\n"
"v_lshlrev_b32 v20, 16, v178\n"
"v_mfma_f32_16x16x32_bf16 v[132:135], a[52:55], v[68:71], v[132:135]\n"
"v_and_b32 v21, 0xffff0000, v178\n"
"v_mfma_f32_16x16x32_bf16 v[132:135], a[48:51], v[64:67], v[132:135]\n"
"v_lshlrev_b32 v22, 16, v179\n"
"s_waitcnt lgkmcnt(1)\n"
"v_mfma_f32_16x16x32_bf16 v[132:135], a[252:255], v[60:63], v[132:135]\n"
"v_and_b32 v23, 0xffff0000, v179\n"
"v_mfma_f32_16x16x32_bf16 v[132:135], a[248:251], v[44:47], v[132:135]\n"
"v_cvt_pk_bf16_f32 v210, v136, v137\n"
"v_mfma_f32_16x16x32_bf16 v[132:135], a[244:247], v[40:43], v[132:135]\n"
"s_add_u32 s54, s26, s45\n"
"s_addc_u32 s55, s27, 0\n"
"v_mfma_f32_16x16x32_bf16 v[132:135], a[240:243], v[36:39], v[132:135]\n"
"ds_read_b128 v[216:219], v246 offset:0\n"
"v_mfma_f32_16x16x32_bf16 v[136:139], a[80:83], v[36:39], v[4:7]\n"
"ds_read_b128 v[220:223], v246 offset:1024\n"
"v_mfma_f32_16x16x32_bf16 v[136:139], a[84:87], v[40:43], v[136:139]\n"
"ds_read_b128 v[224:227], v246 offset:2048\n"
"v_mfma_f32_16x16x32_bf16 v[136:139], a[88:91], v[44:47], v[136:139]\n"
"ds_write_b64 v244, v[210:211] offset:12424\n"
"v_mfma_f32_16x16x32_bf16 v[136:139], a[92:95], v[60:63], v[136:139]\n"
"v_cvt_pk_bf16_f32 v208, v132, v133\n"
"v_mfma_f32_16x16x32_bf16 v[136:139], a[96:99], v[64:67], v[136:139]\n"
"v_cvt_pk_bf16_f32 v209, v134, v135\n"
"v_mfma_f32_16x16x32_bf16 v[136:139], a[100:103], v[68:71], v[136:139]\n"
"s_add_u32 m0, s31, 0x3e00\n"
"v_mfma_f32_16x16x32_bf16 v[136:139], a[104:107], v[84:87], v[136:139]\n"
"global_load_lds_dwordx4 v237, s[52:53] offset:512 nt\n"
"v_mfma_f32_16x16x32_bf16 v[136:139], a[108:111], v[88:91], v[136:139]\n"
"s_waitcnt lgkmcnt(1)\n"
"v_mfma_f32_16x16x32_bf16 v[136:139], a[112:115], v[92:95], v[136:139]\n"
"s_sub_i32 s84, s84, s19\n"
"v_mfma_f32_16x16x32_bf16 v[136:139], a[116:119], v[108:111], v[136:139]\n"
"global_store_dwordx4 v247, v[216:219], s[54:55] offset:0 sc1\n"
"ds_write_b64 v244, v[208:209] offset:24576\n"
"v_mfma_f32_16x16x32_bf16 v[136:139], a[120:123], v[112:115], v[136:139]\n"
"s_add_u32 m0, s31, 0x1000\n"
"v_mfma_f32_16x16x32_bf16 v[136:139], a[124:127], v[116:119], v[136:139]\n"
"global_load_lds_dwordx4 v238, s[52:53] offset:0 nt\n"
"v_mfma_f32_16x16x32_bf16 v[132:135], a[172:175], v[116:119], v[8:11]\n"
"s_max_i32 s88, s88, s84\n"
"v_mfma_f32_16x16x32_bf16 v[132:135], a[168:171], v[112:115], v[132:135]\n"
"v_mfma_f32_16x16x32_bf16 v[132:135], a[164:167], v[108:111], v[132:135]\n"
"v_mfma_f32_16x16x32_bf16 v[132:135], a[160:163], v[92:95], v[132:135]\n"
"v_cvt_pk_bf16_f32 v210, v136, v137\n"
"v_mfma_f32_16x16x32_bf16 v[132:135], a[156:159], v[88:91], v[132:135]\n"
"v_cvt_pk_bf16_f32 v211, v138, v139\n"
"v_mfma_f32_16x16x32_bf16 v[132:135], a[152:155], v[84:87], v[132:135]\n"
"v_mfma_f32_16x16x32_bf16 v[132:135], a[148:151], v[68:71], v[132:135]\n"
"v_mfma_f32_16x16x32_bf16 v[132:135], a[144:147], v[64:67], v[132:135]\n"
"v_mfma_f32_16x16x32_bf16 v[132:135], a[140:143], v[60:63], v[132:135]\n"
"v_mfma_f32_16x16x32_bf16 v[132:135], a[136:139], v[44:47], v[132:135]\n"
"global_store_dwordx4 v252, v[220:223], s[54:55] offset:0 sc1\n"
"ds_write_b64 v244, v[210:211] offset:24584\n"
"v_mfma_f32_16x16x32_bf16 v[132:135], a[132:135], v[40:43], v[132:135]\n"
"s_add_u32 m0, s31, 0x2f00\n"
"v_mfma_f32_16x16x32_bf16 v[132:135], a[128:131], v[36:39], v[132:135]\n"
"global_load_lds_dwordx4 v238, s[52:53] offset:256 nt\n"
"v_mfma_f32_16x16x32_bf16 v[136:139], a[176:179], v[36:39], v[12:15]\n"
"v_mfma_f32_16x16x32_bf16 v[136:139], a[180:183], v[40:43], v[136:139]\n"
"v_mfma_f32_16x16x32_bf16 v[136:139], a[184:187], v[44:47], v[136:139]\n"
"v_mfma_f32_16x16x32_bf16 v[136:139], a[188:191], v[60:63], v[136:139]\n"
"v_cvt_pk_bf16_f32 v212, v132, v133\n"
"v_mfma_f32_16x16x32_bf16 v[136:139], a[192:195], v[64:67], v[136:139]\n"
"v_mov_b32 v242, 29\n"
"v_mfma_f32_16x16x32_bf16 v[136:139], a[196:199], v[68:71], v[136:139]\n"
"v_cvt_pk_bf16_f32 v213, v134, v135\n"
"v_mfma_f32_16x16x32_bf16 v[136:139], a[200:203], v[84:87], v[136:139]\n"
"s_waitcnt lgkmcnt(2)\n"
"ds_write_b32 v240, v242\n"
"v_mfma_f32_16x16x32_bf16 v[136:139], a[204:207], v[88:91], v[136:139]\n"
"v_mfma_f32_16x16x32_bf16 v[136:139], a[208:211], v[92:95], v[136:139]\n"
"v_mfma_f32_16x16x32_bf16 v[136:139], a[212:215], v[108:111], v[136:139]\n"
"v_mfma_f32_16x16x32_bf16 v[136:139], a[216:219], v[112:115], v[136:139]\n"
"global_store_dwordx4 v253, v[224:227], s[54:55] offset:0 sc1\n"
"ds_write_b64 v245, v[212:213] offset:24576\n"
"v_mfma_f32_16x16x32_bf16 v[136:139], a[220:223], v[116:119], v[136:139]\n"
"s_add_u32 m0, s31, 0x4e00\n"
"v_mfma_f32_16x16x32_bf16 v[132:135], v[156:159], v[116:119], v[16:19]\n"
"global_load_lds_dwordx4 v238, s[52:53] offset:512 nt\n"
"v_mfma_f32_16x16x32_bf16 v[132:135], v[140:143], v[112:115], v[132:135]\n"
"v_mfma_f32_16x16x32_bf16 v[132:135], a[20:23], v[108:111], v[132:135]\n"
"ds_read_b32 v243, v241\n"
"v_mfma_f32_16x16x32_bf16 v[132:135], a[16:19], v[92:95], v[132:135]\n"
"v_cvt_pk_bf16_f32 v214, v136, v137\n"
"v_mfma_f32_16x16x32_bf16 v[132:135], a[12:15], v[88:91], v[132:135]\n"
"v_cvt_pk_bf16_f32 v215, v138, v139\n"
"v_mfma_f32_16x16x32_bf16 v[132:135], a[8:11], v[84:87], v[132:135]\n"
"v_mfma_f32_16x16x32_bf16 v[132:135], a[4:7], v[68:71], v[132:135]\n"
"s_waitcnt lgkmcnt(0)\n"
"v_cmp_gt_u32 s[56:57], 29, v243\n"
"v_mfma_f32_16x16x32_bf16 v[132:135], a[0:3], v[64:67], v[132:135]\n"
"v_mfma_f32_16x16x32_bf16 v[132:135], a[236:239], v[60:63], v[132:135]\n"
"s_or_b64 s[56:57], s[56:57], vcc\n"
"s_cbranch_scc1 LB29E\n"
"LB29ER:\n"
"v_mfma_f32_16x16x32_bf16 v[132:135], a[232:235], v[44:47], v[132:135]\n"
"ds_write_b64 v245, v[214:215] offset:24584\n"
"v_mfma_f32_16x16x32_bf16 v[132:135], a[228:231], v[40:43], v[132:135]\n"
"v_mfma_f32_16x16x32_bf16 v[132:135], a[224:227], v[36:39], v[132:135]\n"
"v_mfma_f32_16x16x32_bf16 v[136:139], v[160:163], v[36:39], v[20:23]\n"
"ds_read_b128 v[36:39], v232 offset:256\n"
"v_mfma_f32_16x16x32_bf16 v[136:139], v[164:167], v[40:43], v[136:139]\n"
"ds_read_b128 v[40:43], v233 offset:256\n"
"v_mfma_f32_16x16x32_bf16 v[136:139], v[180:183], v[44:47], v[136:139]\n"
"ds_read_b128 v[44:47], v234 offset:256\n"
"v_mfma_f32_16x16x32_bf16 v[136:139], v[184:187], v[60:63], v[136:139]\n"
"ds_read_b128 v[60:63], v235 offset:256\n"
"v_cvt_pk_bf16_f32 v208, v132, v133\n"
"v_mfma_f32_16x16x32_bf16 v[136:139], v[188:191], v[64:67], v[136:139]\n"
"ds_read_b128 v[64:67], v232 offset:8448\n"
"v_cvt_pk_bf16_f32 v209, v134, v135\n"
"v_mfma_f32_16x16x32_bf16 v[136:139], v[204:207], v[68:71], v[136:139]\n"
"ds_read_b128 v[68:71], v233 offset:8448\n"
"v_mfma_f32_16x16x32_bf16 v[136:139], a[24:27], v[84:87], v[136:139]\n"
"ds_read_b128 v[84:87], v234 offset:8448\n"
"v_mfma_f32_16x16x32_bf16 v[136:139], a[28:31], v[88:91], v[136:139]\n"
"ds_read_b128 v[88:91], v235 offset:8448\n"
"v_mfma_f32_16x16x32_bf16 v[136:139], a[32:35], v[92:95], v[136:139]\n"
"ds_read_b128 v[92:95], v232 offset:16640\n"
"v_mfma_f32_16x16x32_bf16 v[136:139], a[36:39], v[108:111], v[136:139]\n"
"ds_read_b128 v[108:111], v233 offset:16640\n"
"v_mfma_f32_16x16x32_bf16 v[136:139], a[40:43], v[112:115], v[136:139]\n"
"ds_read_b128 v[112:115], v234 offset:16640\n"
"v_mfma_f32_16x16x32_bf16 v[136:139], a[44:47], v[116:119], v[136:139]\n"
"ds_read_b128 v[116:119], v235 offset:16640\n"
"s_waitcnt lgkmcnt(8)\n"
"v_mfma_f32_16x16x32_bf16 v[132:135], a[240:243], v[36:39], v[0:3]\n"
"ds_write_b64 v244, v[208:209] offset:24704\n"
"v_mfma_f32_16x16x32_bf16 v[132:135], a[244:247], v[40:43], v[132:135]\n"
"s_add_u32 s52, s20, s49\n"
"s_addc_u32 s53, s21, 0\n"
"v_mfma_f32_16x16x32_bf16 v[132:135], a[248:251], v[44:47], v[132:135]\n"
"s_add_u32 m0, s31, 0x6000\n"
"v_mfma_f32_16x16x32_bf16 v[132:135], a[252:255], v[60:63], v[132:135]\n"
"global_load_lds_dwordx4 v237, s[52:53] offset:0 nt\n"
"s_waitcnt lgkmcnt(5)\n"
"v_mfma_f32_16x16x32_bf16 v[132:135], a[48:51], v[64:67], v[132:135]\n"
"v_cvt_pk_bf16_f32 v210, v136, v137\n"
"v_mfma_f32_16x16x32_bf16 v[132:135], a[52:55], v[68:71], v[132:135]\n"
"v_cvt_pk_bf16_f32 v211, v138, v139\n"
"v_mfma_f32_16x16x32_bf16 v[132:135], a[56:59], v[84:87], v[132:135]\n"
"ds_read_b128 v[216:219], v246 offset:12288\n"
"v_mfma_f32_16x16x32_bf16 v[132:135], a[60:63], v[88:91], v[132:135]\n"
"ds_read_b128 v[220:223], v246 offset:13312\n"
"s_waitcnt lgkmcnt(3)\n"
"v_mfma_f32_16x16x32_bf16 v[132:135], a[64:67], v[92:95], v[132:135]\n"
"ds_read_b128 v[224:227], v246 offset:14336\n"
"v_mfma_f32_16x16x32_bf16 v[132:135], a[68:71], v[108:111], v[132:135]\n"
"s_add_u32 m0, s31, 0x7f00\n"
"v_mfma_f32_16x16x32_bf16 v[132:135], a[72:75], v[112:115], v[132:135]\n"
"ds_write_b64 v244, v[210:211] offset:24712\n"
"v_mfma_f32_16x16x32_bf16 v[132:135], a[76:79], v[116:119], v[132:135]\n"
"global_load_lds_dwordx4 v237, s[52:53] offset:256 nt\n"
"v_mfma_f32_16x16x32_bf16 v[136:139], a[124:127], v[116:119], v[4:7]\n"
"s_add_u32 s83, s83, s86\n"
"v_mfma_f32_16x16x32_bf16 v[136:139], a[120:123], v[112:115], v[136:139]\n"
"s_sub_i32 s88, s88, 6\n"
"v_mfma_f32_16x16x32_bf16 v[136:139], a[116:119], v[108:111], v[136:139]\n"
"s_max_i32 s88, s88, 0\n"
"v_mfma_f32_16x16x32_bf16 v[136:139], a[112:115], v[92:95], v[136:139]\n"
"v_cvt_pk_bf16_f32 v208, v132, v133\n"
"v_mfma_f32_16x16x32_bf16 v[136:139], a[108:111], v[88:91], v[136:139]\n"
"v_cvt_pk_bf16_f32 v209, v134, v135\n"
"v_mfma_f32_16x16x32_bf16 v[136:139], a[104:107], v[84:87], v[136:139]\n"
"s_add_u32 m0, s31, 0x9e00\n"
"v_mfma_f32_16x16x32_bf16 v[136:139], a[100:103], v[68:71], v[136:139]\n"
"global_load_lds_dwordx4 v237, s[52:53] offset:512 nt\n"
"v_mfma_f32_16x16x32_bf16 v[136:139], a[96:99], v[64:67], v[136:139]\n"
"s_waitcnt lgkmcnt(1)\n"
"v_mfma_f32_16x16x32_bf16 v[136:139], a[92:95], v[60:63], v[136:139]\n"
"v_mfma_f32_16x16x32_bf16 v[136:139], a[88:91], v[44:47], v[136:139]\n"
"global_store_dwordx4 v247, v[216:219], s[54:55] offset:768 sc1\n"
"ds_write_b64 v244, v[208:209] offset:0\n"
"v_mfma_f32_16x16x32_bf16 v[136:139], a[84:87], v[40:43], v[136:139]\n"
"v_mfma_f32_16x16x32_bf16 v[136:139], a[80:83], v[36:39], v[136:139]\n"
"v_mfma_f32_16x16x32_bf16 v[132:135], a[128:131], v[36:39], v[8:11]\n"
"v_mfma_f32_16x16x32_bf16 v[132:135], a[132:135], v[40:43], v[132:135]\n"
"v_mfma_f32_16x16x32_bf16 v[132:135], a[136:139], v[44:47], v[132:135]\n"
"v_mfma_f32_16x16x32_bf16 v[132:135], a[140:143], v[60:63], v[132:135]\n"
"v_cvt_pk_bf16_f32 v210, v136, v137\n"
"v_mfma_f32_16x16x32_bf16 v[132:135], a[144:147], v[64:67], v[132:135]\n"
"v_cvt_pk_bf16_f32 v211, v138, v139\n"
"v_mfma_f32_16x16x32_bf16 v[132:135], a[148:151], v[68:71], v[132:135]\n"
"s_add_u32 m0, s31, 0x7000\n"
"v_mfma_f32_16x16x32_bf16 v[132:135], a[152:155], v[84:87], v[132:135]\n"
"global_load_lds_dwordx4 v238, s[52:53] offset:0 nt\n"
"v_mfma_f32_16x16x32_bf16 v[132:135], a[156:159], v[88:91], v[132:135]\n"
"v_mfma_f32_16x16x32_bf16 v[132:135], a[160:163], v[92:95], v[132:135]\n"
"v_mfma_f32_16x16x32_bf16 v[132:135], a[164:167], v[108:111], v[132:135]\n"
"s_memrealtime s[84:85]\n"
"v_mfma_f32_16x16x32_bf16 v[132:135], a[168:171], v[112:115], v[132:135]\n"
"global_store_dwordx4 v252, v[220:223], s[54:55] offset:768 sc1\n"
"ds_write_b64 v244, v[210:211] offset:8\n"
"v_mfma_f32_16x16x32_bf16 v[132:135], a[172:175], v[116:119], v[132:135]\n"
"v_mfma_f32_16x16x32_bf16 v[136:139], a[220:223], v[116:119], v[12:15]\n"
"v_mfma_f32_16x16x32_bf16 v[136:139], a[216:219], v[112:115], v[136:139]\n"
"v_mfma_f32_16x16x32_bf16 v[136:139], a[212:215], v[108:111], v[136:139]\n"
"v_mfma_f32_16x16x32_bf16 v[136:139], a[208:211], v[92:95], v[136:139]\n"
"v_cvt_pk_bf16_f32 v212, v132, v133\n"
"v_mfma_f32_16x16x32_bf16 v[136:139], a[204:207], v[88:91], v[136:139]\n"
"v_mov_b32 v242, 30\n"
"v_mfma_f32_16x16x32_bf16 v[136:139], a[200:203], v[84:87], v[136:139]\n"
"v_cvt_pk_bf16_f32 v213, v134, v135\n"
"v_mfma_f32_16x16x32_bf16 v[136:139], a[196:199], v[68:71], v[136:139]\n"
"s_waitcnt vmcnt(18) lgkmcnt(2)\n"
"ds_write_b32 v240, v242\n"
"v_mfma_f32_16x16x32_bf16 v[136:139], a[192:195], v[64:67], v[136:139]\n"
"v_mfma_f32_16x16x32_bf16 v[136:139], a[188:191], v[60:63], v[136:139]\n"
"v_mfma_f32_16x16x32_bf16 v[136:139], a[184:187], v[44:47], v[136:139]\n"
"v_mfma_f32_16x16x32_bf16 v[136:139], a[180:183], v[40:43], v[136:139]\n"
"global_store_dwordx4 v253, v[224:227], s[54:55] offset:768 sc1\n"
"ds_write_b64 v245, v[212:213] offset:0\n"
"v_mfma_f32_16x16x32_bf16 v[136:139], a[176:179], v[36:39], v[136:139]\n"
"s_add_u32 m0, s31, 0x8f00\n"
"v_mfma_f32_16x16x32_bf16 v[132:135], a[224:227], v[36:39], v[16:19]\n"
"global_load_lds_dwordx4 v238, s[52:53] offset:256 nt\n"
"v_mfma_f32_16x16x32_bf16 v[132:135], a[228:231], v[40:43], v[132:135]\n"
"s_bitcmp1_b32 s92, 7\n"
"s_cselect_b32 s93, 0x6000, 0\n"
"v_mfma_f32_16x16x32_bf16 v[132:135], a[232:235], v[44:47], v[132:135]\n"
"ds_read_b32 v243, v241\n"
"v_mfma_f32_16x16x32_bf16 v[132:135], a[236:239], v[60:63], v[132:135]\n"
"v_cvt_pk_bf16_f32 v214, v136, v137\n"
"v_mfma_f32_16x16x32_bf16 v[132:135], a[0:3], v[64:67], v[132:135]\n"
"v_cvt_pk_bf16_f32 v215, v138, v139\n"
"v_mfma_f32_16x16x32_bf16 v[132:135], a[4:7], v[68:71], v[132:135]\n"
"s_sub_u32 s94, s93, 0xc000\n"
"v_mfma_f32_16x16x32_bf16 v[132:135], a[8:11], v[84:87], v[132:135]\n"
"s_waitcnt lgkmcnt(0)\n"
"v_cmp_gt_u32 s[56:57], 30, v243\n"
"v_mfma_f32_16x16x32_bf16 v[132:135], a[12:15], v[88:91], v[132:135]\n"
"s_sub_u32 s84, s84, s83\n"
"v_mfma_f32_16x16x32_bf16 v[132:135], a[16:19], v[92:95], v[132:135]\n"
"s_or_b64 s[56:57], s[56:57], vcc\n"
"s_cbranch_scc1 LB30E\n"
"LB30ER:\n"
"v_mfma_f32_16x16x32_bf16 v[132:135], a[20:23], v[108:111], v[132:135]\n"
"s_cmp_lt_i32 s84, s88\n"
"s_cselect_b64 vcc, -1, 0\n"
"v_mfma_f32_16x16x32_bf16 v[132:135], v[140:143], v[112:115], v[132:135]\n"
"ds_write_b64 v245, v[214:215] offset:8\n"
"v_mfma_f32_16x16x32_bf16 v[132:135], v[156:159], v[116:119], v[132:135]\n"
"v_add_u32 v231, s93, v247\n"
"v_mfma_f32_16x16x32_bf16 v[136:139], a[44:47], v[116:119], v[20:23]\n"
"ds_read_b128 v[116:119], v235 offset:40960\n"
"v_mfma_f32_16x16x32_bf16 v[136:139], a[40:43], v[112:115], v[136:139]\n"
"ds_read_b128 v[112:115], v234 offset:40960\n"
"v_mfma_f32_16x16x32_bf16 v[136:139], a[36:39], v[108:111], v[136:139]\n"
"ds_read_b128 v[108:111], v233 offset:40960\n"
"v_mfma_f32_16x16x32_bf16 v[136:139], a[32:35], v[92:95], v[136:139]\n"
"ds_read_b128 v[92:95], v232 offset:40960\n"
"v_cvt_pk_bf16_f32 v208, v132, v133\n"
"v_mfma_f32_16x16x32_bf16 v[136:139], a[28:31], v[88:91], v[136:139]\n"
"ds_read_b128 v[88:91], v235 offset:32768\n"
"v_cvt_pk_bf16_f32 v209, v134, v135\n"
"v_mfma_f32_16x16x32_bf16 v[136:139], a[24:27], v[84:87], v[136:139]\n"
"ds_read_b128 v[84:87], v234 offset:32768\n"
"v_mfma_f32_16x16x32_bf16 v[136:139], v[204:207], v[68:71], v[136:139]\n"
"ds_read_b128 v[68:71], v233 offset:32768\n"
"v_mfma_f32_16x16x32_bf16 v[136:139], v[188:191], v[64:67], v[136:139]\n"
"ds_read_b128 v[64:67], v232 offset:32768\n"
"v_mfma_f32_16x16x32_bf16 v[136:139], v[184:187], v[60:63], v[136:139]\n"
"ds_read_b128 v[60:63], v235 offset:24576\n"
"v_mfma_f32_16x16x32_bf16 v[136:139], v[180:183], v[44:47], v[136:139]\n"
"ds_read_b128 v[44:47], v234 offset:24576\n"
"v_mfma_f32_16x16x32_bf16 v[136:139], v[164:167], v[40:43], v[136:139]\n"
"ds_read_b128 v[40:43], v233 offset:24576\n"
"v_mfma_f32_16x16x32_bf16 v[136:139], v[160:163], v[36:39], v[136:139]\n"
"ds_read_b128 v[36:39], v232 offset:24576\n"
"s_waitcnt lgkmcnt(8)\n"
"v_mfma_f32_16x16x32_bf16 v[132:135], a[76:79], v[116:119], v[0:3]\n"
"ds_write_b64 v244, v[208:209] offset:128\n"
"v_mfma_f32_16x16x32_bf16 v[132:135], a[72:75], v[112:115], v[132:135]\n"
"s_add_u32 m0, s31, 0xae00\n"
"v_mfma_f32_16x16x32_bf16 v[132:135], a[68:71], v[108:111], v[132:135]\n"
"global_load_lds_dwordx4 v238, s[52:53] offset:512 nt\n"
"v_mfma_f32_16x16x32_bf16 v[132:135], a[64:67], v[92:95], v[132:135]\n"
"v_cvt_pk_bf16_f32 v210, v136, v137\n"
"s_waitcnt lgkmcnt(5)\n"
"v_mfma_f32_16x16x32_bf16 v[132:135], a[60:63], v[88:91], v[132:135]\n"
"v_cvt_pk_bf16_f32 v211, v138, v139\n"
"v_mfma_f32_16x16x32_bf16 v[132:135], a[56:59], v[84:87], v[132:135]\n"
"s_add_u32 s54, s26, s46\n"
"s_addc_u32 s55, s27, 0\n"
"v_mfma_f32_16x16x32_bf16 v[132:135], a[52:55], v[68:71], v[132:135]\n"
"ds_read_b128 v[216:219], v246 offset:24576\n"
"v_mfma_f32_16x16x32_bf16 v[132:135], a[48:51], v[64:67], v[132:135]\n"
"ds_read_b128 v[220:223], v246 offset:25600\n"
"s_waitcnt lgkmcnt(3)\n"
"v_mfma_f32_16x16x32_bf16 v[132:135], a[252:255], v[60:63], v[132:135]\n"
"ds_read_b128 v[224:227], v246 offset:26624\n"
"v_mfma_f32_16x16x32_bf16 v[132:135], a[248:251], v[44:47], v[132:135]\n"
"ds_write_b64 v244, v[210:211] offset:136\n"
"v_mfma_f32_16x16x32_bf16 v[132:135], a[244:247], v[40:43], v[132:135]\n"
"s_sub_i32 s84, s84, s19\n"
"v_mfma_f32_16x16x32_bf16 v[132:135], a[240:243], v[36:39], v[132:135]\n"
"s_max_i32 s88, s88, s84\n"
"v_mfma_f32_16x16x32_bf16 v[136:139], a[80:83], v[36:39], v[4:7]\n"
"v_add_u32 v247, s94, v247\n"
"v_mfma_f32_16x16x32_bf16 v[136:139], a[84:87], v[40:43], v[136:139]\n"
"v_min_u32 v247, v247, v231\n"
"v_mfma_f32_16x16x32_bf16 v[136:139], a[88:91], v[44:47], v[136:139]\n"
"v_add_u32 v231, s93, v252\n"
"v_mfma_f32_16x16x32_bf16 v[136:139], a[92:95], v[60:63], v[136:139]\n"
"v_cvt_pk_bf16_f32 v208, v132, v133\n"
"v_add_u32 v252, s94, v252\n"
"v_mfma_f32_16x16x32_bf16 v[136:139], a[96:99], v[64:67], v[136:139]\n"
"v_cvt_pk_bf16_f32 v209, v134, v135\n"
"v_mfma_f32_16x16x32_bf16 v[136:139], a[100:103], v[68:71], v[136:139]\n"
"v_min_u32 v252, v252, v231\n"
"v_mfma_f32_16x16x32_bf16 v[136:139], a[104:107], v[84:87], v[136:139]\n"
"v_add_u32 v231, s93, v253\n"
"v_mfma_f32_16x16x32_bf16 v[136:139], a[108:111], v[88:91], v[136:139]\n"
"s_waitcnt lgkmcnt(1)\n"
"v_add_u32 v253, s94, v253\n"
"v_mfma_f32_16x16x32_bf16 v[136:139], a[112:115], v[92:95], v[136:139]\n"
"v_min_u32 v253, v253, v231\n"
"v_mfma_f32_16x16x32_bf16 v[136:139], a[116:119], v[108:111], v[136:139]\n"
"global_store_dwordx4 v247, v[216:219], s[54:55] offset:0 sc1\n"
"ds_write_b64 v244, v[208:209] offset:12288\n"
"v_mfma_f32_16x16x32_bf16 v[136:139], a[120:123], v[112:115], v[136:139]\n"
"v_mfma_f32_16x16x32_bf16 v[136:139], a[124:127], v[116:119], v[136:139]\n"
"v_mfma_f32_16x16x32_bf16 v[132:135], a[172:175], v[116:119], v[8:11]\n"
"v_mfma_f32_16x16x32_bf16 v[132:135], a[168:171], v[112:115], v[132:135]\n"
"v_mfma_f32_16x16x32_bf16 v[132:135], a[164:167], v[108:111], v[132:135]\n"
"v_mfma_f32_16x16x32_bf16 v[132:135], a[160:163], v[92:95], v[132:135]\n"
"v_cvt_pk_bf16_f32 v210, v136, v137\n"
"v_mfma_f32_16x16x32_bf16 v[132:135], a[156:159], v[88:91], v[132:135]\n"
"v_cvt_pk_bf16_f32 v211, v138, v139\n"
"v_mfma_f32_16x16x32_bf16 v[132:135], a[152:155], v[84:87], v[132:135]\n"
"v_mfma_f32_16x16x32_bf16 v[132:135], a[148:151], v[68:71], v[132:135]\n"
"v_mfma_f32_16x16x32_bf16 v[132:135], a[144:147], v[64:67], v[132:135]\n"
"v_mfma_f32_16x16x32_bf16 v[132:135], a[140:143], v[60:63], v[132:135]\n"
"v_mfma_f32_16x16x32_bf16 v[132:135], a[136:139], v[44:47], v[132:135]\n"
"global_store_dwordx4 v252, v[220:223], s[54:55] offset:0 sc1\n"
"ds_write_b64 v244, v[210:211] offset:12296\n"
"v_mfma_f32_16x16x32_bf16 v[132:135], a[132:135], v[40:43], v[132:135]\n"
"v_mfma_f32_16x16x32_bf16 v[132:135], a[128:131], v[36:39], v[132:135]\n"
"v_mfma_f32_16x16x32_bf16 v[136:139], a[176:179], v[36:39], v[12:15]\n"
"v_mfma_f32_16x16x32_bf16 v[136:139], a[180:183], v[40:43], v[136:139]\n"
"v_mfma_f32_16x16x32_bf16 v[136:139], a[184:187], v[44:47], v[136:139]\n"
"v_mfma_f32_16x16x32_bf16 v[136:139], a[188:191], v[60:63], v[136:139]\n"
"v_cvt_pk_bf16_f32 v212, v132, v133\n"
"v_mfma_f32_16x16x32_bf16 v[136:139], a[192:195], v[64:67], v[136:139]\n"
"v_mov_b32 v242, 31\n"
"v_mfma_f32_16x16x32_bf16 v[136:139], a[196:199], v[68:71], v[136:139]\n"
"v_cvt_pk_bf16_f32 v213, v134, v135\n"
"v_mfma_f32_16x16x32_bf16 v[136:139], a[200:203], v[84:87], v[136:139]\n"
"s_waitcnt lgkmcnt(2)\n"
"ds_write_b32 v240, v242\n"
"v_mfma_f32_16x16x32_bf16 v[136:139], a[204:207], v[88:91], v[136:139]\n"
"v_mfma_f32_16x16x32_bf16 v[136:139], a[208:211], v[92:95], v[136:139]\n"
"v_mfma_f32_16x16x32_bf16 v[136:139], a[212:215], v[108:111], v[136:139]\n"
"v_mfma_f32_16x16x32_bf16 v[136:139], a[216:219], v[112:115], v[136:139]\n"
"global_store_dwordx4 v253, v[224:227], s[54:55] offset:0 sc1\n"
"ds_write_b64 v245, v[212:213] offset:12288\n"
"v_mfma_f32_16x16x32_bf16 v[136:139], a[220:223], v[116:119], v[136:139]\n"
"v_mfma_f32_16x16x32_bf16 v[132:135], v[156:159], v[116:119], v[16:19]\n"
"v_mfma_f32_16x16x32_bf16 v[132:135], v[140:143], v[112:115], v[132:135]\n"
"v_mfma_f32_16x16x32_bf16 v[132:135], a[20:23], v[108:111], v[132:135]\n"
"ds_read_b32 v243, v241\n"
"v_mfma_f32_16x16x32_bf16 v[132:135], a[16:19], v[92:95], v[132:135]\n"
"v_cvt_pk_bf16_f32 v214, v136, v137\n"
"v_mfma_f32_16x16x32_bf16 v[132:135], a[12:15], v[88:91], v[132:135]\n"
"v_cvt_pk_bf16_f32 v215, v138, v139\n"
"v_mfma_f32_16x16x32_bf16 v[132:135], a[8:11], v[84:87], v[132:135]\n"
"v_mfma_f32_16x16x32_bf16 v[132:135], a[4:7], v[68:71], v[132:135]\n"
"s_waitcnt lgkmcnt(0)\n"
"v_cmp_gt_u32 s[56:57], 31, v243\n"
"v_mfma_f32_16x16x32_bf16 v[132:135], a[0:3], v[64:67], v[132:135]\n"
"v_mfma_f32_16x16x32_bf16 v[132:135], a[236:239], v[60:63], v[132:135]\n"
"s_or_b64 s[56:57], s[56:57], vcc\n"
"s_cbranch_scc1 LB31E\n"
"LB31ER:\n"
"v_mfma_f32_16x16x32_bf16 v[132:135], a[232:235], v[44:47], v[132:135]\n"
"ds_write_b64 v245, v[214:215] offset:12296\n"
"v_mfma_f32_16x16x32_bf16 v[132:135], a[228:231], v[40:43], v[132:135]\n"
"v_mfma_f32_16x16x32_bf16 v[132:135], a[224:227], v[36:39], v[132:135]\n"
"v_mfma_f32_16x16x32_bf16 v[136:139], v[160:163], v[36:39], v[20:23]\n"
"ds_read_b128 v[36:39], v232 offset:24832\n"
"v_mfma_f32_16x16x32_bf16 v[136:139], v[164:167], v[40:43], v[136:139]\n"
"ds_read_b128 v[40:43], v233 offset:24832\n"
"v_mfma_f32_16x16x32_bf16 v[136:139], v[180:183], v[44:47], v[136:139]\n"
"ds_read_b128 v[44:47], v234 offset:24832\n"
"v_mfma_f32_16x16x32_bf16 v[136:139], v[184:187], v[60:63], v[136:139]\n"
"ds_read_b128 v[60:63], v235 offset:24832\n"
"v_cvt_pk_bf16_f32 v208, v132, v133\n"
"v_mfma_f32_16x16x32_bf16 v[136:139], v[188:191], v[64:67], v[136:139]\n"
"ds_read_b128 v[64:67], v232 offset:33024\n"
"v_cvt_pk_bf16_f32 v209, v134, v135\n"
"v_mfma_f32_16x16x32_bf16 v[136:139], v[204:207], v[68:71], v[136:139]\n"
"ds_read_b128 v[68:71], v233 offset:33024\n"
"v_mfma_f32_16x16x32_bf16 v[136:139], a[24:27], v[84:87], v[136:139]\n"
"ds_read_b128 v[84:87], v234 offset:33024\n"
"v_mfma_f32_16x16x32_bf16 v[136:139], a[28:31], v[88:91], v[136:139]\n"
"ds_read_b128 v[88:91], v235 offset:33024\n"
"v_mfma_f32_16x16x32_bf16 v[136:139], a[32:35], v[92:95], v[136:139]\n"
"ds_read_b128 v[92:95], v232 offset:41216\n"
"v_mfma_f32_16x16x32_bf16 v[136:139], a[36:39], v[108:111], v[136:139]\n"
"ds_read_b128 v[108:111], v233 offset:41216\n"
"ds_write_b64 v244, v[208:209] offset:12416\n"
"v_mfma_f32_16x16x32_bf16 v[136:139], a[40:43], v[112:115], v[136:139]\n"
"ds_read_b128 v[112:115], v234 offset:41216\n"
"v_mfma_f32_16x16x32_bf16 v[136:139], a[44:47], v[116:119], v[136:139]\n"
"ds_read_b128 v[116:119], v235 offset:41216\n"
"s_waitcnt lgkmcnt(9)\n"
"v_mfma_f32_16x16x32_bf16 v[132:135], a[240:243], v[36:39], v[0:3]\n"
"v_add_u32 v232, 0xffff4000, v232\n"
"v_mfma_f32_16x16x32_bf16 v[132:135], a[244:247], v[40:43], v[132:135]\n"
"v_add_u32 v233, 0xffff4000, v233\n"
"v_mfma_f32_16x16x32_bf16 v[132:135], a[248:251], v[44:47], v[132:135]\n"
"v_add_u32 v234, 0xffff4000, v234\n"
"v_mfma_f32_16x16x32_bf16 v[132:135], a[252:255], v[60:63], v[132:135]\n"
"v_add_u32 v235, 0xffff4000, v235\n"
"s_waitcnt lgkmcnt(5)\n"
"v_mfma_f32_16x16x32_bf16 v[132:135], a[48:51], v[64:67], v[132:135]\n"
"v_cvt_pk_bf16_f32 v210, v136, v137\n"
"v_mfma_f32_16x16x32_bf16 v[132:135], a[52:55], v[68:71], v[132:135]\n"
"v_cvt_pk_bf16_f32 v211, v138, v139\n"
"v_mfma_f32_16x16x32_bf16 v[132:135], a[56:59], v[84:87], v[132:135]\n"
"ds_read_b128 v[216:219], v246 offset:0\n"
"v_mfma_f32_16x16x32_bf16 v[132:135], a[60:63], v[88:91], v[132:135]\n"
"ds_read_b128 v[220:223], v246 offset:1024\n"
"s_waitcnt lgkmcnt(2)\n"
"v_mfma_f32_16x16x32_bf16 v[132:135], a[64:67], v[92:95], v[132:135]\n"
"ds_read_b128 v[224:227], v246 offset:2048\n"
"v_mfma_f32_16x16x32_bf16 v[132:135], a[68:71], v[108:111], v[132:135]\n"
"v_lshlrev_b32 v0, 16, v192\n"
"v_mfma_f32_16x16x32_bf16 v[132:135], a[72:75], v[112:115], v[132:135]\n"
"ds_write_b64 v244, v[210:211] offset:12424\n"
"v_mfma_f32_16x16x32_bf16 v[132:135], a[76:79], v[116:119], v[132:135]\n"
"v_and_b32 v1, 0xffff0000, v192\n"
"v_mfma_f32_16x16x32_bf16 v[136:139], a[124:127], v[116:119], v[4:7]\n"
"v_lshlrev_b32 v2, 16, v193\n"
"v_mfma_f32_16x16x32_bf16 v[136:139], a[120:123], v[112:115], v[136:139]\n"
"v_and_b32 v3, 0xffff0000, v193\n"
"v_mfma_f32_16x16x32_bf16 v[136:139], a[116:119], v[108:111], v[136:139]\n"
"s_add_u32 s83, s83, s86\n"
"v_mfma_f32_16x16x32_bf16 v[136:139], a[112:115], v[92:95], v[136:139]\n"
"v_cvt_pk_bf16_f32 v208, v132, v133\n"
"v_mfma_f32_16x16x32_bf16 v[136:139], a[108:111], v[88:91], v[136:139]\n"
"v_cvt_pk_bf16_f32 v209, v134, v135\n"
"v_mfma_f32_16x16x32_bf16 v[136:139], a[104:107], v[84:87], v[136:139]\n"
"v_lshlrev_b32 v4, 16, v194\n"
"v_mfma_f32_16x16x32_bf16 v[136:139], a[100:103], v[68:71], v[136:139]\n"
"v_and_b32 v5, 0xffff0000, v194\n"
"v_mfma_f32_16x16x32_bf16 v[136:139], a[96:99], v[64:67], v[136:139]\n"
"s_waitcnt lgkmcnt(1)\n"
"v_mfma_f32_16x16x32_bf16 v[136:139], a[92:95], v[60:63], v[136:139]\n"
"v_lshlrev_b32 v6, 16, v195\n"
"v_mfma_f32_16x16x32_bf16 v[136:139], a[88:91], v[44:47], v[136:139]\n"
"global_store_dwordx4 v247, v[216:219], s[54:55] offset:768 sc1\n"
"ds_write_b64 v244, v[208:209] offset:24576\n"
"v_mfma_f32_16x16x32_bf16 v[136:139], a[84:87], v[40:43], v[136:139]\n"
"v_and_b32 v7, 0xffff0000, v195\n"
"v_mfma_f32_16x16x32_bf16 v[136:139], a[80:83], v[36:39], v[136:139]\n"
"s_sub_i32 s88, s88, 6\n"
"v_mfma_f32_16x16x32_bf16 v[132:135], a[128:131], v[36:39], v[8:11]\n"
"s_max_i32 s88, s88, 0\n"
"v_mfma_f32_16x16x32_bf16 v[132:135], a[132:135], v[40:43], v[132:135]\n"
"s_mov_b64 exec, s[8:9]\n"
"s_cbranch_execz LHT14\n"
"LHR14:\n"
"s_mov_b64 exec, -1\n"
"v_mfma_f32_16x16x32_bf16 v[132:135], a[136:139], v[44:47], v[132:135]\n"
"v_mfma_f32_16x16x32_bf16 v[132:135], a[140:143], v[60:63], v[132:135]\n"
"v_cvt_pk_bf16_f32 v210, v136, v137\n"
"v_mfma_f32_16x16x32_bf16 v[132:135], a[144:147], v[64:67], v[132:135]\n"
"v_cvt_pk_bf16_f32 v211, v138, v139\n"
"v_mfma_f32_16x16x32_bf16 v[132:135], a[148:151], v[68:71], v[132:135]\n"
"v_lshlrev_b32 v8, 16, v196\n"
"v_mfma_f32_16x16x32_bf16 v[132:135], a[152:155], v[84:87], v[132:135]\n"
"v_and_b32 v9, 0xffff0000, v196\n"
"v_mfma_f32_16x16x32_bf16 v[132:135], a[156:159], v[88:91], v[132:135]\n"
"v_lshlrev_b32 v10, 16, v197\n"
"v_mfma_f32_16x16x32_bf16 v[132:135], a[160:163], v[92:95], v[132:135]\n"
"v_and_b32 v11, 0xffff0000, v197\n"
"v_mfma_f32_16x16x32_bf16 v[132:135], a[164:167], v[108:111], v[132:135]\n"
"s_memrealtime s[84:85]\n"
"v_mfma_f32_16x16x32_bf16 v[132:135], a[168:171], v[112:115], v[132:135]\n"
"global_store_dwordx4 v252, v[220:223], s[54:55] offset:768 sc1\n"
"ds_write_b64 v244, v[210:211] offset:24584\n"
"v_mfma_f32_16x16x32_bf16 v[132:135], a[172:175], v[116:119], v[132:135]\n"
"v_mfma_f32_16x16x32_bf16 v[136:139], a[220:223], v[116:119], v[12:15]\n"
"v_mfma_f32_16x16x32_bf16 v[136:139], a[216:219], v[112:115], v[136:139]\n"
"v_mfma_f32_16x16x32_bf16 v[136:139], a[212:215], v[108:111], v[136:139]\n"
"v_mfma_f32_16x16x32_bf16 v[136:139], a[208:211], v[92:95], v[136:139]\n"
"v_cvt_pk_bf16_f32 v212, v132, v133\n"
"v_mfma_f32_16x16x32_bf16 v[136:139], a[204:207], v[88:91], v[136:139]\n"
"v_mov_b32 v242, 32\n"
"v_mfma_f32_16x16x32_bf16 v[136:139], a[200:203], v[84:87], v[136:139]\n"
"v_cvt_pk_bf16_f32 v213, v134, v135\n"
"v_mfma_f32_16x16x32_bf16 v[136:139], a[196:199], v[68:71], v[136:139]\n"
"s_waitcnt vmcnt(14) lgkmcnt(2)\n"
"ds_write_b32 v240, v242\n"
"v_mfma_f32_16x16x32_bf16 v[136:139], a[192:195], v[64:67], v[136:139]\n"
"v_lshlrev_b32 v12, 16, v198\n"
"LHT14:\n"
"s_cbranch_execz LHR14\n"
"v_mfma_f32_16x16x32_bf16 v[136:139], a[188:191], v[60:63], v[136:139]\n"
"v_and_b32 v13, 0xffff0000, v198\n"
"v_mfma_f32_16x16x32_bf16 v[136:139], a[184:187], v[44:47], v[136:139]\n"
"v_lshlrev_b32 v14, 16, v199\n"
"v_mfma_f32_16x16x32_bf16 v[136:139], a[180:183], v[40:43], v[136:139]\n"
"global_store_dwordx4 v253, v[224:227], s[54:55] offset:768 sc1\n"
"ds_write_b64 v245, v[212:213] offset:24576\n"
"v_mfma_f32_16x16x32_bf16 v[136:139], a[176:179], v[36:39], v[136:139]\n"
"v_and_b32 v15, 0xffff0000, v199\n"
"v_mfma_f32_16x16x32_bf16 v[132:135], a[224:227], v[36:39], v[16:19]\n"
"v_mfma_f32_16x16x32_bf16 v[132:135], a[228:231], v[40:43], v[132:135]\n"
"v_mfma_f32_16x16x32_bf16 v[132:135], a[232:235], v[44:47], v[132:135]\n"
"ds_read_b32 v243, v241\n"
"v_mfma_f32_16x16x32_bf16 v[132:135], a[236:239], v[60:63], v[132:135]\n"
"v_cvt_pk_bf16_f32 v214, v136, v137\n"
"v_mfma_f32_16x16x32_bf16 v[132:135], a[0:3], v[64:67], v[132:135]\n"
"v_cvt_pk_bf16_f32 v215, v138, v139\n"
"v_mfma_f32_16x16x32_bf16 v[132:135], a[4:7], v[68:71], v[132:135]\n"
"v_lshlrev_b32 v16, 16, v200\n"
"v_mfma_f32_16x16x32_bf16 v[132:135], a[8:11], v[84:87], v[132:135]\n"
"s_waitcnt lgkmcnt(0)\n"
"v_cmp_gt_u32 s[56:57], 32, v243\n"
"v_mfma_f32_16x16x32_bf16 v[132:135], a[12:15], v[88:91], v[132:135]\n"
"s_sub_u32 s84, s84, s83\n"
"v_mfma_f32_16x16x32_bf16 v[132:135], a[16:19], v[92:95], v[132:135]\n"
"s_or_b64 s[56:57], s[56:57], vcc\n"
"s_cbranch_scc1 LB32E\n"
"LB32ER:\n"
"v_mfma_f32_16x16x32_bf16 v[132:135], a[20:23], v[108:111], v[132:135]\n"
"s_cmp_lt_i32 s84, s88\n"
"s_cselect_b64 vcc, -1, 0\n"
"v_mfma_f32_16x16x32_bf16 v[132:135], v[140:143], v[112:115], v[132:135]\n"
"ds_write_b64 v245, v[214:215] offset:24584\n"
"v_mfma_f32_16x16x32_bf16 v[132:135], v[156:159], v[116:119], v[132:135]\n"
"v_and_b32 v17, 0xffff0000, v200\n"
"v_mfma_f32_16x16x32_bf16 v[136:139], a[44:47], v[116:119], v[20:23]\n"
"ds_read_b128 v[116:119], v235 offset:16384\n"
"v_mfma_f32_16x16x32_bf16 v[136:139], a[40:43], v[112:115], v[136:139]\n"
"ds_read_b128 v[112:115], v234 offset:16384\n"
"v_mfma_f32_16x16x32_bf16 v[136:139], a[36:39], v[108:111], v[136:139]\n"
"ds_read_b128 v[108:111], v233 offset:16384\n"
"v_mfma_f32_16x16x32_bf16 v[136:139], a[32:35], v[92:95], v[136:139]\n"
"ds_read_b128 v[92:95], v232 offset:16384\n"
"v_cvt_pk_bf16_f32 v208, v132, v133\n"
"v_mfma_f32_16x16x32_bf16 v[136:139], a[28:31], v[88:91], v[136:139]\n"
"ds_read_b128 v[88:91], v235 offset:8192\n"
"v_cvt_pk_bf16_f32 v209, v134, v135\n"
"v_mfma_f32_16x16x32_bf16 v[136:139], a[24:27], v[84:87], v[136:139]\n"
"ds_read_b128 v[84:87], v234 offset:8192\n"
"v_mfma_f32_16x16x32_bf16 v[136:139], v[204:207], v[68:71], v[136:139]\n"
"ds_read_b128 v[68:71], v233 offset:8192\n"
"v_mfma_f32_16x16x32_bf16 v[136:139], v[188:191], v[64:67], v[136:139]\n"
"ds_read_b128 v[64:67], v232 offset:8192\n"
"v_mfma_f32_16x16x32_bf16 v[136:139], v[184:187], v[60:63], v[136:139]\n"
"ds_read_b128 v[60:63], v235 offset:0\n"
"v_mfma_f32_16x16x32_bf16 v[136:139], v[180:183], v[44:47], v[136:139]\n"
"ds_read_b128 v[44:47], v234 offset:0\n"
"v_mfma_f32_16x16x32_bf16 v[136:139], v[164:167], v[40:43], v[136:139]\n"
"ds_read_b128 v[40:43], v233 offset:0\n"
"v_mfma_f32_16x16x32_bf16 v[136:139], v[160:163], v[36:39], v[136:139]\n"
"ds_read_b128 v[36:39], v232 offset:0\n"
"s_waitcnt lgkmcnt(8)\n"
"v_mfma_f32_16x16x32_bf16 v[132:135], a[76:79], v[116:119], v[0:3]\n"
"ds_write_b64 v244, v[208:209] offset:24704\n"
"v_mfma_f32_16x16x32_bf16 v[132:135], a[72:75], v[112:115], v[132:135]\n"
"v_lshlrev_b32 v18, 16, v201\n"
"v_mfma_f32_16x16x32_bf16 v[132:135], a[68:71], v[108:111], v[132:135]\n"
"v_and_b32 v19, 0xffff0000, v201\n"
"v_mfma_f32_16x16x32_bf16 v[132:135], a[64:67], v[92:95], v[132:135]\n"
"v_lshlrev_b32 v20, 16, v202\n"
"s_waitcnt lgkmcnt(5)\n"
"v_mfma_f32_16x16x32_bf16 v[132:135], a[60:63], v[88:91], v[132:135]\n"
"v_and_b32 v21, 0xffff0000, v202\n"
"v_mfma_f32_16x16x32_bf16 v[132:135], a[56:59], v[84:87], v[132:135]\n"
"v_lshlrev_b32 v22, 16, v203\n"
"v_mfma_f32_16x16x32_bf16 v[132:135], a[52:55], v[68:71], v[132:135]\n"
"v_and_b32 v23, 0xffff0000, v203\n"
"v_mfma_f32_16x16x32_bf16 v[132:135], a[48:51], v[64:67], v[132:135]\n"
"v_cvt_pk_bf16_f32 v210, v136, v137\n"
"s_waitcnt lgkmcnt(1)\n"
"v_mfma_f32_16x16x32_bf16 v[132:135], a[252:255], v[60:63], v[132:135]\n"
"v_cvt_pk_bf16_f32 v211, v138, v139\n"
"v_mfma_f32_16x16x32_bf16 v[132:135], a[248:251], v[44:47], v[132:135]\n"
"s_add_u32 s54, s26, s47\n"
"s_addc_u32 s55, s27, 0\n"
"v_mfma_f32_16x16x32_bf16 v[132:135], a[244:247], v[40:43], v[132:135]\n"
"ds_read_b128 v[216:219], v246 offset:12288\n"
"v_mfma_f32_16x16x32_bf16 v[132:135], a[240:243], v[36:39], v[132:135]\n"
"ds_read_b128 v[220:223], v246 offset:13312\n"
"v_mfma_f32_16x16x32_bf16 v[136:139], a[80:83], v[36:39], v[4:7]\n"
"ds_read_b128 v[224:227], v246 offset:14336\n"
"v_mfma_f32_16x16x32_bf16 v[136:139], a[84:87], v[40:43], v[136:139]\n"
"ds_write_b64 v244, v[210:211] offset:24712\n"
"v_mfma_f32_16x16x32_bf16 v[136:139], a[88:91], v[44:47], v[136:139]\n"
"s_sub_i32 s84, s84, s19\n"
"v_mfma_f32_16x16x32_bf16 v[136:139], a[92:95], v[60:63], v[136:139]\n"
"v_cvt_pk_bf16_f32 v208, v132, v133\n"
"v_mfma_f32_16x16x32_bf16 v[136:139], a[96:99], v[64:67], v[136:139]\n"
"v_cvt_pk_bf16_f32 v209, v134, v135\n"
"v_mfma_f32_16x16x32_bf16 v[136:139], a[100:103], v[68:71], v[136:139]\n"
"s_max_i32 s88, s88, s84\n"
"v_mfma_f32_16x16x32_bf16 v[136:139], a[104:107], v[84:87], v[136:139]\n"
"v_mfma_f32_16x16x32_bf16 v[136:139], a[108:111], v[88:91], v[136:139]\n"
"s_waitcnt lgkmcnt(1)\n"
"v_mfma_f32_16x16x32_bf16 v[136:139], a[112:115], v[92:95], v[136:139]\n"
"v_mfma_f32_16x16x32_bf16 v[136:139], a[116:119], v[108:111], v[136:139]\n"
"global_store_dwordx4 v247, v[216:219], s[54:55] offset:0 sc1\n"
"ds_write_b64 v244, v[208:209] offset:0\n"
"v_mfma_f32_16x16x32_bf16 v[136:139], a[120:123], v[112:115], v[136:139]\n"
"v_mfma_f32_16x16x32_bf16 v[136:139], a[124:127], v[116:119], v[136:139]\n"
"v_mfma_f32_16x16x32_bf16 v[132:135], a[172:175], v[116:119], v[8:11]\n"
"v_mfma_f32_16x16x32_bf16 v[132:135], a[168:171], v[112:115], v[132:135]\n"
"v_mfma_f32_16x16x32_bf16 v[132:135], a[164:167], v[108:111], v[132:135]\n"
"v_mfma_f32_16x16x32_bf16 v[132:135], a[160:163], v[92:95], v[132:135]\n"
"v_cvt_pk_bf16_f32 v210, v136, v137\n"
"v_mfma_f32_16x16x32_bf16 v[132:135], a[156:159], v[88:91], v[132:135]\n"
"v_cvt_pk_bf16_f32 v211, v138, v139\n"
"v_mfma_f32_16x16x32_bf16 v[132:135], a[152:155], v[84:87], v[132:135]\n"
"v_mfma_f32_16x16x32_bf16 v[132:135], a[148:151], v[68:71], v[132:135]\n"
"v_mfma_f32_16x16x32_bf16 v[132:135], a[144:147], v[64:67], v[132:135]\n"
"v_mfma_f32_16x16x32_bf16 v[132:135], a[140:143], v[60:63], v[132:135]\n"
"v_mfma_f32_16x16x32_bf16 v[132:135], a[136:139], v[44:47], v[132:135]\n"
"global_store_dwordx4 v252, v[220:223], s[54:55] offset:0 sc1\n"
"ds_write_b64 v244, v[210:211] offset:8\n"
"v_mfma_f32_16x16x32_bf16 v[132:135], a[132:135], v[40:43], v[132:135]\n"
"v_mfma_f32_16x16x32_bf16 v[132:135], a[128:131], v[36:39], v[132:135]\n"
"v_mfma_f32_16x16x32_bf16 v[136:139], a[176:179], v[36:39], v[12:15]\n"
"v_mfma_f32_16x16x32_bf16 v[136:139], a[180:183], v[40:43], v[136:139]\n"
"v_mfma_f32_16x16x32_bf16 v[136:139], a[184:187], v[44:47], v[136:139]\n"
"v_mfma_f32_16x16x32_bf16 v[136:139], a[188:191], v[60:63], v[136:139]\n"
"v_cvt_pk_bf16_f32 v212, v132, v133\n"
"v_mfma_f32_16x16x32_bf16 v[136:139], a[192:195], v[64:67], v[136:139]\n"
"v_mov_b32 v242, 33\n"
"v_mfma_f32_16x16x32_bf16 v[136:139], a[196:199], v[68:71], v[136:139]\n"
"v_cvt_pk_bf16_f32 v213, v134, v135\n"
"v_mfma_f32_16x16x32_bf16 v[136:139], a[200:203], v[84:87], v[136:139]\n"
"s_waitcnt lgkmcnt(2)\n"
"ds_write_b32 v240, v242\n"
"v_mfma_f32_16x16x32_bf16 v[136:139], a[204:207], v[88:91], v[136:139]\n"
"v_mfma_f32_16x16x32_bf16 v[136:139], a[208:211], v[92:95], v[136:139]\n"
"v_mfma_f32_16x16x32_bf16 v[136:139], a[212:215], v[108:111], v[136:139]\n"
"v_mfma_f32_16x16x32_bf16 v[136:139], a[216:219], v[112:115], v[136:139]\n"
"global_store_dwordx4 v253, v[224:227], s[54:55] offset:0 sc1\n"
"ds_write_b64 v245, v[212:213] offset:0\n"
"v_mfma_f32_16x16x32_bf16 v[136:139], a[220:223], v[116:119], v[136:139]\n"
"v_mfma_f32_16x16x32_bf16 v[132:135], v[156:159], v[116:119], v[16:19]\n"
"v_mfma_f32_16x16x32_bf16 v[132:135], v[140:143], v[112:115], v[132:135]\n"
"v_mfma_f32_16x16x32_bf16 v[132:135], a[20:23], v[108:111], v[132:135]\n"
"ds_read_b32 v243, v241\n"
"v_mfma_f32_16x16x32_bf16 v[132:135], a[16:19], v[92:95], v[132:135]\n"
"v_cvt_pk_bf16_f32 v214, v136, v137\n"
"v_mfma_f32_16x16x32_bf16 v[132:135], a[12:15], v[88:91], v[132:135]\n"
"v_cvt_pk_bf16_f32 v215, v138, v139\n"
"v_mfma_f32_16x16x32_bf16 v[132:135], a[8:11], v[84:87], v[132:135]\n"
"v_mfma_f32_16x16x32_bf16 v[132:135], a[4:7], v[68:71], v[132:135]\n"
"s_waitcnt lgkmcnt(0)\n"
"v_cmp_gt_u32 s[56:57], 33, v243\n"
"v_mfma_f32_16x16x32_bf16 v[132:135], a[0:3], v[64:67], v[132:135]\n"
"v_mfma_f32_16x16x32_bf16 v[132:135], a[236:239], v[60:63], v[132:135]\n"
"s_or_b64 s[56:57], s[56:57], vcc\n"
"s_cbranch_scc1 LB33E\n"
"LB33ER:\n"
"v_mfma_f32_16x16x32_bf16 v[132:135], a[232:235], v[44:47], v[132:135]\n"
"ds_write_b64 v245, v[214:215] offset:8\n"
"v_mfma_f32_16x16x32_bf16 v[132:135], a[228:231], v[40:43], v[132:135]\n"
"v_mfma_f32_16x16x32_bf16 v[132:135], a[224:227], v[36:39], v[132:135]\n"
"v_mfma_f32_16x16x32_bf16 v[136:139], v[160:163], v[36:39], v[20:23]\n"
"ds_read_b128 v[36:39], v232 offset:256\n"
"v_mfma_f32_16x16x32_bf16 v[136:139], v[164:167], v[40:43], v[136:139]\n"
"ds_read_b128 v[40:43], v233 offset:256\n"
"v_mfma_f32_16x16x32_bf16 v[136:139], v[180:183], v[44:47], v[136:139]\n"
"ds_read_b128 v[44:47], v234 offset:256\n"
"v_mfma_f32_16x16x32_bf16 v[136:139], v[184:187], v[60:63], v[136:139]\n"
"ds_read_b128 v[60:63], v235 offset:256\n"
"v_cvt_pk_bf16_f32 v208, v132, v133\n"
"v_mfma_f32_16x16x32_bf16 v[136:139], v[188:191], v[64:67], v[136:139]\n"
"ds_read_b128 v[64:67], v232 offset:8448\n"
"v_cvt_pk_bf16_f32 v209, v134, v135\n"
"v_mfma_f32_16x16x32_bf16 v[136:139], v[204:207], v[68:71], v[136:139]\n"
"ds_read_b128 v[68:71], v233 offset:8448\n"
"v_mfma_f32_16x16x32_bf16 v[136:139], a[24:27], v[84:87], v[136:139]\n"
"ds_read_b128 v[84:87], v234 offset:8448\n"
"v_mfma_f32_16x16x32_bf16 v[136:139], a[28:31], v[88:91], v[136:139]\n"
"ds_read_b128 v[88:91], v235 offset:8448\n"
"v_mfma_f32_16x16x32_bf16 v[136:139], a[32:35], v[92:95], v[136:139]\n"
"ds_read_b128 v[92:95], v232 offset:16640\n"
"v_mfma_f32_16x16x32_bf16 v[136:139], a[36:39], v[108:111], v[136:139]\n"
"ds_read_b128 v[108:111], v233 offset:16640\n"
"v_mfma_f32_16x16x32_bf16 v[136:139], a[40:43], v[112:115], v[136:139]\n"
"ds_read_b128 v[112:115], v234 offset:16640\n"
"v_mfma_f32_16x16x32_bf16 v[136:139], a[44:47], v[116:119], v[136:139]\n"
"ds_read_b128 v[116:119], v235 offset:16640\n"
"s_waitcnt lgkmcnt(8)\n"
"v_mfma_f32_16x16x32_bf16 v[132:135], a[240:243], v[36:39], v[0:3]\n"
"ds_write_b64 v244, v[208:209] offset:128\n"
"v_mfma_f32_16x16x32_bf16 v[132:135], a[244:247], v[40:43], v[132:135]\n"
"v_mfma_f32_16x16x32_bf16 v[132:135], a[248:251], v[44:47], v[132:135]\n"
"v_mfma_f32_16x16x32_bf16 v[132:135], a[252:255], v[60:63], v[132:135]\n"
"v_cvt_pk_bf16_f32 v210, v136, v137\n"
"s_waitcnt lgkmcnt(5)\n"
"v_mfma_f32_16x16x32_bf16 v[132:135], a[48:51], v[64:67], v[132:135]\n"
"v_cvt_pk_bf16_f32 v211, v138, v139\n"
"v_mfma_f32_16x16x32_bf16 v[132:135], a[52:55], v[68:71], v[132:135]\n"
"v_mfma_f32_16x16x32_bf16 v[132:135], a[56:59], v[84:87], v[132:135]\n"
"ds_read_b128 v[216:219], v246 offset:24576\n"
"v_mfma_f32_16x16x32_bf16 v[132:135], a[60:63], v[88:91], v[132:135]\n"
"ds_read_b128 v[220:223], v246 offset:25600\n"
"s_waitcnt lgkmcnt(3)\n"
"v_mfma_f32_16x16x32_bf16 v[132:135], a[64:67], v[92:95], v[132:135]\n"
"ds_read_b128 v[224:227], v246 offset:26624\n"
"v_mfma_f32_16x16x32_bf16 v[132:135], a[68:71], v[108:111], v[132:135]\n"
"ds_write_b64 v244, v[210:211] offset:136\n"
"v_mfma_f32_16x16x32_bf16 v[132:135], a[72:75], v[112:115], v[132:135]\n"
"v_mfma_f32_16x16x32_bf16 v[132:135], a[76:79], v[116:119], v[132:135]\n"
"v_mfma_f32_16x16x32_bf16 v[136:139], a[124:127], v[116:119], v[4:7]\n"
"v_mfma_f32_16x16x32_bf16 v[136:139], a[120:123], v[112:115], v[136:139]\n"
"v_mfma_f32_16x16x32_bf16 v[136:139], a[116:119], v[108:111], v[136:139]\n"
"v_mfma_f32_16x16x32_bf16 v[136:139], a[112:115], v[92:95], v[136:139]\n"
"v_cvt_pk_bf16_f32 v208, v132, v133\n"
"v_mfma_f32_16x16x32_bf16 v[136:139], a[108:111], v[88:91], v[136:139]\n"
"v_cvt_pk_bf16_f32 v209, v134, v135\n"
"v_mfma_f32_16x16x32_bf16 v[136:139], a[104:107], v[84:87], v[136:139]\n"
"v_mfma_f32_16x16x32_bf16 v[136:139], a[100:103], v[68:71], v[136:139]\n"
"v_mfma_f32_16x16x32_bf16 v[136:139], a[96:99], v[64:67], v[136:139]\n"
"s_waitcnt lgkmcnt(1)\n"
"v_mfma_f32_16x16x32_bf16 v[136:139], a[92:95], v[60:63], v[136:139]\n"
"v_mfma_f32_16x16x32_bf16 v[136:139], a[88:91], v[44:47], v[136:139]\n"
"global_store_dwordx4 v247, v[216:219], s[54:55] offset:768 sc1\n"
"ds_write_b64 v244, v[208:209] offset:12288\n"
"v_mfma_f32_16x16x32_bf16 v[136:139], a[84:87], v[40:43], v[136:139]\n"
"v_mfma_f32_16x16x32_bf16 v[136:139], a[80:83], v[36:39], v[136:139]\n"
"v_mfma_f32_16x16x32_bf16 v[132:135], a[128:131], v[36:39], v[8:11]\n"
"v_mfma_f32_16x16x32_bf16 v[132:135], a[132:135], v[40:43], v[132:135]\n"
"v_mfma_f32_16x16x32_bf16 v[132:135], a[136:139], v[44:47], v[132:135]\n"
"v_mfma_f32_16x16x32_bf16 v[132:135], a[140:143], v[60:63], v[132:135]\n"
"v_cvt_pk_bf16_f32 v210, v136, v137\n"
"v_mfma_f32_16x16x32_bf16 v[132:135], a[144:147], v[64:67], v[132:135]\n"
"v_cvt_pk_bf16_f32 v211, v138, v139\n"
"v_mfma_f32_16x16x32_bf16 v[132:135], a[148:151], v[68:71], v[132:135]\n"
"v_mfma_f32_16x16x32_bf16 v[132:135], a[152:155], v[84:87], v[132:135]\n"
"v_mfma_f32_16x16x32_bf16 v[132:135], a[156:159], v[88:91], v[132:135]\n"
"v_mfma_f32_16x16x32_bf16 v[132:135], a[160:163], v[92:95], v[132:135]\n"
"v_mfma_f32_16x16x32_bf16 v[132:135], a[164:167], v[108:111], v[132:135]\n"
"global_store_dwordx4 v252, v[220:223], s[54:55] offset:768 sc1\n"
"ds_write_b64 v244, v[210:211] offset:12296\n"
"v_mfma_f32_16x16x32_bf16 v[132:135], a[168:171], v[112:115], v[132:135]\n"
"v_mfma_f32_16x16x32_bf16 v[132:135], a[172:175], v[116:119], v[132:135]\n"
"v_mfma_f32_16x16x32_bf16 v[136:139], a[220:223], v[116:119], v[12:15]\n"
"v_mfma_f32_16x16x32_bf16 v[136:139], a[216:219], v[112:115], v[136:139]\n"
"v_mfma_f32_16x16x32_bf16 v[136:139], a[212:215], v[108:111], v[136:139]\n"
"v_mfma_f32_16x16x32_bf16 v[136:139], a[208:211], v[92:95], v[136:139]\n"
"v_cvt_pk_bf16_f32 v212, v132, v133\n"
"v_mfma_f32_16x16x32_bf16 v[136:139], a[204:207], v[88:91], v[136:139]\n"
"v_mov_b32 v242, 34\n"
"v_mfma_f32_16x16x32_bf16 v[136:139], a[200:203], v[84:87], v[136:139]\n"
"v_cvt_pk_bf16_f32 v213, v134, v135\n"
"v_mfma_f32_16x16x32_bf16 v[136:139], a[196:199], v[68:71], v[136:139]\n"
"s_waitcnt vmcnt(11) lgkmcnt(2)\n"
"ds_write_b32 v240, v242\n"
"v_mfma_f32_16x16x32_bf16 v[136:139], a[192:195], v[64:67], v[136:139]\n"
"v_mfma_f32_16x16x32_bf16 v[136:139], a[188:191], v[60:63], v[136:139]\n"
"v_mfma_f32_16x16x32_bf16 v[136:139], a[184:187], v[44:47], v[136:139]\n"
"v_mfma_f32_16x16x32_bf16 v[136:139], a[180:183], v[40:43], v[136:139]\n"
"global_store_dwordx4 v253, v[224:227], s[54:55] offset:768 sc1\n"
"ds_write_b64 v245, v[212:213] offset:12288\n"
"v_mfma_f32_16x16x32_bf16 v[136:139], a[176:179], v[36:39], v[136:139]\n"
"s_bitcmp1_b32 s92, 8\n"
"s_cselect_b32 s93, 0x6000, 0\n"
"v_mfma_f32_16x16x32_bf16 v[132:135], a[224:227], v[36:39], v[16:19]\n"
"s_sub_u32 s94, s93, 0xc000\n"
"v_mfma_f32_16x16x32_bf16 v[132:135], a[228:231], v[40:43], v[132:135]\n"
"v_add_u32 v231, s93, v247\n"
"v_mfma_f32_16x16x32_bf16 v[132:135], a[232:235], v[44:47], v[132:135]\n"
"ds_read_b32 v243, v241\n"
"v_mfma_f32_16x16x32_bf16 v[132:135], a[236:239], v[60:63], v[132:135]\n"
"v_cvt_pk_bf16_f32 v214, v136, v137\n"
"v_mfma_f32_16x16x32_bf16 v[132:135], a[0:3], v[64:67], v[132:135]\n"
"v_cvt_pk_bf16_f32 v215, v138, v139\n"
"v_mfma_f32_16x16x32_bf16 v[132:135], a[4:7], v[68:71], v[132:135]\n"
"v_add_u32 v247, s94, v247\n"
"v_mfma_f32_16x16x32_bf16 v[132:135], a[8:11], v[84:87], v[132:135]\n"
"s_waitcnt lgkmcnt(0)\n"
"v_cmp_gt_u32 s[56:57], 34, v243\n"
"v_mfma_f32_16x16x32_bf16 v[132:135], a[12:15], v[88:91], v[132:135]\n"
"v_min_u32 v247, v247, v231\n"
"v_mfma_f32_16x16x32_bf16 v[132:135], a[16:19], v[92:95], v[132:135]\n"
"s_or_b64 s[56:57], s[56:57], vcc\n"
"s_cbranch_scc1 LB34E\n"
"LB34ER:\n"
"v_mfma_f32_16x16x32_bf16 v[132:135], a[20:23], v[108:111], v[132:135]\n"
"ds_write_b64 v245, v[214:215] offset:12296\n"
"v_mfma_f32_16x16x32_bf16 v[132:135], v[140:143], v[112:115], v[132:135]\n"
"v_add_u32 v231, s93, v252\n"
"v_mfma_f32_16x16x32_bf16 v[132:135], v[156:159], v[116:119], v[132:135]\n"
"v_add_u32 v252, s94, v252\n"
"v_mfma_f32_16x16x32_bf16 v[136:139], a[44:47], v[116:119], v[20:23]\n"
"ds_read_b128 v[116:119], v235 offset:40960\n"
"v_mfma_f32_16x16x32_bf16 v[136:139], a[40:43], v[112:115], v[136:139]\n"
"ds_read_b128 v[112:115], v234 offset:40960\n"
"v_mfma_f32_16x16x32_bf16 v[136:139], a[36:39], v[108:111], v[136:139]\n"
"ds_read_b128 v[108:111], v233 offset:40960\n"
"v_mfma_f32_16x16x32_bf16 v[136:139], a[32:35], v[92:95], v[136:139]\n"
"ds_read_b128 v[92:95], v232 offset:40960\n"
"v_cvt_pk_bf16_f32 v208, v132, v133\n"
"v_mfma_f32_16x16x32_bf16 v[136:139], a[28:31], v[88:91], v[136:139]\n"
"ds_read_b128 v[88:91], v235 offset:32768\n"
"v_cvt_pk_bf16_f32 v209, v134, v135\n"
"v_mfma_f32_16x16x32_bf16 v[136:139], a[24:27], v[84:87], v[136:139]\n"
"ds_read_b128 v[84:87], v234 offset:32768\n"
"v_mfma_f32_16x16x32_bf16 v[136:139], v[204:207], v[68:71], v[136:139]\n"
"ds_read_b128 v[68:71], v233 offset:32768\n"
"v_mfma_f32_16x16x32_bf16 v[136:139], v[188:191], v[64:67], v[136:139]\n"
"ds_read_b128 v[64:67], v232 offset:32768\n"
"v_mfma_f32_16x16x32_bf16 v[136:139], v[184:187], v[60:63], v[136:139]\n"
"ds_read_b128 v[60:63], v235 offset:24576\n"
"v_mfma_f32_16x16x32_bf16 v[136:139], v[180:183], v[44:47], v[136:139]\n"
"ds_read_b128 v[44:47], v234 offset:24576\n"
"v_mfma_f32_16x16x32_bf16 v[136:139], v[164:167], v[40:43], v[136:139]\n"
"ds_read_b128 v[40:43], v233 offset:24576\n"
"v_mfma_f32_16x16x32_bf16 v[136:139], v[160:163], v[36:39], v[136:139]\n"
"ds_read_b128 v[36:39], v232 offset:24576\n"
"s_waitcnt lgkmcnt(8)\n"
"v_mfma_f32_16x16x32_bf16 v[132:135], a[76:79], v[116:119], v[0:3]\n"
"ds_write_b64 v244, v[208:209] offset:12416\n"
"v_mfma_f32_16x16x32_bf16 v[132:135], a[72:75], v[112:115], v[132:135]\n"
"s_add_u32 s54, s26, s48\n"
"s_addc_u32 s55, s27, 0\n"
"v_mfma_f32_16x16x32_bf16 v[132:135], a[68:71], v[108:111], v[132:135]\n"
"v_min_u32 v252, v252, v231\n"
"v_mfma_f32_16x16x32_bf16 v[132:135], a[64:67], v[92:95], v[132:135]\n"
"v_cvt_pk_bf16_f32 v210, v136, v137\n"
"s_waitcnt lgkmcnt(5)\n"
"v_mfma_f32_16x16x32_bf16 v[132:135], a[60:63], v[88:91], v[132:135]\n"
"v_cvt_pk_bf16_f32 v211, v138, v139\n"
"v_mfma_f32_16x16x32_bf16 v[132:135], a[56:59], v[84:87], v[132:135]\n"
"v_add_u32 v231, s93, v253\n"
"v_mfma_f32_16x16x32_bf16 v[132:135], a[52:55], v[68:71], v[132:135]\n"
"ds_read_b128 v[216:219], v246 offset:0\n"
"v_mfma_f32_16x16x32_bf16 v[132:135], a[48:51], v[64:67], v[132:135]\n"
"ds_read_b128 v[220:223], v246 offset:1024\n"
"s_waitcnt lgkmcnt(3)\n"
"v_mfma_f32_16x16x32_bf16 v[132:135], a[252:255], v[60:63], v[132:135]\n"
"ds_read_b128 v[224:227], v246 offset:2048\n"
"v_mfma_f32_16x16x32_bf16 v[132:135], a[248:251], v[44:47], v[132:135]\n"
"ds_write_b64 v244, v[210:211] offset:12424\n"
"v_mfma_f32_16x16x32_bf16 v[132:135], a[244:247], v[40:43], v[132:135]\n"
"v_add_u32 v253, s94, v253\n"
"v_mfma_f32_16x16x32_bf16 v[132:135], a[240:243], v[36:39], v[132:135]\n"
"v_min_u32 v253, v253, v231\n"
"v_mfma_f32_16x16x32_bf16 v[136:139], a[80:83], v[36:39], v[4:7]\n"
"v_mfma_f32_16x16x32_bf16 v[136:139], a[84:87], v[40:43], v[136:139]\n"
"v_mfma_f32_16x16x32_bf16 v[136:139], a[88:91], v[44:47], v[136:139]\n"
"v_mfma_f32_16x16x32_bf16 v[136:139], a[92:95], v[60:63], v[136:139]\n"
"v_cvt_pk_bf16_f32 v208, v132, v133\n"
"v_mfma_f32_16x16x32_bf16 v[136:139], a[96:99], v[64:67], v[136:139]\n"
"v_cvt_pk_bf16_f32 v209, v134, v135\n"
"v_mfma_f32_16x16x32_bf16 v[136:139], a[100:103], v[68:71], v[136:139]\n"
"v_mfma_f32_16x16x32_bf16 v[136:139], a[104:107], v[84:87], v[136:139]\n"
"v_mfma_f32_16x16x32_bf16 v[136:139], a[108:111], v[88:91], v[136:139]\n"
"s_waitcnt lgkmcnt(1)\n"
"v_mfma_f32_16x16x32_bf16 v[136:139], a[112:115], v[92:95], v[136:139]\n"
"v_mfma_f32_16x16x32_bf16 v[136:139], a[116:119], v[108:111], v[136:139]\n"
"global_store_dwordx4 v247, v[216:219], s[54:55] offset:0 sc1\n"
"ds_write_b64 v244, v[208:209] offset:24576\n"
"v_mfma_f32_16x16x32_bf16 v[136:139], a[120:123], v[112:115], v[136:139]\n"
"v_mfma_f32_16x16x32_bf16 v[136:139], a[124:127], v[116:119], v[136:139]\n"
"v_mfma_f32_16x16x32_bf16 v[132:135], a[172:175], v[116:119], v[8:11]\n"
"v_mfma_f32_16x16x32_bf16 v[132:135], a[168:171], v[112:115], v[132:135]\n"
"v_mfma_f32_16x16x32_bf16 v[132:135], a[164:167], v[108:111], v[132:135]\n"
"v_mfma_f32_16x16x32_bf16 v[132:135], a[160:163], v[92:95], v[132:135]\n"
"v_cvt_pk_bf16_f32 v210, v136, v137\n"
"v_mfma_f32_16x16x32_bf16 v[132:135], a[156:159], v[88:91], v[132:135]\n"
"v_cvt_pk_bf16_f32 v211, v138, v139\n"
"v_mfma_f32_16x16x32_bf16 v[132:135], a[152:155], v[84:87], v[132:135]\n"
"v_mfma_f32_16x16x32_bf16 v[132:135], a[148:151], v[68:71], v[132:135]\n"
"v_mfma_f32_16x16x32_bf16 v[132:135], a[144:147], v[64:67], v[132:135]\n"
"v_mfma_f32_16x16x32_bf16 v[132:135], a[140:143], v[60:63], v[132:135]\n"
"v_mfma_f32_16x16x32_bf16 v[132:135], a[136:139], v[44:47], v[132:135]\n"
"global_store_dwordx4 v252, v[220:223], s[54:55] offset:0 sc1\n"
"ds_write_b64 v244, v[210:211] offset:24584\n"
"v_mfma_f32_16x16x32_bf16 v[132:135], a[132:135], v[40:43], v[132:135]\n"
"v_mfma_f32_16x16x32_bf16 v[132:135], a[128:131], v[36:39], v[132:135]\n"
"v_mfma_f32_16x16x32_bf16 v[136:139], a[176:179], v[36:39], v[12:15]\n"
"v_mfma_f32_16x16x32_bf16 v[136:139], a[180:183], v[40:43], v[136:139]\n"
"v_mfma_f32_16x16x32_bf16 v[136:139], a[184:187], v[44:47], v[136:139]\n"
"v_mfma_f32_16x16x32_bf16 v[136:139], a[188:191], v[60:63], v[136:139]\n"
"v_cvt_pk_bf16_f32 v212, v132, v133\n"
"v_mfma_f32_16x16x32_bf16 v[136:139], a[192:195], v[64:67], v[136:139]\n"
"v_mov_b32 v242, 35\n"
"v_mfma_f32_16x16x32_bf16 v[136:139], a[196:199], v[68:71], v[136:139]\n"
"v_cvt_pk_bf16_f32 v213, v134, v135\n"
"v_mfma_f32_16x16x32_bf16 v[136:139], a[200:203], v[84:87], v[136:139]\n"
"s_waitcnt lgkmcnt(2)\n"
"ds_write_b32 v240, v242\n"
"v_mfma_f32_16x16x32_bf16 v[136:139], a[204:207], v[88:91], v[136:139]\n"
"v_mfma_f32_16x16x32_bf16 v[136:139], a[208:211], v[92:95], v[136:139]\n"
"v_mfma_f32_16x16x32_bf16 v[136:139], a[212:215], v[108:111], v[136:139]\n"
"v_mfma_f32_16x16x32_bf16 v[136:139], a[216:219], v[112:115], v[136:139]\n"
"global_store_dwordx4 v253, v[224:227], s[54:55] offset:0 sc1\n"
"ds_write_b64 v245, v[212:213] offset:24576\n"
"v_mfma_f32_16x16x32_bf16 v[136:139], a[220:223], v[116:119], v[136:139]\n"
"v_mfma_f32_16x16x32_bf16 v[132:135], v[156:159], v[116:119], v[16:19]\n"
"v_mfma_f32_16x16x32_bf16 v[132:135], v[140:143], v[112:115], v[132:135]\n"
"v_mfma_f32_16x16x32_bf16 v[132:135], a[20:23], v[108:111], v[132:135]\n"
"ds_read_b32 v243, v241\n"
"v_mfma_f32_16x16x32_bf16 v[132:135], a[16:19], v[92:95], v[132:135]\n"
"v_cvt_pk_bf16_f32 v214, v136, v137\n"
"v_mfma_f32_16x16x32_bf16 v[132:135], a[12:15], v[88:91], v[132:135]\n"
"v_cvt_pk_bf16_f32 v215, v138, v139\n"
"v_mfma_f32_16x16x32_bf16 v[132:135], a[8:11], v[84:87], v[132:135]\n"
"v_mfma_f32_16x16x32_bf16 v[132:135], a[4:7], v[68:71], v[132:135]\n"
"s_waitcnt lgkmcnt(0)\n"
"v_cmp_gt_u32 s[56:57], 35, v243\n"
"v_mfma_f32_16x16x32_bf16 v[132:135], a[0:3], v[64:67], v[132:135]\n"
"v_mfma_f32_16x16x32_bf16 v[132:135], a[236:239], v[60:63], v[132:135]\n"
"s_cmp_lg_u64 s[56:57], 0\n"
"s_cbranch_scc1 LB35E\n"
"LB35ER:\n"
"v_mfma_f32_16x16x32_bf16 v[132:135], a[232:235], v[44:47], v[132:135]\n"
"ds_write_b64 v245, v[214:215] offset:24584\n"
"v_mfma_f32_16x16x32_bf16 v[132:135], a[228:231], v[40:43], v[132:135]\n"
"v_mfma_f32_16x16x32_bf16 v[132:135], a[224:227], v[36:39], v[132:135]\n"
"v_mfma_f32_16x16x32_bf16 v[136:139], v[160:163], v[36:39], v[20:23]\n"
"ds_read_b128 v[36:39], v232 offset:24832\n"
"v_mfma_f32_16x16x32_bf16 v[136:139], v[164:167], v[40:43], v[136:139]\n"
"ds_read_b128 v[40:43], v233 offset:24832\n"
"v_mfma_f32_16x16x32_bf16 v[136:139], v[180:183], v[44:47], v[136:139]\n"
"ds_read_b128 v[44:47], v234 offset:24832\n"
"v_mfma_f32_16x16x32_bf16 v[136:139], v[184:187], v[60:63], v[136:139]\n"
"ds_read_b128 v[60:63], v235 offset:24832\n"
"v_cvt_pk_bf16_f32 v208, v132, v133\n"
"v_mfma_f32_16x16x32_bf16 v[136:139], v[188:191], v[64:67], v[136:139]\n"
"ds_read_b128 v[64:67], v232 offset:33024\n"
"v_cvt_pk_bf16_f32 v209, v134, v135\n"
"v_mfma_f32_16x16x32_bf16 v[136:139], v[204:207], v[68:71], v[136:139]\n"
"ds_read_b128 v[68:71], v233 offset:33024\n"
"v_mfma_f32_16x16x32_bf16 v[136:139], a[24:27], v[84:87], v[136:139]\n"
"ds_read_b128 v[84:87], v234 offset:33024\n"
"v_mfma_f32_16x16x32_bf16 v[136:139], a[28:31], v[88:91], v[136:139]\n"
"ds_read_b128 v[88:91], v235 offset:33024\n"
"v_mfma_f32_16x16x32_bf16 v[136:139], a[32:35], v[92:95], v[136:139]\n"
"ds_read_b128 v[92:95], v232 offset:41216\n"
"v_mfma_f32_16x16x32_bf16 v[136:139], a[36:39], v[108:111], v[136:139]\n"
"ds_read_b128 v[108:111], v233 offset:41216\n"
"v_mfma_f32_16x16x32_bf16 v[136:139], a[40:43], v[112:115], v[136:139]\n"
"ds_read_b128 v[112:115], v234 offset:41216\n"
"v_mfma_f32_16x16x32_bf16 v[136:139], a[44:47], v[116:119], v[136:139]\n"
"ds_read_b128 v[116:119], v235 offset:41216\n"
"s_waitcnt lgkmcnt(8)\n"
"v_mfma_f32_16x16x32_bf16 v[132:135], a[240:243], v[36:39], v[0:3]\n"
"ds_write_b64 v244, v[208:209] offset:24704\n"
"v_mfma_f32_16x16x32_bf16 v[132:135], a[244:247], v[40:43], v[132:135]\n"
"v_mfma_f32_16x16x32_bf16 v[132:135], a[248:251], v[44:47], v[132:135]\n"
"v_mfma_f32_16x16x32_bf16 v[132:135], a[252:255], v[60:63], v[132:135]\n"
"v_cvt_pk_bf16_f32 v210, v136, v137\n"
"s_waitcnt lgkmcnt(5)\n"
"v_mfma_f32_16x16x32_bf16 v[132:135], a[48:51], v[64:67], v[132:135]\n"
"v_cvt_pk_bf16_f32 v211, v138, v139\n"
"v_mfma_f32_16x16x32_bf16 v[132:135], a[52:55], v[68:71], v[132:135]\n"
"v_mfma_f32_16x16x32_bf16 v[132:135], a[56:59], v[84:87], v[132:135]\n"
"ds_read_b128 v[216:219], v246 offset:12288\n"
"v_mfma_f32_16x16x32_bf16 v[132:135], a[60:63], v[88:91], v[132:135]\n"
"ds_read_b128 v[220:223], v246 offset:13312\n"
"s_waitcnt lgkmcnt(3)\n"
"v_mfma_f32_16x16x32_bf16 v[132:135], a[64:67], v[92:95], v[132:135]\n"
"ds_read_b128 v[224:227], v246 offset:14336\n"
"v_mfma_f32_16x16x32_bf16 v[132:135], a[68:71], v[108:111], v[132:135]\n"
"ds_write_b64 v244, v[210:211] offset:24712\n"
"v_mfma_f32_16x16x32_bf16 v[132:135], a[72:75], v[112:115], v[132:135]\n"
"v_mfma_f32_16x16x32_bf16 v[132:135], a[76:79], v[116:119], v[132:135]\n"
"v_mfma_f32_16x16x32_bf16 v[136:139], a[124:127], v[116:119], v[4:7]\n"
"v_mfma_f32_16x16x32_bf16 v[136:139], a[120:123], v[112:115], v[136:139]\n"
"v_mfma_f32_16x16x32_bf16 v[136:139], a[116:119], v[108:111], v[136:139]\n"
"v_mfma_f32_16x16x32_bf16 v[136:139], a[112:115], v[92:95], v[136:139]\n"
"v_cvt_pk_bf16_f32 v208, v132, v133\n"
"v_mfma_f32_16x16x32_bf16 v[136:139], a[108:111], v[88:91], v[136:139]\n"
"v_cvt_pk_bf16_f32 v209, v134, v135\n"
"v_mfma_f32_16x16x32_bf16 v[136:139], a[104:107], v[84:87], v[136:139]\n"
"v_mfma_f32_16x16x32_bf16 v[136:139], a[100:103], v[68:71], v[136:139]\n"
"v_mfma_f32_16x16x32_bf16 v[136:139], a[96:99], v[64:67], v[136:139]\n"
"s_waitcnt lgkmcnt(1)\n"
"v_mfma_f32_16x16x32_bf16 v[136:139], a[92:95], v[60:63], v[136:139]\n"
"v_mfma_f32_16x16x32_bf16 v[136:139], a[88:91], v[44:47], v[136:139]\n"
"global_store_dwordx4 v247, v[216:219], s[54:55] offset:768 sc1\n"
"ds_write_b64 v244, v[208:209] offset:0\n"
"v_mfma_f32_16x16x32_bf16 v[136:139], a[84:87], v[40:43], v[136:139]\n"
"v_mfma_f32_16x16x32_bf16 v[136:139], a[80:83], v[36:39], v[136:139]\n"
"v_mfma_f32_16x16x32_bf16 v[132:135], a[128:131], v[36:39], v[8:11]\n"
"v_mfma_f32_16x16x32_bf16 v[132:135], a[132:135], v[40:43], v[132:135]\n"
"v_mfma_f32_16x16x32_bf16 v[132:135], a[136:139], v[44:47], v[132:135]\n"
"v_mfma_f32_16x16x32_bf16 v[132:135], a[140:143], v[60:63], v[132:135]\n"
"v_cvt_pk_bf16_f32 v210, v136, v137\n"
"v_mfma_f32_16x16x32_bf16 v[132:135], a[144:147], v[64:67], v[132:135]\n"
"v_cvt_pk_bf16_f32 v211, v138, v139\n"
"v_mfma_f32_16x16x32_bf16 v[132:135], a[148:151], v[68:71], v[132:135]\n"
"v_mfma_f32_16x16x32_bf16 v[132:135], a[152:155], v[84:87], v[132:135]\n"
"v_mfma_f32_16x16x32_bf16 v[132:135], a[156:159], v[88:91], v[132:135]\n"
"s_mov_b64 exec, s[10:11]\n"
"s_cbranch_execz LHT15\n"
"LHR15:\n"
"s_mov_b64 exec, -1\n"
"v_mfma_f32_16x16x32_bf16 v[132:135], a[160:163], v[92:95], v[132:135]\n"
"v_mfma_f32_16x16x32_bf16 v[132:135], a[164:167], v[108:111], v[132:135]\n"
"global_store_dwordx4 v252, v[220:223], s[54:55] offset:768 sc1\n"
"ds_write_b64 v244, v[210:211] offset:8\n"
"v_mfma_f32_16x16x32_bf16 v[132:135], a[168:171], v[112:115], v[132:135]\n"
"v_mfma_f32_16x16x32_bf16 v[132:135], a[172:175], v[116:119], v[132:135]\n"
"v_mfma_f32_16x16x32_bf16 v[136:139], a[220:223], v[116:119], v[12:15]\n"
"v_mfma_f32_16x16x32_bf16 v[136:139], a[216:219], v[112:115], v[136:139]\n"
"v_mfma_f32_16x16x32_bf16 v[136:139], a[212:215], v[108:111], v[136:139]\n"
"v_mfma_f32_16x16x32_bf16 v[136:139], a[208:211], v[92:95], v[136:139]\n"
"v_cvt_pk_bf16_f32 v212, v132, v133\n"
"v_mfma_f32_16x16x32_bf16 v[136:139], a[204:207], v[88:91], v[136:139]\n"
"v_mov_b32 v242, 36\n"
"v_mfma_f32_16x16x32_bf16 v[136:139], a[200:203], v[84:87], v[136:139]\n"
"v_cvt_pk_bf16_f32 v213, v134, v135\n"
"v_mfma_f32_16x16x32_bf16 v[136:139], a[196:199], v[68:71], v[136:139]\n"
"s_waitcnt lgkmcnt(2)\n"
"ds_write_b32 v240, v242\n"
"v_mfma_f32_16x16x32_bf16 v[136:139], a[192:195], v[64:67], v[136:139]\n"
"v_mfma_f32_16x16x32_bf16 v[136:139], a[188:191], v[60:63], v[136:139]\n"
"v_mfma_f32_16x16x32_bf16 v[136:139], a[184:187], v[44:47], v[136:139]\n"
"v_mfma_f32_16x16x32_bf16 v[136:139], a[180:183], v[40:43], v[136:139]\n"
"global_store_dwordx4 v253, v[224:227], s[54:55] offset:768 sc1\n"
"ds_write_b64 v245, v[212:213] offset:0\n"
"v_mfma_f32_16x16x32_bf16 v[136:139], a[176:179], v[36:39], v[136:139]\n"
"v_mfma_f32_16x16x32_bf16 v[132:135], a[224:227], v[36:39], v[16:19]\n"
"v_mfma_f32_16x16x32_bf16 v[132:135], a[228:231], v[40:43], v[132:135]\n"
"v_mfma_f32_16x16x32_bf16 v[132:135], a[232:235], v[44:47], v[132:135]\n"
"ds_read_b32 v243, v241\n"
"v_mfma_f32_16x16x32_bf16 v[132:135], a[236:239], v[60:63], v[132:135]\n"
"v_cvt_pk_bf16_f32 v214, v136, v137\n"
"v_mfma_f32_16x16x32_bf16 v[132:135], a[0:3], v[64:67], v[132:135]\n"
"v_cvt_pk_bf16_f32 v215, v138, v139\n"
"v_mfma_f32_16x16x32_bf16 v[132:135], a[4:7], v[68:71], v[132:135]\n"
"v_mfma_f32_16x16x32_bf16 v[132:135], a[8:11], v[84:87], v[132:135]\n"
"s_waitcnt lgkmcnt(0)\n"
"v_cmp_gt_u32 s[56:57], 36, v243\n"
"v_mfma_f32_16x16x32_bf16 v[132:135], a[12:15], v[88:91], v[132:135]\n"
"v_mfma_f32_16x16x32_bf16 v[132:135], a[16:19], v[92:95], v[132:135]\n"
"s_cmp_lg_u64 s[56:57], 0\n"
"s_cbranch_scc1 LB36E\n"
"LB36ER:\n"
"v_mfma_f32_16x16x32_bf16 v[132:135], a[20:23], v[108:111], v[132:135]\n"
"ds_write_b64 v245, v[214:215] offset:8\n"
"v_mfma_f32_16x16x32_bf16 v[132:135], v[140:143], v[112:115], v[132:135]\n"
"v_mfma_f32_16x16x32_bf16 v[132:135], v[156:159], v[116:119], v[132:135]\n"
"v_mfma_f32_16x16x32_bf16 v[136:139], a[44:47], v[116:119], v[20:23]\n"
"v_mfma_f32_16x16x32_bf16 v[136:139], a[40:43], v[112:115], v[136:139]\n"
"v_mfma_f32_16x16x32_bf16 v[136:139], a[36:39], v[108:111], v[136:139]\n"
"LHT15:\n"
"s_cbranch_execz LHR15\n"
"v_mfma_f32_16x16x32_bf16 v[136:139], a[32:35], v[92:95], v[136:139]\n"
"v_cvt_pk_bf16_f32 v208, v132, v133\n"
"v_mfma_f32_16x16x32_bf16 v[136:139], a[28:31], v[88:91], v[136:139]\n"
"v_cvt_pk_bf16_f32 v209, v134, v135\n"
"v_mfma_f32_16x16x32_bf16 v[136:139], a[24:27], v[84:87], v[136:139]\n"
"v_mfma_f32_16x16x32_bf16 v[136:139], v[204:207], v[68:71], v[136:139]\n"
"v_mfma_f32_16x16x32_bf16 v[136:139], v[188:191], v[64:67], v[136:139]\n"
"v_mfma_f32_16x16x32_bf16 v[136:139], v[184:187], v[60:63], v[136:139]\n"
"v_mfma_f32_16x16x32_bf16 v[136:139], v[180:183], v[44:47], v[136:139]\n"
"ds_write_b64 v244, v[208:209] offset:128\n"
"v_mfma_f32_16x16x32_bf16 v[136:139], v[164:167], v[40:43], v[136:139]\n"
"v_mfma_f32_16x16x32_bf16 v[136:139], v[160:163], v[36:39], v[136:139]\n"
"s_add_u32 s54, s26, s49\n"
"s_addc_u32 s55, s27, 0\n"
"ds_read_b128 v[216:219], v246 offset:24576\n"
"ds_read_b128 v[220:223], v246 offset:25600\n"
"ds_read_b128 v[224:227], v246 offset:26624\n"
"s_nop 3\n"
"v_cvt_pk_bf16_f32 v210, v136, v137\n"
"v_cvt_pk_bf16_f32 v211, v138, v139\n"
"ds_write_b128 v244, v[208:211] offset:128\n"
"s_waitcnt lgkmcnt(3)\n"
"global_store_dwordx4 v247, v[216:219], s[54:55] offset:0 sc1\n"
"s_waitcnt lgkmcnt(2)\n"
"global_store_dwordx4 v252, v[220:223], s[54:55] offset:0 sc1\n"
"s_waitcnt lgkmcnt(1)\n"
"global_store_dwordx4 v253, v[224:227], s[54:55] offset:0 sc1\n"
"s_waitcnt lgkmcnt(0)\n"
"s_barrier\n"
"ds_read_b128 v[216:219], v246 offset:0\n"
"ds_read_b128 v[220:223], v246 offset:1024\n"
"ds_read_b128 v[224:227], v246 offset:2048\n"
"s_waitcnt lgkmcnt(2)\n"
"global_store_dwordx4 v247, v[216:219], s[54:55] offset:768 sc1\n"
"s_waitcnt lgkmcnt(1)\n"
"global_store_dwordx4 v252, v[220:223], s[54:55] offset:768 sc1\n"
"s_waitcnt lgkmcnt(0)\n"
"global_store_dwordx4 v253, v[224:227], s[54:55] offset:768 sc1\n"
"s_branch LFIN\n"
"LB2_0:\n"
"ds_read_b32 v243, v241\n"
"s_waitcnt lgkmcnt(0)\n"
"v_cmp_gt_u32 s[56:57], 2, v243\n"
"s_nop 3\n"
"s_cmp_lg_u64 s[56:57], 0\n"
"s_cbranch_scc1 LB2_0\n"
"s_branch LB2_0R\n"
"LB3:\n"
"s_cbranch_vccz LB3C\n"
"s_sleep 7\n"
"LB3C:\n"
"ds_read_b32 v243, v241\n"
"s_waitcnt lgkmcnt(0)\n"
"v_cmp_gt_u32 s[56:57], 3, v243\n"
"s_nop 3\n"
"s_cmp_lg_u64 s[56:57], 0\n"
"s_cbranch_scc1 LB3C\n"
"s_branch LB3R\n"
"LB4:\n"
"s_cbranch_vccz LB4C\n"
"s_sleep 7\n"
"LB4C:\n"
"ds_read_b32 v243, v241\n"
"s_waitcnt lgkmcnt(0)\n"
"v_cmp_gt_u32 s[56:57], 4, v243\n"
"s_nop 3\n"
"s_cmp_lg_u64 s[56:57], 0\n"
"s_cbranch_scc1 LB4C\n"
"s_branch LB4R\n"
"LB5:\n"
"s_cbranch_vccz LB5C\n"
"s_sleep 7\n"
"LB5C:\n"
"ds_read_b32 v243, v241\n"
"s_waitcnt lgkmcnt(0)\n"
"v_cmp_gt_u32 s[56:57], 5, v243\n"
"s_nop 3\n"
"s_cmp_lg_u64 s[56:57], 0\n"
"s_cbranch_scc1 LB5C\n"
"s_branch LB5R\n"
"LB6:\n"
"s_cbranch_vccz LB6C\n"
"s_sleep 7\n"
"LB6C:\n"
"ds_read_b32 v243, v241\n"
"s_waitcnt lgkmcnt(0)\n"
"v_cmp_gt_u32 s[56:57], 6, v243\n"
"s_nop 3\n"
"s_cmp_lg_u64 s[56:57], 0\n"
"s_cbranch_scc1 LB6C\n"
"s_branch LB6R\n"
"LB7:\n"
"s_cbranch_vccz LB7C\n"
"s_sleep 7\n"
"LB7C:\n"
"ds_read_b32 v243, v241\n"
"s_waitcnt lgkmcnt(0)\n"
"v_cmp_gt_u32 s[56:57], 7, v243\n"
"s_nop 3\n"
"s_cmp_lg_u64 s[56:57], 0\n"
"s_cbranch_scc1 LB7C\n"
"s_branch LB7R\n"
"LB8:\n"
"s_cbranch_vccz LB8C\n"
"s_sleep 7\n"
"LB8C:\n"
"ds_read_b32 v243, v241\n"
"s_waitcnt lgkmcnt(0)\n"
"v_cmp_gt_u32 s[56:57], 8, v243\n"
"s_nop 3\n"
"s_cmp_lg_u64 s[56:57], 0\n"
"s_cbranch_scc1 LB8C\n"
"s_branch LB8R\n"
"LB9:\n"
"s_cbranch_vccz LB9C\n"
"s_sleep 7\n"
"LB9C:\n"
"ds_read_b32 v243, v241\n"
"s_waitcnt lgkmcnt(0)\n"
"v_cmp_gt_u32 s[56:57], 9, v243\n"
"s_nop 3\n"
"s_cmp_lg_u64 s[56:57], 0\n"
"s_cbranch_scc1 LB9C\n"
"s_branch LB9R\n"
"LB10:\n"
"s_cbranch_vccz LB10C\n"
"s_sleep 7\n"
"LB10C:\n"
"ds_read_b32 v243, v241\n"
"s_waitcnt lgkmcnt(0)\n"
"v_cmp_gt_u32 s[56:57], 10, v243\n"
"s_nop 3\n"
"s_cmp_lg_u64 s[56:57], 0\n"
"s_cbranch_scc1 LB10C\n"
"s_branch LB10R\n"
"LB11:\n"
"s_cbranch_vccz LB11C\n"
"s_sleep 7\n"
"LB11C:\n"
"ds_read_b32 v243, v241\n"
"s_waitcnt lgkmcnt(0)\n"
"v_cmp_gt_u32 s[56:57], 11, v243\n"
"s_nop 3\n"
"s_cmp_lg_u64 s[56:57], 0\n"
"s_cbranch_scc1 LB11C\n"
"s_branch LB11R\n"
"LB12:\n"
"s_cbranch_vccz LB12C\n"
"s_sleep 7\n"
"LB12C:\n"
"ds_read_b32 v243, v241\n"
"s_waitcnt lgkmcnt(0)\n"
"v_cmp_gt_u32 s[56:57], 12, v243\n"
"s_nop 3\n"
"s_cmp_lg_u64 s[56:57], 0\n"
"s_cbranch_scc1 LB12C\n"
"s_branch LB12R\n"
"LB13:\n"
"s_cbranch_vccz LB13C\n"
"s_sleep 7\n"
"LB13C:\n"
"ds_read_b32 v243, v241\n"
"s_waitcnt lgkmcnt(0)\n"
"v_cmp_gt_u32 s[56:57], 13, v243\n"
"s_nop 3\n"
"s_cmp_lg_u64 s[56:57], 0\n"
"s_cbranch_scc1 LB13C\n"
"s_branch LB13R\n"
"LB14:\n"
"s_cbranch_vccz LB14C\n"
"s_sleep 7\n"
"LB14C:\n"
"ds_read_b32 v243, v241\n"
"s_waitcnt lgkmcnt(0)\n"
"v_cmp_gt_u32 s[56:57], 14, v243\n"
"s_nop 3\n"
"s_cmp_lg_u64 s[56:57], 0\n"
"s_cbranch_scc1 LB14C\n"
"s_branch LB14R\n"
"LB15:\n"
"s_cbranch_vccz LB15C\n"
"s_sleep 7\n"
"LB15C:\n"
"ds_read_b32 v243, v241\n"
"s_waitcnt lgkmcnt(0)\n"
"v_cmp_gt_u32 s[56:57], 15, v243\n"
"s_nop 3\n"
"s_cmp_lg_u64 s[56:57], 0\n"
"s_cbranch_scc1 LB15C\n"
"s_branch LB15R\n"
"LB16:\n"
"s_cbranch_vccz LB16C\n"
"s_sleep 7\n"
"LB16C:\n"
"ds_read_b32 v243, v241\n"
"s_waitcnt lgkmcnt(0)\n"
"v_cmp_gt_u32 s[56:57], 16, v243\n"
"s_nop 3\n"
"s_cmp_lg_u64 s[56:57], 0\n"
"s_cbranch_scc1 LB16C\n"
"s_branch LB16R\n"
"LB17:\n"
"s_cbranch_vccz LB17C\n"
"s_sleep 7\n"
"LB17C:\n"
"ds_read_b32 v243, v241\n"
"s_waitcnt lgkmcnt(0)\n"
"v_cmp_gt_u32 s[56:57], 17, v243\n"
"s_nop 3\n"
"s_cmp_lg_u64 s[56:57], 0\n"
"s_cbranch_scc1 LB17C\n"
"s_branch LB17R\n"
"LB18:\n"
"s_cbranch_vccz LB18C\n"
"s_sleep 7\n"
"LB18C:\n"
"ds_read_b32 v243, v241\n"
"s_waitcnt lgkmcnt(0)\n"
"v_cmp_gt_u32 s[56:57], 18, v243\n"
"s_nop 3\n"
"s_cmp_lg_u64 s[56:57], 0\n"
"s_cbranch_scc1 LB18C\n"
"s_branch LB18R\n"
"LB19:\n"
"s_cbranch_vccz LB19C\n"
"s_sleep 7\n"
"LB19C:\n"
"ds_read_b32 v243, v241\n"
"s_waitcnt lgkmcnt(0)\n"
"v_cmp_gt_u32 s[56:57], 19, v243\n"
"s_nop 3\n"
"s_cmp_lg_u64 s[56:57], 0\n"
"s_cbranch_scc1 LB19C\n"
"s_branch LB19R\n"
"LB20:\n"
"s_cbranch_vccz LB20C\n"
"s_sleep 7\n"
"LB20C:\n"
"ds_read_b32 v243, v241\n"
"s_waitcnt lgkmcnt(0)\n"
"v_cmp_gt_u32 s[56:57], 20, v243\n"
"s_nop 3\n"
"s_cmp_lg_u64 s[56:57], 0\n"
"s_cbranch_scc1 LB20C\n"
"s_branch LB20R\n"
"LB21:\n"
"s_cbranch_vccz LB21C\n"
"s_sleep 7\n"
"LB21C:\n"
"ds_read_b32 v243, v241\n"
"s_waitcnt lgkmcnt(0)\n"
"v_cmp_gt_u32 s[56:57], 21, v243\n"
"s_nop 3\n"
"s_cmp_lg_u64 s[56:57], 0\n"
"s_cbranch_scc1 LB21C\n"
"s_branch LB21R\n"
"LB22:\n"
"s_cbranch_vccz LB22C\n"
"s_sleep 7\n"
"LB22C:\n"
"ds_read_b32 v243, v241\n"
"s_waitcnt lgkmcnt(0)\n"
"v_cmp_gt_u32 s[56:57], 22, v243\n"
"s_nop 3\n"
"s_cmp_lg_u64 s[56:57], 0\n"
"s_cbranch_scc1 LB22C\n"
"s_branch LB22R\n"
"LB23:\n"
"s_cbranch_vccz LB23C\n"
"s_sleep 7\n"
"LB23C:\n"
"ds_read_b32 v243, v241\n"
"s_waitcnt lgkmcnt(0)\n"
"v_cmp_gt_u32 s[56:57], 23, v243\n"
"s_nop 3\n"
"s_cmp_lg_u64 s[56:57], 0\n"
"s_cbranch_scc1 LB23C\n"
"s_branch LB23R\n"
"LB24:\n"
"s_cbranch_vccz LB24C\n"
"s_sleep 7\n"
"LB24C:\n"
"ds_read_b32 v243, v241\n"
"s_waitcnt lgkmcnt(0)\n"
"v_cmp_gt_u32 s[56:57], 24, v243\n"
"s_nop 3\n"
"s_cmp_lg_u64 s[56:57], 0\n"
"s_cbranch_scc1 LB24C\n"
"s_branch LB24R\n"
"LB25:\n"
"s_cbranch_vccz LB25C\n"
"s_sleep 7\n"
"LB25C:\n"
"ds_read_b32 v243, v241\n"
"s_waitcnt lgkmcnt(0)\n"
"v_cmp_gt_u32 s[56:57], 25, v243\n"
"s_nop 3\n"
"s_cmp_lg_u64 s[56:57], 0\n"
"s_cbranch_scc1 LB25C\n"
"s_branch LB25R\n"
"LB26:\n"
"s_cbranch_vccz LB26C\n"
"s_sleep 7\n"
"LB26C:\n"
"ds_read_b32 v243, v241\n"
"s_waitcnt lgkmcnt(0)\n"
"v_cmp_gt_u32 s[56:57], 26, v243\n"
"s_nop 3\n"
"s_cmp_lg_u64 s[56:57], 0\n"
"s_cbranch_scc1 LB26C\n"
"s_branch LB26R\n"
"LB27:\n"
"s_cbranch_vccz LB27C\n"
"s_sleep 7\n"
"LB27C:\n"
"ds_read_b32 v243, v241\n"
"s_waitcnt lgkmcnt(0)\n"
"v_cmp_gt_u32 s[56:57], 27, v243\n"
"s_nop 3\n"
"s_cmp_lg_u64 s[56:57], 0\n"
"s_cbranch_scc1 LB27C\n"
"s_branch LB27R\n"
"LB28:\n"
"s_cbranch_vccz LB28C\n"
"s_sleep 7\n"
"LB28C:\n"
"ds_read_b32 v243, v241\n"
"s_waitcnt lgkmcnt(0)\n"
"v_cmp_gt_u32 s[56:57], 28, v243\n"
"s_nop 3\n"
"s_cmp_lg_u64 s[56:57], 0\n"
"s_cbranch_scc1 LB28C\n"
"s_branch LB28R\n"
"LB29:\n"
"s_cbranch_vccz LB29C\n"
"s_sleep 7\n"
"LB29C:\n"
"ds_read_b32 v243, v241\n"
"s_waitcnt lgkmcnt(0)\n"
"v_cmp_gt_u32 s[56:57], 29, v243\n"
"s_nop 3\n"
"s_cmp_lg_u64 s[56:57], 0\n"
"s_cbranch_scc1 LB29C\n"
"s_branch LB29R\n"
"LB30:\n"
"s_cbranch_vccz LB30C\n"
"s_sleep 7\n"
"LB30C:\n"
"ds_read_b32 v243, v241\n"
"s_waitcnt lgkmcnt(0)\n"
"v_cmp_gt_u32 s[56:57], 30, v243\n"
"s_nop 3\n"
"s_cmp_lg_u64 s[56:57], 0\n"
"s_cbranch_scc1 LB30C\n"
"s_branch LB30R\n"
"LB31:\n"
"s_cbranch_vccz LB31C\n"
"s_sleep 7\n"
"LB31C:\n"
"ds_read_b32 v243, v241\n"
"s_waitcnt lgkmcnt(0)\n"
"v_cmp_gt_u32 s[56:57], 31, v243\n"
"s_nop 3\n"
"s_cmp_lg_u64 s[56:57], 0\n"
"s_cbranch_scc1 LB31C\n"
"s_branch LB31R\n"
"LB32:\n"
"s_cbranch_vccz LB32C\n"
"s_sleep 7\n"
"LB32C:\n"
"ds_read_b32 v243, v241\n"
"s_waitcnt lgkmcnt(0)\n"
"v_cmp_gt_u32 s[56:57], 32, v243\n"
"s_nop 3\n"
"s_cmp_lg_u64 s[56:57], 0\n"
"s_cbranch_scc1 LB32C\n"
"s_branch LB32R\n"
"LB33:\n"
"s_cbranch_vccz LB33C\n"
"s_sleep 7\n"
"LB33C:\n"
"ds_read_b32 v243, v241\n"
"s_waitcnt lgkmcnt(0)\n"
"v_cmp_gt_u32 s[56:57], 33, v243\n"
"s_nop 3\n"
"s_cmp_lg_u64 s[56:57], 0\n"
"s_cbranch_scc1 LB33C\n"
"s_branch LB33R\n"
"LB34:\n"
"s_cbranch_vccz LB34C\n"
"s_sleep 7\n"
"LB34C:\n"
"ds_read_b32 v243, v241\n"
"s_waitcnt lgkmcnt(0)\n"
"v_cmp_gt_u32 s[56:57], 34, v243\n"
"s_nop 3\n"
"s_cmp_lg_u64 s[56:57], 0\n"
"s_cbranch_scc1 LB34C\n"
"s_branch LB34R\n"
"LB35:\n"
"ds_read_b32 v243, v241\n"
"s_waitcnt lgkmcnt(0)\n"
"v_cmp_gt_u32 s[56:57], 35, v243\n"
"s_nop 3\n"
"s_cmp_lg_u64 s[56:57], 0\n"
"s_cbranch_scc1 LB35\n"
"s_branch LB35R\n"
"LB36:\n"
"ds_read_b32 v243, v241\n"
"s_waitcnt lgkmcnt(0)\n"
"v_cmp_gt_u32 s[56:57], 36, v243\n"
"s_nop 3\n"
"s_cmp_lg_u64 s[56:57], 0\n"
"s_cbranch_scc1 LB36\n"
"s_branch LB36R\n"
"LB2_1:\n"
"ds_read_b32 v243, v241\n"
"s_waitcnt lgkmcnt(0)\n"
"v_cmp_gt_u32 s[56:57], 2, v243\n"
"s_nop 3\n"
"s_cmp_lg_u64 s[56:57], 0\n"
"s_cbranch_scc1 LB2_1\n"
"s_branch LB2_1R\n"
"LB29E:\n"
"s_cbranch_vccz LB29EC\n"
"s_sleep 7\n"
"LB29EC:\n"
"ds_read_b32 v243, v241\n"
"s_waitcnt lgkmcnt(0)\n"
"v_cmp_gt_u32 s[56:57], 29, v243\n"
"s_nop 3\n"
"s_cmp_lg_u64 s[56:57], 0\n"
"s_cbranch_scc1 LB29EC\n"
"s_branch LB29ER\n"
"LB30E:\n"
"s_cbranch_vccz LB30EC\n"
"s_sleep 7\n"
"LB30EC:\n"
"ds_read_b32 v243, v241\n"
"s_waitcnt lgkmcnt(0)\n"
"v_cmp_gt_u32 s[56:57], 30, v243\n"
"s_nop 3\n"
"s_cmp_lg_u64 s[56:57], 0\n"
"s_cbranch_scc1 LB30EC\n"
"s_branch LB30ER\n"
"LB31E:\n"
"s_cbranch_vccz LB31EC\n"
"s_sleep 7\n"
"LB31EC:\n"
"ds_read_b32 v243, v241\n"
"s_waitcnt lgkmcnt(0)\n"
"v_cmp_gt_u32 s[56:57], 31, v243\n"
"s_nop 3\n"
"s_cmp_lg_u64 s[56:57], 0\n"
"s_cbranch_scc1 LB31EC\n"
"s_branch LB31ER\n"
"LB32E:\n"
"s_cbranch_vccz LB32EC\n"
"s_sleep 7\n"
"LB32EC:\n"
"ds_read_b32 v243, v241\n"
"s_waitcnt lgkmcnt(0)\n"
"v_cmp_gt_u32 s[56:57], 32, v243\n"
"s_nop 3\n"
"s_cmp_lg_u64 s[56:57], 0\n"
"s_cbranch_scc1 LB32EC\n"
"s_branch LB32ER\n"
"LB33E:\n"
"s_cbranch_vccz LB33EC\n"
"s_sleep 7\n"
"LB33EC:\n"
"ds_read_b32 v243, v241\n"
"s_waitcnt lgkmcnt(0)\n"
"v_cmp_gt_u32 s[56:57], 33, v243\n"
"s_nop 3\n"
"s_cmp_lg_u64 s[56:57], 0\n"
"s_cbranch_scc1 LB33EC\n"
"s_branch LB33ER\n"
"LB34E:\n"
"s_cbranch_vccz LB34EC\n"
"s_sleep 7\n"
"LB34EC:\n"
"ds_read_b32 v243, v241\n"
"s_waitcnt lgkmcnt(0)\n"
"v_cmp_gt_u32 s[56:57], 34, v243\n"
"s_nop 3\n"
"s_cmp_lg_u64 s[56:57], 0\n"
"s_cbranch_scc1 LB34EC\n"
"s_branch LB34ER\n"
"LB35E:\n"
"ds_read_b32 v243, v241\n"
"s_waitcnt lgkmcnt(0)\n"
"v_cmp_gt_u32 s[56:57], 35, v243\n"
"s_nop 3\n"
"s_cmp_lg_u64 s[56:57], 0\n"
"s_cbranch_scc1 LB35E\n"
"s_branch LB35ER\n"
"LB36E:\n"
"ds_read_b32 v243, v241\n"
"s_waitcnt lgkmcnt(0)\n"
"v_cmp_gt_u32 s[56:57], 36, v243\n"
"s_nop 3\n"
"s_cmp_lg_u64 s[56:57], 0\n"
"s_cbranch_scc1 LB36E\n"
"s_branch LB36ER\n"
"LCEND:\n"

      : "+{s[0:1]}"(ka), "+{s2}"(b), "+{v0}"(t)
      :
      : "memory", "scc", "vcc", "v1", "v2", "v3", "v4", "v5", "v6", "v7", "v8", "v9", "v10", "v11", "v12", "v13", "v14", "v15", "v16", "v17", "v18", "v19", "v20", "v21", "v22", "v23", "v24", "v25", "v26", "v27", "v28", "v29", "v30", "v31", "v32", "v33", "v34", "v35", "v36", "v37", "v38", "v39", "v40", "v41", "v42", "v43", "v44", "v45", "v46", "v47", "v48", "v49", "v50", "v51", "v52", "v53", "v54", "v55", "v56", "v57", "v58", "v59", "v60", "v61", "v62", "v63", "v64", "v65", "v66", "v67", "v68", "v69", "v70", "v71", "v72", "v73", "v74", "v75", "v76", "v77", "v78", "v79", "v80", "v81", "v82", "v83", "v84", "v85", "v86", "v87", "v88", "v89", "v90", "v91", "v92", "v93", "v94", "v95", "v96", "v97", "v98", "v99", "v100", "v101", "v102", "v103", "v104", "v105", "v106", "v107", "v108", "v109", "v110", "v111", "v112", "v113", "v114", "v115", "v116", "v117", "v118", "v119", "v120", "v121", "v122", "v123", "v124", "v125", "v126", "v127", "v128", "v129", "v130", "v131", "v132", "v133", "v134", "v135", "v136", "v137", "v138", "v139", "v140", "v141", "v142", "v143", "v144", "v145", "v146", "v147", "v148", "v149", "v150", "v151", "v152", "v153", "v154", "v155", "v156", "v157", "v158", "v159", "v160", "v161", "v162", "v163", "v164", "v165", "v166", "v167", "v168", "v169", "v170", "v171", "v172", "v173", "v174", "v175", "v176", "v177", "v178", "v179", "v180", "v181", "v182", "v183", "v184", "v185", "v186", "v187", "v188", "v189", "v190", "v191", "v192", "v193", "v194", "v195", "v196", "v197", "v198", "v199", "v200", "v201", "v202", "v203", "v204", "v205", "v206", "v207", "v208", "v209", "v210", "v211", "v212", "v213", "v214", "v215", "v216", "v217", "v218", "v219", "v220", "v221", "v222", "v223", "v224", "v225", "v226", "v227", "v228", "v229", "v230", "v231", "v232", "v233", "v234", "v235", "v236", "v237", "v238", "v239", "v240", "v241", "v242", "v243", "v244", "v245", "v246", "v247", "v248", "v249", "v250", "v251", "v252", "v253", "v254", "v255", "a0", "a1", "a2", "a3", "a4", "a5", "a6", "a7", "a8", "a9", "a10", "a11", "a12", "a13", "a14", "a15", "a16", "a17", "a18", "a19", "a20", "a21", "a22", "a23", "a24", "a25", "a26", "a27", "a28", "a29", "a30", "a31", "a32", "a33", "a34", "a35", "a36", "a37", "a38", "a39", "a40", "a41", "a42", "a43", "a44", "a45", "a46", "a47", "a48", "a49", "a50", "a51", "a52", "a53", "a54", "a55", "a56", "a57", "a58", "a59", "a60", "a61", "a62", "a63", "a64", "a65", "a66", "a67", "a68", "a69", "a70", "a71", "a72", "a73", "a74", "a75", "a76", "a77", "a78", "a79", "a80", "a81", "a82", "a83", "a84", "a85", "a86", "a87", "a88", "a89", "a90", "a91", "a92", "a93", "a94", "a95", "a96", "a97", "a98", "a99", "a100", "a101", "a102", "a103", "a104", "a105", "a106", "a107", "a108", "a109", "a110", "a111", "a112", "a113", "a114", "a115", "a116", "a117", "a118", "a119", "a120", "a121", "a122", "a123", "a124", "a125", "a126", "a127", "a128", "a129", "a130", "a131", "a132", "a133", "a134", "a135", "a136", "a137", "a138", "a139", "a140", "a141", "a142", "a143", "a144", "a145", "a146", "a147", "a148", "a149", "a150", "a151", "a152", "a153", "a154", "a155", "a156", "a157", "a158", "a159", "a160", "a161", "a162", "a163", "a164", "a165", "a166", "a167", "a168", "a169", "a170", "a171", "a172", "a173", "a174", "a175", "a176", "a177", "a178", "a179", "a180", "a181", "a182", "a183", "a184", "a185", "a186", "a187", "a188", "a189", "a190", "a191", "a192", "a193", "a194", "a195", "a196", "a197", "a198", "a199", "a200", "a201", "a202", "a203", "a204", "a205", "a206", "a207", "a208", "a209", "a210", "a211", "a212", "a213", "a214", "a215", "a216", "a217", "a218", "a219", "a220", "a221", "a222", "a223", "a224", "a225", "a226", "a227", "a228", "a229", "a230", "a231", "a232", "a233", "a234", "a235", "a236", "a237", "a238", "a239", "a240", "a241", "a242", "a243", "a244", "a245", "a246", "a247", "a248", "a249", "a250", "a251", "a252", "a253", "a254", "a255", "s3", "s4", "s5", "s6", "s7", "s8", "s9", "s10", "s11", "s12", "s13", "s14", "s15", "s16", "s17", "s18", "s19", "s20", "s21", "s22", "s23", "s24", "s25", "s26", "s27", "s28", "s29", "s30", "s31", "s32", "s33", "s34", "s35", "s36", "s37", "s38", "s39", "s40", "s41", "s42", "s43", "s44", "s45", "s46", "s47", "s48", "s49", "s50", "s51", "s52", "s53", "s54", "s55", "s56", "s57", "s58", "s59", "s60", "s61", "s62", "s63", "s64", "s65", "s66", "s67", "s68", "s69", "s70", "s71", "s72", "s73", "s74", "s75", "s76", "s77", "s78", "s79", "s80", "s81", "s82", "s83", "s84", "s85", "s86", "s87", "s88", "s89", "s90", "s91", "s92", "s93", "s94", "s95", "s96", "s97", "s98", "s99", "s100", "s101");
}

extern "C" void kernel_launch(void* const* d_in, const int* in_sizes, int n_in,
                              void* d_out, int out_size, void* d_ws, size_t ws_size,
                              hipStream_t stream) {
  (void)hipFuncSetAttribute((const void*)fpn_kernel, hipFuncAttributeMaxDynamicSharedMemorySize, 159808);
  fpn_kernel<<<256, 256, 159808, stream>>>((const char*)d_in[0], (const char*)d_in[1], (const char*)d_in[2],
                                                (char*)d_out, (char*)d_ws, (const char*)d_in[3]);
}
```
